```python
import torch

def _softmax(x, axis=-1):
    return torch.softmax(x, dim=axis)
def _gelu(x):
    return torch.nn.functional.gelu(x, approximate="tanh")
def _silu(x):
    return torch.nn.functional.silu(x)
def _sigmoid(x):
    return torch.sigmoid(x)
def _layernorm(x, axis=-1, eps=1e-5):
    m = x.mean(dim=axis, keepdim=True)
    v = x.var(dim=axis, keepdim=True, unbiased=False)
    return (x - m) / torch.sqrt(v + eps)
def _scatter_add(tgt, idx, src, axis=0):
    return tgt.clone().index_add_(axis, idx, src)
def _segment_sum(src, seg, n):
    out = torch.zeros((n,) + src.shape[1:], dtype=src.dtype)
    return out.index_add_(0, seg, src)
def _pad(x, pw):
    flat = tuple(v for pair in reversed(pw) for v in pair)
    return torch.nn.functional.pad(x, flat)

kernel_name = 'concat_heads__linear_kernel_hedgehog__grouped_kv_mqa__prompt'

def setup_inputs():

    _w = (lambda a: (lambda t: t.to(torch.bfloat16) if t.is_floating_point() else t)(torch.as_tensor(a)))
    def _A_setup():
        B, S, D, H = 1, 2048, 1024, 32
        Dh = D // H
        return {
            "x": _w((lambda *s: torch.randn(*s, dtype=torch.bfloat16) * 0.4)(B, S, D)),
            "wqkv": _w((lambda *s: torch.randn(*s, dtype=torch.bfloat16) * 0.4)(D, 3 * D)),
            "w_phi": _w((lambda *s: torch.randn(*s, dtype=torch.bfloat16) * 0.4)(H, Dh, Dh) * (Dh ** -0.5)),
            "wo": _w((lambda *s: torch.randn(*s, dtype=torch.bfloat16) * 0.05)(D, D)),
        }
    _A_in = _A_setup()

    def _B_setup():
        B, S, D, H = 1, 2048, 1024, 32
        Dh = D // H
        return {
            "x": _w((lambda *s: torch.randn(*s, dtype=torch.bfloat16) * 0.05)(B, S, D)),
            "wq": _w((lambda *s: torch.randn(*s, dtype=torch.bfloat16) * 0.8)(D, H * Dh)),
            "wk": _w((lambda *s: torch.randn(*s, dtype=torch.bfloat16) * 0.8)(D, Dh)),
            "wv": _w((lambda *s: torch.randn(*s, dtype=torch.bfloat16) * 0.1)(D, Dh)),
            "wo": _w((lambda *s: torch.randn(*s, dtype=torch.bfloat16) * 0.1)(H * Dh, D)),
        }
    _B_in = _B_setup()

    return {
        "A_x": _A_in["x"],
        "A_wqkv": _A_in["wqkv"],
        "A_w_phi": _A_in["w_phi"],
        "A_wo": _A_in["wo"],
        "B_x": _B_in["x"],
        "B_wq": _B_in["wq"],
        "B_wk": _B_in["wk"],
        "B_wv": _B_in["wv"],
        "B_wo": _B_in["wo"],
    }

def reference(A_x, A_wqkv, A_w_phi, A_wo, B_x, B_wq, B_wk, B_wv, B_wo):
    def _A_ref(x, wqkv, w_phi, wo):
        B, S, D = x.shape
        H = 32; Dh = D // H
        qkv = (lambda x, *d: x.permute(*d))((x @ wqkv).reshape(B, S, 3, H, Dh), 2, 0, 3, 1, 4)
        q, k, v = qkv[0], qkv[1], qkv[2]
        pq = torch.einsum("bhsd,hdf->bhsf", q, w_phi)
        pk = torch.einsum("bhsd,hdf->bhsf", k, w_phi)
        phi_q = torch.concatenate([_softmax(pq, axis=-1), _softmax(-pq, axis=-1)], axis=-1)
        phi_k = torch.concatenate([_softmax(pk, axis=-1), _softmax(-pk, axis=-1)], axis=-1)
        kv = torch.einsum("bhsf,bhsd->bhfd", phi_k, v)
        z  = torch.einsum("bhsf->bhf", phi_k)
        num = torch.einsum("bhqf,bhfd->bhqd", phi_q, kv)
        den = torch.einsum("bhqf,bhf->bhq", phi_q, z)[..., None] + 1e-6
        o = num / den
        return (lambda x, *d: x.permute(*d))(o, 0, 2, 1, 3).reshape(B, S, D) @ wo

    def _B_ref(x, wq, wk, wv, wo):
        B, S, D = x.shape
        H = 32
        Dh = D // H
        q = (lambda x, *d: x.permute(*d))((x @ wq).reshape(B, S, H, Dh), 0, 2, 1, 3)
        k = x @ wk
        v = x @ wv
        scores = torch.einsum("bhqd,bkd->bhqk", q, k) * (Dh ** -0.5)
        p = _softmax(scores, axis=-1)
        o = torch.einsum("bhqk,bkd->bhqd", p, v)
        return (lambda x, *d: x.permute(*d))(o, 0, 2, 1, 3).reshape(B, S, H * Dh) @ wo

    a = _A_ref(A_x, A_wqkv, A_w_phi, A_wo)
    b = _B_ref(B_x, B_wq, B_wk, B_wv, B_wo)
    return torch.concatenate([a, b], axis=-1)
```

```cpp
#include <hip/hip_runtime.h>
#include <hip/hip_bf16.h>
#include <stdint.h>
#pragma clang diagnostic ignored "-Wint-to-pointer-cast"

typedef __bf16   bf16x8 __attribute__((ext_vector_type(8)));
typedef __bf16   bf16x2 __attribute__((ext_vector_type(2)));
typedef short    s16x4  __attribute__((ext_vector_type(4)));
typedef short    s16x8  __attribute__((ext_vector_type(8)));
typedef float    f32x16 __attribute__((ext_vector_type(16)));
typedef float    f32x4  __attribute__((ext_vector_type(4)));
typedef float    f32x2  __attribute__((ext_vector_type(2)));
typedef unsigned u32x4  __attribute__((ext_vector_type(4)));
typedef unsigned u32x2  __attribute__((ext_vector_type(2)));
typedef __amdgpu_buffer_rsrc_t rsrc_t;

#define AS3 __attribute__((address_space(3)))
#define DI __device__ __forceinline__
#define MFMA32(a, b, c) __builtin_amdgcn_mfma_f32_32x32x16_bf16((a), (b), (c), 0, 0, 0)
#define MFMA16(a, b, c) __builtin_amdgcn_mfma_f32_16x16x32_bf16((a), (b), (c), 0, 0, 0)
#define SBAR() __builtin_amdgcn_sched_barrier(0)
#define WAIT_BAR(N) asm volatile("s_waitcnt vmcnt(" #N ") lgkmcnt(0)\n\ts_barrier" ::: "memory")
#define LDS_BAR() asm volatile("s_waitcnt lgkmcnt(0)\n\ts_barrier" ::: "memory")

constexpr int S = 2048, D = 1024, H = 32;
constexpr float LOG2E = 1.4426950408889634f;
constexpr float QSCALE = 0.17677669529663687f * LOG2E;

constexpr size_t WS_QA  = 0;
constexpr size_t WS_QB  = WS_QA + (4u << 20);
constexpr size_t WS_OA  = WS_QB + (4u << 20);
constexpr size_t WS_OB  = WS_OA + (4u << 20);
constexpr size_t WS_KV  = WS_OB + (4u << 20);
constexpr size_t WS_KVP = WS_KV + (256u << 10);
constexpr size_t WS_ZP  = WS_KVP + (2u << 20);

DI unsigned pk2(float lo, float hi) {
  f32x2 v = {lo, hi};
  return __builtin_bit_cast(unsigned, __builtin_convertvector(v, bf16x2));
}
template <int SS> DI bf16x8 pack8(const f32x16& x) {
  u32x4 p = {pk2(x[8 * SS + 0], x[8 * SS + 1]), pk2(x[8 * SS + 2], x[8 * SS + 3]), pk2(x[8 * SS + 4], x[8 * SS + 5]), pk2(x[8 * SS + 6], x[8 * SS + 7])};
  return __builtin_bit_cast(bf16x8, p);
}
DI bf16x8 lds_r128(unsigned a) { return *(const AS3 bf16x8*)a; }
DI bf16x8 lds_tr2(unsigned a0, unsigned a1) {
  s16x4 lo = __builtin_amdgcn_ds_read_tr16_b64_v4i16((AS3 s16x4*)a0);
  s16x4 hi = __builtin_amdgcn_ds_read_tr16_b64_v4i16((AS3 s16x4*)a1);
  s16x8 r = __builtin_shufflevector(lo, hi, 0, 1, 2, 3, 4, 5, 6, 7);
  return __builtin_bit_cast(bf16x8, r);
}
DI rsrc_t mk_rsrc(const void* p, unsigned bytes) { return __builtin_amdgcn_make_buffer_rsrc((void*)p, (short)0, (int)bytes, 0x00020000); }
DI void set_m0(unsigned v) { asm volatile("s_mov_b32 m0, %0\n\ts_nop 0" ::"s"(v) : "memory"); }
template <int IMM> DI void dma16(unsigned voff, rsrc_t srd, unsigned soff) {
  asm volatile("buffer_load_dwordx4 %0, %1, %2 offen offset:%3 lds" ::"v"(voff), "s"(srd), "s"(soff), "i"(IMM) : "memory");
}
DI float xhalf_max(float v) {
  auto r = __builtin_amdgcn_permlane32_swap(__float_as_uint(v), __float_as_uint(v), false, false);
  return fmaxf(__uint_as_float(r[0]), __uint_as_float(r[1]));
}
DI float xhalf_min(float v) {
  auto r = __builtin_amdgcn_permlane32_swap(__float_as_uint(v), __float_as_uint(v), false, false);
  return fminf(__uint_as_float(r[0]), __uint_as_float(r[1]));
}
DI float xhalf_sum(float v) {
  auto r = __builtin_amdgcn_permlane32_swap(__float_as_uint(v), __float_as_uint(v), false, false);
  return __uint_as_float(r[0]) + __uint_as_float(r[1]);
}
DI float max16(const f32x16& x) {
  float a = fmaxf(fmaxf(x[0], x[1]), x[2]), b = fmaxf(fmaxf(x[3], x[4]), x[5]);
  a = fmaxf(fmaxf(a, x[6]), x[7]);   b = fmaxf(fmaxf(b, x[8]), x[9]);
  a = fmaxf(fmaxf(a, x[10]), x[11]); b = fmaxf(fmaxf(b, x[12]), x[13]);
  a = fmaxf(fmaxf(a, x[14]), x[15]);
  return fmaxf(a, b);
}
DI float min16(const f32x16& x) {
  float a = fminf(fminf(x[0], x[1]), x[2]), b = fminf(fminf(x[3], x[4]), x[5]);
  a = fminf(fminf(a, x[6]), x[7]);   b = fminf(fminf(b, x[8]), x[9]);
  a = fminf(fminf(a, x[10]), x[11]); b = fminf(fminf(b, x[12]), x[13]);
  a = fminf(fminf(a, x[14]), x[15]);
  return fminf(a, b);
}
DI void load_wphi(const unsigned short* __restrict__ wphi, int head, int lane, bf16x8* wf) {
  const int f = lane & 31, h = lane >> 5;
#pragma unroll
  for (int s = 0; s < 2; ++s) {
    s16x8 v;
#pragma unroll
    for (int j = 0; j < 8; ++j) v[j] = (short)wphi[(head * 32 + 16 * s + 8 * (j >> 2) + 4 * h + (j & 3)) * 32 + f];
    wf[s] = __builtin_bit_cast(bf16x8, v);
  }
}
DI void feat_map(const f32x16& p, f32x16& ep, f32x16& em) {
  const float mx = xhalf_max(max16(p)), mn = xhalf_min(min16(p));
  const float a = -mx * LOG2E, b = mn * LOG2E;
  float sp = 0.f, sm = 0.f;
#pragma unroll
  for (int i = 0; i < 16; ++i) {
    ep[i] = __builtin_amdgcn_exp2f(fmaf(p[i], LOG2E, a));
    em[i] = __builtin_amdgcn_exp2f(fmaf(p[i], -LOG2E, b));
    sp += ep[i]; sm += em[i];
  }
  const float rp = __builtin_amdgcn_rcpf(xhalf_sum(sp)), rm = __builtin_amdgcn_rcpf(xhalf_sum(sm));
#pragma unroll
  for (int i = 0; i < 16; ++i) { ep[i] *= rp; em[i] *= rm; }
}

constexpr int K1_WOFF = 32768, K1_EOFF = 49152, K1_STAGE = 53248, K1_LDS = 3 * K1_STAGE;

template <int TYPE>
DI void k1_body(unsigned lds0, const unsigned short* __restrict__ X, const unsigned short* __restrict__ W, const unsigned short* __restrict__ E,
                const unsigned short* __restrict__ wphi, char* __restrict__ ws, int rb, int ct) {
  constexpr unsigned LDW = (TYPE == 2) ? 2048u : 6144u;
  const int tid = threadIdx.x, lane = tid & 63, w = __builtin_amdgcn_readfirstlane(tid >> 6);
  const int wm = w & 3, wn = w >> 2, r32 = lane & 31, h = lane >> 5;
  const rsrc_t sx = mk_rsrc(X + (size_t)rb * 256 * D, 256u * 2048u);
  const rsrc_t sw = mk_rsrc(W, 1024u * LDW);
  const rsrc_t se = mk_rsrc(E, 1024u * 64u);
  unsigned xv[4], wv[2];
#pragma unroll
  for (int j = 0; j < 4; ++j) {
    const int row = 8 * (4 * w + j) + (lane >> 3), c = (lane & 7) ^ ((row >> 1) & 7);
    xv[j] = row * 2048 + c * 16 - j * 1024;
  }
#pragma unroll
  for (int j = 0; j < 2; ++j) {
    const int k = 4 * (2 * w + j) + (lane >> 4), c = (lane & 15) ^ (((lane >> 4) & 3) << 2);
    int col;
    if (TYPE == 0) col = 1024 + ((c >> 2) & 1) * 1024 + (2 * ct + (c >> 3)) * 32 + (c & 3) * 8;
    else col = ct * 128 + c * 8;
    wv[j] = k * LDW + col * 2 - j * 1024;
  }
  const unsigned ev = w * 1024 + lane * 16;
  auto stage = [&](int t, unsigned buf) {
    if (TYPE == 2 && w < 4) { set_m0(buf + K1_EOFF + w * 1024); dma16<0>(lane * 16, se, t * 4096 + w * 1024); }
    set_m0(buf + w * 4096);
    dma16<0>(xv[0], sx, t * 128); dma16<1024>(xv[1], sx, t * 128); dma16<2048>(xv[2], sx, t * 128); dma16<3072>(xv[3], sx, t * 128);
    set_m0(buf + K1_WOFF + w * 2048);
    dma16<0>(wv[0], sw, t * 64 * LDW); dma16<1024>(wv[1], sw, t * 64 * LDW);
  };
  (void)ev;
  stage(0, lds0); stage(1, lds0 + K1_STAGE); stage(2, lds0 + 2 * K1_STAGE);

  bf16x8 wf[2];
  if (TYPE == 0) load_wphi(wphi, 2 * ct + wn, lane, wf);

  const int xrow = wm * 64 + r32;
  const unsigned xa = xrow * 128 + ((h ^ ((xrow >> 1) & 7)) << 4);
  const int q4 = (lane & 15) >> 2, p4 = lane & 3, chalf = (lane >> 4) & 1;
  unsigned wa[2];
#pragma unroll
  for (int ns = 0; ns < 2; ++ns)
    wa[ns] = K1_WOFF + (8 * h + q4) * 256 + ((((wn * 2 + ns) ^ q4) << 2) | (chalf * 2 + (p4 >> 1))) * 16 + (p4 & 1) * 8;
  const int erow = 64 * (ct >> 1) + 16 * wm + (lane & 15);
  const unsigned exa = erow * 128 + (((lane >> 4) ^ ((erow >> 1) & 7)) << 4);
  const unsigned ewa = K1_EOFF + (8 * (lane >> 4) + q4) * 64 + wn * 32 + p4 * 8;

  f32x16 acc[2][2] = {};
  f32x4 eacc = {};
  bf16x8 xf[2][2], wfr[2][2];
  auto rd = [&](int set, unsigned buf, int ks) {
#pragma unroll
    for (int ms = 0; ms < 2; ++ms) xf[set][ms] = lds_r128(buf + ((xa ^ (ks * 32)) + ms * 4096));
#pragma unroll
    for (int ns = 0; ns < 2; ++ns) wfr[set][ns] = lds_tr2(buf + wa[ns] + ks * 4096, buf + wa[ns] + ks * 4096 + 1024);
  };
  auto mm = [&](int set) {
#pragma unroll
    for (int ms = 0; ms < 2; ++ms) {
      acc[ms][0] = MFMA32(wfr[set][0], xf[set][ms], acc[ms][0]);
      if (TYPE == 0) acc[ms][1] = MFMA32(xf[set][ms], wfr[set][1], acc[ms][1]);
      else           acc[ms][1] = MFMA32(wfr[set][1], xf[set][ms], acc[ms][1]);
    }
  };
  auto extra = [&](unsigned buf, int kk) {
    if (TYPE == 2) {
      const bf16x8 ex = lds_r128(buf + (exa ^ (kk * 64)));
      const bf16x8 ew = lds_tr2(buf + ewa + kk * 2048, buf + ewa + kk * 2048 + 256);
      if (ct & 1) eacc = MFMA16(ex, ew, eacc); else eacc = MFMA16(ew, ex, eacc);
    }
  };
  WAIT_BAR(12);
  unsigned cur = lds0, nxt = lds0 + K1_STAGE;
  rd(0, cur, 0);
#pragma unroll 1
  for (int t = 0; t < 16; ++t) {
    SBAR(); rd(1, cur, 1); SBAR(); mm(0); extra(cur, 0);
    SBAR(); rd(0, cur, 2); SBAR(); mm(1); extra(cur, 1);
    SBAR(); rd(1, cur, 3); SBAR(); mm(0);
    SBAR();
    if (t < 14) WAIT_BAR(6); else if (t == 14) WAIT_BAR(0);
    if (t < 13) stage(t + 3, cur);
    if (t < 15) rd(0, nxt, 0);
    SBAR(); mm(1);
    cur = nxt; nxt = (nxt == lds0 + 2 * K1_STAGE) ? lds0 : nxt + K1_STAGE;
  }
  SBAR();

  if (TYPE != 0) {
    char* dst = ws + (TYPE == 1 ? WS_QA : WS_QB);
#pragma unroll
    for (int ms = 0; ms < 2; ++ms)
#pragma unroll
      for (int ns = 0; ns < 2; ++ns) {
        f32x16 v = acc[ms][ns];
        if (TYPE == 2) v *= QSCALE;
        const int tt = rb * 8 + wm * 2 + ms, head = ct * 4 + wn * 2 + ns;
        char* p = dst + (size_t)(tt * 32 + head) * 2048 + lane * 16;
        *(bf16x8*)p = pack8<0>(v);
        *(bf16x8*)(p + 1024) = pack8<1>(v);
      }
    if (TYPE == 2) {
      const int g = lane >> 4, tok0 = rb * 256 + 64 * (ct >> 1) + 16 * wm;
      u32x2 v = {pk2(eacc[0], eacc[1]), pk2(eacc[2], eacc[3])};
      char* p = ws + WS_KV + (size_t)(tok0 >> 5) * 4096;
      if (ct & 1) p += 2048 + (wm & 1) * 1024 + (16 * wn + (lane & 15) + 32 * (g & 1)) * 16 + (g >> 1) * 8;
      else        p += wn * 1024 + (16 * (wm & 1) + (lane & 15) + 32 * (g & 1)) * 16 + (g >> 1) * 8;
      *(u32x2*)p = v;
    }
  } else {
    const unsigned scr = lds0 + K1_STAGE;
    const unsigned img = scr + w * 4096;
    const unsigned red = scr + 32768, zred = red + 65536;
    f32x16 kvp = {}, kvm = {}, zp = {}, zm = {};
    const u32x4 ones_u = {0x3f803f80u, 0x3f803f80u, 0x3f803f80u, 0x3f803f80u};
    const bf16x8 ones = __builtin_bit_cast(bf16x8, ones_u);
#pragma unroll
    for (int ms = 0; ms < 2; ++ms) {
      f32x16 pk = {};
      pk = MFMA32(wf[0], pack8<0>(acc[ms][0]), pk);
      pk = MFMA32(wf[1], pack8<1>(acc[ms][0]), pk);
      f32x16 ep, em;
      feat_map(pk, ep, em);
#pragma unroll
      for (int g = 0; g < 4; ++g) {
        const unsigned a = img + r32 * 64 + (((2 * g + h) ^ ((r32 >> 1) & 7)) << 3);
        *(AS3 u32x2*)a = u32x2{pk2(ep[4 * g], ep[4 * g + 1]), pk2(ep[4 * g + 2], ep[4 * g + 3])};
        *(AS3 u32x2*)(a + 2048) = u32x2{pk2(em[4 * g], em[4 * g + 1]), pk2(em[4 * g + 2], em[4 * g + 3])};
      }
      asm volatile("s_waitcnt lgkmcnt(0)" ::: "memory");
      const bf16x8 vf[2] = {pack8<0>(acc[ms][1]), pack8<1>(acc[ms][1])};
#pragma unroll
      for (int s = 0; s < 2; ++s) {
        const int t0 = 16 * s + 4 * h + q4, t1 = t0 + 8, u = 4 * chalf + p4;
        const unsigned a0 = img + t0 * 64 + ((u ^ ((t0 >> 1) & 7)) << 3), a1 = img + t1 * 64 + ((u ^ ((t1 >> 1) & 7)) << 3);
        const bf16x8 ap = lds_tr2(a0, a1), am = lds_tr2(a0 + 2048, a1 + 2048);
        kvp = MFMA32(ap, vf[s], kvp); kvm = MFMA32(am, vf[s], kvm);
        zp = MFMA32(ap, ones, zp);    zm = MFMA32(am, ones, zm);
      }
      asm volatile("s_waitcnt lgkmcnt(0)" ::: "memory");
    }
#pragma unroll
    for (int g = 0; g < 4; ++g) {
      *(AS3 f32x4*)(red + w * 8192 + g * 1024 + lane * 16) = f32x4{kvp[4 * g], kvp[4 * g + 1], kvp[4 * g + 2], kvp[4 * g + 3]};
      *(AS3 f32x4*)(red + w * 8192 + 4096 + g * 1024 + lane * 16) = f32x4{kvm[4 * g], kvm[4 * g + 1], kvm[4 * g + 2], kvm[4 * g + 3]};
      if (r32 == 0) {
        *(AS3 f32x4*)(zred + w * 256 + h * 64 + g * 16) = f32x4{zp[4 * g], zp[4 * g + 1], zp[4 * g + 2], zp[4 * g + 3]};
        *(AS3 f32x4*)(zred + w * 256 + 128 + h * 64 + g * 16) = f32x4{zm[4 * g], zm[4 * g + 1], zm[4 * g + 2], zm[4 * g + 3]};
      }
    }
    LDS_BAR();
#pragma unroll
    for (int hb = 0; hb < 2; ++hb) {
      f32x4 s = *(AS3 f32x4*)(red + (hb * 4) * 8192 + tid * 16);
#pragma unroll
      for (int m = 1; m < 4; ++m) s += *(AS3 f32x4*)(red + (hb * 4 + m) * 8192 + tid * 16);
      *(f32x4*)(ws + WS_KVP + ((size_t)((2 * ct + hb) * 8 + rb) * 512 + tid) * 16) = s;
    }
    if (tid < 128) {
      const int hb = tid >> 6;
      float s = 0.f;
#pragma unroll
      for (int m = 0; m < 4; ++m) s += *(AS3 float*)(zred + (hb * 4 + m) * 256 + lane * 4);
      *(float*)(ws + WS_ZP + ((size_t)((2 * ct + hb) * 8 + rb) * 64 + lane) * 4) = s;
    }
  }
}

__global__ __launch_bounds__(512, 2) void k1_proj(const unsigned short* __restrict__ xa, const unsigned short* __restrict__ wqkv,
                                                  const unsigned short* __restrict__ wphi, const unsigned short* __restrict__ xb,
                                                  const unsigned short* __restrict__ wq, const unsigned short* __restrict__ wk,
                                                  const unsigned short* __restrict__ wv, char* __restrict__ ws) {
  __shared__ __attribute__((aligned(1024))) char smem[K1_LDS];
  const unsigned lds0 = (unsigned)(uintptr_t)(AS3 char*)smem;
  const int b = blockIdx.x, x = b & 7, idx = b >> 3;
  const int rb = 4 * (x & 1) + (idx & 3);
  if (x < 4)      k1_body<0>(lds0, xa, wqkv, wk, wphi, ws, rb, 8 * (x >> 1) + (idx >> 2));
  else if (x < 6) k1_body<1>(lds0, xa, wqkv, wk, wphi, ws, rb, idx >> 2);
  else            k1_body<2>(lds0, xb, wq, ((idx >> 2) & 1) ? wv : wk, wphi, ws, rb, idx >> 2);
}

constexpr int K2_RING = 4 * 32768, K2_KVF = K2_RING, K2_ZF = K2_KVF + 4096, K2_FLAG = K2_ZF + 128, K2_LDS = K2_FLAG + 128;

DI void k2_issue(unsigned lds0, rsrc_t skv, int c, int w, int lane) {
  set_m0(lds0 + (c & 3) * 32768 + w * 4096);
  const unsigned so = c * 32768 + w * 4096, vo = lane * 16;
  dma16<0>(vo, skv, so); dma16<1024>(vo, skv, so); dma16<2048>(vo, skv, so); dma16<3072>(vo, skv, so);
}

template <bool SAFE>
DI void attn_pass(unsigned lds0, rsrc_t skv, const bf16x8 q0, const bf16x8 q1, int w, int lane, f32x16& O, f32x4& L) {
  const unsigned sel_u = ((((lane >> 2) ^ (lane >> 4)) & 1) == 0) ? 0x3f803f80u : 0u;
  const bf16x8 sel = __builtin_bit_cast(bf16x8, u32x4{sel_u, sel_u, sel_u, sel_u});
  const unsigned la = lds0 + lane * 16;
  O = f32x16{}; L = f32x4{};
  WAIT_BAR(8);
  bf16x8 k0 = lds_r128(la), k1 = lds_r128(la + 1024);
  f32x16 negm, Sc;
  float m = 0.f;
  {
    f32x16 z = {};
    Sc = MFMA32(k0, q0, z); Sc = MFMA32(k1, q1, Sc);
    m = xhalf_max(max16(Sc));
    if (!SAFE) {
#pragma unroll
      for (int i = 0; i < 16; ++i) { negm[i] = -m; Sc[i] -= m; }
    } else negm = z;
  }
#pragma unroll 1
  for (int c = 0; c < 8; ++c) {
    const unsigned cb = la + (c & 3) * 32768, nb = la + ((c + 1) & 3) * 32768;
#pragma unroll
    for (int i = 0; i < 8; ++i) {
      if (i == 7) {
        if (c < 6) WAIT_BAR(4); else if (c == 6) WAIT_BAR(0);
        if (c < 5) k2_issue(lds0, skv, c + 3, w, lane);
      }
      const unsigned ka = (i < 7) ? cb + (i + 1) * 4096 : nb;
      k0 = lds_r128(ka); k1 = lds_r128(ka + 1024);
      const bf16x8 v0 = lds_r128(cb + i * 4096 + 2048), v1 = lds_r128(cb + i * 4096 + 3072);
      if (SAFE) {
        const float mn = fmaxf(m, xhalf_max(max16(Sc)));
        const float al = __builtin_amdgcn_exp2f(m - mn);
        m = mn;
#pragma unroll
        for (int r = 0; r < 16; ++r) { O[r] *= al; Sc[r] -= mn; }
        L *= al;
      }
      f32x16 P;
#pragma unroll
      for (int r = 0; r < 16; ++r) P[r] = __builtin_amdgcn_exp2f(Sc[r]);
      const bf16x8 p0 = pack8<0>(P), p1 = pack8<1>(P);
      Sc = MFMA32(k0, q0, negm); Sc = MFMA32(k1, q1, Sc);
      O = MFMA32(v0, p0, O); L = MFMA16(sel, p0, L);
      O = MFMA32(v1, p1, O); L = MFMA16(sel, p1, L);
    }
  }
}

__global__ __launch_bounds__(512, 2) void k2_attn(const unsigned short* __restrict__ wphi, char* __restrict__ ws) {
  __shared__ __attribute__((aligned(1024))) char smem[K2_LDS];
  const unsigned lds0 = (unsigned)(uintptr_t)(AS3 char*)smem;
  const int tid = threadIdx.x, lane = tid & 63, w = __builtin_amdgcn_readfirstlane(tid >> 6), h = lane >> 5;
  const int b = blockIdx.x;
  const rsrc_t skv = mk_rsrc(ws + WS_KV, 256u << 10);
  k2_issue(lds0, skv, 0, w, lane); k2_issue(lds0, skv, 1, w, lane); k2_issue(lds0, skv, 2, w, lane);
  const size_t qoff = (size_t)(8 * b + w) * 2048 + lane * 16;
  const bf16x8 q0 = *(const bf16x8*)(ws + WS_QB + qoff), q1 = *(const bf16x8*)(ws + WS_QB + qoff + 1024);

  {
    const int head = b & 31, tt = (b >> 5) * 8 + w;
    const size_t aoff = (size_t)(tt * 32 + head) * 2048 + lane * 16;
    const bf16x8 a0 = *(const bf16x8*)(ws + WS_QA + aoff), a1 = *(const bf16x8*)(ws + WS_QA + aoff + 1024);
    bf16x8 wf[2];
    load_wphi(wphi, head, lane, wf);
    f32x4 s = {};
#pragma unroll
    for (int p = 0; p < 8; ++p) s += *(const f32x4*)(ws + WS_KVP + ((size_t)(head * 8 + p) * 512 + tid) * 16);
    *(AS3 u32x2*)(lds0 + K2_KVF + (w >> 1) * 1024 + lane * 16 + (w & 1) * 8) = u32x2{pk2(s[0], s[1]), pk2(s[2], s[3])};
    if (w == 0) {
      float z = 0.f;
#pragma unroll
      for (int p = 0; p < 8; ++p) z += *(const float*)(ws + WS_ZP + ((size_t)(head * 8 + p) * 64 + lane) * 4);
      *(AS3 unsigned short*)(lds0 + K2_ZF + lane * 2) = (unsigned short)(pk2(z, z) & 0xffffu);
      *(AS3 unsigned*)(lds0 + K2_FLAG) = 0u;
    }
    f32x16 pq = {};
    pq = MFMA32(wf[0], a0, pq); pq = MFMA32(wf[1], a1, pq);
    f32x16 ep, em;
    feat_map(pq, ep, em);
    const bf16x8 f0 = pack8<0>(ep), f1 = pack8<1>(ep), f2 = pack8<0>(em), f3 = pack8<1>(em);
    LDS_BAR();
    const unsigned ka = lds0 + K2_KVF + lane * 16, za = lds0 + K2_ZF + h * 32;
    f32x16 num = {}, den = {};
    num = MFMA32(lds_r128(ka), f0, num);        den = MFMA32(lds_r128(za), f0, den);
    num = MFMA32(lds_r128(ka + 1024), f1, num); den = MFMA32(lds_r128(za + 16), f1, den);
    num = MFMA32(lds_r128(ka + 2048), f2, num); den = MFMA32(lds_r128(za + 64), f2, den);
    num = MFMA32(lds_r128(ka + 3072), f3, num); den = MFMA32(lds_r128(za + 80), f3, den);
    const float r = 1.0f / (den[0] + 1e-6f);
    num *= r;
    *(bf16x8*)(ws + WS_OA + aoff) = pack8<0>(num);
    *(bf16x8*)(ws + WS_OA + aoff + 1024) = pack8<1>(num);
  }

  f32x16 O; f32x4 L;
  attn_pass<false>(lds0, skv, q0, q1, w, lane, O, L);
  bool bad = !(L[0] < 1.8446744e19f);
#pragma unroll
  for (int i = 0; i < 16; ++i) bad |= !(fabsf(O[i]) < 3.0e38f);
  if (__any(bad)) *(AS3 unsigned*)(lds0 + K2_FLAG) = 1u;
  LDS_BAR();
  if (__builtin_amdgcn_readfirstlane(*(AS3 unsigned*)(lds0 + K2_FLAG)) != 0u) {
    k2_issue(lds0, skv, 0, w, lane); k2_issue(lds0, skv, 1, w, lane); k2_issue(lds0, skv, 2, w, lane);
    attn_pass<true>(lds0, skv, q0, q1, w, lane, O, L);
  }
  const float r = 1.0f / L[0];
  O *= r;
  *(bf16x8*)(ws + WS_OB + qoff) = pack8<0>(O);
  *(bf16x8*)(ws + WS_OB + qoff + 1024) = pack8<1>(O);
}

constexpr int K3_STAGE = 32768, K3_NBUF = 5, K3_LDS = K3_NBUF * K3_STAGE;

__global__ __launch_bounds__(512, 2) void k3_out(const unsigned short* __restrict__ woa, const unsigned short* __restrict__ wob,
                                                 const char* __restrict__ ws, unsigned short* __restrict__ out) {
  __shared__ __attribute__((aligned(1024))) char smem[K3_LDS];
  const unsigned lds0 = (unsigned)(uintptr_t)(AS3 char*)smem;
  const int tid = threadIdx.x, lane = tid & 63, w = __builtin_amdgcn_readfirstlane(tid >> 6);
  const int b = blockIdx.x, x = b & 7, idx = b >> 3;
  const int br = x >> 2, rb = 8 * (x & 1) + (idx & 7), cb = 4 * ((x >> 1) & 1) + (idx >> 3);
  const int kh = w >> 2, wm = w & 1, wn = (w >> 1) & 1, r32 = lane & 31, h = lane >> 5;
  const rsrc_t so = mk_rsrc(ws + (br ? WS_OB : WS_OA) + (size_t)rb * 4 * 65536, 4u * 65536u);
  const rsrc_t sw = mk_rsrc(br ? wob : woa, 1024u * 2048u);
  const unsigned ov = (w >> 1) * 65536 + (w & 1) * 2048 + lane * 16;
  unsigned wv[2];
#pragma unroll
  for (int j = 0; j < 2; ++j) {
    const int k = 4 * (2 * w + j) + (lane >> 4), c = (lane & 15) ^ (((lane >> 4) & 3) << 2);
    wv[j] = k * 2048 + (cb * 128 + c * 8) * 2 - j * 1024;
  }
  auto stage = [&](int t, unsigned buf) {
    set_m0(buf + w * 2048);
    dma16<0>(ov, so, t * 4096); dma16<1024>(ov, so, t * 4096);
    set_m0(buf + 16384 + w * 2048);
    dma16<0>(wv[0], sw, t * 64 * 2048); dma16<1024>(wv[1], sw, t * 64 * 2048);
  };
#pragma unroll
  for (int t = 0; t < K3_NBUF; ++t) stage(t, lds0 + t * K3_STAGE);

  const int q4 = (lane & 15) >> 2, p4 = lane & 3, chalf = (lane >> 4) & 1;
  const unsigned oa = wm * 8192 + kh * 2048 + lane * 16;
  unsigned wa[2];
#pragma unroll
  for (int ns = 0; ns < 2; ++ns)
    wa[ns] = 16384 + (kh * 32 + 4 * h + q4) * 256 + ((((wn * 2 + ns) ^ q4) << 2) | (chalf * 2 + (p4 >> 1))) * 16 + (p4 & 1) * 8;
  f32x16 acc[2][2] = {};
  bf16x8 xf[2][2], wfr[2][2];
  auto rd = [&](int set, unsigned buf, int ks) {
#pragma unroll
    for (int ms = 0; ms < 2; ++ms) xf[set][ms] = lds_r128(buf + oa + ms * 4096 + ks * 1024);
#pragma unroll
    for (int ns = 0; ns < 2; ++ns) wfr[set][ns] = lds_tr2(buf + wa[ns] + ks * 4096, buf + wa[ns] + ks * 4096 + 2048);
  };
  auto mm = [&](int set) {
#pragma unroll
    for (int ms = 0; ms < 2; ++ms)
#pragma unroll
      for (int ns = 0; ns < 2; ++ns) acc[ms][ns] = MFMA32(wfr[set][ns], xf[set][ms], acc[ms][ns]);
  };
  WAIT_BAR(16);
  unsigned cur = lds0, nxt = lds0 + K3_STAGE;
  rd(0, cur, 0);
#pragma unroll 1
  for (int t = 0; t < 16; ++t) {
    SBAR(); rd(1, cur, 1); SBAR(); mm(0);
    SBAR();
    if (t < 12) WAIT_BAR(12); else if (t == 12) WAIT_BAR(8); else if (t == 13) WAIT_BAR(4); else WAIT_BAR(0);
    if (t < 11) stage(t + 5, cur);
    if (t < 15) rd(0, nxt, 0);
    SBAR(); mm(1);
    cur = nxt; nxt = (nxt == lds0 + (K3_NBUF - 1) * K3_STAGE) ? lds0 : nxt + K3_STAGE;
  }
  SBAR();
  const unsigned xs = lds0 + w * 8192 + lane * 16, xr = lds0 + (w ^ 4) * 8192 + lane * 16;
#pragma unroll
  for (int ns = 0; ns < 2; ++ns)
#pragma unroll
    for (int g = 0; g < 4; ++g) {
      const f32x16& a = kh ? acc[0][ns] : acc[1][ns];
      *(AS3 f32x4*)(xs + ns * 4096 + g * 1024) = f32x4{a[4 * g], a[4 * g + 1], a[4 * g + 2], a[4 * g + 3]};
    }
  LDS_BAR();
  const unsigned img = lds0 + 65536 + w * 4096;
#pragma unroll
  for (int ns = 0; ns < 2; ++ns)
#pragma unroll
    for (int g = 0; g < 4; ++g) {
      const f32x16& a = kh ? acc[1][ns] : acc[0][ns];
      const f32x4 o = *(AS3 f32x4*)(xr + ns * 4096 + g * 1024);
      *(AS3 u32x2*)(img + r32 * 128 + (((ns * 4 + g) ^ (r32 & 7)) << 4) + h * 8) =
          u32x2{pk2(a[4 * g] + o[0], a[4 * g + 1] + o[1]), pk2(a[4 * g + 2] + o[2], a[4 * g + 3] + o[3])};
    }
  asm volatile("s_waitcnt lgkmcnt(0)" ::: "memory");
  const int tok0 = rb * 128 + wm * 64 + kh * 32;
  unsigned short* op = out + (size_t)tok0 * 2048 + br * 1024 + cb * 128 + wn * 64;
#pragma unroll
  for (int i = 0; i < 4; ++i) {
    const int row = 8 * i + (lane >> 3), ch = lane & 7;
    const u32x4 v = *(AS3 u32x4*)(img + row * 128 + ((ch ^ (row & 7)) << 4));
    *(u32x4*)(op + (size_t)row * 2048 + ch * 8) = v;
  }
}

extern "C" void kernel_launch(void* const* d_in, const int* in_sizes, int n_in, void* d_out, int out_size, void* d_ws, size_t ws_size,
                              hipStream_t stream) {
  (void)in_sizes; (void)n_in; (void)out_size; (void)ws_size;
  typedef const unsigned short* cp;
  char* ws = (char*)d_ws;
  k1_proj<<<256, 512, 0, stream>>>((cp)d_in[0], (cp)d_in[1], (cp)d_in[2], (cp)d_in[4], (cp)d_in[5], (cp)d_in[6], (cp)d_in[7], ws);
  k2_attn<<<256, 512, 0, stream>>>((cp)d_in[2], ws);
  k3_out<<<256, 512, 0, stream>>>((cp)d_in[3], (cp)d_in[8], ws, (unsigned short*)d_out);
}
```

```cpp
#include <hip/hip_runtime.h>
#include <hip/hip_bf16.h>
#include <stdint.h>
#pragma clang diagnostic ignored "-Wint-to-pointer-cast"

typedef __bf16   bf16x8 __attribute__((ext_vector_type(8)));
typedef __bf16   bf16x2 __attribute__((ext_vector_type(2)));
typedef short    s16x4  __attribute__((ext_vector_type(4)));
typedef short    s16x8  __attribute__((ext_vector_type(8)));
typedef float    f32x16 __attribute__((ext_vector_type(16)));
typedef float    f32x4  __attribute__((ext_vector_type(4)));
typedef float    f32x2  __attribute__((ext_vector_type(2)));
typedef unsigned u32x4  __attribute__((ext_vector_type(4)));
typedef unsigned u32x2  __attribute__((ext_vector_type(2)));
typedef __amdgpu_buffer_rsrc_t rsrc_t;

#define AS3 __attribute__((address_space(3)))
#define DI __device__ __forceinline__
#define MFMA32(a, b, c) __builtin_amdgcn_mfma_f32_32x32x16_bf16((a), (b), (c), 0, 0, 0)
#define MFMA16(a, b, c) __builtin_amdgcn_mfma_f32_16x16x32_bf16((a), (b), (c), 0, 0, 0)
#define SBAR() __builtin_amdgcn_sched_barrier(0)
#define WAIT_BAR(N) asm volatile("s_waitcnt vmcnt(" #N ") lgkmcnt(0)\n\ts_barrier" ::: "memory")
#define VM_BAR(N) asm volatile("s_waitcnt vmcnt(" #N ")\n\ts_barrier" ::: "memory")
#define LGKM0() __builtin_amdgcn_s_waitcnt(0xC07F)
#define LDS_BAR() do { asm volatile("" ::: "memory"); LGKM0(); asm volatile("s_barrier" ::: "memory"); } while (0)

#define STAMP(ws, kid, i) do {} while (0)
constexpr int S = 2048, D = 1024, H = 32;
constexpr float LOG2E = 1.4426950408889634f;
constexpr float QSCALE = 0.17677669529663687f * LOG2E;

constexpr size_t WS_QA  = 0;
constexpr size_t WS_QB  = WS_QA + (4u << 20);
constexpr size_t WS_OA  = WS_QB + (4u << 20);
constexpr size_t WS_OB  = WS_OA + (4u << 20);
constexpr size_t WS_KV  = WS_OB + (4u << 20);
constexpr size_t WS_KVP = WS_KV + (256u << 10);
constexpr size_t WS_ZP  = WS_KVP + (2u << 20);

DI unsigned pk2(float lo, float hi) {
  f32x2 v = {lo, hi};
  return __builtin_bit_cast(unsigned, __builtin_convertvector(v, bf16x2));
}
template <int SS> DI bf16x8 pack8(const f32x16& x) {
  u32x4 p = {pk2(x[8 * SS + 0], x[8 * SS + 1]), pk2(x[8 * SS + 2], x[8 * SS + 3]), pk2(x[8 * SS + 4], x[8 * SS + 5]), pk2(x[8 * SS + 6], x[8 * SS + 7])};
  return __builtin_bit_cast(bf16x8, p);
}
DI bf16x8 lds_r128(unsigned a) { return *(const AS3 bf16x8*)a; }
DI bf16x8 lds_tr2(unsigned a0, unsigned a1) {
  s16x4 lo = __builtin_amdgcn_ds_read_tr16_b64_v4i16((AS3 s16x4*)a0);
  s16x4 hi = __builtin_amdgcn_ds_read_tr16_b64_v4i16((AS3 s16x4*)a1);
  s16x8 r = __builtin_shufflevector(lo, hi, 0, 1, 2, 3, 4, 5, 6, 7);
  return __builtin_bit_cast(bf16x8, r);
}
DI rsrc_t mk_rsrc(const void* p, unsigned bytes) { return __builtin_amdgcn_make_buffer_rsrc((void*)p, (short)0, (int)bytes, 0x00020000); }
DI void set_m0(unsigned v) { asm volatile("s_mov_b32 m0, %0\n\ts_nop 0" ::"s"(v) : "memory"); }
template <int IMM> DI void dma16(unsigned voff, rsrc_t srd, unsigned soff) {
  asm volatile("buffer_load_dwordx4 %0, %1, %2 offen offset:%3 lds" ::"v"(voff), "s"(srd), "s"(soff), "i"(IMM) : "memory");
}
DI void pf_line(unsigned& sink, const void* p) { asm volatile("global_load_dword %0, %1, off" : "+v"(sink) : "v"(p) : "memory"); }
DI void pf_drain(unsigned& sink) { asm volatile("s_waitcnt vmcnt(0)" : "+v"(sink)::"memory"); }
#define PIN(x) asm volatile("" : "+v"(x))
DI float xhalf_max(float v) {
  auto r = __builtin_amdgcn_permlane32_swap(__float_as_uint(v), __float_as_uint(v), false, false);
  return fmaxf(__uint_as_float(r[0]), __uint_as_float(r[1]));
}
DI float xhalf_min(float v) {
  auto r = __builtin_amdgcn_permlane32_swap(__float_as_uint(v), __float_as_uint(v), false, false);
  return fminf(__uint_as_float(r[0]), __uint_as_float(r[1]));
}
DI float xhalf_sum(float v) {
  auto r = __builtin_amdgcn_permlane32_swap(__float_as_uint(v), __float_as_uint(v), false, false);
  return __uint_as_float(r[0]) + __uint_as_float(r[1]);
}
DI float max16(const f32x16& x) {
  float a = fmaxf(fmaxf(x[0], x[1]), x[2]), b = fmaxf(fmaxf(x[3], x[4]), x[5]);
  a = fmaxf(fmaxf(a, x[6]), x[7]);   b = fmaxf(fmaxf(b, x[8]), x[9]);
  a = fmaxf(fmaxf(a, x[10]), x[11]); b = fmaxf(fmaxf(b, x[12]), x[13]);
  a = fmaxf(fmaxf(a, x[14]), x[15]);
  return fmaxf(a, b);
}
DI float min16(const f32x16& x) {
  float a = fminf(fminf(x[0], x[1]), x[2]), b = fminf(fminf(x[3], x[4]), x[5]);
  a = fminf(fminf(a, x[6]), x[7]);   b = fminf(fminf(b, x[8]), x[9]);
  a = fminf(fminf(a, x[10]), x[11]); b = fminf(fminf(b, x[12]), x[13]);
  a = fminf(fminf(a, x[14]), x[15]);
  return fminf(a, b);
}
DI void load_wphi(const unsigned short* __restrict__ wphi, int head, int lane, bf16x8* wf) {
  const int f = lane & 31, h = lane >> 5;
#pragma unroll
  for (int s = 0; s < 2; ++s) {
    s16x8 v;
#pragma unroll
    for (int j = 0; j < 8; ++j) v[j] = (short)wphi[(head * 32 + 16 * s + 8 * (j >> 2) + 4 * h + (j & 3)) * 32 + f];
    wf[s] = __builtin_bit_cast(bf16x8, v);
  }
}
DI void feat_map(const f32x16& p, f32x16& ep, f32x16& em) {
  const float mx = xhalf_max(max16(p)), mn = xhalf_min(min16(p));
  const f32x2 a = {-mx * LOG2E, -mx * LOG2E}, b = {mn * LOG2E, mn * LOG2E}, c = {LOG2E, LOG2E};
  f32x2 sp = {0.f, 0.f}, sm = {0.f, 0.f};
#pragma unroll
  for (int i = 0; i < 16; i += 2) {
    const f32x2 x = {p[i], p[i + 1]};
    const f32x2 u = __builtin_elementwise_fma(x, c, a), v = __builtin_elementwise_fma(x, -c, b);
    const f32x2 eu = {__builtin_amdgcn_exp2f(u[0]), __builtin_amdgcn_exp2f(u[1])}, ev = {__builtin_amdgcn_exp2f(v[0]), __builtin_amdgcn_exp2f(v[1])};
    ep[i] = eu[0]; ep[i + 1] = eu[1]; em[i] = ev[0]; em[i + 1] = ev[1];
    sp += eu; sm += ev;
  }
  const float rp = __builtin_amdgcn_rcpf(xhalf_sum(sp[0] + sp[1])), rm = __builtin_amdgcn_rcpf(xhalf_sum(sm[0] + sm[1]));
  ep *= rp; em *= rm;
}

constexpr int K1_WOFF = 32768, K1_EOFF = 49152, K1_STAGE = 53248, K1_LDS = 3 * K1_STAGE;
#define K1_PFD 6
#define K1_PF0 2
#define K1_PFSLEEP 10

template <int TYPE>
DI void k1_body(unsigned lds0, const unsigned short* __restrict__ X, const unsigned short* __restrict__ W, const unsigned short* __restrict__ E,
                const unsigned short* __restrict__ wphi, char* __restrict__ ws, int rb, int ct, int x, int idx) {
  constexpr unsigned LDW = (TYPE >= 2) ? 2048u : 6144u;
  const int tid = threadIdx.x, lane = tid & 63, w = __builtin_amdgcn_readfirstlane(tid >> 6);
  STAMP(ws, 0, 0);
  if (w >= 8) {
    const int lw = w - 8;
    const rsrc_t sx = mk_rsrc(X + (size_t)rb * 256 * D, 256u * 2048u);
    const rsrc_t sw = mk_rsrc(W, 1024u * LDW);
    const rsrc_t se = mk_rsrc(E, 1024u * 64u);
    unsigned xv[8], wv[4];
#pragma unroll
    for (int j = 0; j < 8; ++j) {
      const int row = 8 * (8 * lw + j) + (lane >> 3), c = (lane & 7) ^ ((row >> 1) & 7);
      xv[j] = row * 2048 + c * 16 - (j & 3) * 1024;
    }
#pragma unroll
    for (int j = 0; j < 4; ++j) {
      const int k = 4 * (4 * lw + j) + (lane >> 4), c = (lane & 15) ^ (((lane >> 4) & 3) << 2);
      int col;
      if (TYPE == 0) col = 1024 + ((c >> 2) & 1) * 1024 + (2 * ct + (c >> 3)) * 32 + (c & 3) * 8;
      else col = ct * 128 + c * 8;
      wv[j] = k * LDW + col * 2 - j * 1024;
    }
    const unsigned ev = lane * 16;
    auto stage = [&](int t, unsigned buf) {
      const unsigned sox = t * 128, sow = t * 64 * LDW;
      set_m0(buf + K1_WOFF + lw * 4096);
      dma16<0>(wv[0], sw, sow); dma16<1024>(wv[1], sw, sow); dma16<2048>(wv[2], sw, sow); dma16<3072>(wv[3], sw, sow);
      set_m0(buf + lw * 8192);
      dma16<0>(xv[0], sx, sox); dma16<1024>(xv[1], sx, sox); dma16<2048>(xv[2], sx, sox); dma16<3072>(xv[3], sx, sox);
      set_m0(buf + lw * 8192 + 4096);
      dma16<0>(xv[4], sx, sox); dma16<1024>(xv[5], sx, sox); dma16<2048>(xv[6], sx, sox); dma16<3072>(xv[7], sx, sox);
      if (TYPE >= 2) { set_m0(buf + K1_EOFF + lw * 1024); dma16<0>(ev, se, t * 4096 + lw * 1024); }
    };
    __builtin_amdgcn_s_setprio(2);
    stage(0, lds0); stage(1, lds0 + K1_STAGE);
    if (TYPE >= 2) WAIT_BAR(13); else WAIT_BAR(12);
    stage(2, lds0 + 2 * K1_STAGE);
    unsigned buf = lds0;
#pragma unroll 1
    for (int t = 0; t < 13; ++t) {
      if (TYPE >= 2) WAIT_BAR(13); else WAIT_BAR(12);
      stage(t + 3, buf);
      buf = (buf == lds0 + 2 * K1_STAGE) ? lds0 : buf + K1_STAGE;
    }
    if (TYPE >= 2) WAIT_BAR(13); else WAIT_BAR(12);
    WAIT_BAR(0);
    return;
  }
  const int wm = w & 3, wn = w >> 2, r32 = lane & 31, h = lane >> 5;
  unsigned pfs = 0;
  const char* pfp; unsigned pfstep;
  {
    const int j = lane & 15;
    const unsigned wcol = (TYPE == 0) ? 2048u + (j >> 3) * 2048u + (x >> 1) * 1024u + (j & 7) * 128u : j * 128u;
    pfp = (lane < 32) ? (const char*)X + (size_t)(1024 * (x & 1) + idx * 32 + lane) * 2048 : (const char*)W + (size_t)(2 * idx + ((lane >> 4) & 1)) * LDW + wcol;
    pfstep = (lane < 32) ? 128u : 64u * LDW;
  }
  if (K1_PFD > 0 && w >= K1_PF0 && w < K1_PFD) { __builtin_amdgcn_s_sleep(K1_PFSLEEP); pf_line(pfs, pfp + (size_t)w * pfstep); }

  bf16x8 wf[2];
  if (TYPE == 0) load_wphi(wphi, 2 * ct + wn, lane, wf);

  const int xrow = wm * 64 + r32;
  const unsigned xa = xrow * 128 + ((h ^ ((xrow >> 1) & 7)) << 4);
  const int q4 = (lane & 15) >> 2, p4 = lane & 3, chalf = (lane >> 4) & 1;
  unsigned wa[2];
#pragma unroll
  for (int ns = 0; ns < 2; ++ns)
    wa[ns] = K1_WOFF + (8 * h + q4) * 256 + ((((wn * 2 + ns) ^ q4) << 2) | (chalf * 2 + (p4 >> 1))) * 16 + (p4 & 1) * 8;
  const int erow = 64 * (ct >> 1) + 16 * wm + (lane & 15);
  const unsigned exa = erow * 128 + (((lane >> 4) ^ ((erow >> 1) & 7)) << 4);
  const unsigned ewa = K1_EOFF + (8 * (lane >> 4) + q4) * 64 + wn * 32 + p4 * 8;

  f32x16 acc[2][2] = {};
  f32x4 eacc = {};
  bf16x8 xf[2][2], wfr[2][2], ex[2], ew[2];
  auto rdx = [&](int set, unsigned buf, int ks) {
#pragma unroll
    for (int ms = 0; ms < 2; ++ms) xf[set][ms] = lds_r128(buf + ((xa ^ (ks * 32)) + ms * 4096));
  };
  auto rdw = [&](int set, unsigned buf, int ks, int ns) { wfr[set][ns] = lds_tr2(buf + wa[ns] + ks * 4096, buf + wa[ns] + ks * 4096 + 1024); };
  auto mm1 = [&](int set, int ms, int ns) {
    if (TYPE == 0 && ns == 1) acc[ms][1] = MFMA32(xf[set][ms], wfr[set][1], acc[ms][1]);
    else                      acc[ms][ns] = MFMA32(wfr[set][ns], xf[set][ms], acc[ms][ns]);
  };
  auto erd = [&](unsigned buf, int kk) {
    if (TYPE >= 2) { ex[kk] = lds_r128(buf + (exa ^ (kk * 64))); ew[kk] = lds_tr2(buf + ewa + kk * 2048, buf + ewa + kk * 2048 + 256); }
  };
  auto emm = [&](int kk) {
    if (TYPE == 2) eacc = MFMA16(ew[kk], ex[kk], eacc);
    if (TYPE == 3) eacc = MFMA16(ex[kk], ew[kk], eacc);
  };
  auto sub = [&](int set, bool rd, unsigned buf, int ks) {
    SBAR(); mm1(set, 0, 0); SBAR(); if (rd) rdx(set ^ 1, buf, ks);
    SBAR(); mm1(set, 1, 0); SBAR(); if (rd) rdw(set ^ 1, buf, ks, 0);
    SBAR(); mm1(set, 0, 1); SBAR(); if (rd) rdw(set ^ 1, buf, ks, 1);
    SBAR(); mm1(set, 1, 1); SBAR();
  };
  LDS_BAR();
  STAMP(ws, 0, 1);
  unsigned cur = lds0, nxt = lds0 + K1_STAGE;
  rdx(0, cur, 0); rdw(0, cur, 0, 0); rdw(0, cur, 0, 1);
  LGKM0();
#pragma unroll 1
  for (int t = 0; t < 15; ++t) {
    sub(0, true, cur, 1); erd(cur, 0);
    sub(1, true, cur, 2); erd(cur, 1); SBAR(); emm(0);
    sub(0, true, cur, 3); emm(1); SBAR();
    if (K1_PFD > 0 && t + K1_PFD < 16 && w == ((t + K1_PFD) & 7)) pf_line(pfs, pfp + (size_t)(t + K1_PFD) * pfstep);
    LDS_BAR();
    sub(1, true, nxt, 0);
    cur = nxt; nxt = (nxt == lds0 + 2 * K1_STAGE) ? lds0 : nxt + K1_STAGE;
  }
  sub(0, true, cur, 1); erd(cur, 0);
  sub(1, true, cur, 2); erd(cur, 1); SBAR(); emm(0);
  sub(0, true, cur, 3); emm(1);
  sub(1, false, cur, 0);
  pf_drain(pfs);
  STAMP(ws, 0, 2);

  if (TYPE != 0) {
    char* dst = ws + (TYPE == 1 ? WS_QA : WS_QB);
#pragma unroll
    for (int ms = 0; ms < 2; ++ms)
#pragma unroll
      for (int ns = 0; ns < 2; ++ns) {
        f32x16 v = acc[ms][ns];
        if (TYPE >= 2) v *= QSCALE;
        const int tt = rb * 8 + wm * 2 + ms, head = ct * 4 + wn * 2 + ns;
        char* p = dst + (size_t)(tt * 32 + head) * 2048 + lane * 16;
        *(bf16x8*)p = pack8<0>(v);
        *(bf16x8*)(p + 1024) = pack8<1>(v);
      }
    if (TYPE >= 2) {
      const int g = lane >> 4, tok0 = rb * 256 + 64 * (ct >> 1) + 16 * wm;
      u32x2 v = {pk2(eacc[0], eacc[1]), pk2(eacc[2], eacc[3])};
      char* p = ws + WS_KV + (size_t)(tok0 >> 5) * 4096;
      if (TYPE == 3) p += 2048 + (wm & 1) * 1024 + (16 * wn + (lane & 15) + 32 * (g & 1)) * 16 + (g >> 1) * 8;
      else           p += wn * 1024 + (16 * (wm & 1) + (lane & 15) + 32 * (g & 1)) * 16 + (g >> 1) * 8;
      *(u32x2*)p = v;
    }
  } else {
    const unsigned scr = lds0 + K1_STAGE;
    const unsigned img = scr + w * 4096;
    const unsigned red = scr + 32768, zred = red + 65536;
    f32x16 kvp = {}, kvm = {}, zp = {}, zm = {};
    const u32x4 ones_u = {0x3f803f80u, 0x3f803f80u, 0x3f803f80u, 0x3f803f80u};
    const bf16x8 ones = __builtin_bit_cast(bf16x8, ones_u);
#pragma unroll
    for (int ms = 0; ms < 2; ++ms) {
      f32x16 pk = {};
      pk = MFMA32(wf[0], pack8<0>(acc[ms][0]), pk);
      pk = MFMA32(wf[1], pack8<1>(acc[ms][0]), pk);
      f32x16 ep, em;
      feat_map(pk, ep, em);
#pragma unroll
      for (int g = 0; g < 4; ++g) {
        const unsigned a = img + r32 * 64 + (((2 * g + h) ^ ((r32 >> 1) & 7)) << 3);
        *(AS3 u32x2*)a = u32x2{pk2(ep[4 * g], ep[4 * g + 1]), pk2(ep[4 * g + 2], ep[4 * g + 3])};
        *(AS3 u32x2*)(a + 2048) = u32x2{pk2(em[4 * g], em[4 * g + 1]), pk2(em[4 * g + 2], em[4 * g + 3])};
      }
      asm volatile("s_waitcnt lgkmcnt(0)" ::: "memory");
      const bf16x8 vf[2] = {pack8<0>(acc[ms][1]), pack8<1>(acc[ms][1])};
#pragma unroll
      for (int s = 0; s < 2; ++s) {
        const int t0 = 16 * s + 4 * h + q4, t1 = t0 + 8, u = 4 * chalf + p4;
        const unsigned a0 = img + t0 * 64 + ((u ^ ((t0 >> 1) & 7)) << 3), a1 = img + t1 * 64 + ((u ^ ((t1 >> 1) & 7)) << 3);
        const bf16x8 ap = lds_tr2(a0, a1), am = lds_tr2(a0 + 2048, a1 + 2048);
        kvp = MFMA32(ap, vf[s], kvp); kvm = MFMA32(am, vf[s], kvm);
        zp = MFMA32(ap, ones, zp);    zm = MFMA32(am, ones, zm);
      }
      asm volatile("s_waitcnt lgkmcnt(0)" ::: "memory");
    }
#pragma unroll
    for (int g = 0; g < 4; ++g) {
      *(AS3 f32x4*)(red + w * 8192 + g * 1024 + lane * 16) = f32x4{kvp[4 * g], kvp[4 * g + 1], kvp[4 * g + 2], kvp[4 * g + 3]};
      *(AS3 f32x4*)(red + w * 8192 + 4096 + g * 1024 + lane * 16) = f32x4{kvm[4 * g], kvm[4 * g + 1], kvm[4 * g + 2], kvm[4 * g + 3]};
      if (r32 == 0) {
        *(AS3 f32x4*)(zred + w * 256 + h * 64 + g * 16) = f32x4{zp[4 * g], zp[4 * g + 1], zp[4 * g + 2], zp[4 * g + 3]};
        *(AS3 f32x4*)(zred + w * 256 + 128 + h * 64 + g * 16) = f32x4{zm[4 * g], zm[4 * g + 1], zm[4 * g + 2], zm[4 * g + 3]};
      }
    }
    LDS_BAR();
#pragma unroll
    for (int hb = 0; hb < 2; ++hb) {
      f32x4 s = *(AS3 f32x4*)(red + (hb * 4) * 8192 + tid * 16);
#pragma unroll
      for (int m = 1; m < 4; ++m) s += *(AS3 f32x4*)(red + (hb * 4 + m) * 8192 + tid * 16);
      *(f32x4*)(ws + WS_KVP + ((size_t)((2 * ct + hb) * 8 + rb) * 512 + tid) * 16) = s;
    }
    if (tid < 128) {
      const int hb = tid >> 6;
      float s = 0.f;
#pragma unroll
      for (int m = 0; m < 4; ++m) s += *(AS3 float*)(zred + (hb * 4 + m) * 256 + lane * 4);
      *(float*)(ws + WS_ZP + ((size_t)((2 * ct + hb) * 8 + rb) * 64 + lane) * 4) = s;
    }
  }
  STAMP(ws, 0, 3);
}

__global__ __launch_bounds__(768, 3) void k1_proj(const unsigned short* __restrict__ xa, const unsigned short* __restrict__ wqkv,
                                                  const unsigned short* __restrict__ wphi, const unsigned short* __restrict__ xb,
                                                  const unsigned short* __restrict__ wq, const unsigned short* __restrict__ wk,
                                                  const unsigned short* __restrict__ wv, char* __restrict__ ws) {
  __shared__ __attribute__((aligned(1024))) char smem[K1_LDS];
  const unsigned lds0 = (unsigned)(uintptr_t)(AS3 char*)smem;
  const int b = blockIdx.x, x = b & 7, idx = b >> 3;
  const int rb = 4 * (x & 1) + (idx & 3);
  if (x < 4)              k1_body<0>(lds0, xa, wqkv, wk, wphi, ws, rb, 8 * (x >> 1) + (idx >> 2), x, idx);
  else if (x < 6)         k1_body<1>(lds0, xa, wqkv, wk, wphi, ws, rb, idx >> 2, x, idx);
  else if (!(idx & 4))    k1_body<2>(lds0, xb, wq, wk, wphi, ws, rb, idx >> 2, x, idx);
  else                    k1_body<3>(lds0, xb, wq, wv, wphi, ws, rb, idx >> 2, x, idx);
}

#define K2_PRIO 1
constexpr int K2_RING = 4 * 32768, K2_KVF = K2_RING, K2_ZB = K2_KVF + 4096, K2_LDS = K2_ZB + 128;

DI void k2_issue(unsigned lds0, rsrc_t skv, int c, int lw, int lane) {
  const unsigned so = c * 32768 + lw * 8192, vo = lane * 16;
  set_m0(lds0 + (c & 3) * 32768 + lw * 8192);
  dma16<0>(vo, skv, so); dma16<1024>(vo, skv, so); dma16<2048>(vo, skv, so); dma16<3072>(vo, skv, so);
  set_m0(lds0 + (c & 3) * 32768 + lw * 8192 + 4096);
  dma16<0>(vo, skv, so + 4096); dma16<1024>(vo, skv, so + 4096); dma16<2048>(vo, skv, so + 4096); dma16<3072>(vo, skv, so + 4096);
}

DI void attn_pass_fast(unsigned lds0, const bf16x8 (&q)[2][2], const bf16x8 sel, int kh, int lane, f32x16 (&O)[2], f32x4 (&L)[2]) {
  typedef const AS3 char* lp;
  const lp l0 = (lp)(lds0 + lane * 16), la = l0 + kh * 16384;
#define RD(p) (*(const AS3 bf16x8*)(p))
  bf16x8 ka = RD(l0), kb = RD(l0 + 1024);
  f32x16 negm[2], S0, S1;
#pragma unroll
  for (int r = 0; r < 2; ++r) {
    f32x16 z = {};
    z = MFMA32(ka, q[r][0], z); z = MFMA32(kb, q[r][1], z);
    const float m = xhalf_max(max16(z));
#pragma unroll
    for (int i = 0; i < 16; ++i) negm[r][i] = -m;
    PIN(negm[r]);
    O[r] = f32x16{}; L[r] = f32x4{};
  }
  ka = RD(la); kb = RD(la + 1024);
  bf16x8 va = RD(la + 2048), vb = bf16x8{};
  S0 = MFMA32(ka, q[0][0], negm[0]); S0 = MFMA32(kb, q[0][1], S0);
  u32x4 p0 = {}, p1 = {};
  LGKM0();
#define EX4(SC, i) do { SC[i] = __builtin_amdgcn_exp2f(SC[i]); SC[i + 1] = __builtin_amdgcn_exp2f(SC[i + 1]); \
    SC[i + 2] = __builtin_amdgcn_exp2f(SC[i + 2]); SC[i + 3] = __builtin_amdgcn_exp2f(SC[i + 3]); PIN(SC); } while (0)
#define CV4(P, SC, i) do { P[0] = pk2(SC[i], SC[i + 1]); P[1] = pk2(SC[i + 2], SC[i + 3]); P[2] = pk2(SC[i + 4], SC[i + 5]); P[3] = pk2(SC[i + 6], SC[i + 7]); PIN(P); } while (0)
#define BF(P) __builtin_bit_cast(bf16x8, P)
#define TILE(TA, TN) do { \
    SBAR(); S1 = MFMA32(ka, q[1][0], negm[1]);  SBAR(); ka = RD(TN);          EX4(S0, 0);  \
    SBAR(); O[1] = MFMA32(vb, BF(p1), O[1]);    SBAR(); vb = RD((TA) + 3072); EX4(S0, 4);  \
    SBAR(); L[1] = MFMA16(sel, BF(p1), L[1]);   SBAR(); CV4(p0, S0, 0); \
    SBAR(); S1 = MFMA32(kb, q[1][1], S1);       SBAR(); kb = RD((TN) + 1024); EX4(S0, 8);  \
    SBAR(); O[0] = MFMA32(va, BF(p0), O[0]);    SBAR(); EX4(S0, 12); \
    SBAR(); L[0] = MFMA16(sel, BF(p0), L[0]);   SBAR(); CV4(p1, S0, 8); \
    SBAR(); S0 = MFMA32(ka, q[0][0], negm[0]);  SBAR(); EX4(S1, 0);  \
    SBAR(); O[0] = MFMA32(vb, BF(p1), O[0]);    SBAR(); EX4(S1, 4);  \
    SBAR(); L[0] = MFMA16(sel, BF(p1), L[0]);   SBAR(); CV4(p0, S1, 0); \
    SBAR(); S0 = MFMA32(kb, q[0][1], S0);       SBAR(); EX4(S1, 8);  \
    SBAR(); O[1] = MFMA32(va, BF(p0), O[1]);    SBAR(); va = RD((TN) + 2048); EX4(S1, 12); \
    SBAR(); L[1] = MFMA16(sel, BF(p0), L[1]);   SBAR(); CV4(p1, S1, 8); } while (0)
#pragma unroll 1
  for (int c = 0; c < 8; ++c) {
    const lp cb = la + (c & 3) * 32768, nb = la + ((c + 1) & 3) * 32768;
    TILE(cb, cb + 4096);
    SBAR(); if (kh) __builtin_amdgcn_s_setprio(0);
    TILE(cb + 4096, cb + 8192);
    TILE(cb + 8192, cb + 12288);
    SBAR();
    if (c < 7) asm volatile("s_barrier" ::: "memory");
    if (kh) __builtin_amdgcn_s_setprio(1);
    TILE(cb + 12288, nb);
  }
  SBAR();
  O[1] = MFMA32(vb, BF(p1), O[1]); L[1] = MFMA16(sel, BF(p1), L[1]);
#undef TILE
#undef EX4
#undef CV4
#undef BF
#undef RD
}

DI void attn_pass_safe(const char* __restrict__ kv, const bf16x8 q0, const bf16x8 q1, const bf16x8 sel, int lane, f32x16& O, f32x4& L) {
  O = f32x16{}; L = f32x4{};
  float m = -INFINITY;
#pragma unroll 1
  for (int t = 0; t < 64; ++t) {
    const bf16x8* a = (const bf16x8*)(kv + t * 4096 + lane * 16);
    f32x16 Sc = {};
    Sc = MFMA32(a[0], q0, Sc); Sc = MFMA32(a[64], q1, Sc);
    const float mn = fmaxf(m, xhalf_max(max16(Sc)));
    const float al = __builtin_amdgcn_exp2f(m - mn);
    m = mn;
#pragma unroll
    for (int r = 0; r < 16; ++r) { O[r] *= al; Sc[r] = __builtin_amdgcn_exp2f(Sc[r] - mn); }
    L *= al;
    const bf16x8 p0 = pack8<0>(Sc), p1 = pack8<1>(Sc);
    O = MFMA32(a[128], p0, O); L = MFMA16(sel, p0, L);
    O = MFMA32(a[192], p1, O); L = MFMA16(sel, p1, L);
  }
}

__global__ __launch_bounds__(768, 3) void k2_attn(const unsigned short* __restrict__ wphi, const unsigned short* __restrict__ woa,
                                                  const unsigned short* __restrict__ wob, char* __restrict__ ws) {
  __shared__ __attribute__((aligned(1024))) char smem[K2_LDS];
  const unsigned lds0 = (unsigned)(uintptr_t)(AS3 char*)smem;
  const int tid = threadIdx.x, lane = tid & 63, w = __builtin_amdgcn_readfirstlane(tid >> 6), h = lane >> 5;
  const int b = blockIdx.x, x = b & 7, idx = b >> 3;
  STAMP(ws, 1, 0);
  if (w >= 8) {
    const int lw = w - 8;
    const rsrc_t skv = mk_rsrc(ws + WS_KV, 256u << 10);
    __builtin_amdgcn_s_setprio(3);
    k2_issue(lds0, skv, 0, lw, lane);
    WAIT_BAR(0);
    k2_issue(lds0, skv, 1, lw, lane);
    const int head = idx;
    const size_t aoff = (size_t)((8 * x + 2 * lw) * 32 + head) * 2048 + lane * 16;
#pragma unroll
    for (int e = 0; e < 2; ++e) {
      f32x4 kvp[8];
#pragma unroll
      for (int p = 0; p < 8; ++p) kvp[p] = *(const f32x4*)(ws + WS_KVP + ((size_t)(head * 8 + p) * 512 + (2 * lw + e) * 64 + lane) * 16);
      const f32x4 sm = ((kvp[0] + kvp[1]) + (kvp[2] + kvp[3])) + ((kvp[4] + kvp[5]) + (kvp[6] + kvp[7]));
      *(AS3 u32x2*)(lds0 + K2_KVF + lw * 1024 + lane * 16 + e * 8) = u32x2{pk2(sm[0], sm[1]), pk2(sm[2], sm[3])};
    }
    if (lw == 0) {
      float z = 0.f;
#pragma unroll
      for (int p = 0; p < 8; ++p) z += *(const float*)(ws + WS_ZP + ((size_t)(head * 8 + p) * 64 + lane) * 4);
      *(AS3 unsigned short*)(lds0 + K2_ZB + lane * 2) = (unsigned short)(pk2(z, z) & 0xffffu);
    }
    bf16x8 wf[2], aq[2][2];
    load_wphi(wphi, head, lane, wf);
#pragma unroll
    for (int e = 0; e < 2; ++e) { aq[e][0] = *(const bf16x8*)(ws + WS_QA + aoff + e * 65536); aq[e][1] = *(const bf16x8*)(ws + WS_QA + aoff + e * 65536 + 1024); }
    k2_issue(lds0, skv, 2, lw, lane);
    unsigned pfs = 0;
    pf_line(pfs, (const char*)woa + (size_t)(idx * 32 + lw * 8 + (lane >> 3)) * 2048 + (x & 1) * 1024 + (lane & 7) * 128);
    pf_line(pfs, (const char*)wob + (size_t)(idx * 32 + lw * 8 + (lane >> 3)) * 2048 + (x & 1) * 1024 + (lane & 7) * 128);
    pf_drain(pfs);
#pragma unroll
    for (int e = 0; e < 2; ++e) {
      WAIT_BAR(0);
      k2_issue(lds0, skv, 3 + e, lw, lane);
      f32x16 pq = {};
      pq = MFMA32(wf[0], aq[e][0], pq); pq = MFMA32(wf[1], aq[e][1], pq);
      f32x16 ep, em;
      feat_map(pq, ep, em);
      const bf16x8 f0 = pack8<0>(ep), f1 = pack8<1>(ep), f2 = pack8<0>(em), f3 = pack8<1>(em);
      const unsigned ka = lds0 + K2_KVF + lane * 16, za = lds0 + K2_ZB + h * 32;
      f32x16 num = {}, den = {};
      num = MFMA32(lds_r128(ka), f0, num);        den = MFMA32(lds_r128(za), f0, den);
      num = MFMA32(lds_r128(ka + 1024), f1, num); den = MFMA32(lds_r128(za + 16), f1, den);
      num = MFMA32(lds_r128(ka + 2048), f2, num); den = MFMA32(lds_r128(za + 64), f2, den);
      num = MFMA32(lds_r128(ka + 3072), f3, num); den = MFMA32(lds_r128(za + 80), f3, den);
      const float r = __builtin_amdgcn_rcpf(den[0] + 1e-6f);
      num *= r;
      *(bf16x8*)(ws + WS_OA + aoff + e * 65536) = pack8<0>(num);
      *(bf16x8*)(ws + WS_OA + aoff + e * 65536 + 1024) = pack8<1>(num);
    }
#pragma unroll 1
    for (int c = 2; c < 7; ++c) {
      WAIT_BAR(0);
      if (c < 5) k2_issue(lds0, skv, c + 3, lw, lane);
    }
    return;
  }
  unsigned pfs = 0;
  if (w == 0) pf_line(pfs, ws + WS_KV + (size_t)((lane >> 3) * 32768 + idx * 1024 + (lane & 7) * 128));
  const int kh = w >> 2;
  const size_t qoff = (size_t)((8 * x + (idx >> 2)) * 32 + 8 * (idx & 3) + 2 * (w & 3) + kh) * 2048 + lane * 16;
  bf16x8 q[2][2];
  q[0][0] = *(const bf16x8*)(ws + WS_QB + qoff);          q[0][1] = *(const bf16x8*)(ws + WS_QB + qoff + 1024);
  q[1][0] = *(const bf16x8*)(ws + WS_QB + (qoff ^ 2048)); q[1][1] = *(const bf16x8*)(ws + WS_QB + (qoff ^ 2048) + 1024);
  const unsigned sel_u = ((lane & 11) == 0 && (((lane >> 2) ^ (lane >> 4)) & 1) == 0) ? 0x3f803f80u : 0u;
  const bf16x8 sel = __builtin_bit_cast(bf16x8, u32x4{sel_u, sel_u, sel_u, sel_u});
  pf_drain(pfs);
  asm volatile("s_barrier" ::: "memory");
  STAMP(ws, 1, 1);
  f32x16 O[2]; f32x4 L[2];
  attn_pass_fast(lds0, q, sel, kh, lane, O, L);
  STAMP(ws, 1, 2);
  {
    const unsigned xs = lds0 + 32768 + w * 4096 + lane * 16, xr = lds0 + 32768 + (w ^ 4) * 4096 + lane * 16;
#pragma unroll
    for (int g = 0; g < 4; ++g) *(AS3 f32x4*)(xs + g * 1024) = f32x4{O[1][4 * g], O[1][4 * g + 1], O[1][4 * g + 2], O[1][4 * g + 3]};
    *(AS3 float*)(lds0 + 28672 + w * 256 + lane * 4) = L[1][0];
    LDS_BAR();
#pragma unroll
    for (int g = 0; g < 4; ++g) {
      const f32x4 o = *(AS3 f32x4*)(xr + g * 1024);
      O[0][4 * g] += o[0]; O[0][4 * g + 1] += o[1]; O[0][4 * g + 2] += o[2]; O[0][4 * g + 3] += o[3];
    }
    L[0][0] += *(AS3 float*)(lds0 + 28672 + (w ^ 4) * 256 + lane * 4);
  }
  bool bad = !(L[0][0] < 1.8446744e19f);
#pragma unroll
  for (int i = 0; i < 16; ++i) bad |= !(fabsf(O[0][i]) < 3.0e38f);
  if (__any(bad)) attn_pass_safe(ws + WS_KV, q[0][0], q[0][1], sel, lane, O[0], L[0]);
  const float r = __builtin_amdgcn_rcpf(xhalf_sum(L[0][0]));
  O[0] *= r;
  *(bf16x8*)(ws + WS_OB + qoff) = pack8<0>(O[0]);
  *(bf16x8*)(ws + WS_OB + qoff + 1024) = pack8<1>(O[0]);
  STAMP(ws, 1, 3);
}

constexpr int K3_STAGE = 32768, K3_NBUF = 5, K3_LDS = K3_NBUF * K3_STAGE;

__global__ __launch_bounds__(768, 3) void k3_out(const unsigned short* __restrict__ woa, const unsigned short* __restrict__ wob,
                                                 const char* __restrict__ ws, unsigned short* __restrict__ out) {
  __shared__ __attribute__((aligned(1024))) char smem[K3_LDS];
  const unsigned lds0 = (unsigned)(uintptr_t)(AS3 char*)smem;
  const int tid = threadIdx.x, lane = tid & 63, w = __builtin_amdgcn_readfirstlane(tid >> 6);
  const int b = blockIdx.x, x = b & 7, idx = b >> 3;
  const int br = idx >> 4, rb = 4 * (x >> 1) + (idx & 3), cb = 4 * (x & 1) + ((idx >> 2) & 3);
  STAMP(ws, 2, 0);
  if (w >= 8) {
    const int lw = w - 8;
    const rsrc_t so = mk_rsrc(ws + (br ? WS_OB : WS_OA) + (size_t)rb * 4 * 65536, 4u * 65536u);
    const rsrc_t sw = mk_rsrc(br ? wob : woa, 1024u * 2048u);
    const unsigned ov = lw * 65536 + lane * 16;
    unsigned wv[4];
#pragma unroll
    for (int j = 0; j < 4; ++j) {
      const int k = 4 * (4 * lw + j) + (lane >> 4), c = (lane & 15) ^ (((lane >> 4) & 3) << 2);
      wv[j] = k * 2048 + (cb * 128 + c * 8) * 2 - j * 1024;
    }
    auto stage = [&](int t, unsigned buf) {
      const unsigned soo = t * 4096, sow = t * 64 * 2048;
      set_m0(buf + lw * 4096);
      dma16<0>(ov, so, soo); dma16<1024>(ov, so, soo); dma16<2048>(ov, so, soo); dma16<3072>(ov, so, soo);
      set_m0(buf + 16384 + lw * 4096);
      dma16<0>(wv[0], sw, sow); dma16<1024>(wv[1], sw, sow); dma16<2048>(wv[2], sw, sow); dma16<3072>(wv[3], sw, sow);
    };
    __builtin_amdgcn_s_setprio(2);
    stage(0, lds0); stage(1, lds0 + K3_STAGE);
    WAIT_BAR(8);
    stage(2, lds0 + 2 * K3_STAGE); stage(3, lds0 + 3 * K3_STAGE); stage(4, lds0 + 4 * K3_STAGE);
    unsigned buf = lds0;
#pragma unroll 1
    for (int t = 0; t < 11; ++t) {
      WAIT_BAR(24);
      stage(t + 5, buf);
      buf = (buf == lds0 + (K3_NBUF - 1) * K3_STAGE) ? lds0 : buf + K3_STAGE;
    }
    WAIT_BAR(24); WAIT_BAR(16); WAIT_BAR(8); WAIT_BAR(0);
    return;
  }
  const int kh = w >> 2, wm = w & 1, wn = (w >> 1) & 1, r32 = lane & 31, h = lane >> 5;
  const int q4 = (lane & 15) >> 2, p4 = lane & 3, chalf = (lane >> 4) & 1;
  const unsigned oa = wm * 8192 + kh * (4096 + 2048) + lane * 16;
  unsigned wa[2];
#pragma unroll
  for (int ns = 0; ns < 2; ++ns)
    wa[ns] = 16384 + (kh * 32 + 4 * h + q4) * 256 + ((((wn * 2 + ns) ^ q4) << 2) | (chalf * 2 + (p4 >> 1))) * 16 + (p4 & 1) * 8;
  f32x16 acc[2][2] = {};
  bf16x8 xf[2][2], wfr[2][2];
  auto rdx = [&](int set, unsigned buf, int ks) {
#pragma unroll
    for (int ms = 0; ms < 2; ++ms) xf[set][ms] = lds_r128(buf + (oa ^ (ms * 4096)) + ks * 1024);
  };
  auto rdw = [&](int set, unsigned buf, int ks, int ns) { wfr[set][ns] = lds_tr2(buf + wa[ns] + ks * 4096, buf + wa[ns] + ks * 4096 + 2048); };
  auto mm1 = [&](int set, int ms, int ns) { acc[ms][ns] = MFMA32(wfr[set][ns], xf[set][ms], acc[ms][ns]); };
  auto sub = [&](int set, bool rd, unsigned buf, int ks) {
    SBAR(); mm1(set, 0, 0); SBAR(); if (rd) rdx(set ^ 1, buf, ks);
    SBAR(); mm1(set, 1, 0); SBAR(); if (rd) rdw(set ^ 1, buf, ks, 0);
    SBAR(); mm1(set, 0, 1); SBAR(); if (rd) rdw(set ^ 1, buf, ks, 1);
    SBAR(); mm1(set, 1, 1); SBAR();
  };
  LDS_BAR();
  STAMP(ws, 2, 1);
  unsigned cur = lds0, nxt = lds0 + K3_STAGE;
  rdx(0, cur, 0); rdw(0, cur, 0, 0); rdw(0, cur, 0, 1);
  LGKM0();
#pragma unroll 1
  for (int t = 0; t < 15; ++t) {
    sub(0, true, cur, 1);
    LDS_BAR();
    sub(1, true, nxt, 0);
    cur = nxt; nxt = (nxt == lds0 + (K3_NBUF - 1) * K3_STAGE) ? lds0 : nxt + K3_STAGE;
  }
  sub(0, true, cur, 1);
  sub(1, false, cur, 0);
  STAMP(ws, 2, 2);
  const unsigned xs = lds0 + K3_STAGE + w * 8192 + lane * 16, xr = lds0 + K3_STAGE + (w ^ 4) * 8192 + lane * 16;
#pragma unroll
  for (int ns = 0; ns < 2; ++ns)
#pragma unroll
    for (int g = 0; g < 4; ++g) {
      const f32x16& a = acc[1][ns];
      *(AS3 f32x4*)(xs + ns * 4096 + g * 1024) = f32x4{a[4 * g], a[4 * g + 1], a[4 * g + 2], a[4 * g + 3]};
    }
  LDS_BAR();
  const unsigned img = lds0 + K3_STAGE + 65536 + w * 4096;
#pragma unroll
  for (int ns = 0; ns < 2; ++ns)
#pragma unroll
    for (int g = 0; g < 4; ++g) {
      const f32x16& a = acc[0][ns];
      const f32x4 o = *(AS3 f32x4*)(xr + ns * 4096 + g * 1024);
      *(AS3 u32x2*)(img + r32 * 128 + (((ns * 4 + g) ^ (r32 & 7)) << 4) + h * 8) =
          u32x2{pk2(a[4 * g] + o[0], a[4 * g + 1] + o[1]), pk2(a[4 * g + 2] + o[2], a[4 * g + 3] + o[3])};
    }
  asm volatile("s_waitcnt lgkmcnt(0)" ::: "memory");
  const int tok0 = rb * 128 + wm * 64 + kh * 32;
  unsigned short* op = out + (size_t)tok0 * 2048 + br * 1024 + cb * 128 + wn * 64;
#pragma unroll
  for (int i = 0; i < 4; ++i) {
    const int row = 8 * i + (lane >> 3), ch = lane & 7;
    const u32x4 v = *(AS3 u32x4*)(img + row * 128 + ((ch ^ (row & 7)) << 4));
    *(u32x4*)(op + (size_t)row * 2048 + ch * 8) = v;
  }
  STAMP(ws, 2, 3);
}

extern "C" void kernel_launch(void* const* d_in, const int* in_sizes, int n_in, void* d_out, int out_size, void* d_ws, size_t ws_size,
                              hipStream_t stream) {
  (void)in_sizes; (void)n_in; (void)out_size; (void)ws_size;
  typedef const unsigned short* cp;
  char* ws = (char*)d_ws;
  k1_proj<<<256, 768, 0, stream>>>((cp)d_in[0], (cp)d_in[1], (cp)d_in[2], (cp)d_in[4], (cp)d_in[5], (cp)d_in[6], (cp)d_in[7], ws);
  k2_attn<<<256, 768, 0, stream>>>((cp)d_in[2], (cp)d_in[3], (cp)d_in[8], ws);
  k3_out<<<256, 768, 0, stream>>>((cp)d_in[3], (cp)d_in[8], ws, (unsigned short*)d_out);
}
```

```cpp
#include <hip/hip_runtime.h>
#include <hip/hip_bf16.h>
#include <stdint.h>
#pragma clang diagnostic ignored "-Wint-to-pointer-cast"

typedef __bf16   bf16x8 __attribute__((ext_vector_type(8)));
typedef __bf16   bf16x2 __attribute__((ext_vector_type(2)));
typedef short    s16x4  __attribute__((ext_vector_type(4)));
typedef short    s16x8  __attribute__((ext_vector_type(8)));
typedef float    f32x16 __attribute__((ext_vector_type(16)));
typedef float    f32x4  __attribute__((ext_vector_type(4)));
typedef float    f32x2  __attribute__((ext_vector_type(2)));
typedef unsigned u32x4  __attribute__((ext_vector_type(4)));
typedef unsigned u32x2  __attribute__((ext_vector_type(2)));
typedef unsigned long long u64;
typedef __amdgpu_buffer_rsrc_t rsrc_t;

#define AS1 __attribute__((address_space(1)))
#define AS3 __attribute__((address_space(3)))
#define DI __device__ __forceinline__
#define MFMA32(a, b, c) __builtin_amdgcn_mfma_f32_32x32x16_bf16((a), (b), (c), 0, 0, 0)
#define MFMA16(a, b, c) __builtin_amdgcn_mfma_f32_16x16x32_bf16((a), (b), (c), 0, 0, 0)
#define SBAR() __builtin_amdgcn_sched_barrier(0)
#define WAIT_BAR(N) asm volatile("s_waitcnt vmcnt(" #N ") lgkmcnt(0)\n\ts_barrier" ::: "memory")
#define LGKM0() __builtin_amdgcn_s_waitcnt(0xC07F)
#define LDS_BAR() do { asm volatile("" ::: "memory"); LGKM0(); asm volatile("s_barrier" ::: "memory"); } while (0)

#define STAMP(th, i) do {} while (0)
#define MAPV 1
constexpr int D = 1024;
constexpr float LOG2E = 1.4426950408889634f;
constexpr float QSCALE = 0.17677669529663687f * LOG2E;

constexpr unsigned WS_QA  = 0;
constexpr unsigned WS_QB  = WS_QA + (8u << 20);
constexpr unsigned WS_OA  = WS_QB + (4u << 20);
constexpr unsigned WS_OB  = WS_OA + (4u << 20);
constexpr unsigned WS_KV  = WS_OB + (4u << 20);
constexpr unsigned WS_KVP = WS_KV + (256u << 10);
constexpr unsigned WS_ZP  = WS_KVP + (2u << 20);
constexpr unsigned WS_FLG = WS_ZP + (64u << 10);
constexpr unsigned WS_END = WS_FLG + (16u << 10);
constexpr int F_K = 0, F_Q = 128, F_OA = 256, F_OB = 512, F_KV = 768, F_EPOCH = 1024;

DI unsigned pk2(float lo, float hi) {
  f32x2 v = {lo, hi};
  return __builtin_bit_cast(unsigned, __builtin_convertvector(v, bf16x2));
}
template <int SS> DI bf16x8 pack8(const f32x16& x) {
  u32x4 p = {pk2(x[8 * SS + 0], x[8 * SS + 1]), pk2(x[8 * SS + 2], x[8 * SS + 3]), pk2(x[8 * SS + 4], x[8 * SS + 5]), pk2(x[8 * SS + 6], x[8 * SS + 7])};
  return __builtin_bit_cast(bf16x8, p);
}
DI bf16x8 lds_r128(unsigned a) { return *(const AS3 bf16x8*)a; }
DI bf16x8 lds_tr2(unsigned a0, unsigned a1) {
  s16x4 lo = __builtin_amdgcn_ds_read_tr16_b64_v4i16((AS3 s16x4*)a0);
  s16x4 hi = __builtin_amdgcn_ds_read_tr16_b64_v4i16((AS3 s16x4*)a1);
  s16x8 r = __builtin_shufflevector(lo, hi, 0, 1, 2, 3, 4, 5, 6, 7);
  return __builtin_bit_cast(bf16x8, r);
}
DI rsrc_t mk_rsrc(const void* p, unsigned bytes) { return __builtin_amdgcn_make_buffer_rsrc((void*)p, (short)0, (int)bytes, 0x00020000); }
DI void set_m0(unsigned v) { asm volatile("s_mov_b32 m0, %0\n\ts_nop 0" ::"s"(v) : "memory"); }
template <int IMM> DI void dma16(unsigned voff, rsrc_t srd, unsigned soff) {
  asm volatile("buffer_load_dwordx4 %0, %1, %2 offen offset:%3 lds" ::"v"(voff), "s"(srd), "s"(soff), "i"(IMM) : "memory");
}
template <int IMM> DI void dma16d(unsigned voff, rsrc_t srd, unsigned soff) {
  asm volatile("buffer_load_dwordx4 %0, %1, %2 offen offset:%3 sc1 lds" ::"v"(voff), "s"(srd), "s"(soff), "i"(IMM) : "memory");
}
DI void dma4k(unsigned lds, unsigned voff, rsrc_t srd, unsigned soff) {
  set_m0(lds); dma16d<0>(voff, srd, soff); dma16d<1024>(voff, srd, soff); dma16d<2048>(voff, srd, soff); dma16d<3072>(voff, srd, soff);
}
template <class T> DI void st16(rsrc_t r, unsigned voff, T v) { __builtin_amdgcn_raw_buffer_store_b128(__builtin_bit_cast(u32x4, v), r, (int)voff, 0, 16); }
DI void st8(rsrc_t r, unsigned voff, u32x2 v) { __builtin_amdgcn_raw_buffer_store_b64(v, r, (int)voff, 0, 16); }
DI void st4(rsrc_t r, unsigned voff, float v) { __builtin_amdgcn_raw_buffer_store_b32(__float_as_uint(v), r, (int)voff, 0, 16); }
DI u32x4 ld16(rsrc_t r, unsigned voff) { return __builtin_amdgcn_raw_buffer_load_b128(r, (int)voff, 0, 16); }
DI float ld4(rsrc_t r, unsigned voff) { return __uint_as_float(__builtin_amdgcn_raw_buffer_load_b32(r, (int)voff, 0, 16)); }
DI void post(char* ws, int f, u64 tag) { __hip_atomic_store((AS1 u64*)(ws + WS_FLG) + f, tag, __ATOMIC_RELAXED, __HIP_MEMORY_SCOPE_AGENT); }
DI void poll(char* ws, int f, u64 tag) {
#pragma unroll 1
  for (int it = 0; it < (1 << 17); ++it) {
    const u64 v = __hip_atomic_load((AS1 u64*)(ws + WS_FLG) + f, __ATOMIC_RELAXED, __HIP_MEMORY_SCOPE_AGENT);
    if (__all(v == tag)) break;
    __builtin_amdgcn_s_sleep(2);
  }
  asm volatile("" ::: "memory");
}
DI void pf_line(unsigned& sink, const void* p) { asm volatile("global_load_dword %0, %1, off" : "+v"(sink) : "v"(p) : "memory"); }
#define PIN(x) asm volatile("" : "+v"(x))
DI float xhalf_max(float v) {
  auto r = __builtin_amdgcn_permlane32_swap(__float_as_uint(v), __float_as_uint(v), false, false);
  return fmaxf(__uint_as_float(r[0]), __uint_as_float(r[1]));
}
DI float xhalf_min(float v) {
  auto r = __builtin_amdgcn_permlane32_swap(__float_as_uint(v), __float_as_uint(v), false, false);
  return fminf(__uint_as_float(r[0]), __uint_as_float(r[1]));
}
DI float xhalf_sum(float v) {
  auto r = __builtin_amdgcn_permlane32_swap(__float_as_uint(v), __float_as_uint(v), false, false);
  return __uint_as_float(r[0]) + __uint_as_float(r[1]);
}
DI float max16(const f32x16& x) {
  float a = fmaxf(fmaxf(x[0], x[1]), x[2]), b = fmaxf(fmaxf(x[3], x[4]), x[5]);
  a = fmaxf(fmaxf(a, x[6]), x[7]);   b = fmaxf(fmaxf(b, x[8]), x[9]);
  a = fmaxf(fmaxf(a, x[10]), x[11]); b = fmaxf(fmaxf(b, x[12]), x[13]);
  a = fmaxf(fmaxf(a, x[14]), x[15]);
  return fmaxf(a, b);
}
DI float min16(const f32x16& x) {
  float a = fminf(fminf(x[0], x[1]), x[2]), b = fminf(fminf(x[3], x[4]), x[5]);
  a = fminf(fminf(a, x[6]), x[7]);   b = fminf(fminf(b, x[8]), x[9]);
  a = fminf(fminf(a, x[10]), x[11]); b = fminf(fminf(b, x[12]), x[13]);
  a = fminf(fminf(a, x[14]), x[15]);
  return fminf(a, b);
}
template <bool NORM> DI void feat_map(const f32x16& p, f32x16& ep, f32x16& em) {
  const float mx = xhalf_max(max16(p)), mn = xhalf_min(min16(p));
  const f32x2 a = {-mx * LOG2E, -mx * LOG2E}, b = {mn * LOG2E, mn * LOG2E}, c = {LOG2E, LOG2E};
  f32x2 sp = {0.f, 0.f}, sm = {0.f, 0.f};
#pragma unroll
  for (int i = 0; i < 16; i += 2) {
    const f32x2 x = {p[i], p[i + 1]};
    const f32x2 u = __builtin_elementwise_fma(x, c, a), v = __builtin_elementwise_fma(x, -c, b);
    const f32x2 eu = {__builtin_amdgcn_exp2f(u[0]), __builtin_amdgcn_exp2f(u[1])}, ev = {__builtin_amdgcn_exp2f(v[0]), __builtin_amdgcn_exp2f(v[1])};
    ep[i] = eu[0]; ep[i + 1] = eu[1]; em[i] = ev[0]; em[i + 1] = ev[1];
    if (i == 0) { sp = eu; sm = ev; } else { sp += eu; sm += ev; }
  }
  if (NORM) {
    const float rp = __builtin_amdgcn_rcpf(xhalf_sum(sp[0] + sp[1])), rm = __builtin_amdgcn_rcpf(xhalf_sum(sm[0] + sm[1]));
    ep *= rp; em *= rm;
  }
}
DI bf16x8 row_sum_sel(int lane) {
  const unsigned u = ((lane & 11) == 0 && (((lane >> 2) ^ (lane >> 4)) & 1) == 0) ? 0x3f803f80u : 0u;
  return __builtin_bit_cast(bf16x8, u32x4{u, u, u, u});
}

constexpr int LDS_BYTES = 163840;
constexpr int P1_WOFF = 32768, P1_EOFF = 49152, P1_STAGE = 53248, P1_ZRED = 3 * P1_STAGE, P1_CNT = P1_ZRED + 2048, P1_GO = P1_CNT + 16, P1_PHI = 65536;
constexpr int P2_Q = 131072, P2_KVF = 147456, P2_ZB = 151552;
constexpr int P2_XO = 32768, P2_XL = 65536;
constexpr int P3_O = 32768;
DI constexpr int p3_wslot(int t) { return (98304 + (t % 6) * 16384) % 163840; }

template <int TYPE>
DI void p1_compute(unsigned lds0, char* __restrict__ ws, rsrc_t sws, int rb, int ct, int w, int lane) {
  const int tid = threadIdx.x;
  const int wm = w & 3, wn = w >> 2, r32 = lane & 31, h = lane >> 5;
  if (tid < 8) *(AS3 unsigned*)(lds0 + P1_CNT + tid * 4) = 0u;

  const int xrow = wm * 64 + r32;
  const unsigned xa = xrow * 128 + ((h ^ ((xrow >> 1) & 7)) << 4);
  const int q4 = (lane & 15) >> 2, p4 = lane & 3, chalf = (lane >> 4) & 1;
  unsigned wa[2];
#pragma unroll
  for (int ns = 0; ns < 2; ++ns)
    wa[ns] = P1_WOFF + (8 * h + q4) * 256 + ((((wn * 2 + ns) ^ q4) << 2) | (chalf * 2 + (p4 >> 1))) * 16 + (p4 & 1) * 8;
  const int erow = 64 * (ct >> 1) + 16 * wm + (lane & 15);
  const unsigned exa = erow * 128 + (((lane >> 4) ^ ((erow >> 1) & 7)) << 4);
  const unsigned ewa = P1_EOFF + (8 * (lane >> 4) + q4) * 64 + wn * 32 + p4 * 8;

  f32x16 acc[2][2] = {};
  f32x4 eacc = {};
  bf16x8 xf[2][2], wfr[2][2], ex[2], ew[2];
  auto rdx = [&](int set, unsigned buf, int ks) {
#pragma unroll
    for (int ms = 0; ms < 2; ++ms) xf[set][ms] = lds_r128(buf + ((xa ^ (ks * 32)) + ms * 4096));
  };
  auto rdw = [&](int set, unsigned buf, int ks, int ns) { wfr[set][ns] = lds_tr2(buf + wa[ns] + ks * 4096, buf + wa[ns] + ks * 4096 + 1024); };
  auto mm1 = [&](int set, int ms, int ns) {
    if (TYPE == 0 && ns == 1) acc[ms][1] = MFMA32(xf[set][ms], wfr[set][1], acc[ms][1]);
    else                      acc[ms][ns] = MFMA32(wfr[set][ns], xf[set][ms], acc[ms][ns]);
  };
  auto erd = [&](unsigned buf, int kk) {
    if (TYPE >= 2) { ex[kk] = lds_r128(buf + (exa ^ (kk * 64))); ew[kk] = lds_tr2(buf + ewa + kk * 2048, buf + ewa + kk * 2048 + 256); }
  };
  auto emm = [&](int kk) {
    if (TYPE == 2) eacc = MFMA16(ew[kk], ex[kk], eacc);
    if (TYPE == 3) eacc = MFMA16(ex[kk], ew[kk], eacc);
  };
  auto sub = [&](int set, unsigned buf, int ks) {
    SBAR(); mm1(set, 0, 0); SBAR(); rdx(set ^ 1, buf, ks);
    SBAR(); mm1(set, 1, 0); SBAR(); rdw(set ^ 1, buf, ks, 0);
    SBAR(); mm1(set, 0, 1); SBAR(); rdw(set ^ 1, buf, ks, 1);
    SBAR(); mm1(set, 1, 1); SBAR();
  };
  LDS_BAR();
  STAMP(0, 1);
  unsigned cur = lds0, nxt = lds0 + P1_STAGE;
  rdx(0, cur, 0); rdw(0, cur, 0, 0); rdw(0, cur, 0, 1);
  LGKM0();
#pragma unroll 1
  for (int t = 0; t < 16; ++t) {
    sub(0, cur, 1); erd(cur, 0);
    sub(1, cur, 2); erd(cur, 1); SBAR(); emm(0);
    sub(0, cur, 3); emm(1); SBAR();
    LDS_BAR();
    sub(1, nxt, 0);
    cur = nxt; nxt = (nxt == lds0 + 2 * P1_STAGE) ? lds0 : nxt + P1_STAGE;
  }
  STAMP(0, 2);
  bf16x8 wf[2][2];
  if constexpr (TYPE <= 1) {
    const unsigned a = lds0 + P1_PHI + (TYPE == 0 ? wn : 2 * wn) * 2048 + (4 * h + q4) * 64 + chalf * 32 + p4 * 8;
#pragma unroll
    for (int n = 0; n <= TYPE; ++n)
#pragma unroll
      for (int s = 0; s < 2; ++s) wf[n][s] = lds_tr2(a + n * 2048 + s * 1024, a + n * 2048 + s * 1024 + 512);
  }

  if (TYPE >= 2) {
#pragma unroll
    for (int ms = 0; ms < 2; ++ms)
#pragma unroll
      for (int ns = 0; ns < 2; ++ns) {
        const f32x16 v = acc[ms][ns] * QSCALE;
        const unsigned p = WS_QB + ((rb * 8 + wm * 2 + ms) * 32 + ct * 4 + wn * 2 + ns) * 2048 + lane * 16;
        st16(sws, p, pack8<0>(v)); st16(sws, p + 1024, pack8<1>(v));
      }
    const int g = lane >> 4, tok0 = rb * 256 + 64 * (ct >> 1) + 16 * wm;
    unsigned p = WS_KV + (tok0 >> 5) * 4096;
    if (TYPE == 3) p += 2048 + (wm & 1) * 1024 + (16 * wn + (lane & 15) + 32 * (g & 1)) * 16 + (g >> 1) * 8;
    else           p += wn * 1024 + (16 * (wm & 1) + (lane & 15) + 32 * (g & 1)) * 16 + (g >> 1) * 8;
    st8(sws, p, u32x2{pk2(eacc[0], eacc[1]), pk2(eacc[2], eacc[3])});
    WAIT_BAR(0);
  } else if (TYPE == 1) {
    f32x16 pq[2][2];
#pragma unroll
    for (int ms = 0; ms < 2; ++ms)
#pragma unroll
      for (int ns = 0; ns < 2; ++ns) {
        pq[ms][ns] = MFMA32(wf[ns][0], pack8<0>(acc[ms][ns]), f32x16{});
        pq[ms][ns] = MFMA32(wf[ns][1], pack8<1>(acc[ms][ns]), pq[ms][ns]);
      }
#pragma unroll
    for (int ms = 0; ms < 2; ++ms)
#pragma unroll
      for (int ns = 0; ns < 2; ++ns) {
        f32x16 ep, em;
        feat_map<false>(pq[ms][ns], ep, em);
        const unsigned p = WS_QA + ((rb * 8 + wm * 2 + ms) * 32 + ct * 4 + wn * 2 + ns) * 4096 + lane * 16;
        st16(sws, p, pack8<0>(ep)); st16(sws, p + 1024, pack8<1>(ep)); st16(sws, p + 2048, pack8<0>(em)); st16(sws, p + 3072, pack8<1>(em));
      }
  } else {
    const unsigned img = lds0 + w * 8192;
    const bf16x8 sel = row_sum_sel(lane);
    f32x16 pk[2], kvp = {}, kvm = {};
    f32x4 zp = {}, zm = {};
    bf16x8 vf[2][2], ap[2], am[2];
#pragma unroll
    for (int ms = 0; ms < 2; ++ms) {
      pk[ms] = MFMA32(wf[0][0], pack8<0>(acc[ms][0]), f32x16{});
      pk[ms] = MFMA32(wf[0][1], pack8<1>(acc[ms][0]), pk[ms]);
    }
#pragma unroll
    for (int ms = 0; ms < 2; ++ms) { vf[ms][0] = pack8<0>(acc[ms][1]); vf[ms][1] = pack8<1>(acc[ms][1]); }
    auto kvmm = [&](int ms) {
#pragma unroll
      for (int s = 0; s < 2; ++s) {
        kvp = MFMA32(ap[s], vf[ms][s], kvp); kvm = MFMA32(am[s], vf[ms][s], kvm);
        zp = MFMA16(sel, ap[s], zp);         zm = MFMA16(sel, am[s], zm);
      }
    };
#pragma unroll
    for (int ms = 0; ms < 2; ++ms) {
      f32x16 ep, em;
      feat_map<true>(pk[ms], ep, em);
      if (ms == 1) kvmm(0);
#pragma unroll
      for (int g = 0; g < 4; ++g) {
        const unsigned a = img + r32 * 64 + (((2 * g + h) ^ ((r32 >> 1) & 7)) << 3);
        *(AS3 u32x2*)a = u32x2{pk2(ep[4 * g], ep[4 * g + 1]), pk2(ep[4 * g + 2], ep[4 * g + 3])};
        *(AS3 u32x2*)(a + 2048) = u32x2{pk2(em[4 * g], em[4 * g + 1]), pk2(em[4 * g + 2], em[4 * g + 3])};
      }
      asm volatile("" ::: "memory");
#pragma unroll
      for (int s = 0; s < 2; ++s) {
        const int t0 = 16 * s + 4 * h + q4, t1 = t0 + 8, u = 4 * chalf + p4;
        const unsigned a0 = img + t0 * 64 + ((u ^ ((t0 >> 1) & 7)) << 3), a1 = img + t1 * 64 + ((u ^ ((t1 >> 1) & 7)) << 3);
        ap[s] = lds_tr2(a0, a1); am[s] = lds_tr2(a0 + 2048, a1 + 2048);
      }
      asm volatile("" ::: "memory");
    }
    kvmm(1);
#pragma unroll
    for (int g = 0; g < 4; ++g) {
      *(AS3 f32x4*)(img + g * 1024 + lane * 16) = f32x4{kvp[4 * g], kvp[4 * g + 1], kvp[4 * g + 2], kvp[4 * g + 3]};
      *(AS3 f32x4*)(img + 4096 + g * 1024 + lane * 16) = f32x4{kvm[4 * g], kvm[4 * g + 1], kvm[4 * g + 2], kvm[4 * g + 3]};
    }
    if (lane < 32) {
      const unsigned a = lds0 + P1_ZRED + w * 256 + ((lane >> 2) & 1) * 64 + (lane >> 3) * 16 + (lane & 3) * 4;
      *(AS3 float*)a = zp[0]; *(AS3 float*)(a + 128) = zm[0];
    }
    asm volatile("" ::: "memory"); LGKM0();
    if (lane == 0) __hip_atomic_fetch_add((AS3 unsigned*)(lds0 + P1_CNT), 1u, __ATOMIC_RELAXED, __HIP_MEMORY_SCOPE_WORKGROUP);
    while (__hip_atomic_load((AS3 unsigned*)(lds0 + P1_CNT), __ATOMIC_RELAXED, __HIP_MEMORY_SCOPE_WORKGROUP) < 8u) __builtin_amdgcn_s_sleep(1);
    asm volatile("" ::: "memory");
#pragma unroll
    for (int hb = 0; hb < 2; ++hb) {
      f32x4 s = *(AS3 f32x4*)(lds0 + (hb * 4) * 8192 + tid * 16);
#pragma unroll
      for (int m = 1; m < 4; ++m) s += *(AS3 f32x4*)(lds0 + (hb * 4 + m) * 8192 + tid * 16);
      st16(sws, WS_KVP + (((2 * ct + hb) * 8 + rb) * 512 + tid) * 16, s);
    }
    if (tid < 128) {
      const int hb = tid >> 6;
      float s = 0.f;
#pragma unroll
      for (int m = 0; m < 4; ++m) s += *(AS3 float*)(lds0 + P1_ZRED + (hb * 4 + m) * 256 + lane * 4);
      st4(sws, WS_ZP + (((2 * ct + hb) * 8 + rb) * 64 + lane) * 4, s);
    }
  }
  STAMP(0, 3);
}

DI void attn_pass_fast(unsigned lds0, const bf16x8 (&q)[2][2], const bf16x8 sel, int kh, int lane, f32x16 (&O)[2], f32x4 (&L)[2]) {
  typedef const AS3 char* lp;
  const lp l0 = (lp)(lds0 + lane * 16), la = l0 + kh * 16384;
#define RD(p) (*(const AS3 bf16x8*)(p))
  bf16x8 ka = RD(l0 + 98304), kb = RD(l0 + 98304 + 1024);
  f32x16 negm[2], S0, S1;
#pragma unroll
  for (int r = 0; r < 2; ++r) {
    f32x16 z = {};
    z = MFMA32(ka, q[r][0], z); z = MFMA32(kb, q[r][1], z);
    const float m = xhalf_max(max16(z));
#pragma unroll
    for (int i = 0; i < 16; ++i) negm[r][i] = -m;
    PIN(negm[r]);
    O[r] = f32x16{}; L[r] = f32x4{};
  }
  ka = RD(la + 98304); kb = RD(la + 98304 + 1024);
  bf16x8 va = RD(la + 98304 + 2048), vb = bf16x8{};
  S0 = MFMA32(ka, q[0][0], negm[0]); S0 = MFMA32(kb, q[0][1], S0);
  u32x4 p0 = {}, p1 = {};
  LGKM0();
#define EX4(SC, i) do { SC[i] = __builtin_amdgcn_exp2f(SC[i]); SC[i + 1] = __builtin_amdgcn_exp2f(SC[i + 1]); \
    SC[i + 2] = __builtin_amdgcn_exp2f(SC[i + 2]); SC[i + 3] = __builtin_amdgcn_exp2f(SC[i + 3]); PIN(SC); } while (0)
#define CV4(P, SC, i) do { P[0] = pk2(SC[i], SC[i + 1]); P[1] = pk2(SC[i + 2], SC[i + 3]); P[2] = pk2(SC[i + 4], SC[i + 5]); P[3] = pk2(SC[i + 6], SC[i + 7]); PIN(P); } while (0)
#define BF(P) __builtin_bit_cast(bf16x8, P)
#define TILE(TA, TN) do { \
    SBAR(); S1 = MFMA32(ka, q[1][0], negm[1]);  SBAR(); ka = RD(TN);          EX4(S0, 0);  \
    SBAR(); O[1] = MFMA32(vb, BF(p1), O[1]);    SBAR(); vb = RD((TA) + 3072); EX4(S0, 4);  \
    SBAR(); L[1] = MFMA16(sel, BF(p1), L[1]);   SBAR(); CV4(p0, S0, 0); \
    SBAR(); S1 = MFMA32(kb, q[1][1], S1);       SBAR(); kb = RD((TN) + 1024); EX4(S0, 8);  \
    SBAR(); O[0] = MFMA32(va, BF(p0), O[0]);    SBAR(); EX4(S0, 12); \
    SBAR(); L[0] = MFMA16(sel, BF(p0), L[0]);   SBAR(); CV4(p1, S0, 8); \
    SBAR(); S0 = MFMA32(ka, q[0][0], negm[0]);  SBAR(); EX4(S1, 0);  \
    SBAR(); O[0] = MFMA32(vb, BF(p1), O[0]);    SBAR(); EX4(S1, 4);  \
    SBAR(); L[0] = MFMA16(sel, BF(p1), L[0]);   SBAR(); CV4(p0, S1, 0); \
    SBAR(); S0 = MFMA32(kb, q[0][1], S0);       SBAR(); EX4(S1, 8);  \
    SBAR(); O[1] = MFMA32(va, BF(p0), O[1]);    SBAR(); va = RD((TN) + 2048); EX4(S1, 12); \
    SBAR(); L[1] = MFMA16(sel, BF(p0), L[1]);   SBAR(); CV4(p1, S1, 8); } while (0)
#pragma unroll 1
  for (int c = 0; c < 8; ++c) {
    const lp cb = la + ((c + 3) & 3) * 32768, nb = la + (c & 3) * 32768;
    TILE(cb, cb + 4096);
    SBAR(); if (kh) __builtin_amdgcn_s_setprio(0);
    TILE(cb + 4096, cb + 8192);
    TILE(cb + 8192, cb + 12288);
    SBAR();
    asm volatile("s_waitcnt vmcnt(0)\n\ts_barrier" ::: "memory");
    if (kh) __builtin_amdgcn_s_setprio(1);
    TILE(cb + 12288, nb);
  }
  SBAR();
  O[1] = MFMA32(vb, BF(p1), O[1]); L[1] = MFMA16(sel, BF(p1), L[1]);
  __builtin_amdgcn_s_setprio(0);
#undef TILE
#undef EX4
#undef CV4
#undef BF
#undef RD
}

DI void attn_pass_safe(rsrc_t sws, const bf16x8 q0, const bf16x8 q1, const bf16x8 sel, int lane, f32x16& O, f32x4& L) {
  O = f32x16{}; L = f32x4{};
  float m = -INFINITY;
#pragma unroll 1
  for (int t = 0; t < 64; ++t) {
    bf16x8 a[4];
#pragma unroll
    for (int j = 0; j < 4; ++j) a[j] = __builtin_bit_cast(bf16x8, ld16(sws, WS_KV + t * 4096 + j * 1024 + lane * 16));
    f32x16 Sc = {};
    Sc = MFMA32(a[0], q0, Sc); Sc = MFMA32(a[1], q1, Sc);
    const float mn = fmaxf(m, xhalf_max(max16(Sc)));
    const float al = __builtin_amdgcn_exp2f(m - mn);
    m = mn;
#pragma unroll
    for (int r = 0; r < 16; ++r) { O[r] *= al; Sc[r] = __builtin_amdgcn_exp2f(Sc[r] - mn); }
    L *= al;
    const bf16x8 p0 = pack8<0>(Sc), p1 = pack8<1>(Sc);
    O = MFMA32(a[2], p0, O); L = MFMA16(sel, p0, L);
    O = MFMA32(a[3], p1, O); L = MFMA16(sel, p1, L);
  }
}

DI void helper_program(unsigned lds0, const unsigned short* __restrict__ X, const unsigned short* __restrict__ W, const unsigned short* __restrict__ E,
                       const unsigned short* __restrict__ woa, const unsigned short* __restrict__ wob, char* __restrict__ ws, rsrc_t sws,
                       int type, int rb, int ct, int x, int idx, int lw, int lane) {
  __builtin_amdgcn_s_setprio(3);
  const u64 e0 = __hip_atomic_load((AS1 u64*)(ws + WS_FLG) + F_EPOCH, __ATOMIC_RELAXED, __HIP_MEMORY_SCOPE_AGENT);
  const unsigned vl = lane * 16;
  {
    const bool ext = type >= 2;
    const unsigned ldw = ext ? 2048u : 6144u;
    const rsrc_t sx = mk_rsrc(X + (size_t)rb * 256 * D, 256u * 2048u);
    const rsrc_t sw = mk_rsrc(W, 1024u * ldw);
    const rsrc_t se = mk_rsrc(E, 1024u * 64u);
    unsigned xv[8], wv[4];
#pragma unroll
    for (int j = 0; j < 8; ++j) {
      const int row = 8 * (8 * lw + j) + (lane >> 3), c = (lane & 7) ^ ((row >> 1) & 7);
      xv[j] = row * 2048 + c * 16 - (j & 3) * 1024;
    }
#pragma unroll
    for (int j = 0; j < 4; ++j) {
      const int k = 4 * (4 * lw + j) + (lane >> 4), c = (lane & 15) ^ (((lane >> 4) & 3) << 2);
      const int col = (type == 0) ? 1024 + ((c >> 2) & 1) * 1024 + (2 * ct + (c >> 3)) * 32 + (c & 3) * 8 : ct * 128 + c * 8;
      wv[j] = k * ldw + col * 2 - j * 1024;
    }
    auto stage = [&](int t, unsigned buf) {
      const unsigned sox = t * 128, sow = t * 64 * ldw;
      set_m0(buf + P1_WOFF + lw * 4096);
      dma16<0>(wv[0], sw, sow); dma16<1024>(wv[1], sw, sow); dma16<2048>(wv[2], sw, sow); dma16<3072>(wv[3], sw, sow);
      set_m0(buf + lw * 8192);
      dma16<0>(xv[0], sx, sox); dma16<1024>(xv[1], sx, sox); dma16<2048>(xv[2], sx, sox); dma16<3072>(xv[3], sx, sox);
      set_m0(buf + lw * 8192 + 4096);
      dma16<0>(xv[4], sx, sox); dma16<1024>(xv[5], sx, sox); dma16<2048>(xv[6], sx, sox); dma16<3072>(xv[7], sx, sox);
      if (ext) { set_m0(buf + P1_EOFF + lw * 1024); dma16<0>(vl, se, t * 4096 + lw * 1024); }
    };
    stage(0, lds0); stage(1, lds0 + P1_STAGE);
    if (ext) WAIT_BAR(13); else WAIT_BAR(12);
    stage(2, lds0 + 2 * P1_STAGE);
    unsigned buf = lds0;
#pragma unroll 1
    for (int t = 0; t < 13; ++t) {
      if (ext) WAIT_BAR(13); else WAIT_BAR(12);
      stage(t + 3, buf);
      buf = (buf == lds0 + 2 * P1_STAGE) ? lds0 : buf + P1_STAGE;
    }
    if (ext) WAIT_BAR(13); else WAIT_BAR(12);
    if (!ext && (type == 1 || lw < 2)) {
      set_m0(lds0 + P1_PHI + lw * 2048);
      const unsigned so = ((type == 0 ? 2 * ct : 4 * ct) + lw) * 2048;
      dma16<0>(vl, se, so); dma16<1024>(vl, se, so);
    }
    WAIT_BAR(0);
    WAIT_BAR(0);
  }
  const u64 tag = ((u64)(unsigned)__builtin_amdgcn_readfirstlane((unsigned)(e0 >> 32)) << 32 | (unsigned)__builtin_amdgcn_readfirstlane((unsigned)e0))
                  * 0x5851F42D4C957F2Dull + 0x14057B7EF767814Full;
  const bool lead = (lw == 0) && (lane == 0);
  if (type >= 2) { WAIT_BAR(0); if (lead) post(ws, F_KV + rb * 16 + ct, tag); }
  auto chunk = [&](int c) {
    const unsigned l = lds0 + ((c + 3) & 3) * 32768 + lw * 8192, so = WS_KV + ((c + x) & 7) * 32768 + lw * 8192;
    dma4k(l, vl, sws, so); dma4k(l + 4096, vl, sws, so + 4096);
  };
  STAMP(512, 10);
  if (lw == 0) {
    poll(ws, F_KV + x * 16 + (lane & 7), tag);
    __hip_atomic_store((AS3 unsigned*)(lds0 + P1_GO), 1u, __ATOMIC_RELAXED, __HIP_MEMORY_SCOPE_WORKGROUP);
  } else {
    while (__hip_atomic_load((AS3 unsigned*)(lds0 + P1_GO), __ATOMIC_RELAXED, __HIP_MEMORY_SCOPE_WORKGROUP) == 0u) __builtin_amdgcn_s_sleep(1);
    asm volatile("" ::: "memory");
  }
  STAMP(512, 11);
  dma4k(lds0 + P2_Q + lw * 4096, vl, sws, WS_QB + ((8 * x + (idx >> 2)) * 32 + 8 * (idx & 3)) * 2048 + lw * 4096);
  chunk(0);
  WAIT_BAR(0);
  poll(ws, F_KV + (lane >> 3) * 16 + (lane & 7), tag);
  chunk(1); chunk(2); chunk(3);
  WAIT_BAR(16);
  if (lead && type < 2) post(ws, type == 0 ? F_K + ct * 8 + rb : F_Q + rb * 8 + ct, tag);
  {
    unsigned pfs = 0;
    pf_line(pfs, (const char*)woa + (size_t)(idx * 32 + lw * 8 + (lane >> 3)) * 2048 + (x & 1) * 1024 + (lane & 7) * 128);
    pf_line(pfs, (const char*)wob + (size_t)(idx * 32 + lw * 8 + (lane >> 3)) * 2048 + (x & 1) * 1024 + (lane & 7) * 128);
    if (lw == 1) *(AS3 unsigned*)(lds0 + P2_ZB + 128 + lane * 4) = lane < 32 ? 0x3f803f80u : 0u;
    asm volatile("s_waitcnt vmcnt(0) lgkmcnt(0)\n\ts_barrier" : "+v"(pfs)::"memory");
  }
  const int head = idx;
  poll(ws, lane < 8 ? F_K + (head >> 1) * 8 + lane : F_Q + x * 8 + (head >> 2), tag);
  STAMP(512, 12);
  u32x4 kvp[2][8], fq[2][4];
  float zs[8];
#pragma unroll
  for (int e = 0; e < 2; ++e)
#pragma unroll
    for (int p = 0; p < 8; ++p) kvp[e][p] = ld16(sws, WS_KVP + ((head * 8 + p) * 512 + (2 * lw + e) * 64 + lane) * 16);
#pragma unroll
  for (int p = 0; p < 8; ++p) zs[p] = ld4(sws, WS_ZP + ((head * 8 + p) * 64 + lane) * 4);
#pragma unroll
  for (int e = 0; e < 2; ++e)
#pragma unroll
    for (int j = 0; j < 4; ++j) fq[e][j] = ld16(sws, WS_QA + ((8 * x + 2 * lw + e) * 32 + head) * 4096 + j * 1024 + vl);
  chunk(4);
  asm volatile("s_barrier" ::: "memory");
#pragma unroll
  for (int e = 0; e < 2; ++e) {
#pragma unroll
    for (int p = 0; p < 8; ++p) PIN(kvp[e][p]);
#define F4(v) __builtin_bit_cast(f32x4, v)
    const f32x4 sm = ((F4(kvp[e][0]) + F4(kvp[e][1])) + (F4(kvp[e][2]) + F4(kvp[e][3]))) + ((F4(kvp[e][4]) + F4(kvp[e][5])) + (F4(kvp[e][6]) + F4(kvp[e][7])));
#undef F4
    *(AS3 u32x2*)(lds0 + P2_KVF + lw * 1024 + vl + e * 8) = u32x2{pk2(sm[0], sm[1]), pk2(sm[2], sm[3])};
  }
  {
#pragma unroll
    for (int p = 0; p < 8; ++p) PIN(zs[p]);
    const float z = ((zs[0] + zs[1]) + (zs[2] + zs[3])) + ((zs[4] + zs[5]) + (zs[6] + zs[7]));
    if (lw == 0) *(AS3 unsigned short*)(lds0 + P2_ZB + lane * 2) = (unsigned short)(pk2(z, z) & 0xffffu);
  }
#pragma unroll
  for (int e = 0; e < 2; ++e)
#pragma unroll
    for (int j = 0; j < 4; ++j) PIN(fq[e][j]);
  chunk(5);
  WAIT_BAR(8);
  chunk(6);
  {
    const int row = lane & 15, kg = lane >> 4;
    const bool on = (((row >> 2) ^ kg) & 1) == 0;
    const unsigned za = lds0 + P2_ZB + ((on && (row & 3) == 0) ? (kg >> 1) * 32 : (on && (row & 3) == 1) ? 128 : 256);
    const unsigned ka = lds0 + P2_KVF + vl;
#pragma unroll
    for (int e = 0; e < 2; ++e) {
#define BF(v) __builtin_bit_cast(bf16x8, v)
      f32x16 np = MFMA32(lds_r128(ka), BF(fq[e][0]), f32x16{});        f32x4 dp = MFMA16(lds_r128(za), BF(fq[e][0]), f32x4{});
      np = MFMA32(lds_r128(ka + 1024), BF(fq[e][1]), np);              dp = MFMA16(lds_r128(za + 16), BF(fq[e][1]), dp);
      f32x16 nm = MFMA32(lds_r128(ka + 2048), BF(fq[e][2]), f32x16{}); f32x4 dm = MFMA16(lds_r128(za + 64), BF(fq[e][2]), f32x4{});
      nm = MFMA32(lds_r128(ka + 3072), BF(fq[e][3]), nm);              dm = MFMA16(lds_r128(za + 80), BF(fq[e][3]), dm);
#undef BF
      const float a = __builtin_amdgcn_rcpf(dp[1]), b = __builtin_amdgcn_rcpf(dm[1]);
      const float r = __builtin_amdgcn_rcpf(dp[0] * a + dm[0] * b + 1e-6f);
      const f32x16 o = np * (a * r) + nm * (b * r);
      const unsigned p = WS_OA + ((8 * x + 2 * lw + e) * 32 + head) * 2048 + vl;
      st16(sws, p, pack8<0>(o)); st16(sws, p + 1024, pack8<1>(o));
    }
  }
  WAIT_BAR(0);
  if (lead) post(ws, F_OA + x * 32 + idx, tag);
  chunk(7);
  if (blockIdx.x == 0 && lw == 1) {
    poll(ws, lane, tag); poll(ws, 64 + lane, tag); poll(ws, 128 + lane, tag);
    if (lane == 0) post(ws, F_EPOCH, tag);
  }
  const int br = idx >> 4, rb3 = 4 * (x >> 1) + (idx & 3), cb = 4 * (x & 1) + ((idx >> 2) & 3);
  const rsrc_t sw = mk_rsrc(br ? wob : woa, 1024u * 2048u);
  unsigned wv[4];
#pragma unroll
  for (int j = 0; j < 4; ++j) {
    const int k = 4 * (4 * lw + j) + (lane >> 4), c = (lane & 15) ^ (((lane >> 4) & 3) << 2);
    wv[j] = k * 2048 + (cb * 128 + c * 8) * 2 - j * 1024;
  }
  const unsigned osrc = (br ? WS_OB : WS_OA) + (rb3 * 4 + lw) * 65536;
  auto wst = [&](int t) {
    const unsigned sow = t * 64 * 2048;
    set_m0(lds0 + p3_wslot(t) + lw * 4096);
    dma16<0>(wv[0], sw, sow); dma16<1024>(wv[1], sw, sow); dma16<2048>(wv[2], sw, sow); dma16<3072>(wv[3], sw, sow);
  };
  auto ost = [&](int t) { dma4k(lds0 + P3_O + (t & 3) * 16384 + lw * 4096, vl, sws, osrc + t * 4096); };
  wst(2); wst(3);
  asm volatile("s_barrier" ::: "memory");
  wst(0); wst(1);
  WAIT_BAR(16);
  wst(4); wst(5);
  asm volatile("s_barrier" ::: "memory");
  asm volatile("s_barrier" ::: "memory");
  asm volatile("s_barrier" ::: "memory");
  if (lead) post(ws, F_OB + x * 32 + idx, tag);
  STAMP(512, 13);
  if (lw == 0) poll(ws, br ? F_OB + (rb3 >> 1) * 32 + (rb3 & 1) * 16 + (lane & 15) : F_OA + (rb3 >> 1) * 32 + (lane & 31), tag);
  asm volatile("s_barrier" ::: "memory");
  STAMP(512, 14);
  ost(0); ost(1); ost(2); ost(3);
  WAIT_BAR(12);
  WAIT_BAR(8);  ost(4); wst(6);
  WAIT_BAR(12); ost(5); wst(7);
  WAIT_BAR(16); ost(6); wst(8);
  WAIT_BAR(20); ost(7); wst(9);
  WAIT_BAR(20); ost(8); wst(10);
  WAIT_BAR(20); ost(9); wst(11);
  WAIT_BAR(20); ost(10); wst(12);
  WAIT_BAR(20); ost(11); wst(13);
  WAIT_BAR(20); ost(12); wst(14);
  WAIT_BAR(20); ost(13); wst(15);
  WAIT_BAR(20); ost(14);
  WAIT_BAR(16); ost(15);
  WAIT_BAR(12);
  WAIT_BAR(4);
  WAIT_BAR(0);
}

__global__ __launch_bounds__(768, 3) void fused_fwd(const unsigned short* __restrict__ xa, const unsigned short* __restrict__ wqkv,
                                                    const unsigned short* __restrict__ wphi, const unsigned short* __restrict__ woa,
                                                    const unsigned short* __restrict__ xb, const unsigned short* __restrict__ wq,
                                                    const unsigned short* __restrict__ wk, const unsigned short* __restrict__ wv,
                                                    const unsigned short* __restrict__ wob, char* __restrict__ ws, unsigned short* __restrict__ out) {
  __shared__ __attribute__((aligned(1024))) char smem[LDS_BYTES];
  const unsigned lds0 = (unsigned)(uintptr_t)(AS3 char*)smem;
  const int tid = threadIdx.x, lane = tid & 63, w = __builtin_amdgcn_readfirstlane(tid >> 6);
  const int b = blockIdx.x, x = b & 7, idx = b >> 3;
  const int rb = idx < 24 ? 4 * (x & 1) + (idx & 3) : 2 * (x >> 1) + (idx & 1);
  const int ct = idx < 16 ? 4 * (x >> 1) + (idx >> 2) : idx < 24 ? 2 * (x >> 1) + ((idx >> 2) & 1) : 4 * (x & 1) + ((idx >> 1) & 3);
  const int type = idx < 16 ? 0 : idx < 24 ? 1 : 2 + (ct & 1);
  const rsrc_t sws = mk_rsrc(ws, WS_END);
  STAMP(0, 0);
  if (w >= 8) {
    helper_program(lds0, type >= 2 ? xb : xa, type >= 2 ? wq : wqkv, type == 3 ? wv : type == 2 ? wk : wphi, woa, wob, ws, sws, type, rb, ct, x, idx, w - 8, lane);
    return;
  }
  if (type == 0)      p1_compute<0>(lds0, ws, sws, rb, ct, w, lane);
  else if (type == 1) p1_compute<1>(lds0, ws, sws, rb, ct, w, lane);
  else if (type == 2) p1_compute<2>(lds0, ws, sws, rb, ct, w, lane);
  else                p1_compute<3>(lds0, ws, sws, rb, ct, w, lane);

  const int kh = w >> 2, r32 = lane & 31, h = lane >> 5;
  {
    const bf16x8 sel = row_sum_sel(lane);
    LDS_BAR();
    STAMP(0, 4);
    const unsigned qa = lds0 + P2_Q + (2 * (w & 3) + kh) * 2048 + lane * 16;
    bf16x8 q[2][2];
    q[0][0] = lds_r128(qa);        q[0][1] = lds_r128(qa + 1024);
    q[1][0] = lds_r128(qa ^ 2048); q[1][1] = lds_r128((qa ^ 2048) + 1024);
    f32x16 O[2]; f32x4 L[2];
    attn_pass_fast(lds0, q, sel, kh, lane, O, L);
    STAMP(0, 5);
    const unsigned xs = lds0 + P2_XO + w * 4096 + lane * 16, xr = lds0 + P2_XO + (w ^ 4) * 4096 + lane * 16;
#pragma unroll
    for (int g = 0; g < 4; ++g) *(AS3 f32x4*)(xs + g * 1024) = f32x4{O[1][4 * g], O[1][4 * g + 1], O[1][4 * g + 2], O[1][4 * g + 3]};
    *(AS3 float*)(lds0 + P2_XL + w * 256 + lane * 4) = L[1][0];
    LDS_BAR();
#pragma unroll
    for (int g = 0; g < 4; ++g) {
      const f32x4 o = *(AS3 f32x4*)(xr + g * 1024);
      O[0][4 * g] += o[0]; O[0][4 * g + 1] += o[1]; O[0][4 * g + 2] += o[2]; O[0][4 * g + 3] += o[3];
    }
    L[0][0] += *(AS3 float*)(lds0 + P2_XL + (w ^ 4) * 256 + lane * 4);
    bool bad = !(L[0][0] < 1.8446744e19f);
#pragma unroll
    for (int i = 0; i < 16; ++i) bad |= !(fabsf(O[0][i]) < 3.0e38f);
    if (__any(bad)) attn_pass_safe(sws, q[0][0], q[0][1], sel, lane, O[0], L[0]);
    const float r = __builtin_amdgcn_rcpf(xhalf_sum(L[0][0]));
    O[0] *= r;
    const unsigned p = WS_OB + ((8 * x + (idx >> 2)) * 32 + 8 * (idx & 3) + 2 * (w & 3) + kh) * 2048 + lane * 16;
    st16(sws, p, pack8<0>(O[0])); st16(sws, p + 1024, pack8<1>(O[0]));
    WAIT_BAR(0);
    STAMP(0, 6);
  }
  const int br = idx >> 4, rb3 = 4 * (x >> 1) + (idx & 3), cb = 4 * (x & 1) + ((idx >> 2) & 3);
  const int wm = w & 1, wn = (w >> 1) & 1;
  const int q4 = (lane & 15) >> 2, p4 = lane & 3, chalf = (lane >> 4) & 1;
  const unsigned oa = wm * 8192 + kh * (4096 + 2048) + lane * 16;
  unsigned wa[2];
#pragma unroll
  for (int ns = 0; ns < 2; ++ns)
    wa[ns] = (kh * 32 + 4 * h + q4) * 256 + ((((wn * 2 + ns) ^ q4) << 2) | (chalf * 2 + (p4 >> 1))) * 16 + (p4 & 1) * 8;
  f32x16 acc[2][2] = {};
  bf16x8 xf[2][2], wfr[2][2];
  auto rdx = [&](int set, unsigned buf, int ks) {
#pragma unroll
    for (int ms = 0; ms < 2; ++ms) xf[set][ms] = lds_r128(buf + (oa ^ (ms * 4096)) + ks * 1024);
  };
  auto rdw = [&](int set, unsigned buf, int ks, int ns) { wfr[set][ns] = lds_tr2(buf + wa[ns] + ks * 4096, buf + wa[ns] + ks * 4096 + 2048); };
  auto mm1 = [&](int set, int ms, int ns) { acc[ms][ns] = MFMA32(wfr[set][ns], xf[set][ms], acc[ms][ns]); };
  auto sub = [&](int set, bool rd, unsigned ob, unsigned wb, int ks) {
    SBAR(); mm1(set, 0, 0); SBAR(); if (rd) rdx(set ^ 1, ob, ks);
    SBAR(); mm1(set, 1, 0); SBAR(); if (rd) rdw(set ^ 1, wb, ks, 0);
    SBAR(); mm1(set, 0, 1); SBAR(); if (rd) rdw(set ^ 1, wb, ks, 1);
    SBAR(); mm1(set, 1, 1); SBAR();
  };
  asm volatile("s_barrier" ::: "memory");
  LDS_BAR();
  STAMP(0, 7);
  unsigned ob = lds0 + P3_O, wb = lds0 + p3_wslot(0);
  rdx(0, ob, 0); rdw(0, wb, 0, 0); rdw(0, wb, 0, 1);
  LGKM0();
#pragma unroll 1
  for (int t = 0; t < 15; ++t) {
    sub(0, true, ob, wb, 1);
    ob = (ob == lds0 + P3_O + 3 * 16384) ? lds0 + P3_O : ob + 16384;
    wb = (wb == lds0 + 147456) ? lds0 : (wb == lds0 + 16384) ? lds0 + 98304 : wb + 16384;
    LDS_BAR();
    sub(1, true, ob, wb, 0);
  }
  sub(0, true, ob, wb, 1);
  sub(1, false, ob, wb, 0);
  STAMP(0, 8);
  const unsigned xs = lds0 + w * 8192 + lane * 16, xr = lds0 + (w ^ 4) * 8192 + lane * 16;
#pragma unroll
  for (int ns = 0; ns < 2; ++ns)
#pragma unroll
    for (int g = 0; g < 4; ++g) {
      const f32x16& a = acc[1][ns];
      *(AS3 f32x4*)(xs + ns * 4096 + g * 1024) = f32x4{a[4 * g], a[4 * g + 1], a[4 * g + 2], a[4 * g + 3]};
    }
  LDS_BAR();
  const unsigned img = lds0 + 98304 + w * 4096;
#pragma unroll
  for (int ns = 0; ns < 2; ++ns)
#pragma unroll
    for (int g = 0; g < 4; ++g) {
      const f32x16& a = acc[0][ns];
      const f32x4 o = *(AS3 f32x4*)(xr + ns * 4096 + g * 1024);
      *(AS3 u32x2*)(img + r32 * 128 + (((ns * 4 + g) ^ (r32 & 7)) << 4) + h * 8) =
          u32x2{pk2(a[4 * g] + o[0], a[4 * g + 1] + o[1]), pk2(a[4 * g + 2] + o[2], a[4 * g + 3] + o[3])};
    }
  asm volatile("" ::: "memory");
  const int tok0 = rb3 * 128 + wm * 64 + kh * 32;
  unsigned short* op = out + (size_t)tok0 * 2048 + br * 1024 + cb * 128 + wn * 64;
#pragma unroll
  for (int i = 0; i < 4; ++i) {
    const int row = 8 * i + (lane >> 3), ch = lane & 7;
    const u32x4 v = *(AS3 u32x4*)(img + row * 128 + ((ch ^ (row & 7)) << 4));
    *(u32x4*)(op + (size_t)row * 2048 + ch * 8) = v;
  }
  STAMP(0, 9);
}

extern "C" void kernel_launch(void* const* d_in, const int* in_sizes, int n_in, void* d_out, int out_size, void* d_ws, size_t ws_size,
                              hipStream_t stream) {
  (void)in_sizes; (void)n_in; (void)out_size; (void)ws_size;
  typedef const unsigned short* cp;
  fused_fwd<<<256, 768, 0, stream>>>((cp)d_in[0], (cp)d_in[1], (cp)d_in[2], (cp)d_in[3], (cp)d_in[4], (cp)d_in[5], (cp)d_in[6], (cp)d_in[7],
                                     (cp)d_in[8], (char*)d_ws, (unsigned short*)d_out);
}
```

```cpp
#include <hip/hip_runtime.h>
#include <hip/hip_bf16.h>
#include <stdint.h>
#pragma clang diagnostic ignored "-Wint-to-pointer-cast"

typedef __bf16   bf16x8 __attribute__((ext_vector_type(8)));
typedef __bf16   bf16x2 __attribute__((ext_vector_type(2)));
typedef short    s16x4  __attribute__((ext_vector_type(4)));
typedef short    s16x8  __attribute__((ext_vector_type(8)));
typedef float    f32x16 __attribute__((ext_vector_type(16)));
typedef float    f32x4  __attribute__((ext_vector_type(4)));
typedef float    f32x2  __attribute__((ext_vector_type(2)));
typedef unsigned u32x4  __attribute__((ext_vector_type(4)));
typedef unsigned u32x2  __attribute__((ext_vector_type(2)));
typedef unsigned long long u64;
typedef __amdgpu_buffer_rsrc_t rsrc_t;

#define AS1 __attribute__((address_space(1)))
#define AS3 __attribute__((address_space(3)))
#define DI __device__ __forceinline__
#define MFMA32(a, b, c) __builtin_amdgcn_mfma_f32_32x32x16_bf16((a), (b), (c), 0, 0, 0)
#define MFMA16(a, b, c) __builtin_amdgcn_mfma_f32_16x16x32_bf16((a), (b), (c), 0, 0, 0)
#define SBAR() __builtin_amdgcn_sched_barrier(0)
#define WAIT_BAR(N) asm volatile("s_waitcnt vmcnt(" #N ") lgkmcnt(0)\n\ts_barrier" ::: "memory")
#define LGKM0() __builtin_amdgcn_s_waitcnt(0xC07F)
#define LDS_BAR() do { asm volatile("" ::: "memory"); LGKM0(); asm volatile("s_barrier" ::: "memory"); } while (0)

#define STAMP(th, i) do {} while (0)
#define TS(i) do {} while (0)
#define TSOUT() do {} while (0)
#define KROT (2 * x)
#define MAPV 1
constexpr int D = 1024;
constexpr float LOG2E = 1.4426950408889634f;
constexpr float QSCALE = 0.17677669529663687f * LOG2E;

constexpr unsigned WS_QA  = 0;
constexpr unsigned WS_QB  = WS_QA + (8u << 20);
constexpr unsigned WS_OA  = WS_QB + (4u << 20);
constexpr unsigned WS_OB  = WS_OA + (4u << 20);
constexpr unsigned WS_KV  = WS_OB + (4u << 20);
constexpr unsigned WS_KVP = WS_KV + (256u << 10);
constexpr unsigned WS_ZP  = WS_KVP + (2u << 20);
constexpr unsigned WS_FLG = WS_ZP + (64u << 10);
constexpr unsigned WS_END = WS_FLG + (16u << 10);
constexpr int F_K = 0, F_Q = 128, F_OA = 256, F_OB = 512, F_KV = 768, F_EPOCH = 1024;

DI unsigned pk2(float lo, float hi) {
  f32x2 v = {lo, hi};
  return __builtin_bit_cast(unsigned, __builtin_convertvector(v, bf16x2));
}
template <int SS> DI bf16x8 pack8(const f32x16& x) {
  u32x4 p = {pk2(x[8 * SS + 0], x[8 * SS + 1]), pk2(x[8 * SS + 2], x[8 * SS + 3]), pk2(x[8 * SS + 4], x[8 * SS + 5]), pk2(x[8 * SS + 6], x[8 * SS + 7])};
  return __builtin_bit_cast(bf16x8, p);
}
DI bf16x8 lds_r128(unsigned a) { return *(const AS3 bf16x8*)a; }
DI bf16x8 lds_tr2(unsigned a0, unsigned a1) {
  s16x4 lo = __builtin_amdgcn_ds_read_tr16_b64_v4i16((AS3 s16x4*)a0);
  s16x4 hi = __builtin_amdgcn_ds_read_tr16_b64_v4i16((AS3 s16x4*)a1);
  s16x8 r = __builtin_shufflevector(lo, hi, 0, 1, 2, 3, 4, 5, 6, 7);
  return __builtin_bit_cast(bf16x8, r);
}
DI rsrc_t mk_rsrc(const void* p, unsigned bytes) { return __builtin_amdgcn_make_buffer_rsrc((void*)p, (short)0, (int)bytes, 0x00020000); }
DI void set_m0(unsigned v) { asm volatile("s_mov_b32 m0, %0\n\ts_nop 0" ::"s"(v) : "memory"); }
template <int IMM> DI void dma16(unsigned voff, rsrc_t srd, unsigned soff) {
  asm volatile("buffer_load_dwordx4 %0, %1, %2 offen offset:%3 lds" ::"v"(voff), "s"(srd), "s"(soff), "i"(IMM) : "memory");
}
template <int IMM> DI void dma16d(unsigned voff, rsrc_t srd, unsigned soff) {
  asm volatile("buffer_load_dwordx4 %0, %1, %2 offen offset:%3 sc1 lds" ::"v"(voff), "s"(srd), "s"(soff), "i"(IMM) : "memory");
}
DI void dma4k(unsigned lds, unsigned voff, rsrc_t srd, unsigned soff) {
  set_m0(lds); dma16d<0>(voff, srd, soff); dma16d<1024>(voff, srd, soff); dma16d<2048>(voff, srd, soff); dma16d<3072>(voff, srd, soff);
}
template <class T> DI void st16(rsrc_t r, unsigned voff, T v) { __builtin_amdgcn_raw_buffer_store_b128(__builtin_bit_cast(u32x4, v), r, (int)voff, 0, 16); }
DI void st8(rsrc_t r, unsigned voff, u32x2 v) { __builtin_amdgcn_raw_buffer_store_b64(v, r, (int)voff, 0, 16); }
DI void st4(rsrc_t r, unsigned voff, float v) { __builtin_amdgcn_raw_buffer_store_b32(__float_as_uint(v), r, (int)voff, 0, 16); }
DI u32x4 ld16(rsrc_t r, unsigned voff) { return __builtin_amdgcn_raw_buffer_load_b128(r, (int)voff, 0, 16); }
DI float ld4(rsrc_t r, unsigned voff) { return __uint_as_float(__builtin_amdgcn_raw_buffer_load_b32(r, (int)voff, 0, 16)); }
DI void post(char* ws, int f, u64 tag) { __hip_atomic_store((AS1 u64*)(ws + WS_FLG) + f, tag, __ATOMIC_RELAXED, __HIP_MEMORY_SCOPE_AGENT); }
DI void poll(char* ws, int f, u64 tag) {
#pragma unroll 1
  for (int it = 0; it < (1 << 17); ++it) {
    const u64 v = __hip_atomic_load((AS1 u64*)(ws + WS_FLG) + f, __ATOMIC_RELAXED, __HIP_MEMORY_SCOPE_AGENT);
    if (__all(v == tag)) break;
    __builtin_amdgcn_s_sleep(2);
  }
  asm volatile("" ::: "memory");
}
DI void pf_line(unsigned& sink, const void* p) { asm volatile("global_load_dword %0, %1, off" : "+v"(sink) : "v"(p) : "memory"); }
#define PIN(x) asm volatile("" : "+v"(x))
DI float xhalf_max(float v) {
  auto r = __builtin_amdgcn_permlane32_swap(__float_as_uint(v), __float_as_uint(v), false, false);
  return fmaxf(__uint_as_float(r[0]), __uint_as_float(r[1]));
}
DI float xhalf_min(float v) {
  auto r = __builtin_amdgcn_permlane32_swap(__float_as_uint(v), __float_as_uint(v), false, false);
  return fminf(__uint_as_float(r[0]), __uint_as_float(r[1]));
}
DI float xhalf_sum(float v) {
  auto r = __builtin_amdgcn_permlane32_swap(__float_as_uint(v), __float_as_uint(v), false, false);
  return __uint_as_float(r[0]) + __uint_as_float(r[1]);
}
DI float max16(const f32x16& x) {
  float a = fmaxf(fmaxf(x[0], x[1]), x[2]), b = fmaxf(fmaxf(x[3], x[4]), x[5]);
  a = fmaxf(fmaxf(a, x[6]), x[7]);   b = fmaxf(fmaxf(b, x[8]), x[9]);
  a = fmaxf(fmaxf(a, x[10]), x[11]); b = fmaxf(fmaxf(b, x[12]), x[13]);
  a = fmaxf(fmaxf(a, x[14]), x[15]);
  return fmaxf(a, b);
}
DI float min16(const f32x16& x) {
  float a = fminf(fminf(x[0], x[1]), x[2]), b = fminf(fminf(x[3], x[4]), x[5]);
  a = fminf(fminf(a, x[6]), x[7]);   b = fminf(fminf(b, x[8]), x[9]);
  a = fminf(fminf(a, x[10]), x[11]); b = fminf(fminf(b, x[12]), x[13]);
  a = fminf(fminf(a, x[14]), x[15]);
  return fminf(a, b);
}
template <bool NORM> DI void feat_map(const f32x16& p, f32x16& ep, f32x16& em) {
  const float mx = xhalf_max(max16(p)), mn = xhalf_min(min16(p));
  const f32x2 a = {-mx * LOG2E, -mx * LOG2E}, b = {mn * LOG2E, mn * LOG2E}, c = {LOG2E, LOG2E};
  f32x2 sp = {0.f, 0.f}, sm = {0.f, 0.f};
#pragma unroll
  for (int i = 0; i < 16; i += 2) {
    const f32x2 x = {p[i], p[i + 1]};
    const f32x2 u = __builtin_elementwise_fma(x, c, a), v = __builtin_elementwise_fma(x, -c, b);
    const f32x2 eu = {__builtin_amdgcn_exp2f(u[0]), __builtin_amdgcn_exp2f(u[1])}, ev = {__builtin_amdgcn_exp2f(v[0]), __builtin_amdgcn_exp2f(v[1])};
    ep[i] = eu[0]; ep[i + 1] = eu[1]; em[i] = ev[0]; em[i + 1] = ev[1];
    if (i == 0) { sp = eu; sm = ev; } else { sp += eu; sm += ev; }
  }
  if (NORM) {
    const float rp = __builtin_amdgcn_rcpf(xhalf_sum(sp[0] + sp[1])), rm = __builtin_amdgcn_rcpf(xhalf_sum(sm[0] + sm[1]));
    ep *= rp; em *= rm;
  }
}
DI bf16x8 row_sum_sel(int lane) {
  const unsigned u = ((lane & 11) == 0 && (((lane >> 2) ^ (lane >> 4)) & 1) == 0) ? 0x3f803f80u : 0u;
  return __builtin_bit_cast(bf16x8, u32x4{u, u, u, u});
}

constexpr int LDS_BYTES = 163840;
constexpr int P1_WOFF = 32768, P1_EOFF = 49152, P1_STAGE = 53248, P1_ZRED = 3 * P1_STAGE, P1_CNT = P1_ZRED + 2048, P1_GO = P1_CNT + 16, P1_PHI = 65536;
constexpr int P2_Q = 131072, P2_KVF = 147456, P2_ZB = 151552;
constexpr int P2_XO = 32768, P2_XL = 65536;
constexpr int P3_O = 32768;
DI constexpr int p3_wslot(int t) { return (98304 + (t % 6) * 16384) % 163840; }

template <int TYPE>
DI void p1_compute(unsigned lds0, char* __restrict__ ws, rsrc_t sws, int rb, int ct, int w, int lane) {
  const int tid = threadIdx.x;
  const int wm = w & 3, wn = w >> 2, r32 = lane & 31, h = lane >> 5;
  if (tid < 8) *(AS3 unsigned*)(lds0 + P1_CNT + tid * 4) = 0u;

  const int xrow = wm * 64 + r32;
  const unsigned xa = xrow * 128 + ((h ^ ((xrow >> 1) & 7)) << 4);
  const int q4 = (lane & 15) >> 2, p4 = lane & 3, chalf = (lane >> 4) & 1;
  unsigned wa[2];
#pragma unroll
  for (int ns = 0; ns < 2; ++ns)
    wa[ns] = P1_WOFF + (8 * h + q4) * 256 + ((((wn * 2 + ns) ^ q4) << 2) | (chalf * 2 + (p4 >> 1))) * 16 + (p4 & 1) * 8;
  const int erow = 64 * (ct >> 1) + 16 * wm + (lane & 15);
  const unsigned exa = erow * 128 + (((lane >> 4) ^ ((erow >> 1) & 7)) << 4);
  const unsigned ewa = P1_EOFF + (8 * (lane >> 4) + q4) * 64 + wn * 32 + p4 * 8;

  f32x16 acc[2][2] = {};
  f32x4 eacc = {};
  bf16x8 xf[2][2], wfr[2][2], ex[2], ew[2];
  auto rdx = [&](int set, unsigned buf, int ks) {
#pragma unroll
    for (int ms = 0; ms < 2; ++ms) xf[set][ms] = lds_r128(buf + ((xa ^ (ks * 32)) + ms * 4096));
  };
  auto rdw = [&](int set, unsigned buf, int ks, int ns) { wfr[set][ns] = lds_tr2(buf + wa[ns] + ks * 4096, buf + wa[ns] + ks * 4096 + 1024); };
  auto mm1 = [&](int set, int ms, int ns) {
    if (TYPE == 0 && ns == 1) acc[ms][1] = MFMA32(xf[set][ms], wfr[set][1], acc[ms][1]);
    else                      acc[ms][ns] = MFMA32(wfr[set][ns], xf[set][ms], acc[ms][ns]);
  };
  auto erd = [&](unsigned buf, int kk) {
    if (TYPE >= 2) { ex[kk] = lds_r128(buf + (exa ^ (kk * 64))); ew[kk] = lds_tr2(buf + ewa + kk * 2048, buf + ewa + kk * 2048 + 256); }
  };
  auto emm = [&](int kk) {
    if (TYPE == 2) eacc = MFMA16(ew[kk], ex[kk], eacc);
    if (TYPE == 3) eacc = MFMA16(ex[kk], ew[kk], eacc);
  };
  auto sub = [&](int set, unsigned buf, int ks) {
    SBAR(); mm1(set, 0, 0); SBAR(); rdx(set ^ 1, buf, ks);
    SBAR(); mm1(set, 1, 0); SBAR(); rdw(set ^ 1, buf, ks, 0);
    SBAR(); mm1(set, 0, 1); SBAR(); rdw(set ^ 1, buf, ks, 1);
    SBAR(); mm1(set, 1, 1); SBAR();
  };
  LDS_BAR();
  STAMP(0, 1);
  unsigned cur = lds0, nxt = lds0 + P1_STAGE;
  rdx(0, cur, 0); rdw(0, cur, 0, 0); rdw(0, cur, 0, 1);
  LGKM0();
#pragma unroll 1
  for (int t = 0; t < 16; ++t) {
    sub(0, cur, 1); erd(cur, 0);
    sub(1, cur, 2); erd(cur, 1); SBAR(); emm(0);
    sub(0, cur, 3); emm(1); SBAR();
    LDS_BAR();
    sub(1, nxt, 0);
    cur = nxt; nxt = (nxt == lds0 + 2 * P1_STAGE) ? lds0 : nxt + P1_STAGE;
  }
  STAMP(0, 2);
  bf16x8 wf[2][2];
  if constexpr (TYPE <= 1) {
    const unsigned a = lds0 + P1_PHI + (TYPE == 0 ? wn : 2 * wn) * 2048 + (4 * h + q4) * 64 + chalf * 32 + p4 * 8;
#pragma unroll
    for (int n = 0; n <= TYPE; ++n)
#pragma unroll
      for (int s = 0; s < 2; ++s) wf[n][s] = lds_tr2(a + n * 2048 + s * 1024, a + n * 2048 + s * 1024 + 512);
  }

  if (TYPE >= 2) {
#pragma unroll
    for (int ms = 0; ms < 2; ++ms)
#pragma unroll
      for (int ns = 0; ns < 2; ++ns) {
        const f32x16 v = acc[ms][ns] * QSCALE;
        const unsigned p = WS_QB + ((rb * 8 + wm * 2 + ms) * 32 + ct * 4 + wn * 2 + ns) * 2048 + lane * 16;
        st16(sws, p, pack8<0>(v)); st16(sws, p + 1024, pack8<1>(v));
      }
    const int g = lane >> 4, tok0 = rb * 256 + 64 * (ct >> 1) + 16 * wm;
    unsigned p = WS_KV + (tok0 >> 5) * 4096;
    if (TYPE == 3) p += 2048 + (wm & 1) * 1024 + (16 * wn + (lane & 15) + 32 * (g & 1)) * 16 + (g >> 1) * 8;
    else           p += wn * 1024 + (16 * (wm & 1) + (lane & 15) + 32 * (g & 1)) * 16 + (g >> 1) * 8;
    st8(sws, p, u32x2{pk2(eacc[0], eacc[1]), pk2(eacc[2], eacc[3])});
    WAIT_BAR(0);
  } else if (TYPE == 1) {
    f32x16 pq[2][2];
#pragma unroll
    for (int ms = 0; ms < 2; ++ms)
#pragma unroll
      for (int ns = 0; ns < 2; ++ns) {
        pq[ms][ns] = MFMA32(wf[ns][0], pack8<0>(acc[ms][ns]), f32x16{});
        pq[ms][ns] = MFMA32(wf[ns][1], pack8<1>(acc[ms][ns]), pq[ms][ns]);
      }
#pragma unroll
    for (int ms = 0; ms < 2; ++ms)
#pragma unroll
      for (int ns = 0; ns < 2; ++ns) {
        f32x16 ep, em;
        feat_map<false>(pq[ms][ns], ep, em);
        const unsigned p = WS_QA + ((rb * 8 + wm * 2 + ms) * 32 + ct * 4 + wn * 2 + ns) * 4096 + lane * 16;
        st16(sws, p, pack8<0>(ep)); st16(sws, p + 1024, pack8<1>(ep)); st16(sws, p + 2048, pack8<0>(em)); st16(sws, p + 3072, pack8<1>(em));
      }
  } else {
    const unsigned img = lds0 + w * 8192;
    const bf16x8 sel = row_sum_sel(lane);
    f32x16 pk[2], kvp = {}, kvm = {};
    f32x4 zp = {}, zm = {};
    bf16x8 vf[2][2], ap[2], am[2];
#pragma unroll
    for (int ms = 0; ms < 2; ++ms) {
      pk[ms] = MFMA32(wf[0][0], pack8<0>(acc[ms][0]), f32x16{});
      pk[ms] = MFMA32(wf[0][1], pack8<1>(acc[ms][0]), pk[ms]);
    }
#pragma unroll
    for (int ms = 0; ms < 2; ++ms) { vf[ms][0] = pack8<0>(acc[ms][1]); vf[ms][1] = pack8<1>(acc[ms][1]); }
    auto kvmm = [&](int ms) {
#pragma unroll
      for (int s = 0; s < 2; ++s) {
        kvp = MFMA32(ap[s], vf[ms][s], kvp); kvm = MFMA32(am[s], vf[ms][s], kvm);
        zp = MFMA16(sel, ap[s], zp);         zm = MFMA16(sel, am[s], zm);
      }
    };
#pragma unroll
    for (int ms = 0; ms < 2; ++ms) {
      f32x16 ep, em;
      feat_map<true>(pk[ms], ep, em);
      if (ms == 1) kvmm(0);
#pragma unroll
      for (int g = 0; g < 4; ++g) {
        const unsigned a = img + r32 * 64 + (((2 * g + h) ^ ((r32 >> 1) & 7)) << 3);
        *(AS3 u32x2*)a = u32x2{pk2(ep[4 * g], ep[4 * g + 1]), pk2(ep[4 * g + 2], ep[4 * g + 3])};
        *(AS3 u32x2*)(a + 2048) = u32x2{pk2(em[4 * g], em[4 * g + 1]), pk2(em[4 * g + 2], em[4 * g + 3])};
      }
      asm volatile("" ::: "memory");
#pragma unroll
      for (int s = 0; s < 2; ++s) {
        const int t0 = 16 * s + 4 * h + q4, t1 = t0 + 8, u = 4 * chalf + p4;
        const unsigned a0 = img + t0 * 64 + ((u ^ ((t0 >> 1) & 7)) << 3), a1 = img + t1 * 64 + ((u ^ ((t1 >> 1) & 7)) << 3);
        ap[s] = lds_tr2(a0, a1); am[s] = lds_tr2(a0 + 2048, a1 + 2048);
      }
      asm volatile("" ::: "memory");
    }
    kvmm(1);
#pragma unroll
    for (int g = 0; g < 4; ++g) {
      *(AS3 f32x4*)(img + g * 1024 + lane * 16) = f32x4{kvp[4 * g], kvp[4 * g + 1], kvp[4 * g + 2], kvp[4 * g + 3]};
      *(AS3 f32x4*)(img + 4096 + g * 1024 + lane * 16) = f32x4{kvm[4 * g], kvm[4 * g + 1], kvm[4 * g + 2], kvm[4 * g + 3]};
    }
    if (lane < 32) {
      const unsigned a = lds0 + P1_ZRED + w * 256 + ((lane >> 2) & 1) * 64 + (lane >> 3) * 16 + (lane & 3) * 4;
      *(AS3 float*)a = zp[0]; *(AS3 float*)(a + 128) = zm[0];
    }
    asm volatile("" ::: "memory"); LGKM0();
    if (lane == 0) __hip_atomic_fetch_add((AS3 unsigned*)(lds0 + P1_CNT), 1u, __ATOMIC_RELAXED, __HIP_MEMORY_SCOPE_WORKGROUP);
    while (__hip_atomic_load((AS3 unsigned*)(lds0 + P1_CNT), __ATOMIC_RELAXED, __HIP_MEMORY_SCOPE_WORKGROUP) < 8u) __builtin_amdgcn_s_sleep(1);
    asm volatile("" ::: "memory");
#pragma unroll
    for (int hb = 0; hb < 2; ++hb) {
      f32x4 s = *(AS3 f32x4*)(lds0 + (hb * 4) * 8192 + tid * 16);
#pragma unroll
      for (int m = 1; m < 4; ++m) s += *(AS3 f32x4*)(lds0 + (hb * 4 + m) * 8192 + tid * 16);
      st16(sws, WS_KVP + (((2 * ct + hb) * 8 + rb) * 512 + tid) * 16, s);
    }
    if (tid < 128) {
      const int hb = tid >> 6;
      float s = 0.f;
#pragma unroll
      for (int m = 0; m < 4; ++m) s += *(AS3 float*)(lds0 + P1_ZRED + (hb * 4 + m) * 256 + lane * 4);
      st4(sws, WS_ZP + (((2 * ct + hb) * 8 + rb) * 64 + lane) * 4, s);
    }
  }
  STAMP(0, 3);
}

DI void attn_pass_fast(unsigned lds0, const bf16x8 (&q)[2][2], const bf16x8 sel, int kh, int lane, f32x16 (&O)[2], f32x4 (&L)[2], unsigned& tv) {
  TS(0);
  typedef const AS3 char* lp;
  const lp l0 = (lp)(lds0 + lane * 16), la = l0 + kh * 16384;
#define RD(p) (*(const AS3 bf16x8*)(p))
  if (kh) __builtin_amdgcn_s_setprio(1);
  bf16x8 ka = RD(l0 + 98304), kb = RD(l0 + 98304 + 1024);
  f32x16 negm[2], S0, S1;
#pragma unroll
  for (int r = 0; r < 2; ++r) {
    f32x16 z = {};
    z = MFMA32(ka, q[r][0], z); z = MFMA32(kb, q[r][1], z);
    const float m = xhalf_max(max16(z));
#pragma unroll
    for (int i = 0; i < 16; ++i) negm[r][i] = -m;
    PIN(negm[r]);
    O[r] = f32x16{}; L[r] = f32x4{};
  }
  ka = RD(la + 98304); kb = RD(la + 98304 + 1024);
  bf16x8 va = RD(la + 98304 + 2048), vb = bf16x8{};
  S0 = MFMA32(ka, q[0][0], negm[0]); S0 = MFMA32(kb, q[0][1], S0);
  u32x4 p0 = {}, p1 = {};
  LGKM0();
#define EX4(SC, i) do { SC[i] = __builtin_amdgcn_exp2f(SC[i]); SC[i + 1] = __builtin_amdgcn_exp2f(SC[i + 1]); \
    SC[i + 2] = __builtin_amdgcn_exp2f(SC[i + 2]); SC[i + 3] = __builtin_amdgcn_exp2f(SC[i + 3]); PIN(SC); } while (0)
#define CV4(P, SC, i) do { P[0] = pk2(SC[i], SC[i + 1]); P[1] = pk2(SC[i + 2], SC[i + 3]); P[2] = pk2(SC[i + 4], SC[i + 5]); P[3] = pk2(SC[i + 6], SC[i + 7]); PIN(P); } while (0)
#define BF(P) __builtin_bit_cast(bf16x8, P)
#define TILE(TA, TN) do { \
    SBAR(); S1 = MFMA32(ka, q[1][0], negm[1]);  SBAR(); ka = RD(TN);          EX4(S0, 0);  \
    SBAR(); O[1] = MFMA32(vb, BF(p1), O[1]);    SBAR(); vb = RD((TA) + 3072); EX4(S0, 4);  \
    SBAR(); L[1] = MFMA16(sel, BF(p1), L[1]);   SBAR(); CV4(p0, S0, 0); \
    SBAR(); S1 = MFMA32(kb, q[1][1], S1);       SBAR(); kb = RD((TN) + 1024); EX4(S0, 8);  \
    SBAR(); O[0] = MFMA32(va, BF(p0), O[0]);    SBAR(); EX4(S0, 12); \
    SBAR(); L[0] = MFMA16(sel, BF(p0), L[0]);   SBAR(); CV4(p1, S0, 8); \
    SBAR(); S0 = MFMA32(ka, q[0][0], negm[0]);  SBAR(); EX4(S1, 0);  \
    SBAR(); O[0] = MFMA32(vb, BF(p1), O[0]);    SBAR(); EX4(S1, 4);  \
    SBAR(); L[0] = MFMA16(sel, BF(p1), L[0]);   SBAR(); CV4(p0, S1, 0); \
    SBAR(); S0 = MFMA32(kb, q[0][1], S0);       SBAR(); EX4(S1, 8);  \
    SBAR(); O[1] = MFMA32(va, BF(p0), O[1]);    SBAR(); va = RD((TN) + 2048); EX4(S1, 12); \
    SBAR(); L[1] = MFMA16(sel, BF(p0), L[1]);   SBAR(); CV4(p1, S1, 8); } while (0)
  TS(1);
#pragma unroll 1
  for (int c = 0; c < 8; ++c) {
    const lp cb = la + ((c + 3) & 3) * 32768, nb = la + (c & 3) * 32768;
    TILE(cb, cb + 4096);
    TILE(cb + 4096, cb + 8192);
    SBAR(); if (kh) __builtin_amdgcn_s_setprio(0);
    TILE(cb + 8192, cb + 12288);
    SBAR(); TS(2 + 2 * c);
    asm volatile("s_waitcnt vmcnt(0)\n\ts_barrier" ::: "memory"); TS(3 + 2 * c);
    if (kh) __builtin_amdgcn_s_setprio(1);
    TILE(cb + 12288, nb);
  }
  SBAR();
  O[1] = MFMA32(vb, BF(p1), O[1]); L[1] = MFMA16(sel, BF(p1), L[1]);
  __builtin_amdgcn_s_setprio(0);
  TS(18);
#undef TILE
#undef EX4
#undef CV4
#undef BF
#undef RD
}

DI void attn_pass_safe(rsrc_t sws, const bf16x8 q0, const bf16x8 q1, const bf16x8 sel, int lane, f32x16& O, f32x4& L) {
  O = f32x16{}; L = f32x4{};
  float m = -INFINITY;
#pragma unroll 1
  for (int t = 0; t < 64; ++t) {
    bf16x8 a[4];
#pragma unroll
    for (int j = 0; j < 4; ++j) a[j] = __builtin_bit_cast(bf16x8, ld16(sws, WS_KV + t * 4096 + j * 1024 + lane * 16));
    f32x16 Sc = {};
    Sc = MFMA32(a[0], q0, Sc); Sc = MFMA32(a[1], q1, Sc);
    const float mn = fmaxf(m, xhalf_max(max16(Sc)));
    const float al = __builtin_amdgcn_exp2f(m - mn);
    m = mn;
#pragma unroll
    for (int r = 0; r < 16; ++r) { O[r] *= al; Sc[r] = __builtin_amdgcn_exp2f(Sc[r] - mn); }
    L *= al;
    const bf16x8 p0 = pack8<0>(Sc), p1 = pack8<1>(Sc);
    O = MFMA32(a[2], p0, O); L = MFMA16(sel, p0, L);
    O = MFMA32(a[3], p1, O); L = MFMA16(sel, p1, L);
  }
}

DI void helper_program(unsigned lds0, const unsigned short* __restrict__ X, const unsigned short* __restrict__ W, const unsigned short* __restrict__ E,
                       const unsigned short* __restrict__ woa, const unsigned short* __restrict__ wob, char* __restrict__ ws, rsrc_t sws,
                       int type, int rb, int ct, int x, int idx, int lw, int lane) {
  __builtin_amdgcn_s_setprio(3);
  const u64 e0 = __hip_atomic_load((AS1 u64*)(ws + WS_FLG) + F_EPOCH, __ATOMIC_RELAXED, __HIP_MEMORY_SCOPE_AGENT);
  const unsigned vl = lane * 16;
  {
    const bool ext = type >= 2;
    const unsigned ldw = ext ? 2048u : 6144u;
    const rsrc_t sx = mk_rsrc(X + (size_t)rb * 256 * D, 256u * 2048u);
    const rsrc_t sw = mk_rsrc(W, 1024u * ldw);
    const rsrc_t se = mk_rsrc(E, 1024u * 64u);
    unsigned xv[8], wv[4];
#pragma unroll
    for (int j = 0; j < 8; ++j) {
      const int row = 8 * (8 * lw + j) + (lane >> 3), c = (lane & 7) ^ ((row >> 1) & 7);
      xv[j] = row * 2048 + c * 16 - (j & 3) * 1024;
    }
#pragma unroll
    for (int j = 0; j < 4; ++j) {
      const int k = 4 * (4 * lw + j) + (lane >> 4), c = (lane & 15) ^ (((lane >> 4) & 3) << 2);
      const int col = (type == 0) ? 1024 + ((c >> 2) & 1) * 1024 + (2 * ct + (c >> 3)) * 32 + (c & 3) * 8 : ct * 128 + c * 8;
      wv[j] = k * ldw + col * 2 - j * 1024;
    }
    auto stage = [&](int t0, unsigned buf) {
      const int t = (t0 + KROT) & 15;
      const unsigned sox = t * 128, sow = t * 64 * ldw;
      set_m0(buf + P1_WOFF + lw * 4096);
      dma16<0>(wv[0], sw, sow); dma16<1024>(wv[1], sw, sow); dma16<2048>(wv[2], sw, sow); dma16<3072>(wv[3], sw, sow);
      set_m0(buf + lw * 8192);
      dma16<0>(xv[0], sx, sox); dma16<1024>(xv[1], sx, sox); dma16<2048>(xv[2], sx, sox); dma16<3072>(xv[3], sx, sox);
      set_m0(buf + lw * 8192 + 4096);
      dma16<0>(xv[4], sx, sox); dma16<1024>(xv[5], sx, sox); dma16<2048>(xv[6], sx, sox); dma16<3072>(xv[7], sx, sox);
      if (ext) { set_m0(buf + P1_EOFF + lw * 1024); dma16<0>(vl, se, t * 4096 + lw * 1024); }
    };
    stage(0, lds0); stage(1, lds0 + P1_STAGE);
    if (ext) WAIT_BAR(13); else WAIT_BAR(12);
    stage(2, lds0 + 2 * P1_STAGE);
    unsigned buf = lds0;
#pragma unroll 1
    for (int t = 0; t < 13; ++t) {
      if (ext) WAIT_BAR(13); else WAIT_BAR(12);
      stage(t + 3, buf);
      buf = (buf == lds0 + 2 * P1_STAGE) ? lds0 : buf + P1_STAGE;
    }
    if (ext) WAIT_BAR(13); else WAIT_BAR(12);
    if (!ext && (type == 1 || lw < 2)) {
      set_m0(lds0 + P1_PHI + lw * 2048);
      const unsigned so = ((type == 0 ? 2 * ct : 4 * ct) + lw) * 2048;
      dma16<0>(vl, se, so); dma16<1024>(vl, se, so);
    }
    WAIT_BAR(0);
    WAIT_BAR(0);
  }
  const u64 tag = ((u64)(unsigned)__builtin_amdgcn_readfirstlane((unsigned)(e0 >> 32)) << 32 | (unsigned)__builtin_amdgcn_readfirstlane((unsigned)e0))
                  * 0x5851F42D4C957F2Dull + 0x14057B7EF767814Full;
  const bool lead = (lw == 0) && (lane == 0);
  if (type >= 2) { WAIT_BAR(0); if (lead) post(ws, F_KV + rb * 16 + ct, tag); }
  auto chunk = [&](int c) {
    const unsigned l = lds0 + ((c + 3) & 3) * 32768 + lw * 8192, so = WS_KV + ((c + x) & 7) * 32768 + lw * 8192;
    dma4k(l, vl, sws, so); dma4k(l + 4096, vl, sws, so + 4096);
  };
  STAMP(512, 10);
  if (lw == 0) {
    poll(ws, F_KV + x * 16 + (lane & 7), tag);
    __hip_atomic_store((AS3 unsigned*)(lds0 + P1_GO), 1u, __ATOMIC_RELAXED, __HIP_MEMORY_SCOPE_WORKGROUP);
  } else {
    while (__hip_atomic_load((AS3 unsigned*)(lds0 + P1_GO), __ATOMIC_RELAXED, __HIP_MEMORY_SCOPE_WORKGROUP) == 0u) __builtin_amdgcn_s_sleep(1);
    asm volatile("" ::: "memory");
  }
  STAMP(512, 11);
  dma4k(lds0 + P2_Q + lw * 4096, vl, sws, WS_QB + ((8 * x + (idx >> 2)) * 32 + 8 * (idx & 3)) * 2048 + lw * 4096);
  chunk(0);
  WAIT_BAR(0);
  poll(ws, F_KV + (lane >> 3) * 16 + (lane & 7), tag);
  chunk(1); chunk(2); chunk(3);
  WAIT_BAR(16);
  if (lead && type < 2) post(ws, type == 0 ? F_K + ct * 8 + rb : F_Q + rb * 8 + ct, tag);
  if (lw == 1) *(AS3 unsigned*)(lds0 + P2_ZB + 128 + lane * 4) = lane < 32 ? 0x3f803f80u : 0u;
  WAIT_BAR(0);
  const int head = 4 * x + (idx & 3), tb = idx >> 2;
  poll(ws, lane < 8 ? F_K + (head >> 1) * 8 + lane : F_Q + tb * 8 + x, tag);
  STAMP(512, 12);
  chunk(4);
  u32x4 kvp[2][8], fq[2][4];
  float zs[8];
#pragma unroll
  for (int e = 0; e < 2; ++e)
#pragma unroll
    for (int p = 0; p < 8; ++p) kvp[e][p] = ld16(sws, WS_KVP + ((head * 8 + p) * 512 + (2 * lw + e) * 64 + lane) * 16);
#pragma unroll
  for (int p = 0; p < 8; ++p) zs[p] = ld4(sws, WS_ZP + ((head * 8 + p) * 64 + lane) * 4);
  asm volatile("s_barrier" ::: "memory");
#pragma unroll
  for (int e = 0; e < 2; ++e) {
#pragma unroll
    for (int p = 0; p < 8; ++p) PIN(kvp[e][p]);
#define F4(v) __builtin_bit_cast(f32x4, v)
    const f32x4 sm = ((F4(kvp[e][0]) + F4(kvp[e][1])) + (F4(kvp[e][2]) + F4(kvp[e][3]))) + ((F4(kvp[e][4]) + F4(kvp[e][5])) + (F4(kvp[e][6]) + F4(kvp[e][7])));
#undef F4
    *(AS3 u32x2*)(lds0 + P2_KVF + lw * 1024 + vl + e * 8) = u32x2{pk2(sm[0], sm[1]), pk2(sm[2], sm[3])};
  }
  {
#pragma unroll
    for (int p = 0; p < 8; ++p) PIN(zs[p]);
    const float z = ((zs[0] + zs[1]) + (zs[2] + zs[3])) + ((zs[4] + zs[5]) + (zs[6] + zs[7]));
    if (lw == 0) *(AS3 unsigned short*)(lds0 + P2_ZB + lane * 2) = (unsigned short)(pk2(z, z) & 0xffffu);
  }
  chunk(5);
#pragma unroll
  for (int e = 0; e < 2; ++e)
#pragma unroll
    for (int j = 0; j < 4; ++j) fq[e][j] = ld16(sws, WS_QA + ((8 * tb + 2 * lw + e) * 32 + head) * 4096 + j * 1024 + vl);
  asm volatile("s_waitcnt lgkmcnt(0)\n\ts_barrier" ::: "memory");
#pragma unroll
  for (int e = 0; e < 2; ++e)
#pragma unroll
    for (int j = 0; j < 4; ++j) PIN(fq[e][j]);
  chunk(6);
  {
    const int row = lane & 15, kg = lane >> 4;
    const bool on = (((row >> 2) ^ kg) & 1) == 0;
    const unsigned za = lds0 + P2_ZB + ((on && (row & 3) == 0) ? (kg >> 1) * 32 : (on && (row & 3) == 1) ? 128 : 256);
    const unsigned ka = lds0 + P2_KVF + vl;
#pragma unroll
    for (int e = 0; e < 2; ++e) {
#define BF(v) __builtin_bit_cast(bf16x8, v)
      f32x16 np = MFMA32(lds_r128(ka), BF(fq[e][0]), f32x16{});        f32x4 dp = MFMA16(lds_r128(za), BF(fq[e][0]), f32x4{});
      np = MFMA32(lds_r128(ka + 1024), BF(fq[e][1]), np);              dp = MFMA16(lds_r128(za + 16), BF(fq[e][1]), dp);
      f32x16 nm = MFMA32(lds_r128(ka + 2048), BF(fq[e][2]), f32x16{}); f32x4 dm = MFMA16(lds_r128(za + 64), BF(fq[e][2]), f32x4{});
      nm = MFMA32(lds_r128(ka + 3072), BF(fq[e][3]), nm);              dm = MFMA16(lds_r128(za + 80), BF(fq[e][3]), dm);
#undef BF
      const float a = __builtin_amdgcn_rcpf(dp[1]), b = __builtin_amdgcn_rcpf(dm[1]);
      const float r = __builtin_amdgcn_rcpf(dp[0] * a + dm[0] * b + 1e-6f);
      const f32x16 o = np * (a * r) + nm * (b * r);
      const unsigned p = WS_OA + ((8 * tb + 2 * lw + e) * 32 + head) * 2048 + vl;
      st16(sws, p, pack8<0>(o)); st16(sws, p + 1024, pack8<1>(o));
    }
  }
  WAIT_BAR(0);
  if (lead) post(ws, F_OA + tb * 32 + head, tag);
  chunk(7);
  if (blockIdx.x == 0 && lw == 1) {
    poll(ws, lane, tag); poll(ws, 64 + lane, tag); poll(ws, 128 + lane, tag);
    if (lane == 0) post(ws, F_EPOCH, tag);
  }
  unsigned pfs = 0;
  pf_line(pfs, (const char*)woa + (size_t)(idx * 32 + lw * 8 + (lane >> 3)) * 2048 + (x & 1) * 1024 + (lane & 7) * 128);
  pf_line(pfs, (const char*)wob + (size_t)(idx * 32 + lw * 8 + (lane >> 3)) * 2048 + (x & 1) * 1024 + (lane & 7) * 128);
  const int br = idx >> 4, rb3 = 4 * (x >> 1) + (idx & 3), cb = 4 * (x & 1) + ((idx >> 2) & 3);
  const rsrc_t sw = mk_rsrc(br ? wob : woa, 1024u * 2048u);
  unsigned wv[4];
#pragma unroll
  for (int j = 0; j < 4; ++j) {
    const int k = 4 * (4 * lw + j) + (lane >> 4), c = (lane & 15) ^ (((lane >> 4) & 3) << 2);
    wv[j] = k * 2048 + (cb * 128 + c * 8) * 2 - j * 1024;
  }
  const unsigned osrc = (br ? WS_OB : WS_OA) + (rb3 * 4 + lw) * 65536;
  auto wst = [&](int t) {
    const unsigned sow = t * 64 * 2048;
    set_m0(lds0 + p3_wslot(t) + lw * 4096);
    dma16<0>(wv[0], sw, sow); dma16<1024>(wv[1], sw, sow); dma16<2048>(wv[2], sw, sow); dma16<3072>(wv[3], sw, sow);
  };
  auto ost = [&](int t) { dma4k(lds0 + P3_O + (t & 3) * 16384 + lw * 4096, vl, sws, osrc + t * 4096); };
  asm volatile("s_barrier" ::: "memory");
  wst(0); wst(1); wst(2); wst(3);
  WAIT_BAR(18);
  wst(4); wst(5);
  asm volatile("s_barrier" ::: "memory");
  asm volatile("s_barrier" ::: "memory");
  asm volatile("s_barrier" ::: "memory");
  if (lead) post(ws, F_OB + x * 32 + idx, tag);
  STAMP(512, 13);
  if (lw == 0) poll(ws, br ? F_OB + (rb3 >> 1) * 32 + (rb3 & 1) * 16 + (lane & 15) : F_OA + (rb3 >> 1) * 32 + (lane & 31), tag);
  asm volatile("s_barrier" ::: "memory");
  STAMP(512, 14);
  ost(0); ost(1); ost(2); ost(3);
  WAIT_BAR(12);
  PIN(pfs);
  WAIT_BAR(8);  ost(4); wst(6);
  WAIT_BAR(12); ost(5); wst(7);
  WAIT_BAR(16); ost(6); wst(8);
  WAIT_BAR(20); ost(7); wst(9);
  WAIT_BAR(20); ost(8); wst(10);
  WAIT_BAR(20); ost(9); wst(11);
  WAIT_BAR(20); ost(10); wst(12);
  WAIT_BAR(20); ost(11); wst(13);
  WAIT_BAR(20); ost(12); wst(14);
  WAIT_BAR(20); ost(13); wst(15);
  WAIT_BAR(20); ost(14);
  WAIT_BAR(16); ost(15);
  WAIT_BAR(12);
  WAIT_BAR(4);
  WAIT_BAR(0);
}

__global__ __launch_bounds__(768, 3) void fused_fwd(const unsigned short* __restrict__ xa, const unsigned short* __restrict__ wqkv,
                                                    const unsigned short* __restrict__ wphi, const unsigned short* __restrict__ woa,
                                                    const unsigned short* __restrict__ xb, const unsigned short* __restrict__ wq,
                                                    const unsigned short* __restrict__ wk, const unsigned short* __restrict__ wv,
                                                    const unsigned short* __restrict__ wob, char* __restrict__ ws, unsigned short* __restrict__ out) {
  __shared__ __attribute__((aligned(1024))) char smem[LDS_BYTES];
  const unsigned lds0 = (unsigned)(uintptr_t)(AS3 char*)smem;
  const int tid = threadIdx.x, lane = tid & 63, w = __builtin_amdgcn_readfirstlane(tid >> 6);
  const int b = blockIdx.x, x = b & 7, idx = b >> 3;
  const int rb = idx < 24 ? 4 * (x & 1) + (idx & 3) : 2 * (x >> 1) + (idx & 1);
  const int ct = idx < 16 ? 4 * (x >> 1) + (idx >> 2) : idx < 24 ? 2 * (x >> 1) + ((idx >> 2) & 1) : 4 * (x & 1) + ((idx >> 1) & 3);
  const int type = idx < 16 ? 0 : idx < 24 ? 1 : 2 + (ct & 1);
  const rsrc_t sws = mk_rsrc(ws, WS_END);
  STAMP(0, 0);
  if (w >= 8) {
    helper_program(lds0, type >= 2 ? xb : xa, type >= 2 ? wq : wqkv, type == 3 ? wv : type == 2 ? wk : wphi, woa, wob, ws, sws, type, rb, ct, x, idx, w - 8, lane);
    return;
  }
  if (type == 0)      p1_compute<0>(lds0, ws, sws, rb, ct, w, lane);
  else if (type == 1) p1_compute<1>(lds0, ws, sws, rb, ct, w, lane);
  else if (type == 2) p1_compute<2>(lds0, ws, sws, rb, ct, w, lane);
  else                p1_compute<3>(lds0, ws, sws, rb, ct, w, lane);

  const int kh = w >> 2, r32 = lane & 31, h = lane >> 5;
  {
    const bf16x8 sel = row_sum_sel(lane);
    LDS_BAR();
    STAMP(0, 4);
    const unsigned qa = lds0 + P2_Q + (2 * (w & 3) + kh) * 2048 + lane * 16;
    bf16x8 q[2][2];
    q[0][0] = lds_r128(qa);        q[0][1] = lds_r128(qa + 1024);
    q[1][0] = lds_r128(qa ^ 2048); q[1][1] = lds_r128((qa ^ 2048) + 1024);
    f32x16 O[2]; f32x4 L[2];
    unsigned tv = 0;
    attn_pass_fast(lds0, q, sel, kh, lane, O, L, tv);
    TSOUT();
    STAMP(0, 5);
    const unsigned xs = lds0 + P2_XO + w * 4096 + lane * 16, xr = lds0 + P2_XO + (w ^ 4) * 4096 + lane * 16;
#pragma unroll
    for (int g = 0; g < 4; ++g) *(AS3 f32x4*)(xs + g * 1024) = f32x4{O[1][4 * g], O[1][4 * g + 1], O[1][4 * g + 2], O[1][4 * g + 3]};
    *(AS3 float*)(lds0 + P2_XL + w * 256 + lane * 4) = L[1][0];
    LDS_BAR();
#pragma unroll
    for (int g = 0; g < 4; ++g) {
      const f32x4 o = *(AS3 f32x4*)(xr + g * 1024);
      O[0][4 * g] += o[0]; O[0][4 * g + 1] += o[1]; O[0][4 * g + 2] += o[2]; O[0][4 * g + 3] += o[3];
    }
    L[0][0] += *(AS3 float*)(lds0 + P2_XL + (w ^ 4) * 256 + lane * 4);
    bool bad = !(L[0][0] < 1.8446744e19f);
#pragma unroll
    for (int i = 0; i < 16; ++i) bad |= !(fabsf(O[0][i]) < 3.0e38f);
    if (__any(bad)) attn_pass_safe(sws, q[0][0], q[0][1], sel, lane, O[0], L[0]);
    const float r = __builtin_amdgcn_rcpf(xhalf_sum(L[0][0]));
    O[0] *= r;
    const unsigned p = WS_OB + ((8 * x + (idx >> 2)) * 32 + 8 * (idx & 3) + 2 * (w & 3) + kh) * 2048 + lane * 16;
    st16(sws, p, pack8<0>(O[0])); st16(sws, p + 1024, pack8<1>(O[0]));
    WAIT_BAR(0);
    STAMP(0, 6);
  }
  const int br = idx >> 4, rb3 = 4 * (x >> 1) + (idx & 3), cb = 4 * (x & 1) + ((idx >> 2) & 3);
  const int wm = w & 1, wn = (w >> 1) & 1;
  const int q4 = (lane & 15) >> 2, p4 = lane & 3, chalf = (lane >> 4) & 1;
  const unsigned oa = wm * 8192 + kh * (4096 + 2048) + lane * 16;
  unsigned wa[2];
#pragma unroll
  for (int ns = 0; ns < 2; ++ns)
    wa[ns] = (kh * 32 + 4 * h + q4) * 256 + ((((wn * 2 + ns) ^ q4) << 2) | (chalf * 2 + (p4 >> 1))) * 16 + (p4 & 1) * 8;
  f32x16 acc[2][2] = {};
  bf16x8 xf[2][2], wfr[2][2];
  auto rdx = [&](int set, unsigned buf, int ks) {
#pragma unroll
    for (int ms = 0; ms < 2; ++ms) xf[set][ms] = lds_r128(buf + (oa ^ (ms * 4096)) + ks * 1024);
  };
  auto rdw = [&](int set, unsigned buf, int ks, int ns) { wfr[set][ns] = lds_tr2(buf + wa[ns] + ks * 4096, buf + wa[ns] + ks * 4096 + 2048); };
  auto mm1 = [&](int set, int ms, int ns) { acc[ms][ns] = MFMA32(wfr[set][ns], xf[set][ms], acc[ms][ns]); };
  auto sub = [&](int set, bool rd, unsigned ob, unsigned wb, int ks) {
    SBAR(); mm1(set, 0, 0); SBAR(); if (rd) rdx(set ^ 1, ob, ks);
    SBAR(); mm1(set, 1, 0); SBAR(); if (rd) rdw(set ^ 1, wb, ks, 0);
    SBAR(); mm1(set, 0, 1); SBAR(); if (rd) rdw(set ^ 1, wb, ks, 1);
    SBAR(); mm1(set, 1, 1); SBAR();
  };
  asm volatile("s_barrier" ::: "memory");
  LDS_BAR();
  STAMP(0, 7);
  unsigned ob = lds0 + P3_O, wb = lds0 + p3_wslot(0);
  rdx(0, ob, 0); rdw(0, wb, 0, 0); rdw(0, wb, 0, 1);
  LGKM0();
#pragma unroll 1
  for (int t = 0; t < 15; ++t) {
    sub(0, true, ob, wb, 1);
    ob = (ob == lds0 + P3_O + 3 * 16384) ? lds0 + P3_O : ob + 16384;
    wb = (wb == lds0 + 147456) ? lds0 : (wb == lds0 + 16384) ? lds0 + 98304 : wb + 16384;
    LDS_BAR();
    sub(1, true, ob, wb, 0);
  }
  sub(0, true, ob, wb, 1);
  sub(1, false, ob, wb, 0);
  STAMP(0, 8);
  const unsigned xs = lds0 + w * 8192 + lane * 16, xr = lds0 + (w ^ 4) * 8192 + lane * 16;
#pragma unroll
  for (int ns = 0; ns < 2; ++ns)
#pragma unroll
    for (int g = 0; g < 4; ++g) {
      const f32x16& a = acc[1][ns];
      *(AS3 f32x4*)(xs + ns * 4096 + g * 1024) = f32x4{a[4 * g], a[4 * g + 1], a[4 * g + 2], a[4 * g + 3]};
    }
  LDS_BAR();
  const unsigned img = lds0 + 98304 + w * 4096;
#pragma unroll
  for (int ns = 0; ns < 2; ++ns)
#pragma unroll
    for (int g = 0; g < 4; ++g) {
      const f32x16& a = acc[0][ns];
      const f32x4 o = *(AS3 f32x4*)(xr + ns * 4096 + g * 1024);
      *(AS3 u32x2*)(img + r32 * 128 + (((ns * 4 + g) ^ (r32 & 7)) << 4) + h * 8) =
          u32x2{pk2(a[4 * g] + o[0], a[4 * g + 1] + o[1]), pk2(a[4 * g + 2] + o[2], a[4 * g + 3] + o[3])};
    }
  asm volatile("" ::: "memory");
  const int tok0 = rb3 * 128 + wm * 64 + kh * 32;
  unsigned short* op = out + (size_t)tok0 * 2048 + br * 1024 + cb * 128 + wn * 64;
#pragma unroll
  for (int i = 0; i < 4; ++i) {
    const int row = 8 * i + (lane >> 3), ch = lane & 7;
    const u32x4 v = *(AS3 u32x4*)(img + row * 128 + ((ch ^ (row & 7)) << 4));
    *(u32x4*)(op + (size_t)row * 2048 + ch * 8) = v;
  }
  STAMP(0, 9);
}

extern "C" void kernel_launch(void* const* d_in, const int* in_sizes, int n_in, void* d_out, int out_size, void* d_ws, size_t ws_size,
                              hipStream_t stream) {
  (void)in_sizes; (void)n_in; (void)out_size; (void)ws_size;
  typedef const unsigned short* cp;
  fused_fwd<<<256, 768, 0, stream>>>((cp)d_in[0], (cp)d_in[1], (cp)d_in[2], (cp)d_in[3], (cp)d_in[4], (cp)d_in[5], (cp)d_in[6], (cp)d_in[7],
                                     (cp)d_in[8], (char*)d_ws, (unsigned short*)d_out);
}
```

```cpp
#define STAMPS
#include <hip/hip_runtime.h>
#include <hip/hip_bf16.h>
#include <stdint.h>
#pragma clang diagnostic ignored "-Wint-to-pointer-cast"

typedef __bf16   bf16x8 __attribute__((ext_vector_type(8)));
typedef __bf16   bf16x2 __attribute__((ext_vector_type(2)));
typedef short    s16x4  __attribute__((ext_vector_type(4)));
typedef short    s16x8  __attribute__((ext_vector_type(8)));
typedef float    f32x16 __attribute__((ext_vector_type(16)));
typedef float    f32x4  __attribute__((ext_vector_type(4)));
typedef float    f32x2  __attribute__((ext_vector_type(2)));
typedef unsigned u32x4  __attribute__((ext_vector_type(4)));
typedef unsigned u32x2  __attribute__((ext_vector_type(2)));
typedef unsigned long long u64;
typedef __amdgpu_buffer_rsrc_t rsrc_t;

#define AS1 __attribute__((address_space(1)))
#define AS3 __attribute__((address_space(3)))
#define DI __device__ __forceinline__
#define MFMA32(a, b, c) __builtin_amdgcn_mfma_f32_32x32x16_bf16((a), (b), (c), 0, 0, 0)
#define MFMA16(a, b, c) __builtin_amdgcn_mfma_f32_16x16x32_bf16((a), (b), (c), 0, 0, 0)
#define SBAR() __builtin_amdgcn_sched_barrier(0)
#define WAIT_BAR(N) asm volatile("s_waitcnt vmcnt(" #N ") lgkmcnt(0)\n\ts_barrier" ::: "memory")
#define LGKM0() __builtin_amdgcn_s_waitcnt(0xC07F)
#define LDS_BAR() do { asm volatile("" ::: "memory"); LGKM0(); asm volatile("s_barrier" ::: "memory"); } while (0)

#define STAMP(th, i) do { if (threadIdx.x == (th)) { u64* sp_ = (u64*)(ws + (64u << 20)) + (size_t)blockIdx.x * 32 + 2 * (i); \
    sp_[0] = __builtin_amdgcn_s_memtime(); sp_[1] = __builtin_amdgcn_s_memrealtime(); } } while (0)
#define TS(i) do {} while (0)
#define TSOUT() do {} while (0)
#define KROT (2 * x)
#define MAPV 1
constexpr int D = 1024;
constexpr float LOG2E = 1.4426950408889634f;
constexpr float QSCALE = 0.17677669529663687f * LOG2E;

constexpr unsigned WS_QA  = 0;
constexpr unsigned WS_QB  = WS_QA + (8u << 20);
constexpr unsigned WS_OA  = WS_QB + (4u << 20);
constexpr unsigned WS_OB  = WS_OA + (4u << 20);
constexpr unsigned WS_KV  = WS_OB + (4u << 20);
constexpr unsigned WS_KVP = WS_KV + (256u << 10);
constexpr unsigned WS_ZP  = WS_KVP + (2u << 20);
constexpr unsigned WS_FLG = WS_ZP + (64u << 10);
constexpr unsigned WS_END = WS_FLG + (16u << 10);
constexpr int F_K = 0, F_Q = 128, F_OA = 256, F_OB = 512, F_KV = 768, F_EPOCH = 1024;

DI unsigned pk2(float lo, float hi) {
  f32x2 v = {lo, hi};
  return __builtin_bit_cast(unsigned, __builtin_convertvector(v, bf16x2));
}
template <int SS> DI bf16x8 pack8(const f32x16& x) {
  u32x4 p = {pk2(x[8 * SS + 0], x[8 * SS + 1]), pk2(x[8 * SS + 2], x[8 * SS + 3]), pk2(x[8 * SS + 4], x[8 * SS + 5]), pk2(x[8 * SS + 6], x[8 * SS + 7])};
  return __builtin_bit_cast(bf16x8, p);
}
DI bf16x8 lds_r128(unsigned a) { return *(const AS3 bf16x8*)a; }
DI bf16x8 lds_tr2(unsigned a0, unsigned a1) {
  s16x4 lo = __builtin_amdgcn_ds_read_tr16_b64_v4i16((AS3 s16x4*)a0);
  s16x4 hi = __builtin_amdgcn_ds_read_tr16_b64_v4i16((AS3 s16x4*)a1);
  s16x8 r = __builtin_shufflevector(lo, hi, 0, 1, 2, 3, 4, 5, 6, 7);
  return __builtin_bit_cast(bf16x8, r);
}
DI rsrc_t mk_rsrc(const void* p, unsigned bytes) { return __builtin_amdgcn_make_buffer_rsrc((void*)p, (short)0, (int)bytes, 0x00020000); }
DI void set_m0(unsigned v) { asm volatile("s_mov_b32 m0, %0\n\ts_nop 0" ::"s"(v) : "memory"); }
template <int IMM> DI void dma16(unsigned voff, rsrc_t srd, unsigned soff) {
  asm volatile("buffer_load_dwordx4 %0, %1, %2 offen offset:%3 lds" ::"v"(voff), "s"(srd), "s"(soff), "i"(IMM) : "memory");
}
template <int IMM> DI void dma16d(unsigned voff, rsrc_t srd, unsigned soff) {
  asm volatile("buffer_load_dwordx4 %0, %1, %2 offen offset:%3 sc1 lds" ::"v"(voff), "s"(srd), "s"(soff), "i"(IMM) : "memory");
}
DI void dma4k(unsigned lds, unsigned voff, rsrc_t srd, unsigned soff) {
  set_m0(lds); dma16d<0>(voff, srd, soff); dma16d<1024>(voff, srd, soff); dma16d<2048>(voff, srd, soff); dma16d<3072>(voff, srd, soff);
}
template <class T> DI void st16(rsrc_t r, unsigned voff, T v) { __builtin_amdgcn_raw_buffer_store_b128(__builtin_bit_cast(u32x4, v), r, (int)voff, 0, 16); }
DI void st8(rsrc_t r, unsigned voff, u32x2 v) { __builtin_amdgcn_raw_buffer_store_b64(v, r, (int)voff, 0, 16); }
DI void st4(rsrc_t r, unsigned voff, float v) { __builtin_amdgcn_raw_buffer_store_b32(__float_as_uint(v), r, (int)voff, 0, 16); }
DI u32x4 ld16(rsrc_t r, unsigned voff) { return __builtin_amdgcn_raw_buffer_load_b128(r, (int)voff, 0, 16); }
DI float ld4(rsrc_t r, unsigned voff) { return __uint_as_float(__builtin_amdgcn_raw_buffer_load_b32(r, (int)voff, 0, 16)); }
DI void post(char* ws, int f, u64 tag) { __hip_atomic_store((AS1 u64*)(ws + WS_FLG) + f, tag, __ATOMIC_RELAXED, __HIP_MEMORY_SCOPE_AGENT); }
DI void poll(char* ws, int f, u64 tag) {
#pragma unroll 1
  for (int it = 0; it < (1 << 17); ++it) {
    const u64 v = __hip_atomic_load((AS1 u64*)(ws + WS_FLG) + f, __ATOMIC_RELAXED, __HIP_MEMORY_SCOPE_AGENT);
    if (__all(v == tag)) break;
    __builtin_amdgcn_s_sleep(2);
  }
  asm volatile("" ::: "memory");
}
DI void pf_line(unsigned& sink, const void* p) { asm volatile("global_load_dword %0, %1, off" : "+v"(sink) : "v"(p) : "memory"); }
#define PIN(x) asm volatile("" : "+v"(x))
DI float xhalf_max(float v) {
  auto r = __builtin_amdgcn_permlane32_swap(__float_as_uint(v), __float_as_uint(v), false, false);
  return fmaxf(__uint_as_float(r[0]), __uint_as_float(r[1]));
}
DI float xhalf_min(float v) {
  auto r = __builtin_amdgcn_permlane32_swap(__float_as_uint(v), __float_as_uint(v), false, false);
  return fminf(__uint_as_float(r[0]), __uint_as_float(r[1]));
}
DI float xhalf_sum(float v) {
  auto r = __builtin_amdgcn_permlane32_swap(__float_as_uint(v), __float_as_uint(v), false, false);
  return __uint_as_float(r[0]) + __uint_as_float(r[1]);
}
DI float max16(const f32x16& x) {
  float a = fmaxf(fmaxf(x[0], x[1]), x[2]), b = fmaxf(fmaxf(x[3], x[4]), x[5]);
  a = fmaxf(fmaxf(a, x[6]), x[7]);   b = fmaxf(fmaxf(b, x[8]), x[9]);
  a = fmaxf(fmaxf(a, x[10]), x[11]); b = fmaxf(fmaxf(b, x[12]), x[13]);
  a = fmaxf(fmaxf(a, x[14]), x[15]);
  return fmaxf(a, b);
}
DI float min16(const f32x16& x) {
  float a = fminf(fminf(x[0], x[1]), x[2]), b = fminf(fminf(x[3], x[4]), x[5]);
  a = fminf(fminf(a, x[6]), x[7]);   b = fminf(fminf(b, x[8]), x[9]);
  a = fminf(fminf(a, x[10]), x[11]); b = fminf(fminf(b, x[12]), x[13]);
  a = fminf(fminf(a, x[14]), x[15]);
  return fminf(a, b);
}
template <bool NORM> DI void feat_map(const f32x16& p, f32x16& ep, f32x16& em) {
  const float mx = xhalf_max(max16(p)), mn = xhalf_min(min16(p));
  const f32x2 a = {-mx * LOG2E, -mx * LOG2E}, b = {mn * LOG2E, mn * LOG2E}, c = {LOG2E, LOG2E};
  f32x2 sp = {0.f, 0.f}, sm = {0.f, 0.f};
#pragma unroll
  for (int i = 0; i < 16; i += 2) {
    const f32x2 x = {p[i], p[i + 1]};
    const f32x2 u = __builtin_elementwise_fma(x, c, a), v = __builtin_elementwise_fma(x, -c, b);
    const f32x2 eu = {__builtin_amdgcn_exp2f(u[0]), __builtin_amdgcn_exp2f(u[1])}, ev = {__builtin_amdgcn_exp2f(v[0]), __builtin_amdgcn_exp2f(v[1])};
    ep[i] = eu[0]; ep[i + 1] = eu[1]; em[i] = ev[0]; em[i + 1] = ev[1];
    if (i == 0) { sp = eu; sm = ev; } else { sp += eu; sm += ev; }
  }
  if (NORM) {
    const float rp = __builtin_amdgcn_rcpf(xhalf_sum(sp[0] + sp[1])), rm = __builtin_amdgcn_rcpf(xhalf_sum(sm[0] + sm[1]));
    ep *= rp; em *= rm;
  }
}
DI bf16x8 row_sum_sel(int lane) {
  const unsigned u = ((lane & 11) == 0 && (((lane >> 2) ^ (lane >> 4)) & 1) == 0) ? 0x3f803f80u : 0u;
  return __builtin_bit_cast(bf16x8, u32x4{u, u, u, u});
}

constexpr int LDS_BYTES = 163840;
constexpr int P1_WOFF = 32768, P1_EOFF = 49152, P1_STAGE = 53248, P1_ZRED = 3 * P1_STAGE, P1_CNT = P1_ZRED + 2048, P1_GO = P1_CNT + 16, P1_PHI = 65536;
constexpr int P2_Q = 131072, P2_KVF = 147456, P2_ZB = 151552;
constexpr int P2_XO = 32768, P2_XL = 65536;
constexpr int P3_O = 32768;
DI constexpr int p3_wslot(int t) { return (98304 + (t % 6) * 16384) % 163840; }

template <int TYPE>
DI void p1_compute(unsigned lds0, char* __restrict__ ws, rsrc_t sws, int rb, int ct, int w, int lane) {
  const int tid = threadIdx.x;
  const int wm = w & 3, wn = w >> 2, r32 = lane & 31, h = lane >> 5;
  if (tid < 8) *(AS3 unsigned*)(lds0 + P1_CNT + tid * 4) = 0u;

  const int xrow = wm * 64 + r32;
  const unsigned xa = xrow * 128 + ((h ^ ((xrow >> 1) & 7)) << 4);
  const int q4 = (lane & 15) >> 2, p4 = lane & 3, chalf = (lane >> 4) & 1;
  unsigned wa[2];
#pragma unroll
  for (int ns = 0; ns < 2; ++ns)
    wa[ns] = P1_WOFF + (8 * h + q4) * 256 + ((((wn * 2 + ns) ^ q4) << 2) | (chalf * 2 + (p4 >> 1))) * 16 + (p4 & 1) * 8;
  const int erow = 64 * (ct >> 1) + 16 * wm + (lane & 15);
  const unsigned exa = erow * 128 + (((lane >> 4) ^ ((erow >> 1) & 7)) << 4);
  const unsigned ewa = P1_EOFF + (8 * (lane >> 4) + q4) * 64 + wn * 32 + p4 * 8;

  f32x16 acc[2][2] = {};
  f32x4 eacc = {};
  bf16x8 xf[2][2], wfr[2][2], ex[2], ew[2];
  auto rdx = [&](int set, unsigned buf, int ks) {
#pragma unroll
    for (int ms = 0; ms < 2; ++ms) xf[set][ms] = lds_r128(buf + ((xa ^ (ks * 32)) + ms * 4096));
  };
  auto rdw = [&](int set, unsigned buf, int ks, int ns) { wfr[set][ns] = lds_tr2(buf + wa[ns] + ks * 4096, buf + wa[ns] + ks * 4096 + 1024); };
  auto mm1 = [&](int set, int ms, int ns) {
    if (TYPE == 0 && ns == 1) acc[ms][1] = MFMA32(xf[set][ms], wfr[set][1], acc[ms][1]);
    else                      acc[ms][ns] = MFMA32(wfr[set][ns], xf[set][ms], acc[ms][ns]);
  };
  auto erd = [&](unsigned buf, int kk) {
    if (TYPE >= 2) { ex[kk] = lds_r128(buf + (exa ^ (kk * 64))); ew[kk] = lds_tr2(buf + ewa + kk * 2048, buf + ewa + kk * 2048 + 256); }
  };
  auto emm = [&](int kk) {
    if (TYPE == 2) eacc = MFMA16(ew[kk], ex[kk], eacc);
    if (TYPE == 3) eacc = MFMA16(ex[kk], ew[kk], eacc);
  };
  auto sub = [&](int set, unsigned buf, int ks) {
    SBAR(); mm1(set, 0, 0); SBAR(); rdx(set ^ 1, buf, ks);
    SBAR(); mm1(set, 1, 0); SBAR(); rdw(set ^ 1, buf, ks, 0);
    SBAR(); mm1(set, 0, 1); SBAR(); rdw(set ^ 1, buf, ks, 1);
    SBAR(); mm1(set, 1, 1); SBAR();
  };
  LDS_BAR();
  STAMP(0, 1);
  unsigned cur = lds0, nxt = lds0 + P1_STAGE;
  rdx(0, cur, 0); rdw(0, cur, 0, 0); rdw(0, cur, 0, 1);
  LGKM0();
#pragma unroll 1
  for (int t = 0; t < 16; ++t) {
    sub(0, cur, 1); erd(cur, 0);
    sub(1, cur, 2); erd(cur, 1); SBAR(); emm(0);
    sub(0, cur, 3); emm(1); SBAR();
    LDS_BAR();
    sub(1, nxt, 0);
    cur = nxt; nxt = (nxt == lds0 + 2 * P1_STAGE) ? lds0 : nxt + P1_STAGE;
  }
  STAMP(0, 2);
  bf16x8 wf[2][2];
  if constexpr (TYPE <= 1) {
    const unsigned a = lds0 + P1_PHI + (TYPE == 0 ? wn : 2 * wn) * 2048 + (4 * h + q4) * 64 + chalf * 32 + p4 * 8;
#pragma unroll
    for (int n = 0; n <= TYPE; ++n)
#pragma unroll
      for (int s = 0; s < 2; ++s) wf[n][s] = lds_tr2(a + n * 2048 + s * 1024, a + n * 2048 + s * 1024 + 512);
  }

  if (TYPE >= 2) {
#pragma unroll
    for (int ms = 0; ms < 2; ++ms)
#pragma unroll
      for (int ns = 0; ns < 2; ++ns) {
        const f32x16 v = acc[ms][ns] * QSCALE;
        const unsigned p = WS_QB + ((rb * 8 + wm * 2 + ms) * 32 + ct * 4 + wn * 2 + ns) * 2048 + lane * 16;
        st16(sws, p, pack8<0>(v)); st16(sws, p + 1024, pack8<1>(v));
      }
    const int g = lane >> 4, tok0 = rb * 256 + 64 * (ct >> 1) + 16 * wm;
    unsigned p = WS_KV + (tok0 >> 5) * 4096;
    if (TYPE == 3) p += 2048 + (wm & 1) * 1024 + (16 * wn + (lane & 15) + 32 * (g & 1)) * 16 + (g >> 1) * 8;
    else           p += wn * 1024 + (16 * (wm & 1) + (lane & 15) + 32 * (g & 1)) * 16 + (g >> 1) * 8;
    st8(sws, p, u32x2{pk2(eacc[0], eacc[1]), pk2(eacc[2], eacc[3])});
    WAIT_BAR(0);
  } else if (TYPE == 1) {
    f32x16 pq[2][2];
#pragma unroll
    for (int ms = 0; ms < 2; ++ms)
#pragma unroll
      for (int ns = 0; ns < 2; ++ns) {
        pq[ms][ns] = MFMA32(wf[ns][0], pack8<0>(acc[ms][ns]), f32x16{});
        pq[ms][ns] = MFMA32(wf[ns][1], pack8<1>(acc[ms][ns]), pq[ms][ns]);
      }
#pragma unroll
    for (int ms = 0; ms < 2; ++ms)
#pragma unroll
      for (int ns = 0; ns < 2; ++ns) {
        f32x16 ep, em;
        feat_map<false>(pq[ms][ns], ep, em);
        const unsigned p = WS_QA + ((rb * 8 + wm * 2 + ms) * 32 + ct * 4 + wn * 2 + ns) * 4096 + lane * 16;
        st16(sws, p, pack8<0>(ep)); st16(sws, p + 1024, pack8<1>(ep)); st16(sws, p + 2048, pack8<0>(em)); st16(sws, p + 3072, pack8<1>(em));
      }
  } else {
    const unsigned img = lds0 + w * 8192;
    const bf16x8 sel = row_sum_sel(lane);
    f32x16 pk[2], kvp = {}, kvm = {};
    f32x4 zp = {}, zm = {};
    bf16x8 vf[2][2], ap[2], am[2];
#pragma unroll
    for (int ms = 0; ms < 2; ++ms) {
      pk[ms] = MFMA32(wf[0][0], pack8<0>(acc[ms][0]), f32x16{});
      pk[ms] = MFMA32(wf[0][1], pack8<1>(acc[ms][0]), pk[ms]);
    }
#pragma unroll
    for (int ms = 0; ms < 2; ++ms) { vf[ms][0] = pack8<0>(acc[ms][1]); vf[ms][1] = pack8<1>(acc[ms][1]); }
    auto kvmm = [&](int ms) {
#pragma unroll
      for (int s = 0; s < 2; ++s) {
        kvp = MFMA32(ap[s], vf[ms][s], kvp); kvm = MFMA32(am[s], vf[ms][s], kvm);
        zp = MFMA16(sel, ap[s], zp);         zm = MFMA16(sel, am[s], zm);
      }
    };
#pragma unroll
    for (int ms = 0; ms < 2; ++ms) {
      f32x16 ep, em;
      feat_map<true>(pk[ms], ep, em);
      if (ms == 1) kvmm(0);
#pragma unroll
      for (int g = 0; g < 4; ++g) {
        const unsigned a = img + r32 * 64 + (((2 * g + h) ^ ((r32 >> 1) & 7)) << 3);
        *(AS3 u32x2*)a = u32x2{pk2(ep[4 * g], ep[4 * g + 1]), pk2(ep[4 * g + 2], ep[4 * g + 3])};
        *(AS3 u32x2*)(a + 2048) = u32x2{pk2(em[4 * g], em[4 * g + 1]), pk2(em[4 * g + 2], em[4 * g + 3])};
      }
      asm volatile("" ::: "memory");
#pragma unroll
      for (int s = 0; s < 2; ++s) {
        const int t0 = 16 * s + 4 * h + q4, t1 = t0 + 8, u = 4 * chalf + p4;
        const unsigned a0 = img + t0 * 64 + ((u ^ ((t0 >> 1) & 7)) << 3), a1 = img + t1 * 64 + ((u ^ ((t1 >> 1) & 7)) << 3);
        ap[s] = lds_tr2(a0, a1); am[s] = lds_tr2(a0 + 2048, a1 + 2048);
      }
      asm volatile("" ::: "memory");
    }
    kvmm(1);
#pragma unroll
    for (int g = 0; g < 4; ++g) {
      *(AS3 f32x4*)(img + g * 1024 + lane * 16) = f32x4{kvp[4 * g], kvp[4 * g + 1], kvp[4 * g + 2], kvp[4 * g + 3]};
      *(AS3 f32x4*)(img + 4096 + g * 1024 + lane * 16) = f32x4{kvm[4 * g], kvm[4 * g + 1], kvm[4 * g + 2], kvm[4 * g + 3]};
    }
    if (lane < 32) {
      const unsigned a = lds0 + P1_ZRED + w * 256 + ((lane >> 2) & 1) * 64 + (lane >> 3) * 16 + (lane & 3) * 4;
      *(AS3 float*)a = zp[0]; *(AS3 float*)(a + 128) = zm[0];
    }
    asm volatile("" ::: "memory"); LGKM0();
    if (lane == 0) __hip_atomic_fetch_add((AS3 unsigned*)(lds0 + P1_CNT), 1u, __ATOMIC_RELAXED, __HIP_MEMORY_SCOPE_WORKGROUP);
    while (__hip_atomic_load((AS3 unsigned*)(lds0 + P1_CNT), __ATOMIC_RELAXED, __HIP_MEMORY_SCOPE_WORKGROUP) < 8u) __builtin_amdgcn_s_sleep(1);
    asm volatile("" ::: "memory");
#pragma unroll
    for (int hb = 0; hb < 2; ++hb) {
      f32x4 s = *(AS3 f32x4*)(lds0 + (hb * 4) * 8192 + tid * 16);
#pragma unroll
      for (int m = 1; m < 4; ++m) s += *(AS3 f32x4*)(lds0 + (hb * 4 + m) * 8192 + tid * 16);
      st16(sws, WS_KVP + (((2 * ct + hb) * 8 + rb) * 512 + tid) * 16, s);
    }
    if (tid < 128) {
      const int hb = tid >> 6;
      float s = 0.f;
#pragma unroll
      for (int m = 0; m < 4; ++m) s += *(AS3 float*)(lds0 + P1_ZRED + (hb * 4 + m) * 256 + lane * 4);
      st4(sws, WS_ZP + (((2 * ct + hb) * 8 + rb) * 64 + lane) * 4, s);
    }
  }
  STAMP(0, 3);
}

DI void attn_pass_fast(unsigned lds0, const bf16x8 (&q)[2][2], const bf16x8 sel, int kh, int lane, f32x16 (&O)[2], f32x4 (&L)[2], unsigned& tv) {
  TS(0);
  typedef const AS3 char* lp;
  const lp l0 = (lp)(lds0 + lane * 16), la = l0 + kh * 16384;
#define RD(p) (*(const AS3 bf16x8*)(p))
  if (kh) __builtin_amdgcn_s_setprio(1);
  bf16x8 ka = RD(l0 + 98304), kb = RD(l0 + 98304 + 1024);
  f32x16 negm[2], S0, S1;
#pragma unroll
  for (int r = 0; r < 2; ++r) {
    f32x16 z = {};
    z = MFMA32(ka, q[r][0], z); z = MFMA32(kb, q[r][1], z);
    const float m = xhalf_max(max16(z));
#pragma unroll
    for (int i = 0; i < 16; ++i) negm[r][i] = -m;
    PIN(negm[r]);
    O[r] = f32x16{}; L[r] = f32x4{};
  }
  ka = RD(la + 98304); kb = RD(la + 98304 + 1024);
  bf16x8 va = RD(la + 98304 + 2048), vb = bf16x8{};
  S0 = MFMA32(ka, q[0][0], negm[0]); S0 = MFMA32(kb, q[0][1], S0);
  u32x4 p0 = {}, p1 = {};
  LGKM0();
#define EX4(SC, i) do { SC[i] = __builtin_amdgcn_exp2f(SC[i]); SC[i + 1] = __builtin_amdgcn_exp2f(SC[i + 1]); \
    SC[i + 2] = __builtin_amdgcn_exp2f(SC[i + 2]); SC[i + 3] = __builtin_amdgcn_exp2f(SC[i + 3]); PIN(SC); } while (0)
#define CV4(P, SC, i) do { P[0] = pk2(SC[i], SC[i + 1]); P[1] = pk2(SC[i + 2], SC[i + 3]); P[2] = pk2(SC[i + 4], SC[i + 5]); P[3] = pk2(SC[i + 6], SC[i + 7]); PIN(P); } while (0)
#define BF(P) __builtin_bit_cast(bf16x8, P)
#define TILE(TA, TN) do { \
    SBAR(); S1 = MFMA32(ka, q[1][0], negm[1]);  SBAR(); ka = RD(TN);          EX4(S0, 0);  \
    SBAR(); O[1] = MFMA32(vb, BF(p1), O[1]);    SBAR(); vb = RD((TA) + 3072); EX4(S0, 4);  \
    SBAR(); L[1] = MFMA16(sel, BF(p1), L[1]);   SBAR(); CV4(p0, S0, 0); \
    SBAR(); S1 = MFMA32(kb, q[1][1], S1);       SBAR(); kb = RD((TN) + 1024); EX4(S0, 8);  \
    SBAR(); O[0] = MFMA32(va, BF(p0), O[0]);    SBAR(); EX4(S0, 12); \
    SBAR(); L[0] = MFMA16(sel, BF(p0), L[0]);   SBAR(); CV4(p1, S0, 8); \
    SBAR(); S0 = MFMA32(ka, q[0][0], negm[0]);  SBAR(); EX4(S1, 0);  \
    SBAR(); O[0] = MFMA32(vb, BF(p1), O[0]);    SBAR(); EX4(S1, 4);  \
    SBAR(); L[0] = MFMA16(sel, BF(p1), L[0]);   SBAR(); CV4(p0, S1, 0); \
    SBAR(); S0 = MFMA32(kb, q[0][1], S0);       SBAR(); EX4(S1, 8);  \
    SBAR(); O[1] = MFMA32(va, BF(p0), O[1]);    SBAR(); va = RD((TN) + 2048); EX4(S1, 12); \
    SBAR(); L[1] = MFMA16(sel, BF(p0), L[1]);   SBAR(); CV4(p1, S1, 8); } while (0)
  TS(1);
#pragma unroll 1
  for (int c = 0; c < 8; ++c) {
    const lp cb = la + ((c + 3) & 3) * 32768, nb = la + (c & 3) * 32768;
    TILE(cb, cb + 4096);
    TILE(cb + 4096, cb + 8192);
    SBAR(); if (kh) __builtin_amdgcn_s_setprio(0);
    TILE(cb + 8192, cb + 12288);
    SBAR(); TS(2 + 2 * c);
    asm volatile("s_waitcnt vmcnt(0)\n\ts_barrier" ::: "memory"); TS(3 + 2 * c);
    if (kh) __builtin_amdgcn_s_setprio(1);
    TILE(cb + 12288, nb);
  }
  SBAR();
  O[1] = MFMA32(vb, BF(p1), O[1]); L[1] = MFMA16(sel, BF(p1), L[1]);
  __builtin_amdgcn_s_setprio(0);
  TS(18);
#undef TILE
#undef EX4
#undef CV4
#undef BF
#undef RD
}

DI void attn_pass_safe(rsrc_t sws, const bf16x8 q0, const bf16x8 q1, const bf16x8 sel, int lane, f32x16& O, f32x4& L) {
  O = f32x16{}; L = f32x4{};
  float m = -INFINITY;
#pragma unroll 1
  for (int t = 0; t < 64; ++t) {
    bf16x8 a[4];
#pragma unroll
    for (int j = 0; j < 4; ++j) a[j] = __builtin_bit_cast(bf16x8, ld16(sws, WS_KV + t * 4096 + j * 1024 + lane * 16));
    f32x16 Sc = {};
    Sc = MFMA32(a[0], q0, Sc); Sc = MFMA32(a[1], q1, Sc);
    const float mn = fmaxf(m, xhalf_max(max16(Sc)));
    const float al = __builtin_amdgcn_exp2f(m - mn);
    m = mn;
#pragma unroll
    for (int r = 0; r < 16; ++r) { O[r] *= al; Sc[r] = __builtin_amdgcn_exp2f(Sc[r] - mn); }
    L *= al;
    const bf16x8 p0 = pack8<0>(Sc), p1 = pack8<1>(Sc);
    O = MFMA32(a[2], p0, O); L = MFMA16(sel, p0, L);
    O = MFMA32(a[3], p1, O); L = MFMA16(sel, p1, L);
  }
}

DI void helper_program(unsigned lds0, const unsigned short* __restrict__ X, const unsigned short* __restrict__ W, const unsigned short* __restrict__ E,
                       const unsigned short* __restrict__ woa, const unsigned short* __restrict__ wob, char* __restrict__ ws, rsrc_t sws,
                       int type, int rb, int ct, int x, int idx, int lw, int lane) {
  __builtin_amdgcn_s_setprio(3);
  const u64 e0 = __hip_atomic_load((AS1 u64*)(ws + WS_FLG) + F_EPOCH, __ATOMIC_RELAXED, __HIP_MEMORY_SCOPE_AGENT);
  const unsigned vl = lane * 16;
  {
    const bool ext = type >= 2;
    const unsigned ldw = ext ? 2048u : 6144u;
    const rsrc_t sx = mk_rsrc(X + (size_t)rb * 256 * D, 256u * 2048u);
    const rsrc_t sw = mk_rsrc(W, 1024u * ldw);
    const rsrc_t se = mk_rsrc(E, 1024u * 64u);
    unsigned xv[8], wv[4];
#pragma unroll
    for (int j = 0; j < 8; ++j) {
      const int row = 8 * (8 * lw + j) + (lane >> 3), c = (lane & 7) ^ ((row >> 1) & 7);
      xv[j] = row * 2048 + c * 16 - (j & 3) * 1024;
    }
#pragma unroll
    for (int j = 0; j < 4; ++j) {
      const int k = 4 * (4 * lw + j) + (lane >> 4), c = (lane & 15) ^ (((lane >> 4) & 3) << 2);
      const int col = (type == 0) ? 1024 + ((c >> 2) & 1) * 1024 + (2 * ct + (c >> 3)) * 32 + (c & 3) * 8 : ct * 128 + c * 8;
      wv[j] = k * ldw + col * 2 - j * 1024;
    }
    auto stage = [&](int t0, unsigned buf) {
      const int t = (t0 + KROT) & 15;
      const unsigned sox = t * 128, sow = t * 64 * ldw;
      set_m0(buf + P1_WOFF + lw * 4096);
      dma16<0>(wv[0], sw, sow); dma16<1024>(wv[1], sw, sow); dma16<2048>(wv[2], sw, sow); dma16<3072>(wv[3], sw, sow);
      set_m0(buf + lw * 8192);
      dma16<0>(xv[0], sx, sox); dma16<1024>(xv[1], sx, sox); dma16<2048>(xv[2], sx, sox); dma16<3072>(xv[3], sx, sox);
      set_m0(buf + lw * 8192 + 4096);
      dma16<0>(xv[4], sx, sox); dma16<1024>(xv[5], sx, sox); dma16<2048>(xv[6], sx, sox); dma16<3072>(xv[7], sx, sox);
      if (ext) { set_m0(buf + P1_EOFF + lw * 1024); dma16<0>(vl, se, t * 4096 + lw * 1024); }
    };
    stage(0, lds0); stage(1, lds0 + P1_STAGE);
    if (ext) WAIT_BAR(13); else WAIT_BAR(12);
    stage(2, lds0 + 2 * P1_STAGE);
    unsigned buf = lds0;
#pragma unroll 1
    for (int t = 0; t < 13; ++t) {
      if (ext) WAIT_BAR(13); else WAIT_BAR(12);
      stage(t + 3, buf);
      buf = (buf == lds0 + 2 * P1_STAGE) ? lds0 : buf + P1_STAGE;
    }
    if (ext) WAIT_BAR(13); else WAIT_BAR(12);
    if (!ext && (type == 1 || lw < 2)) {
      set_m0(lds0 + P1_PHI + lw * 2048);
      const unsigned so = ((type == 0 ? 2 * ct : 4 * ct) + lw) * 2048;
      dma16<0>(vl, se, so); dma16<1024>(vl, se, so);
    }
    WAIT_BAR(0);
    WAIT_BAR(0);
  }
  const u64 tag = ((u64)(unsigned)__builtin_amdgcn_readfirstlane((unsigned)(e0 >> 32)) << 32 | (unsigned)__builtin_amdgcn_readfirstlane((unsigned)e0))
                  * 0x5851F42D4C957F2Dull + 0x14057B7EF767814Full;
  const bool lead = (lw == 0) && (lane == 0);
  if (type >= 2) { WAIT_BAR(0); if (lead) post(ws, F_KV + rb * 16 + ct, tag); }
  auto chunk = [&](int c) {
    const unsigned l = lds0 + ((c + 3) & 3) * 32768 + lw * 8192, so = WS_KV + ((c + x) & 7) * 32768 + lw * 8192;
    dma4k(l, vl, sws, so); dma4k(l + 4096, vl, sws, so + 4096);
  };
  STAMP(512, 10);
  if (lw == 0) {
    poll(ws, F_KV + x * 16 + (lane & 7), tag);
    __hip_atomic_store((AS3 unsigned*)(lds0 + P1_GO), 1u, __ATOMIC_RELAXED, __HIP_MEMORY_SCOPE_WORKGROUP);
  } else {
    while (__hip_atomic_load((AS3 unsigned*)(lds0 + P1_GO), __ATOMIC_RELAXED, __HIP_MEMORY_SCOPE_WORKGROUP) == 0u) __builtin_amdgcn_s_sleep(1);
    asm volatile("" ::: "memory");
  }
  STAMP(512, 11);
  dma4k(lds0 + P2_Q + lw * 4096, vl, sws, WS_QB + ((8 * x + (idx >> 2)) * 32 + 8 * (idx & 3)) * 2048 + lw * 4096);
  chunk(0);
  WAIT_BAR(0);
  poll(ws, F_KV + (lane >> 3) * 16 + (lane & 7), tag);
  chunk(1); chunk(2); chunk(3);
  WAIT_BAR(16);
  if (lead && type < 2) post(ws, type == 0 ? F_K + ct * 8 + rb : F_Q + rb * 8 + ct, tag);
  if (lw == 1) *(AS3 unsigned*)(lds0 + P2_ZB + 128 + lane * 4) = lane < 32 ? 0x3f803f80u : 0u;
  WAIT_BAR(0);
  const int head = 4 * x + (idx & 3), tb = idx >> 2;
  poll(ws, lane < 8 ? F_K + (head >> 1) * 8 + lane : F_Q + tb * 8 + x, tag);
  STAMP(512, 12);
  chunk(4);
  u32x4 kvp[2][8], fq[2][4];
  float zs[8];
#pragma unroll
  for (int e = 0; e < 2; ++e)
#pragma unroll
    for (int p = 0; p < 8; ++p) kvp[e][p] = ld16(sws, WS_KVP + ((head * 8 + p) * 512 + (2 * lw + e) * 64 + lane) * 16);
#pragma unroll
  for (int p = 0; p < 8; ++p) zs[p] = ld4(sws, WS_ZP + ((head * 8 + p) * 64 + lane) * 4);
  asm volatile("s_barrier" ::: "memory");
#pragma unroll
  for (int e = 0; e < 2; ++e) {
#pragma unroll
    for (int p = 0; p < 8; ++p) PIN(kvp[e][p]);
#define F4(v) __builtin_bit_cast(f32x4, v)
    const f32x4 sm = ((F4(kvp[e][0]) + F4(kvp[e][1])) + (F4(kvp[e][2]) + F4(kvp[e][3]))) + ((F4(kvp[e][4]) + F4(kvp[e][5])) + (F4(kvp[e][6]) + F4(kvp[e][7])));
#undef F4
    *(AS3 u32x2*)(lds0 + P2_KVF + lw * 1024 + vl + e * 8) = u32x2{pk2(sm[0], sm[1]), pk2(sm[2], sm[3])};
  }
  {
#pragma unroll
    for (int p = 0; p < 8; ++p) PIN(zs[p]);
    const float z = ((zs[0] + zs[1]) + (zs[2] + zs[3])) + ((zs[4] + zs[5]) + (zs[6] + zs[7]));
    if (lw == 0) *(AS3 unsigned short*)(lds0 + P2_ZB + lane * 2) = (unsigned short)(pk2(z, z) & 0xffffu);
  }
  chunk(5);
#pragma unroll
  for (int e = 0; e < 2; ++e)
#pragma unroll
    for (int j = 0; j < 4; ++j) fq[e][j] = ld16(sws, WS_QA + ((8 * tb + 2 * lw + e) * 32 + head) * 4096 + j * 1024 + vl);
  asm volatile("s_waitcnt lgkmcnt(0)\n\ts_barrier" ::: "memory");
#pragma unroll
  for (int e = 0; e < 2; ++e)
#pragma unroll
    for (int j = 0; j < 4; ++j) PIN(fq[e][j]);
  chunk(6);
  {
    const int row = lane & 15, kg = lane >> 4;
    const bool on = (((row >> 2) ^ kg) & 1) == 0;
    const unsigned za = lds0 + P2_ZB + ((on && (row & 3) == 0) ? (kg >> 1) * 32 : (on && (row & 3) == 1) ? 128 : 256);
    const unsigned ka = lds0 + P2_KVF + vl;
#pragma unroll
    for (int e = 0; e < 2; ++e) {
#define BF(v) __builtin_bit_cast(bf16x8, v)
      f32x16 np = MFMA32(lds_r128(ka), BF(fq[e][0]), f32x16{});        f32x4 dp = MFMA16(lds_r128(za), BF(fq[e][0]), f32x4{});
      np = MFMA32(lds_r128(ka + 1024), BF(fq[e][1]), np);              dp = MFMA16(lds_r128(za + 16), BF(fq[e][1]), dp);
      f32x16 nm = MFMA32(lds_r128(ka + 2048), BF(fq[e][2]), f32x16{}); f32x4 dm = MFMA16(lds_r128(za + 64), BF(fq[e][2]), f32x4{});
      nm = MFMA32(lds_r128(ka + 3072), BF(fq[e][3]), nm);              dm = MFMA16(lds_r128(za + 80), BF(fq[e][3]), dm);
#undef BF
      const float a = __builtin_amdgcn_rcpf(dp[1]), b = __builtin_amdgcn_rcpf(dm[1]);
      const float r = __builtin_amdgcn_rcpf(dp[0] * a + dm[0] * b + 1e-6f);
      const f32x16 o = np * (a * r) + nm * (b * r);
      const unsigned p = WS_OA + ((8 * tb + 2 * lw + e) * 32 + head) * 2048 + vl;
      st16(sws, p, pack8<0>(o)); st16(sws, p + 1024, pack8<1>(o));
    }
  }
  WAIT_BAR(0);
  if (lead) post(ws, F_OA + tb * 32 + head, tag);
  chunk(7);
  if (blockIdx.x == 0 && lw == 1) {
    poll(ws, lane, tag); poll(ws, 64 + lane, tag); poll(ws, 128 + lane, tag);
    if (lane == 0) post(ws, F_EPOCH, tag);
  }
  unsigned pfs = 0;
  pf_line(pfs, (const char*)woa + (size_t)(idx * 32 + lw * 8 + (lane >> 3)) * 2048 + (x & 1) * 1024 + (lane & 7) * 128);
  pf_line(pfs, (const char*)wob + (size_t)(idx * 32 + lw * 8 + (lane >> 3)) * 2048 + (x & 1) * 1024 + (lane & 7) * 128);
  const int br = idx >> 4, rb3 = 4 * (x >> 1) + (idx & 3), cb = 4 * (x & 1) + ((idx >> 2) & 3);
  const rsrc_t sw = mk_rsrc(br ? wob : woa, 1024u * 2048u);
  unsigned wv[4];
#pragma unroll
  for (int j = 0; j < 4; ++j) {
    const int k = 4 * (4 * lw + j) + (lane >> 4), c = (lane & 15) ^ (((lane >> 4) & 3) << 2);
    wv[j] = k * 2048 + (cb * 128 + c * 8) * 2 - j * 1024;
  }
  const unsigned osrc = (br ? WS_OB : WS_OA) + (rb3 * 4 + lw) * 65536;
  auto wst = [&](int t) {
    const unsigned sow = t * 64 * 2048;
    set_m0(lds0 + p3_wslot(t) + lw * 4096);
    dma16<0>(wv[0], sw, sow); dma16<1024>(wv[1], sw, sow); dma16<2048>(wv[2], sw, sow); dma16<3072>(wv[3], sw, sow);
  };
  auto ost = [&](int t) { dma4k(lds0 + P3_O + (t & 3) * 16384 + lw * 4096, vl, sws, osrc + t * 4096); };
  asm volatile("s_barrier" ::: "memory");
  wst(0); wst(1); wst(2); wst(3);
  WAIT_BAR(18);
  wst(4); wst(5);
  asm volatile("s_barrier" ::: "memory");
  asm volatile("s_barrier" ::: "memory");
  asm volatile("s_barrier" ::: "memory");
  if (lead) post(ws, F_OB + x * 32 + idx, tag);
  STAMP(512, 13);
  if (lw == 0) poll(ws, br ? F_OB + (rb3 >> 1) * 32 + (rb3 & 1) * 16 + (lane & 15) : F_OA + (rb3 >> 1) * 32 + (lane & 31), tag);
  asm volatile("s_barrier" ::: "memory");
  STAMP(512, 14);
  ost(0); ost(1); ost(2); ost(3);
  WAIT_BAR(12);
  PIN(pfs);
  WAIT_BAR(8);  ost(4); wst(6);
  WAIT_BAR(12); ost(5); wst(7);
  WAIT_BAR(16); ost(6); wst(8);
  WAIT_BAR(20); ost(7); wst(9);
  WAIT_BAR(20); ost(8); wst(10);
  WAIT_BAR(20); ost(9); wst(11);
  WAIT_BAR(20); ost(10); wst(12);
  WAIT_BAR(20); ost(11); wst(13);
  WAIT_BAR(20); ost(12); wst(14);
  WAIT_BAR(20); ost(13); wst(15);
  WAIT_BAR(20); ost(14);
  WAIT_BAR(16); ost(15);
  WAIT_BAR(12);
  WAIT_BAR(4);
  WAIT_BAR(0);
}

__global__ __launch_bounds__(768, 3) void fused_fwd(const unsigned short* __restrict__ xa, const unsigned short* __restrict__ wqkv,
                                                    const unsigned short* __restrict__ wphi, const unsigned short* __restrict__ woa,
                                                    const unsigned short* __restrict__ xb, const unsigned short* __restrict__ wq,
                                                    const unsigned short* __restrict__ wk, const unsigned short* __restrict__ wv,
                                                    const unsigned short* __restrict__ wob, char* __restrict__ ws, unsigned short* __restrict__ out) {
  __shared__ __attribute__((aligned(1024))) char smem[LDS_BYTES];
  const unsigned lds0 = (unsigned)(uintptr_t)(AS3 char*)smem;
  const int tid = threadIdx.x, lane = tid & 63, w = __builtin_amdgcn_readfirstlane(tid >> 6);
  const int b = blockIdx.x, x = b & 7, idx = b >> 3;
  const int rb = idx < 24 ? 4 * (x & 1) + (idx & 3) : 2 * (x >> 1) + (idx & 1);
  const int ct = idx < 16 ? 4 * (x >> 1) + (idx >> 2) : idx < 24 ? 2 * (x >> 1) + ((idx >> 2) & 1) : 4 * (x & 1) + ((idx >> 1) & 3);
  const int type = idx < 16 ? 0 : idx < 24 ? 1 : 2 + (ct & 1);
  const rsrc_t sws = mk_rsrc(ws, WS_END);
  STAMP(0, 0);
  if (w >= 8) {
    helper_program(lds0, type >= 2 ? xb : xa, type >= 2 ? wq : wqkv, type == 3 ? wv : type == 2 ? wk : wphi, woa, wob, ws, sws, type, rb, ct, x, idx, w - 8, lane);
    return;
  }
  if (type == 0)      p1_compute<0>(lds0, ws, sws, rb, ct, w, lane);
  else if (type == 1) p1_compute<1>(lds0, ws, sws, rb, ct, w, lane);
  else if (type == 2) p1_compute<2>(lds0, ws, sws, rb, ct, w, lane);
  else                p1_compute<3>(lds0, ws, sws, rb, ct, w, lane);

  const int kh = w >> 2, r32 = lane & 31, h = lane >> 5;
  {
    const bf16x8 sel = row_sum_sel(lane);
    LDS_BAR();
    STAMP(0, 4);
    const unsigned qa = lds0 + P2_Q + (2 * (w & 3) + kh) * 2048 + lane * 16;
    bf16x8 q[2][2];
    q[0][0] = lds_r128(qa);        q[0][1] = lds_r128(qa + 1024);
    q[1][0] = lds_r128(qa ^ 2048); q[1][1] = lds_r128((qa ^ 2048) + 1024);
    f32x16 O[2]; f32x4 L[2];
    unsigned tv = 0;
    attn_pass_fast(lds0, q, sel, kh, lane, O, L, tv);
    TSOUT();
    STAMP(0, 5);
    const unsigned xs = lds0 + P2_XO + w * 4096 + lane * 16, xr = lds0 + P2_XO + (w ^ 4) * 4096 + lane * 16;
#pragma unroll
    for (int g = 0; g < 4; ++g) *(AS3 f32x4*)(xs + g * 1024) = f32x4{O[1][4 * g], O[1][4 * g + 1], O[1][4 * g + 2], O[1][4 * g + 3]};
    *(AS3 float*)(lds0 + P2_XL + w * 256 + lane * 4) = L[1][0];
    LDS_BAR();
#pragma unroll
    for (int g = 0; g < 4; ++g) {
      const f32x4 o = *(AS3 f32x4*)(xr + g * 1024);
      O[0][4 * g] += o[0]; O[0][4 * g + 1] += o[1]; O[0][4 * g + 2] += o[2]; O[0][4 * g + 3] += o[3];
    }
    L[0][0] += *(AS3 float*)(lds0 + P2_XL + (w ^ 4) * 256 + lane * 4);
    bool bad = !(L[0][0] < 1.8446744e19f);
#pragma unroll
    for (int i = 0; i < 16; ++i) bad |= !(fabsf(O[0][i]) < 3.0e38f);
    if (__any(bad)) attn_pass_safe(sws, q[0][0], q[0][1], sel, lane, O[0], L[0]);
    const float r = __builtin_amdgcn_rcpf(xhalf_sum(L[0][0]));
    O[0] *= r;
    const unsigned p = WS_OB + ((8 * x + (idx >> 2)) * 32 + 8 * (idx & 3) + 2 * (w & 3) + kh) * 2048 + lane * 16;
    st16(sws, p, pack8<0>(O[0])); st16(sws, p + 1024, pack8<1>(O[0]));
    WAIT_BAR(0);
    STAMP(0, 6);
  }
  const int br = idx >> 4, rb3 = 4 * (x >> 1) + (idx & 3), cb = 4 * (x & 1) + ((idx >> 2) & 3);
  const int wm = w & 1, wn = (w >> 1) & 1;
  const int q4 = (lane & 15) >> 2, p4 = lane & 3, chalf = (lane >> 4) & 1;
  const unsigned oa = wm * 8192 + kh * (4096 + 2048) + lane * 16;
  unsigned wa[2];
#pragma unroll
  for (int ns = 0; ns < 2; ++ns)
    wa[ns] = (kh * 32 + 4 * h + q4) * 256 + ((((wn * 2 + ns) ^ q4) << 2) | (chalf * 2 + (p4 >> 1))) * 16 + (p4 & 1) * 8;
  f32x16 acc[2][2] = {};
  bf16x8 xf[2][2], wfr[2][2];
  auto rdx = [&](int set, unsigned buf, int ks) {
#pragma unroll
    for (int ms = 0; ms < 2; ++ms) xf[set][ms] = lds_r128(buf + (oa ^ (ms * 4096)) + ks * 1024);
  };
  auto rdw = [&](int set, unsigned buf, int ks, int ns) { wfr[set][ns] = lds_tr2(buf + wa[ns] + ks * 4096, buf + wa[ns] + ks * 4096 + 2048); };
  auto mm1 = [&](int set, int ms, int ns) { acc[ms][ns] = MFMA32(wfr[set][ns], xf[set][ms], acc[ms][ns]); };
  auto sub = [&](int set, bool rd, unsigned ob, unsigned wb, int ks) {
    SBAR(); mm1(set, 0, 0); SBAR(); if (rd) rdx(set ^ 1, ob, ks);
    SBAR(); mm1(set, 1, 0); SBAR(); if (rd) rdw(set ^ 1, wb, ks, 0);
    SBAR(); mm1(set, 0, 1); SBAR(); if (rd) rdw(set ^ 1, wb, ks, 1);
    SBAR(); mm1(set, 1, 1); SBAR();
  };
  asm volatile("s_barrier" ::: "memory");
  LDS_BAR();
  STAMP(0, 7);
  unsigned ob = lds0 + P3_O, wb = lds0 + p3_wslot(0);
  rdx(0, ob, 0); rdw(0, wb, 0, 0); rdw(0, wb, 0, 1);
  LGKM0();
#pragma unroll 1
  for (int t = 0; t < 15; ++t) {
    sub(0, true, ob, wb, 1);
    ob = (ob == lds0 + P3_O + 3 * 16384) ? lds0 + P3_O : ob + 16384;
    wb = (wb == lds0 + 147456) ? lds0 : (wb == lds0 + 16384) ? lds0 + 98304 : wb + 16384;
    LDS_BAR();
    sub(1, true, ob, wb, 0);
  }
  sub(0, true, ob, wb, 1);
  sub(1, false, ob, wb, 0);
  STAMP(0, 8);
  const unsigned xs = lds0 + w * 8192 + lane * 16, xr = lds0 + (w ^ 4) * 8192 + lane * 16;
#pragma unroll
  for (int ns = 0; ns < 2; ++ns)
#pragma unroll
    for (int g = 0; g < 4; ++g) {
      const f32x16& a = acc[1][ns];
      *(AS3 f32x4*)(xs + ns * 4096 + g * 1024) = f32x4{a[4 * g], a[4 * g + 1], a[4 * g + 2], a[4 * g + 3]};
    }
  LDS_BAR();
  const unsigned img = lds0 + 98304 + w * 4096;
#pragma unroll
  for (int ns = 0; ns < 2; ++ns)
#pragma unroll
    for (int g = 0; g < 4; ++g) {
      const f32x16& a = acc[0][ns];
      const f32x4 o = *(AS3 f32x4*)(xr + ns * 4096 + g * 1024);
      *(AS3 u32x2*)(img + r32 * 128 + (((ns * 4 + g) ^ (r32 & 7)) << 4) + h * 8) =
          u32x2{pk2(a[4 * g] + o[0], a[4 * g + 1] + o[1]), pk2(a[4 * g + 2] + o[2], a[4 * g + 3] + o[3])};
    }
  asm volatile("" ::: "memory");
  const int tok0 = rb3 * 128 + wm * 64 + kh * 32;
  unsigned short* op = out + (size_t)tok0 * 2048 + br * 1024 + cb * 128 + wn * 64;
#pragma unroll
  for (int i = 0; i < 4; ++i) {
    const int row = 8 * i + (lane >> 3), ch = lane & 7;
    const u32x4 v = *(AS3 u32x4*)(img + row * 128 + ((ch ^ (row & 7)) << 4));
    *(u32x4*)(op + (size_t)row * 2048 + ch * 8) = v;
  }
  STAMP(0, 9);
  if (threadIdx.x == 0 && blockIdx.x == 0 && 0 != 0) {
    asm volatile("s_waitcnt vmcnt(0)" ::: "memory");
    volatile u64* sp_ = (volatile u64*)(ws + (64u << 20)) + (size_t)blockIdx.x * 32;
    const u64 d = 0 ? (sp_[2 * 0] - sp_[2 * 0]) / 2 : (sp_[2 * 0 + 1] - sp_[2 * 0 + 1]) * 10;
    const u64 t0 = __builtin_amdgcn_s_memrealtime();
    while (__builtin_amdgcn_s_memrealtime() - t0 < (d < 200000 ? d : 200000)) __builtin_amdgcn_s_sleep(1);
  }
}

extern "C" void kernel_launch(void* const* d_in, const int* in_sizes, int n_in, void* d_out, int out_size, void* d_ws, size_t ws_size,
                              hipStream_t stream) {
  (void)in_sizes; (void)n_in; (void)out_size; (void)ws_size;
  typedef const unsigned short* cp;
  fused_fwd<<<256, 768, 0, stream>>>((cp)d_in[0], (cp)d_in[1], (cp)d_in[2], (cp)d_in[3], (cp)d_in[4], (cp)d_in[5], (cp)d_in[6], (cp)d_in[7],
                                     (cp)d_in[8], (char*)d_ws, (unsigned short*)d_out);
}
```

```cpp
#include <hip/hip_runtime.h>
#include <hip/hip_bf16.h>
#include <stdint.h>
#pragma clang diagnostic ignored "-Wint-to-pointer-cast"

typedef __bf16   bf16x8 __attribute__((ext_vector_type(8)));
typedef __bf16   bf16x2 __attribute__((ext_vector_type(2)));
typedef short    s16x4  __attribute__((ext_vector_type(4)));
typedef short    s16x8  __attribute__((ext_vector_type(8)));
typedef float    f32x16 __attribute__((ext_vector_type(16)));
typedef float    f32x4  __attribute__((ext_vector_type(4)));
typedef float    f32x2  __attribute__((ext_vector_type(2)));
typedef unsigned u32x4  __attribute__((ext_vector_type(4)));
typedef unsigned u32x2  __attribute__((ext_vector_type(2)));
typedef unsigned long long u64;
typedef __amdgpu_buffer_rsrc_t rsrc_t;

#define AS1 __attribute__((address_space(1)))
#define AS3 __attribute__((address_space(3)))
#define DI __device__ __forceinline__
#define MFMA32(a, b, c) __builtin_amdgcn_mfma_f32_32x32x16_bf16((a), (b), (c), 0, 0, 0)
#define MFMA16(a, b, c) __builtin_amdgcn_mfma_f32_16x16x32_bf16((a), (b), (c), 0, 0, 0)
#define SBAR() __builtin_amdgcn_sched_barrier(0)
#define WAIT_BAR(N) asm volatile("s_waitcnt vmcnt(" #N ") lgkmcnt(0)\n\ts_barrier" ::: "memory")
#define LGKM0() __builtin_amdgcn_s_waitcnt(0xC07F)
#define LDS_BAR() do { asm volatile("" ::: "memory"); LGKM0(); asm volatile("s_barrier" ::: "memory"); } while (0)

#define STAMP(th, i) do {} while (0)
#define TS(i) do {} while (0)
#define TSOUT() do {} while (0)
#define KROT (2 * x)
#define MAPV 1
constexpr int D = 1024;
constexpr float LOG2E = 1.4426950408889634f;
constexpr float QSCALE = 0.17677669529663687f * LOG2E;

constexpr unsigned WS_QA  = 0;
constexpr unsigned WS_QB  = WS_QA + (8u << 20);
constexpr unsigned WS_OA  = WS_QB + (4u << 20);
constexpr unsigned WS_OB  = WS_OA + (4u << 20);
constexpr unsigned WS_KV  = WS_OB + (4u << 20);
constexpr unsigned WS_KVP = WS_KV + (256u << 10);
constexpr unsigned WS_ZP  = WS_KVP + (2u << 20);
constexpr unsigned WS_FLG = WS_ZP + (64u << 10);
constexpr unsigned WS_END = WS_FLG + (16u << 10);
constexpr int F_K = 0, F_Q = 128, F_OA = 256, F_OB = 512, F_KV = 768, F_EPOCH = 1024;

DI unsigned pk2(float lo, float hi) {
  f32x2 v = {lo, hi};
  return __builtin_bit_cast(unsigned, __builtin_convertvector(v, bf16x2));
}
template <int SS> DI bf16x8 pack8(const f32x16& x) {
  u32x4 p = {pk2(x[8 * SS + 0], x[8 * SS + 1]), pk2(x[8 * SS + 2], x[8 * SS + 3]), pk2(x[8 * SS + 4], x[8 * SS + 5]), pk2(x[8 * SS + 6], x[8 * SS + 7])};
  return __builtin_bit_cast(bf16x8, p);
}
DI bf16x8 lds_r128(unsigned a) { return *(const AS3 bf16x8*)a; }
DI bf16x8 lds_tr2(unsigned a0, unsigned a1) {
  s16x4 lo = __builtin_amdgcn_ds_read_tr16_b64_v4i16((AS3 s16x4*)a0);
  s16x4 hi = __builtin_amdgcn_ds_read_tr16_b64_v4i16((AS3 s16x4*)a1);
  s16x8 r = __builtin_shufflevector(lo, hi, 0, 1, 2, 3, 4, 5, 6, 7);
  return __builtin_bit_cast(bf16x8, r);
}
DI rsrc_t mk_rsrc(const void* p, unsigned bytes) { return __builtin_amdgcn_make_buffer_rsrc((void*)p, (short)0, (int)bytes, 0x00020000); }
DI void set_m0(unsigned v) { asm volatile("s_mov_b32 m0, %0\n\ts_nop 0" ::"s"(v) : "memory"); }
template <int IMM> DI void dma16(unsigned voff, rsrc_t srd, unsigned soff) {
  asm volatile("buffer_load_dwordx4 %0, %1, %2 offen offset:%3 lds" ::"v"(voff), "s"(srd), "s"(soff), "i"(IMM) : "memory");
}
template <int IMM> DI void dma16d(unsigned voff, rsrc_t srd, unsigned soff) {
  asm volatile("buffer_load_dwordx4 %0, %1, %2 offen offset:%3 sc1 lds" ::"v"(voff), "s"(srd), "s"(soff), "i"(IMM) : "memory");
}
DI void dma4k(unsigned lds, unsigned voff, rsrc_t srd, unsigned soff) {
  set_m0(lds); dma16d<0>(voff, srd, soff); dma16d<1024>(voff, srd, soff); dma16d<2048>(voff, srd, soff); dma16d<3072>(voff, srd, soff);
}
template <class T> DI void st16(rsrc_t r, unsigned voff, T v) { __builtin_amdgcn_raw_buffer_store_b128(__builtin_bit_cast(u32x4, v), r, (int)voff, 0, 16); }
DI void st8(rsrc_t r, unsigned voff, u32x2 v) { __builtin_amdgcn_raw_buffer_store_b64(v, r, (int)voff, 0, 16); }
DI void st4(rsrc_t r, unsigned voff, float v) { __builtin_amdgcn_raw_buffer_store_b32(__float_as_uint(v), r, (int)voff, 0, 16); }
DI u32x4 ld16(rsrc_t r, unsigned voff) { return __builtin_amdgcn_raw_buffer_load_b128(r, (int)voff, 0, 16); }
DI float ld4(rsrc_t r, unsigned voff) { return __uint_as_float(__builtin_amdgcn_raw_buffer_load_b32(r, (int)voff, 0, 16)); }
DI void post(char* ws, int f, u64 tag) { __hip_atomic_store((AS1 u64*)(ws + WS_FLG) + f, tag, __ATOMIC_RELAXED, __HIP_MEMORY_SCOPE_AGENT); }
DI void poll(char* ws, int f, u64 tag) {
#pragma unroll 1
  for (int it = 0; it < (1 << 17); ++it) {
    const u64 v = __hip_atomic_load((AS1 u64*)(ws + WS_FLG) + f, __ATOMIC_RELAXED, __HIP_MEMORY_SCOPE_AGENT);
    if (__all(v == tag)) break;
    __builtin_amdgcn_s_sleep(2);
  }
  asm volatile("" ::: "memory");
}
DI void pf_line(unsigned& sink, const void* p) { asm volatile("global_load_dword %0, %1, off" : "+v"(sink) : "v"(p) : "memory"); }
#define PIN(x) asm volatile("" : "+v"(x))
DI float xhalf_max(float v) {
  auto r = __builtin_amdgcn_permlane32_swap(__float_as_uint(v), __float_as_uint(v), false, false);
  return fmaxf(__uint_as_float(r[0]), __uint_as_float(r[1]));
}
DI float xhalf_min(float v) {
  auto r = __builtin_amdgcn_permlane32_swap(__float_as_uint(v), __float_as_uint(v), false, false);
  return fminf(__uint_as_float(r[0]), __uint_as_float(r[1]));
}
DI float xhalf_sum(float v) {
  auto r = __builtin_amdgcn_permlane32_swap(__float_as_uint(v), __float_as_uint(v), false, false);
  return __uint_as_float(r[0]) + __uint_as_float(r[1]);
}
DI float max16(const f32x16& x) {
  float a = fmaxf(fmaxf(x[0], x[1]), x[2]), b = fmaxf(fmaxf(x[3], x[4]), x[5]);
  a = fmaxf(fmaxf(a, x[6]), x[7]);   b = fmaxf(fmaxf(b, x[8]), x[9]);
  a = fmaxf(fmaxf(a, x[10]), x[11]); b = fmaxf(fmaxf(b, x[12]), x[13]);
  a = fmaxf(fmaxf(a, x[14]), x[15]);
  return fmaxf(a, b);
}
DI float min16(const f32x16& x) {
  float a = fminf(fminf(x[0], x[1]), x[2]), b = fminf(fminf(x[3], x[4]), x[5]);
  a = fminf(fminf(a, x[6]), x[7]);   b = fminf(fminf(b, x[8]), x[9]);
  a = fminf(fminf(a, x[10]), x[11]); b = fminf(fminf(b, x[12]), x[13]);
  a = fminf(fminf(a, x[14]), x[15]);
  return fminf(a, b);
}
template <bool NORM> DI void feat_map(const f32x16& p, f32x16& ep, f32x16& em) {
  const float mx = xhalf_max(max16(p)), mn = xhalf_min(min16(p));
  const f32x2 a = {-mx * LOG2E, -mx * LOG2E}, b = {mn * LOG2E, mn * LOG2E}, c = {LOG2E, LOG2E};
  f32x2 sp = {0.f, 0.f}, sm = {0.f, 0.f};
#pragma unroll
  for (int i = 0; i < 16; i += 2) {
    const f32x2 x = {p[i], p[i + 1]};
    const f32x2 u = __builtin_elementwise_fma(x, c, a), v = __builtin_elementwise_fma(x, -c, b);
    const f32x2 eu = {__builtin_amdgcn_exp2f(u[0]), __builtin_amdgcn_exp2f(u[1])}, ev = {__builtin_amdgcn_exp2f(v[0]), __builtin_amdgcn_exp2f(v[1])};
    ep[i] = eu[0]; ep[i + 1] = eu[1]; em[i] = ev[0]; em[i + 1] = ev[1];
    if (i == 0) { sp = eu; sm = ev; } else { sp += eu; sm += ev; }
  }
  if (NORM) {
    const float rp = __builtin_amdgcn_rcpf(xhalf_sum(sp[0] + sp[1])), rm = __builtin_amdgcn_rcpf(xhalf_sum(sm[0] + sm[1]));
    ep *= rp; em *= rm;
  }
}
DI bf16x8 row_sum_sel(int lane) {
  const unsigned u = ((lane & 11) == 0 && (((lane >> 2) ^ (lane >> 4)) & 1) == 0) ? 0x3f803f80u : 0u;
  return __builtin_bit_cast(bf16x8, u32x4{u, u, u, u});
}

constexpr int LDS_BYTES = 163840;
constexpr int P1_WOFF = 32768, P1_EOFF = 49152, P1_STAGE = 53248, P1_ZRED = 3 * P1_STAGE, P1_CNT = P1_ZRED + 2048, P1_GO = P1_CNT + 16, P1_PHI = 65536;
constexpr int P2_Q = 131072, P2_KVF = 147456, P2_ZB = 151552;
constexpr int P2_XO = 32768, P2_XL = 65536;
constexpr int P3_O = 32768;
DI constexpr int p3_wslot(int t) { return (98304 + (t % 6) * 16384) % 163840; }

template <int TYPE>
DI void p1_compute(unsigned lds0, char* __restrict__ ws, rsrc_t sws, int rb, int ct, int w, int lane) {
  const int tid = threadIdx.x;
  const int wm = w & 3, wn = w >> 2, r32 = lane & 31, h = lane >> 5;
  if (tid < 8) *(AS3 unsigned*)(lds0 + P1_CNT + tid * 4) = 0u;

  const int xrow = wm * 64 + r32;
  const unsigned xa = xrow * 128 + ((h ^ ((xrow >> 1) & 7)) << 4);
  const int q4 = (lane & 15) >> 2, p4 = lane & 3, chalf = (lane >> 4) & 1;
  unsigned wa[2];
#pragma unroll
  for (int ns = 0; ns < 2; ++ns)
    wa[ns] = P1_WOFF + (8 * h + q4) * 256 + ((((wn * 2 + ns) ^ q4) << 2) | (chalf * 2 + (p4 >> 1))) * 16 + (p4 & 1) * 8;
  const int erow = 64 * (ct >> 1) + 16 * wm + (lane & 15);
  const unsigned exa = erow * 128 + (((lane >> 4) ^ ((erow >> 1) & 7)) << 4);
  const unsigned ewa = P1_EOFF + (8 * (lane >> 4) + q4) * 64 + wn * 32 + p4 * 8;

  f32x16 acc[2][2] = {};
  f32x4 eacc = {};
  bf16x8 xf[2][2], wfr[2][2], ex[2], ew[2];
  auto rdx = [&](int set, unsigned buf, int ks) {
#pragma unroll
    for (int ms = 0; ms < 2; ++ms) xf[set][ms] = lds_r128(buf + ((xa ^ (ks * 32)) + ms * 4096));
  };
  auto rdw = [&](int set, unsigned buf, int ks, int ns) { wfr[set][ns] = lds_tr2(buf + wa[ns] + ks * 4096, buf + wa[ns] + ks * 4096 + 1024); };
  auto mm1 = [&](int set, int ms, int ns) {
    if (TYPE == 0 && ns == 1) acc[ms][1] = MFMA32(xf[set][ms], wfr[set][1], acc[ms][1]);
    else                      acc[ms][ns] = MFMA32(wfr[set][ns], xf[set][ms], acc[ms][ns]);
  };
  auto erd = [&](unsigned buf, int kk) {
    if (TYPE >= 2) { ex[kk] = lds_r128(buf + (exa ^ (kk * 64))); ew[kk] = lds_tr2(buf + ewa + kk * 2048, buf + ewa + kk * 2048 + 256); }
  };
  auto emm = [&](int kk) {
    if (TYPE == 2) eacc = MFMA16(ew[kk], ex[kk], eacc);
    if (TYPE == 3) eacc = MFMA16(ex[kk], ew[kk], eacc);
  };
  auto sub = [&](int set, unsigned buf, int ks) {
    SBAR(); mm1(set, 0, 0); SBAR(); rdx(set ^ 1, buf, ks);
    SBAR(); mm1(set, 1, 0); SBAR(); rdw(set ^ 1, buf, ks, 0);
    SBAR(); mm1(set, 0, 1); SBAR(); rdw(set ^ 1, buf, ks, 1);
    SBAR(); mm1(set, 1, 1); SBAR();
  };
  LDS_BAR();
  STAMP(0, 1);
  unsigned cur = lds0, nxt = lds0 + P1_STAGE;
  rdx(0, cur, 0); rdw(0, cur, 0, 0); rdw(0, cur, 0, 1);
  LGKM0();
#pragma unroll 1
  for (int t = 0; t < 16; ++t) {
    sub(0, cur, 1); erd(cur, 0);
    sub(1, cur, 2); erd(cur, 1); SBAR(); emm(0);
    sub(0, cur, 3); emm(1); SBAR();
    LDS_BAR();
    sub(1, nxt, 0);
    cur = nxt; nxt = (nxt == lds0 + 2 * P1_STAGE) ? lds0 : nxt + P1_STAGE;
  }
  STAMP(0, 2);
  bf16x8 wf[2][2];
  if constexpr (TYPE <= 1) {
    const unsigned a = lds0 + P1_PHI + (TYPE == 0 ? wn : 2 * wn) * 2048 + (4 * h + q4) * 64 + chalf * 32 + p4 * 8;
#pragma unroll
    for (int n = 0; n <= TYPE; ++n)
#pragma unroll
      for (int s = 0; s < 2; ++s) wf[n][s] = lds_tr2(a + n * 2048 + s * 1024, a + n * 2048 + s * 1024 + 512);
  }

  if (TYPE >= 2) {
#pragma unroll
    for (int ms = 0; ms < 2; ++ms)
#pragma unroll
      for (int ns = 0; ns < 2; ++ns) {
        const f32x16 v = acc[ms][ns] * QSCALE;
        const unsigned p = WS_QB + ((rb * 8 + wm * 2 + ms) * 32 + ct * 4 + wn * 2 + ns) * 2048 + lane * 16;
        st16(sws, p, pack8<0>(v)); st16(sws, p + 1024, pack8<1>(v));
      }
    const int g = lane >> 4, tok0 = rb * 256 + 64 * (ct >> 1) + 16 * wm;
    unsigned p = WS_KV + (tok0 >> 5) * 4096;
    if (TYPE == 3) p += 2048 + (wm & 1) * 1024 + (16 * wn + (lane & 15) + 32 * (g & 1)) * 16 + (g >> 1) * 8;
    else           p += wn * 1024 + (16 * (wm & 1) + (lane & 15) + 32 * (g & 1)) * 16 + (g >> 1) * 8;
    st8(sws, p, u32x2{pk2(eacc[0], eacc[1]), pk2(eacc[2], eacc[3])});
    WAIT_BAR(0);
  } else if (TYPE == 1) {
    f32x16 pq[2][2];
#pragma unroll
    for (int ms = 0; ms < 2; ++ms)
#pragma unroll
      for (int ns = 0; ns < 2; ++ns) {
        pq[ms][ns] = MFMA32(wf[ns][0], pack8<0>(acc[ms][ns]), f32x16{});
        pq[ms][ns] = MFMA32(wf[ns][1], pack8<1>(acc[ms][ns]), pq[ms][ns]);
      }
#pragma unroll
    for (int ms = 0; ms < 2; ++ms)
#pragma unroll
      for (int ns = 0; ns < 2; ++ns) {
        f32x16 ep, em;
        feat_map<false>(pq[ms][ns], ep, em);
        const unsigned p = WS_QA + ((rb * 8 + wm * 2 + ms) * 32 + ct * 4 + wn * 2 + ns) * 4096 + lane * 16;
        st16(sws, p, pack8<0>(ep)); st16(sws, p + 1024, pack8<1>(ep)); st16(sws, p + 2048, pack8<0>(em)); st16(sws, p + 3072, pack8<1>(em));
      }
  } else {
    const unsigned img = lds0 + w * 8192;
    const bf16x8 sel = row_sum_sel(lane);
    f32x16 pk[2], kvp = {}, kvm = {};
    f32x4 zp = {}, zm = {};
    bf16x8 vf[2][2], ap[2], am[2];
#pragma unroll
    for (int ms = 0; ms < 2; ++ms) {
      pk[ms] = MFMA32(wf[0][0], pack8<0>(acc[ms][0]), f32x16{});
      pk[ms] = MFMA32(wf[0][1], pack8<1>(acc[ms][0]), pk[ms]);
    }
#pragma unroll
    for (int ms = 0; ms < 2; ++ms) { vf[ms][0] = pack8<0>(acc[ms][1]); vf[ms][1] = pack8<1>(acc[ms][1]); }
    auto kvmm = [&](int ms) {
#pragma unroll
      for (int s = 0; s < 2; ++s) {
        kvp = MFMA32(ap[s], vf[ms][s], kvp); kvm = MFMA32(am[s], vf[ms][s], kvm);
        zp = MFMA16(sel, ap[s], zp);         zm = MFMA16(sel, am[s], zm);
      }
    };
#pragma unroll
    for (int ms = 0; ms < 2; ++ms) {
      f32x16 ep, em;
      feat_map<true>(pk[ms], ep, em);
      if (ms == 1) kvmm(0);
#pragma unroll
      for (int g = 0; g < 4; ++g) {
        const unsigned a = img + r32 * 64 + (((2 * g + h) ^ ((r32 >> 1) & 7)) << 3);
        *(AS3 u32x2*)a = u32x2{pk2(ep[4 * g], ep[4 * g + 1]), pk2(ep[4 * g + 2], ep[4 * g + 3])};
        *(AS3 u32x2*)(a + 2048) = u32x2{pk2(em[4 * g], em[4 * g + 1]), pk2(em[4 * g + 2], em[4 * g + 3])};
      }
      asm volatile("" ::: "memory");
#pragma unroll
      for (int s = 0; s < 2; ++s) {
        const int t0 = 16 * s + 4 * h + q4, t1 = t0 + 8, u = 4 * chalf + p4;
        const unsigned a0 = img + t0 * 64 + ((u ^ ((t0 >> 1) & 7)) << 3), a1 = img + t1 * 64 + ((u ^ ((t1 >> 1) & 7)) << 3);
        ap[s] = lds_tr2(a0, a1); am[s] = lds_tr2(a0 + 2048, a1 + 2048);
      }
      asm volatile("" ::: "memory");
    }
    kvmm(1);
#pragma unroll
    for (int g = 0; g < 4; ++g) {
      *(AS3 f32x4*)(img + g * 1024 + lane * 16) = f32x4{kvp[4 * g], kvp[4 * g + 1], kvp[4 * g + 2], kvp[4 * g + 3]};
      *(AS3 f32x4*)(img + 4096 + g * 1024 + lane * 16) = f32x4{kvm[4 * g], kvm[4 * g + 1], kvm[4 * g + 2], kvm[4 * g + 3]};
    }
    if (lane < 32) {
      const unsigned a = lds0 + P1_ZRED + w * 256 + ((lane >> 2) & 1) * 64 + (lane >> 3) * 16 + (lane & 3) * 4;
      *(AS3 float*)a = zp[0]; *(AS3 float*)(a + 128) = zm[0];
    }
    asm volatile("" ::: "memory"); LGKM0();
    if (lane == 0) __hip_atomic_fetch_add((AS3 unsigned*)(lds0 + P1_CNT), 1u, __ATOMIC_RELAXED, __HIP_MEMORY_SCOPE_WORKGROUP);
    while (__hip_atomic_load((AS3 unsigned*)(lds0 + P1_CNT), __ATOMIC_RELAXED, __HIP_MEMORY_SCOPE_WORKGROUP) < 8u) __builtin_amdgcn_s_sleep(1);
    asm volatile("" ::: "memory");
#pragma unroll
    for (int hb = 0; hb < 2; ++hb) {
      f32x4 s = *(AS3 f32x4*)(lds0 + (hb * 4) * 8192 + tid * 16);
#pragma unroll
      for (int m = 1; m < 4; ++m) s += *(AS3 f32x4*)(lds0 + (hb * 4 + m) * 8192 + tid * 16);
      st16(sws, WS_KVP + (((2 * ct + hb) * 8 + rb) * 512 + tid) * 16, s);
    }
    if (tid < 128) {
      const int hb = tid >> 6;
      float s = 0.f;
#pragma unroll
      for (int m = 0; m < 4; ++m) s += *(AS3 float*)(lds0 + P1_ZRED + (hb * 4 + m) * 256 + lane * 4);
      st4(sws, WS_ZP + (((2 * ct + hb) * 8 + rb) * 64 + lane) * 4, s);
    }
  }
  STAMP(0, 3);
}

DI void attn_pass_fast(unsigned lds0, const bf16x8 (&q)[2][2], const bf16x8 sel, int kh, int lane, f32x16 (&O)[2], f32x4 (&L)[2], unsigned& tv) {
  TS(0);
  typedef const AS3 char* lp;
  const lp l0 = (lp)(lds0 + lane * 16), la = l0 + kh * 16384;
#define RD(p) (*(const AS3 bf16x8*)(p))
  if (kh) __builtin_amdgcn_s_setprio(1);
  bf16x8 ka = RD(l0 + 98304), kb = RD(l0 + 98304 + 1024);
  f32x16 negm[2], S0, S1;
#pragma unroll
  for (int r = 0; r < 2; ++r) {
    f32x16 z = {};
    z = MFMA32(ka, q[r][0], z); z = MFMA32(kb, q[r][1], z);
    const float m = xhalf_max(max16(z));
#pragma unroll
    for (int i = 0; i < 16; ++i) negm[r][i] = -m;
    PIN(negm[r]);
    O[r] = f32x16{}; L[r] = f32x4{};
  }
  ka = RD(la + 98304); kb = RD(la + 98304 + 1024);
  bf16x8 va = RD(la + 98304 + 2048), vb = bf16x8{};
  S0 = MFMA32(ka, q[0][0], negm[0]); S0 = MFMA32(kb, q[0][1], S0);
  u32x4 p0 = {}, p1 = {};
  LGKM0();
#define EX4(SC, i) do { SC[i] = __builtin_amdgcn_exp2f(SC[i]); SC[i + 1] = __builtin_amdgcn_exp2f(SC[i + 1]); \
    SC[i + 2] = __builtin_amdgcn_exp2f(SC[i + 2]); SC[i + 3] = __builtin_amdgcn_exp2f(SC[i + 3]); PIN(SC); } while (0)
#define CV4(P, SC, i) do { P[0] = pk2(SC[i], SC[i + 1]); P[1] = pk2(SC[i + 2], SC[i + 3]); P[2] = pk2(SC[i + 4], SC[i + 5]); P[3] = pk2(SC[i + 6], SC[i + 7]); PIN(P); } while (0)
#define BF(P) __builtin_bit_cast(bf16x8, P)
#define TILE(TA, TN) do { \
    SBAR(); S1 = MFMA32(ka, q[1][0], negm[1]);  SBAR(); ka = RD(TN);          EX4(S0, 0);  \
    SBAR(); O[1] = MFMA32(vb, BF(p1), O[1]);    SBAR(); vb = RD((TA) + 3072); EX4(S0, 4);  \
    SBAR(); L[1] = MFMA16(sel, BF(p1), L[1]);   SBAR(); CV4(p0, S0, 0); \
    SBAR(); S1 = MFMA32(kb, q[1][1], S1);       SBAR(); kb = RD((TN) + 1024); EX4(S0, 8);  \
    SBAR(); O[0] = MFMA32(va, BF(p0), O[0]);    SBAR(); EX4(S0, 12); \
    SBAR(); L[0] = MFMA16(sel, BF(p0), L[0]);   SBAR(); CV4(p1, S0, 8); \
    SBAR(); S0 = MFMA32(ka, q[0][0], negm[0]);  SBAR(); EX4(S1, 0);  \
    SBAR(); O[0] = MFMA32(vb, BF(p1), O[0]);    SBAR(); EX4(S1, 4);  \
    SBAR(); L[0] = MFMA16(sel, BF(p1), L[0]);   SBAR(); CV4(p0, S1, 0); \
    SBAR(); S0 = MFMA32(kb, q[0][1], S0);       SBAR(); EX4(S1, 8);  \
    SBAR(); O[1] = MFMA32(va, BF(p0), O[1]);    SBAR(); va = RD((TN) + 2048); EX4(S1, 12); \
    SBAR(); L[1] = MFMA16(sel, BF(p0), L[1]);   SBAR(); CV4(p1, S1, 8); } while (0)
  TS(1);
#pragma unroll 1
  for (int c = 0; c < 8; ++c) {
    const lp cb = la + ((c + 3) & 3) * 32768, nb = la + (c & 3) * 32768;
    TILE(cb, cb + 4096);
    TILE(cb + 4096, cb + 8192);
    SBAR(); if (kh) __builtin_amdgcn_s_setprio(0);
    TILE(cb + 8192, cb + 12288);
    SBAR(); TS(2 + 2 * c);
    asm volatile("s_waitcnt vmcnt(0)\n\ts_barrier" ::: "memory"); TS(3 + 2 * c);
    if (kh) __builtin_amdgcn_s_setprio(1);
    TILE(cb + 12288, nb);
  }
  SBAR();
  O[1] = MFMA32(vb, BF(p1), O[1]); L[1] = MFMA16(sel, BF(p1), L[1]);
  __builtin_amdgcn_s_setprio(0);
  TS(18);
#undef TILE
#undef EX4
#undef CV4
#undef BF
#undef RD
}

DI void attn_pass_safe(rsrc_t sws, const bf16x8 q0, const bf16x8 q1, const bf16x8 sel, int lane, f32x16& O, f32x4& L) {
  O = f32x16{}; L = f32x4{};
  float m = -INFINITY;
#pragma unroll 1
  for (int t = 0; t < 64; ++t) {
    bf16x8 a[4];
#pragma unroll
    for (int j = 0; j < 4; ++j) a[j] = __builtin_bit_cast(bf16x8, ld16(sws, WS_KV + t * 4096 + j * 1024 + lane * 16));
    f32x16 Sc = {};
    Sc = MFMA32(a[0], q0, Sc); Sc = MFMA32(a[1], q1, Sc);
    const float mn = fmaxf(m, xhalf_max(max16(Sc)));
    const float al = __builtin_amdgcn_exp2f(m - mn);
    m = mn;
#pragma unroll
    for (int r = 0; r < 16; ++r) { O[r] *= al; Sc[r] = __builtin_amdgcn_exp2f(Sc[r] - mn); }
    L *= al;
    const bf16x8 p0 = pack8<0>(Sc), p1 = pack8<1>(Sc);
    O = MFMA32(a[2], p0, O); L = MFMA16(sel, p0, L);
    O = MFMA32(a[3], p1, O); L = MFMA16(sel, p1, L);
  }
}

DI void helper_program(unsigned lds0, const unsigned short* __restrict__ X, const unsigned short* __restrict__ W, const unsigned short* __restrict__ E,
                       const unsigned short* __restrict__ woa, const unsigned short* __restrict__ wob, char* __restrict__ ws, rsrc_t sws,
                       int type, int rb, int ct, int x, int idx, int lw, int lane) {
  __builtin_amdgcn_s_setprio(3);
  const u64 e0 = __hip_atomic_load((AS1 u64*)(ws + WS_FLG) + F_EPOCH, __ATOMIC_RELAXED, __HIP_MEMORY_SCOPE_AGENT);
  const unsigned vl = lane * 16;
  {
    const bool ext = type >= 2;
    const unsigned ldw = ext ? 2048u : 6144u;
    const rsrc_t sx = mk_rsrc(X + (size_t)rb * 256 * D, 256u * 2048u);
    const rsrc_t sw = mk_rsrc(W, 1024u * ldw);
    const rsrc_t se = mk_rsrc(E, 1024u * 64u);
    unsigned xv[8], wv[4];
#pragma unroll
    for (int j = 0; j < 8; ++j) {
      const int row = 8 * (8 * lw + j) + (lane >> 3), c = (lane & 7) ^ ((row >> 1) & 7);
      xv[j] = row * 2048 + c * 16 - (j & 3) * 1024;
    }
#pragma unroll
    for (int j = 0; j < 4; ++j) {
      const int k = 4 * (4 * lw + j) + (lane >> 4), c = (lane & 15) ^ (((lane >> 4) & 3) << 2);
      const int col = (type == 0) ? 1024 + ((c >> 2) & 1) * 1024 + (2 * ct + (c >> 3)) * 32 + (c & 3) * 8 : ct * 128 + c * 8;
      wv[j] = k * ldw + col * 2 - j * 1024;
    }
    auto stage = [&](int t0, unsigned buf) {
      const int t = (t0 + KROT) & 15;
      const unsigned sox = t * 128, sow = t * 64 * ldw;
      set_m0(buf + P1_WOFF + lw * 4096);
      dma16<0>(wv[0], sw, sow); dma16<1024>(wv[1], sw, sow); dma16<2048>(wv[2], sw, sow); dma16<3072>(wv[3], sw, sow);
      set_m0(buf + lw * 8192);
      dma16<0>(xv[0], sx, sox); dma16<1024>(xv[1], sx, sox); dma16<2048>(xv[2], sx, sox); dma16<3072>(xv[3], sx, sox);
      set_m0(buf + lw * 8192 + 4096);
      dma16<0>(xv[4], sx, sox); dma16<1024>(xv[5], sx, sox); dma16<2048>(xv[6], sx, sox); dma16<3072>(xv[7], sx, sox);
      if (ext) { set_m0(buf + P1_EOFF + lw * 1024); dma16<0>(vl, se, t * 4096 + lw * 1024); }
    };
    stage(0, lds0); stage(1, lds0 + P1_STAGE);
    if (ext) WAIT_BAR(13); else WAIT_BAR(12);
    stage(2, lds0 + 2 * P1_STAGE);
    unsigned buf = lds0;
#pragma unroll 1
    for (int t = 0; t < 13; ++t) {
      if (ext) WAIT_BAR(13); else WAIT_BAR(12);
      stage(t + 3, buf);
      buf = (buf == lds0 + 2 * P1_STAGE) ? lds0 : buf + P1_STAGE;
    }
    if (ext) WAIT_BAR(13); else WAIT_BAR(12);
    if (!ext && (type == 1 || lw < 2)) {
      set_m0(lds0 + P1_PHI + lw * 2048);
      const unsigned so = ((type == 0 ? 2 * ct : 4 * ct) + lw) * 2048;
      dma16<0>(vl, se, so); dma16<1024>(vl, se, so);
    }
    WAIT_BAR(0);
    WAIT_BAR(0);
  }
  const u64 tag = ((u64)(unsigned)__builtin_amdgcn_readfirstlane((unsigned)(e0 >> 32)) << 32 | (unsigned)__builtin_amdgcn_readfirstlane((unsigned)e0))
                  * 0x5851F42D4C957F2Dull + 0x14057B7EF767814Full;
  const bool lead = (lw == 0) && (lane == 0);
  if (type >= 2) { WAIT_BAR(0); if (lead) post(ws, F_KV + rb * 16 + ct, tag); }
  auto chunk = [&](int c) {
    const unsigned l = lds0 + ((c + 3) & 3) * 32768 + lw * 8192, so = WS_KV + ((c + x) & 7) * 32768 + lw * 8192;
    dma4k(l, vl, sws, so); dma4k(l + 4096, vl, sws, so + 4096);
  };
  STAMP(512, 10);
  if (lw == 0) {
    poll(ws, F_KV + x * 16 + (lane & 7), tag);
    __hip_atomic_store((AS3 unsigned*)(lds0 + P1_GO), 1u, __ATOMIC_RELAXED, __HIP_MEMORY_SCOPE_WORKGROUP);
  } else {
    while (__hip_atomic_load((AS3 unsigned*)(lds0 + P1_GO), __ATOMIC_RELAXED, __HIP_MEMORY_SCOPE_WORKGROUP) == 0u) __builtin_amdgcn_s_sleep(1);
    asm volatile("" ::: "memory");
  }
  STAMP(512, 11);
  dma4k(lds0 + P2_Q + lw * 4096, vl, sws, WS_QB + ((8 * x + (idx >> 2)) * 32 + 8 * (idx & 3)) * 2048 + lw * 4096);
  chunk(0);
  WAIT_BAR(0);
  poll(ws, F_KV + (lane >> 3) * 16 + (lane & 7), tag);
  chunk(1); chunk(2); chunk(3);
  WAIT_BAR(16);
  if (lead && type < 2) post(ws, type == 0 ? F_K + ct * 8 + rb : F_Q + rb * 8 + ct, tag);
  if (lw == 1) *(AS3 unsigned*)(lds0 + P2_ZB + 128 + lane * 4) = lane < 32 ? 0x3f803f80u : 0u;
  WAIT_BAR(0);
  const int head = 4 * x + (idx & 3), tb = idx >> 2;
  poll(ws, lane < 8 ? F_K + (head >> 1) * 8 + lane : F_Q + tb * 8 + x, tag);
  STAMP(512, 12);
  chunk(4);
  u32x4 kvp[2][8], fq[2][4];
  float zs[8];
#pragma unroll
  for (int e = 0; e < 2; ++e)
#pragma unroll
    for (int p = 0; p < 8; ++p) kvp[e][p] = ld16(sws, WS_KVP + ((head * 8 + p) * 512 + (2 * lw + e) * 64 + lane) * 16);
#pragma unroll
  for (int p = 0; p < 8; ++p) zs[p] = ld4(sws, WS_ZP + ((head * 8 + p) * 64 + lane) * 4);
  asm volatile("s_barrier" ::: "memory");
#pragma unroll
  for (int e = 0; e < 2; ++e) {
#pragma unroll
    for (int p = 0; p < 8; ++p) PIN(kvp[e][p]);
#define F4(v) __builtin_bit_cast(f32x4, v)
    const f32x4 sm = ((F4(kvp[e][0]) + F4(kvp[e][1])) + (F4(kvp[e][2]) + F4(kvp[e][3]))) + ((F4(kvp[e][4]) + F4(kvp[e][5])) + (F4(kvp[e][6]) + F4(kvp[e][7])));
#undef F4
    *(AS3 u32x2*)(lds0 + P2_KVF + lw * 1024 + vl + e * 8) = u32x2{pk2(sm[0], sm[1]), pk2(sm[2], sm[3])};
  }
  {
#pragma unroll
    for (int p = 0; p < 8; ++p) PIN(zs[p]);
    const float z = ((zs[0] + zs[1]) + (zs[2] + zs[3])) + ((zs[4] + zs[5]) + (zs[6] + zs[7]));
    if (lw == 0) *(AS3 unsigned short*)(lds0 + P2_ZB + lane * 2) = (unsigned short)(pk2(z, z) & 0xffffu);
  }
  chunk(5);
#pragma unroll
  for (int e = 0; e < 2; ++e)
#pragma unroll
    for (int j = 0; j < 4; ++j) fq[e][j] = ld16(sws, WS_QA + ((8 * tb + 2 * lw + e) * 32 + head) * 4096 + j * 1024 + vl);
  asm volatile("s_waitcnt lgkmcnt(0)\n\ts_barrier" ::: "memory");
#pragma unroll
  for (int e = 0; e < 2; ++e)
#pragma unroll
    for (int j = 0; j < 4; ++j) PIN(fq[e][j]);
  chunk(6);
  {
    const int row = lane & 15, kg = lane >> 4;
    const bool on = (((row >> 2) ^ kg) & 1) == 0;
    const unsigned za = lds0 + P2_ZB + ((on && (row & 3) == 0) ? (kg >> 1) * 32 : (on && (row & 3) == 1) ? 128 : 256);
    const unsigned ka = lds0 + P2_KVF + vl;
#pragma unroll
    for (int e = 0; e < 2; ++e) {
#define BF(v) __builtin_bit_cast(bf16x8, v)
      f32x16 np = MFMA32(lds_r128(ka), BF(fq[e][0]), f32x16{});        f32x4 dp = MFMA16(lds_r128(za), BF(fq[e][0]), f32x4{});
      np = MFMA32(lds_r128(ka + 1024), BF(fq[e][1]), np);              dp = MFMA16(lds_r128(za + 16), BF(fq[e][1]), dp);
      f32x16 nm = MFMA32(lds_r128(ka + 2048), BF(fq[e][2]), f32x16{}); f32x4 dm = MFMA16(lds_r128(za + 64), BF(fq[e][2]), f32x4{});
      nm = MFMA32(lds_r128(ka + 3072), BF(fq[e][3]), nm);              dm = MFMA16(lds_r128(za + 80), BF(fq[e][3]), dm);
#undef BF
      const float a = __builtin_amdgcn_rcpf(dp[1]), b = __builtin_amdgcn_rcpf(dm[1]);
      const float r = __builtin_amdgcn_rcpf(dp[0] * a + dm[0] * b + 1e-6f);
      const f32x16 o = np * (a * r) + nm * (b * r);
      const unsigned p = WS_OA + ((8 * tb + 2 * lw + e) * 32 + head) * 2048 + vl;
      st16(sws, p, pack8<0>(o)); st16(sws, p + 1024, pack8<1>(o));
    }
  }
  WAIT_BAR(0);
  if (lead) post(ws, F_OA + tb * 32 + head, tag);
  chunk(7);
  if (blockIdx.x == 0 && lw == 1) {
    poll(ws, lane, tag); poll(ws, 64 + lane, tag); poll(ws, 128 + lane, tag);
    if (lane == 0) post(ws, F_EPOCH, tag);
  }
  unsigned pfs = 0;
  pf_line(pfs, (const char*)woa + (size_t)(idx * 32 + lw * 8 + (lane >> 3)) * 2048 + (x & 1) * 1024 + (lane & 7) * 128);
  pf_line(pfs, (const char*)wob + (size_t)(idx * 32 + lw * 8 + (lane >> 3)) * 2048 + (x & 1) * 1024 + (lane & 7) * 128);
  const int br = idx >> 4, rb3 = 4 * (x >> 1) + (idx & 3), cb = 4 * (x & 1) + ((idx >> 2) & 3);
  const rsrc_t sw = mk_rsrc(br ? wob : woa, 1024u * 2048u);
  unsigned wv[4];
#pragma unroll
  for (int j = 0; j < 4; ++j) {
    const int k = 4 * (4 * lw + j) + (lane >> 4), c = (lane & 15) ^ (((lane >> 4) & 3) << 2);
    wv[j] = k * 2048 + (cb * 128 + c * 8) * 2 - j * 1024;
  }
  const unsigned osrc = (br ? WS_OB : WS_OA) + (rb3 * 4 + lw) * 65536;
  auto wst = [&](int t) {
    const unsigned sow = t * 64 * 2048;
    set_m0(lds0 + p3_wslot(t) + lw * 4096);
    dma16<0>(wv[0], sw, sow); dma16<1024>(wv[1], sw, sow); dma16<2048>(wv[2], sw, sow); dma16<3072>(wv[3], sw, sow);
  };
  auto ost = [&](int t) { dma4k(lds0 + P3_O + (t & 3) * 16384 + lw * 4096, vl, sws, osrc + t * 4096); };
  asm volatile("s_barrier" ::: "memory");
  wst(0); wst(1); wst(2); wst(3);
  WAIT_BAR(18);
  wst(4); wst(5);
  asm volatile("s_barrier" ::: "memory");
  asm volatile("s_barrier" ::: "memory");
  asm volatile("s_barrier" ::: "memory");
  if (lead) post(ws, F_OB + x * 32 + idx, tag);
  STAMP(512, 13);
  if (lw == 0) poll(ws, br ? F_OB + (rb3 >> 1) * 32 + (rb3 & 1) * 16 + (lane & 15) : F_OA + (rb3 >> 1) * 32 + (lane & 31), tag);
  asm volatile("s_barrier" ::: "memory");
  STAMP(512, 14);
  ost(0); ost(1); ost(2); ost(3);
  WAIT_BAR(12);
  PIN(pfs);
  WAIT_BAR(8);  ost(4); wst(6);
  WAIT_BAR(12); ost(5); wst(7);
  WAIT_BAR(16); ost(6); wst(8);
  WAIT_BAR(20); ost(7); wst(9);
  WAIT_BAR(20); ost(8); wst(10);
  WAIT_BAR(20); ost(9); wst(11);
  WAIT_BAR(20); ost(10); wst(12);
  WAIT_BAR(20); ost(11); wst(13);
  WAIT_BAR(20); ost(12); wst(14);
  WAIT_BAR(20); ost(13); wst(15);
  WAIT_BAR(20); ost(14);
  WAIT_BAR(16); ost(15);
  WAIT_BAR(12);
  WAIT_BAR(4);
  WAIT_BAR(0);
}

__global__ __launch_bounds__(768, 3) void fused_fwd(const unsigned short* __restrict__ xa, const unsigned short* __restrict__ wqkv,
                                                    const unsigned short* __restrict__ wphi, const unsigned short* __restrict__ woa,
                                                    const unsigned short* __restrict__ xb, const unsigned short* __restrict__ wq,
                                                    const unsigned short* __restrict__ wk, const unsigned short* __restrict__ wv,
                                                    const unsigned short* __restrict__ wob, char* __restrict__ ws, unsigned short* __restrict__ out) {
  __shared__ __attribute__((aligned(1024))) char smem[LDS_BYTES];
  const unsigned lds0 = (unsigned)(uintptr_t)(AS3 char*)smem;
  const int tid = threadIdx.x, lane = tid & 63, w = __builtin_amdgcn_readfirstlane(tid >> 6);
  const int b = blockIdx.x, x = b & 7, idx = b >> 3;
  const int rb = idx < 24 ? 4 * (x & 1) + (idx & 3) : 2 * (x >> 1) + (idx & 1);
  const int ct = idx < 16 ? 4 * (x >> 1) + (idx >> 2) : idx < 24 ? 2 * (x >> 1) + ((idx >> 2) & 1) : 4 * (x & 1) + ((idx >> 1) & 3);
  const int type = idx < 16 ? 0 : idx < 24 ? 1 : 2 + (ct & 1);
  const rsrc_t sws = mk_rsrc(ws, WS_END);
  STAMP(0, 0);
  if (w >= 8) {
    helper_program(lds0, type >= 2 ? xb : xa, type >= 2 ? wq : wqkv, type == 3 ? wv : type == 2 ? wk : wphi, woa, wob, ws, sws, type, rb, ct, x, idx, w - 8, lane);
    return;
  }
  if (type == 0)      p1_compute<0>(lds0, ws, sws, rb, ct, w, lane);
  else if (type == 1) p1_compute<1>(lds0, ws, sws, rb, ct, w, lane);
  else if (type == 2) p1_compute<2>(lds0, ws, sws, rb, ct, w, lane);
  else                p1_compute<3>(lds0, ws, sws, rb, ct, w, lane);

  const int kh = w >> 2, r32 = lane & 31, h = lane >> 5;
  {
    const bf16x8 sel = row_sum_sel(lane);
    LDS_BAR();
    STAMP(0, 4);
    const unsigned qa = lds0 + P2_Q + (2 * (w & 3) + kh) * 2048 + lane * 16;
    bf16x8 q[2][2];
    q[0][0] = lds_r128(qa);        q[0][1] = lds_r128(qa + 1024);
    q[1][0] = lds_r128(qa ^ 2048); q[1][1] = lds_r128((qa ^ 2048) + 1024);
    f32x16 O[2]; f32x4 L[2];
    unsigned tv = 0;
    attn_pass_fast(lds0, q, sel, kh, lane, O, L, tv);
    TSOUT();
    STAMP(0, 5);
    const unsigned xs = lds0 + P2_XO + w * 4096 + lane * 16, xr = lds0 + P2_XO + (w ^ 4) * 4096 + lane * 16;
#pragma unroll
    for (int g = 0; g < 4; ++g) *(AS3 f32x4*)(xs + g * 1024) = f32x4{O[1][4 * g], O[1][4 * g + 1], O[1][4 * g + 2], O[1][4 * g + 3]};
    *(AS3 float*)(lds0 + P2_XL + w * 256 + lane * 4) = L[1][0];
    LDS_BAR();
#pragma unroll
    for (int g = 0; g < 4; ++g) {
      const f32x4 o = *(AS3 f32x4*)(xr + g * 1024);
      O[0][4 * g] += o[0]; O[0][4 * g + 1] += o[1]; O[0][4 * g + 2] += o[2]; O[0][4 * g + 3] += o[3];
    }
    L[0][0] += *(AS3 float*)(lds0 + P2_XL + (w ^ 4) * 256 + lane * 4);
    bool bad = !(L[0][0] < 1.8446744e19f);
#pragma unroll
    for (int i = 0; i < 16; ++i) bad |= !(fabsf(O[0][i]) < 3.0e38f);
    if (__any(bad)) attn_pass_safe(sws, q[0][0], q[0][1], sel, lane, O[0], L[0]);
    const float r = __builtin_amdgcn_rcpf(xhalf_sum(L[0][0]));
    O[0] *= r;
    const unsigned p = WS_OB + ((8 * x + (idx >> 2)) * 32 + 8 * (idx & 3) + 2 * (w & 3) + kh) * 2048 + lane * 16;
    st16(sws, p, pack8<0>(O[0])); st16(sws, p + 1024, pack8<1>(O[0]));
    WAIT_BAR(0);
    STAMP(0, 6);
  }
  const int br = idx >> 4, rb3 = 4 * (x >> 1) + (idx & 3), cb = 4 * (x & 1) + ((idx >> 2) & 3);
  const int wm = w & 1, wn = (w >> 1) & 1;
  const int q4 = (lane & 15) >> 2, p4 = lane & 3, chalf = (lane >> 4) & 1;
  const unsigned oa = wm * 8192 + kh * (4096 + 2048) + lane * 16;
  unsigned wa[2];
#pragma unroll
  for (int ns = 0; ns < 2; ++ns)
    wa[ns] = (kh * 32 + 4 * h + q4) * 256 + ((((wn * 2 + ns) ^ q4) << 2) | (chalf * 2 + (p4 >> 1))) * 16 + (p4 & 1) * 8;
  f32x16 acc[2][2] = {};
  bf16x8 xf[2][2], wfr[2][2];
  auto rdx = [&](int set, unsigned buf, int ks) {
#pragma unroll
    for (int ms = 0; ms < 2; ++ms) xf[set][ms] = lds_r128(buf + (oa ^ (ms * 4096)) + ks * 1024);
  };
  auto rdw = [&](int set, unsigned buf, int ks, int ns) { wfr[set][ns] = lds_tr2(buf + wa[ns] + ks * 4096, buf + wa[ns] + ks * 4096 + 2048); };
  auto mm1 = [&](int set, int ms, int ns) { acc[ms][ns] = MFMA32(wfr[set][ns], xf[set][ms], acc[ms][ns]); };
  auto sub = [&](int set, bool rd, unsigned ob, unsigned wb, int ks) {
    SBAR(); mm1(set, 0, 0); SBAR(); if (rd) rdx(set ^ 1, ob, ks);
    SBAR(); mm1(set, 1, 0); SBAR(); if (rd) rdw(set ^ 1, wb, ks, 0);
    SBAR(); mm1(set, 0, 1); SBAR(); if (rd) rdw(set ^ 1, wb, ks, 1);
    SBAR(); mm1(set, 1, 1); SBAR();
  };
  asm volatile("s_barrier" ::: "memory");
  LDS_BAR();
  STAMP(0, 7);
  unsigned ob = lds0 + P3_O, wb = lds0 + p3_wslot(0);
  rdx(0, ob, 0); rdw(0, wb, 0, 0); rdw(0, wb, 0, 1);
  LGKM0();
#pragma unroll 1
  for (int t = 0; t < 15; ++t) {
    sub(0, true, ob, wb, 1);
    ob = (ob == lds0 + P3_O + 3 * 16384) ? lds0 + P3_O : ob + 16384;
    wb = (wb == lds0 + 147456) ? lds0 : (wb == lds0 + 16384) ? lds0 + 98304 : wb + 16384;
    LDS_BAR();
    sub(1, true, ob, wb, 0);
  }
  sub(0, true, ob, wb, 1);
  sub(1, false, ob, wb, 0);
  STAMP(0, 8);
  const unsigned xs = lds0 + w * 8192 + lane * 16, xr = lds0 + (w ^ 4) * 8192 + lane * 16;
#pragma unroll
  for (int ns = 0; ns < 2; ++ns)
#pragma unroll
    for (int g = 0; g < 4; ++g) {
      const f32x16& a = acc[1][ns];
      *(AS3 f32x4*)(xs + ns * 4096 + g * 1024) = f32x4{a[4 * g], a[4 * g + 1], a[4 * g + 2], a[4 * g + 3]};
    }
  LDS_BAR();
  const unsigned img = lds0 + 98304 + w * 4096;
#pragma unroll
  for (int ns = 0; ns < 2; ++ns)
#pragma unroll
    for (int g = 0; g < 4; ++g) {
      const f32x16& a = acc[0][ns];
      const f32x4 o = *(AS3 f32x4*)(xr + ns * 4096 + g * 1024);
      *(AS3 u32x2*)(img + r32 * 128 + (((ns * 4 + g) ^ (r32 & 7)) << 4) + h * 8) =
          u32x2{pk2(a[4 * g] + o[0], a[4 * g + 1] + o[1]), pk2(a[4 * g + 2] + o[2], a[4 * g + 3] + o[3])};
    }
  asm volatile("" ::: "memory");
  const int tok0 = rb3 * 128 + wm * 64 + kh * 32;
  unsigned short* op = out + (size_t)tok0 * 2048 + br * 1024 + cb * 128 + wn * 64;
#pragma unroll
  for (int i = 0; i < 4; ++i) {
    const int row = 8 * i + (lane >> 3), ch = lane & 7;
    const u32x4 v = *(AS3 u32x4*)(img + row * 128 + ((ch ^ (row & 7)) << 4));
    __builtin_nontemporal_store(v, (u32x4*)(op + (size_t)row * 2048 + ch * 8));
  }
  STAMP(0, 9);
}

extern "C" void kernel_launch(void* const* d_in, const int* in_sizes, int n_in, void* d_out, int out_size, void* d_ws, size_t ws_size,
                              hipStream_t stream) {
  (void)in_sizes; (void)n_in; (void)out_size; (void)ws_size;
  typedef const unsigned short* cp;
  fused_fwd<<<256, 768, 0, stream>>>((cp)d_in[0], (cp)d_in[1], (cp)d_in[2], (cp)d_in[3], (cp)d_in[4], (cp)d_in[5], (cp)d_in[6], (cp)d_in[7],
                                     (cp)d_in[8], (char*)d_ws, (unsigned short*)d_out);
}
```

```cpp
#define STAMPS
#include <hip/hip_runtime.h>
#include <hip/hip_bf16.h>
#include <stdint.h>
#pragma clang diagnostic ignored "-Wint-to-pointer-cast"

typedef __bf16   bf16x8 __attribute__((ext_vector_type(8)));
typedef __bf16   bf16x2 __attribute__((ext_vector_type(2)));
typedef short    s16x4  __attribute__((ext_vector_type(4)));
typedef short    s16x8  __attribute__((ext_vector_type(8)));
typedef float    f32x16 __attribute__((ext_vector_type(16)));
typedef float    f32x4  __attribute__((ext_vector_type(4)));
typedef float    f32x2  __attribute__((ext_vector_type(2)));
typedef unsigned u32x4  __attribute__((ext_vector_type(4)));
typedef unsigned u32x2  __attribute__((ext_vector_type(2)));
typedef unsigned long long u64;
typedef __amdgpu_buffer_rsrc_t rsrc_t;

#define AS1 __attribute__((address_space(1)))
#define AS3 __attribute__((address_space(3)))
#define DI __device__ __forceinline__
#define MFMA32(a, b, c) __builtin_amdgcn_mfma_f32_32x32x16_bf16((a), (b), (c), 0, 0, 0)
#define MFMA16(a, b, c) __builtin_amdgcn_mfma_f32_16x16x32_bf16((a), (b), (c), 0, 0, 0)
#define SBAR() __builtin_amdgcn_sched_barrier(0)
#define WAIT_BAR(N) asm volatile("s_waitcnt vmcnt(" #N ") lgkmcnt(0)\n\ts_barrier" ::: "memory")
#define LGKM0() __builtin_amdgcn_s_waitcnt(0xC07F)
#define LDS_BAR() do { asm volatile("" ::: "memory"); LGKM0(); asm volatile("s_barrier" ::: "memory"); } while (0)

#define STAMP(th, i) do { if (threadIdx.x == (th)) { u64* sp_ = (u64*)(ws + (64u << 20)) + (size_t)blockIdx.x * 32 + 2 * (i); \
    sp_[0] = __builtin_amdgcn_s_memtime(); sp_[1] = __builtin_amdgcn_s_memrealtime(); } } while (0)
#define TS(i) do {} while (0)
#define TSOUT() do {} while (0)
#define KROT (2 * x)
#define MAPV 1
constexpr int D = 1024;
constexpr float LOG2E = 1.4426950408889634f;
constexpr float QSCALE = 0.17677669529663687f * LOG2E;

constexpr unsigned WS_QA  = 0;
constexpr unsigned WS_QB  = WS_QA + (8u << 20);
constexpr unsigned WS_OA  = WS_QB + (4u << 20);
constexpr unsigned WS_OB  = WS_OA + (4u << 20);
constexpr unsigned WS_KV  = WS_OB + (4u << 20);
constexpr unsigned WS_KVP = WS_KV + (256u << 10);
constexpr unsigned WS_ZP  = WS_KVP + (2u << 20);
constexpr unsigned WS_FLG = WS_ZP + (64u << 10);
constexpr unsigned WS_END = WS_FLG + (16u << 10);
constexpr int F_K = 0, F_Q = 128, F_OA = 256, F_OB = 512, F_KV = 768, F_EPOCH = 1024;

DI unsigned pk2(float lo, float hi) {
  f32x2 v = {lo, hi};
  return __builtin_bit_cast(unsigned, __builtin_convertvector(v, bf16x2));
}
template <int SS> DI bf16x8 pack8(const f32x16& x) {
  u32x4 p = {pk2(x[8 * SS + 0], x[8 * SS + 1]), pk2(x[8 * SS + 2], x[8 * SS + 3]), pk2(x[8 * SS + 4], x[8 * SS + 5]), pk2(x[8 * SS + 6], x[8 * SS + 7])};
  return __builtin_bit_cast(bf16x8, p);
}
DI bf16x8 lds_r128(unsigned a) { return *(const AS3 bf16x8*)a; }
DI bf16x8 lds_tr2(unsigned a0, unsigned a1) {
  s16x4 lo = __builtin_amdgcn_ds_read_tr16_b64_v4i16((AS3 s16x4*)a0);
  s16x4 hi = __builtin_amdgcn_ds_read_tr16_b64_v4i16((AS3 s16x4*)a1);
  s16x8 r = __builtin_shufflevector(lo, hi, 0, 1, 2, 3, 4, 5, 6, 7);
  return __builtin_bit_cast(bf16x8, r);
}
DI rsrc_t mk_rsrc(const void* p, unsigned bytes) { return __builtin_amdgcn_make_buffer_rsrc((void*)p, (short)0, (int)bytes, 0x00020000); }
DI void set_m0(unsigned v) { asm volatile("s_mov_b32 m0, %0\n\ts_nop 0" ::"s"(v) : "memory"); }
template <int IMM> DI void dma16(unsigned voff, rsrc_t srd, unsigned soff) {
  asm volatile("buffer_load_dwordx4 %0, %1, %2 offen offset:%3 lds" ::"v"(voff), "s"(srd), "s"(soff), "i"(IMM) : "memory");
}
template <int IMM> DI void dma16d(unsigned voff, rsrc_t srd, unsigned soff) {
  asm volatile("buffer_load_dwordx4 %0, %1, %2 offen offset:%3 sc1 lds" ::"v"(voff), "s"(srd), "s"(soff), "i"(IMM) : "memory");
}
DI void dma4k(unsigned lds, unsigned voff, rsrc_t srd, unsigned soff) {
  set_m0(lds); dma16d<0>(voff, srd, soff); dma16d<1024>(voff, srd, soff); dma16d<2048>(voff, srd, soff); dma16d<3072>(voff, srd, soff);
}
template <class T> DI void st16(rsrc_t r, unsigned voff, T v) { __builtin_amdgcn_raw_buffer_store_b128(__builtin_bit_cast(u32x4, v), r, (int)voff, 0, 16); }
DI void st8(rsrc_t r, unsigned voff, u32x2 v) { __builtin_amdgcn_raw_buffer_store_b64(v, r, (int)voff, 0, 16); }
DI void st4(rsrc_t r, unsigned voff, float v) { __builtin_amdgcn_raw_buffer_store_b32(__float_as_uint(v), r, (int)voff, 0, 16); }
DI u32x4 ld16(rsrc_t r, unsigned voff) { return __builtin_amdgcn_raw_buffer_load_b128(r, (int)voff, 0, 16); }
DI float ld4(rsrc_t r, unsigned voff) { return __uint_as_float(__builtin_amdgcn_raw_buffer_load_b32(r, (int)voff, 0, 16)); }
DI void post(char* ws, int f, u64 tag) { __hip_atomic_store((AS1 u64*)(ws + WS_FLG) + f, tag, __ATOMIC_RELAXED, __HIP_MEMORY_SCOPE_AGENT); }
DI void poll(char* ws, int f, u64 tag) {
#pragma unroll 1
  for (int it = 0; it < (1 << 17); ++it) {
    const u64 v = __hip_atomic_load((AS1 u64*)(ws + WS_FLG) + f, __ATOMIC_RELAXED, __HIP_MEMORY_SCOPE_AGENT);
    if (__all(v == tag)) break;
    __builtin_amdgcn_s_sleep(2);
  }
  asm volatile("" ::: "memory");
}
DI void pf_line(unsigned& sink, const void* p) { asm volatile("global_load_dword %0, %1, off" : "+v"(sink) : "v"(p) : "memory"); }
#define PIN(x) asm volatile("" : "+v"(x))
DI float xhalf_max(float v) {
  auto r = __builtin_amdgcn_permlane32_swap(__float_as_uint(v), __float_as_uint(v), false, false);
  return fmaxf(__uint_as_float(r[0]), __uint_as_float(r[1]));
}
DI float xhalf_min(float v) {
  auto r = __builtin_amdgcn_permlane32_swap(__float_as_uint(v), __float_as_uint(v), false, false);
  return fminf(__uint_as_float(r[0]), __uint_as_float(r[1]));
}
DI float xhalf_sum(float v) {
  auto r = __builtin_amdgcn_permlane32_swap(__float_as_uint(v), __float_as_uint(v), false, false);
  return __uint_as_float(r[0]) + __uint_as_float(r[1]);
}
DI float max16(const f32x16& x) {
  float a = fmaxf(fmaxf(x[0], x[1]), x[2]), b = fmaxf(fmaxf(x[3], x[4]), x[5]);
  a = fmaxf(fmaxf(a, x[6]), x[7]);   b = fmaxf(fmaxf(b, x[8]), x[9]);
  a = fmaxf(fmaxf(a, x[10]), x[11]); b = fmaxf(fmaxf(b, x[12]), x[13]);
  a = fmaxf(fmaxf(a, x[14]), x[15]);
  return fmaxf(a, b);
}
DI float min16(const f32x16& x) {
  float a = fminf(fminf(x[0], x[1]), x[2]), b = fminf(fminf(x[3], x[4]), x[5]);
  a = fminf(fminf(a, x[6]), x[7]);   b = fminf(fminf(b, x[8]), x[9]);
  a = fminf(fminf(a, x[10]), x[11]); b = fminf(fminf(b, x[12]), x[13]);
  a = fminf(fminf(a, x[14]), x[15]);
  return fminf(a, b);
}
template <bool NORM> DI void feat_map(const f32x16& p, f32x16& ep, f32x16& em) {
  const float mx = xhalf_max(max16(p)), mn = xhalf_min(min16(p));
  const f32x2 a = {-mx * LOG2E, -mx * LOG2E}, b = {mn * LOG2E, mn * LOG2E}, c = {LOG2E, LOG2E};
  f32x2 sp = {0.f, 0.f}, sm = {0.f, 0.f};
#pragma unroll
  for (int i = 0; i < 16; i += 2) {
    const f32x2 x = {p[i], p[i + 1]};
    const f32x2 u = __builtin_elementwise_fma(x, c, a), v = __builtin_elementwise_fma(x, -c, b);
    const f32x2 eu = {__builtin_amdgcn_exp2f(u[0]), __builtin_amdgcn_exp2f(u[1])}, ev = {__builtin_amdgcn_exp2f(v[0]), __builtin_amdgcn_exp2f(v[1])};
    ep[i] = eu[0]; ep[i + 1] = eu[1]; em[i] = ev[0]; em[i + 1] = ev[1];
    if (i == 0) { sp = eu; sm = ev; } else { sp += eu; sm += ev; }
  }
  if (NORM) {
    const float rp = __builtin_amdgcn_rcpf(xhalf_sum(sp[0] + sp[1])), rm = __builtin_amdgcn_rcpf(xhalf_sum(sm[0] + sm[1]));
    ep *= rp; em *= rm;
  }
}
DI bf16x8 row_sum_sel(int lane) {
  const unsigned u = ((lane & 11) == 0 && (((lane >> 2) ^ (lane >> 4)) & 1) == 0) ? 0x3f803f80u : 0u;
  return __builtin_bit_cast(bf16x8, u32x4{u, u, u, u});
}

constexpr int LDS_BYTES = 163840;
constexpr int P1_WOFF = 32768, P1_EOFF = 49152, P1_STAGE = 53248, P1_ZRED = 3 * P1_STAGE, P1_CNT = P1_ZRED + 2048, P1_GO = P1_CNT + 16, P1_PHI = 65536;
constexpr int P2_Q = 131072, P2_KVF = 147456, P2_ZB = 151552;
constexpr int P2_XO = 32768, P2_XL = 65536;
constexpr int P3_O = 32768;
DI constexpr int p3_wslot(int t) { return (98304 + (t % 6) * 16384) % 163840; }

template <int TYPE>
DI void p1_compute(unsigned lds0, char* __restrict__ ws, rsrc_t sws, int rb, int ct, int w, int lane) {
  const int tid = threadIdx.x;
  const int wm = w & 3, wn = w >> 2, r32 = lane & 31, h = lane >> 5;
  if (tid < 8) *(AS3 unsigned*)(lds0 + P1_CNT + tid * 4) = 0u;

  const int xrow = wm * 64 + r32;
  const unsigned xa = xrow * 128 + ((h ^ ((xrow >> 1) & 7)) << 4);
  const int q4 = (lane & 15) >> 2, p4 = lane & 3, chalf = (lane >> 4) & 1;
  unsigned wa[2];
#pragma unroll
  for (int ns = 0; ns < 2; ++ns)
    wa[ns] = P1_WOFF + (8 * h + q4) * 256 + ((((wn * 2 + ns) ^ q4) << 2) | (chalf * 2 + (p4 >> 1))) * 16 + (p4 & 1) * 8;
  const int erow = 64 * (ct >> 1) + 16 * wm + (lane & 15);
  const unsigned exa = erow * 128 + (((lane >> 4) ^ ((erow >> 1) & 7)) << 4);
  const unsigned ewa = P1_EOFF + (8 * (lane >> 4) + q4) * 64 + wn * 32 + p4 * 8;

  f32x16 acc[2][2] = {};
  f32x4 eacc = {};
  bf16x8 xf[2][2], wfr[2][2], ex[2], ew[2];
  auto rdx = [&](int set, unsigned buf, int ks) {
#pragma unroll
    for (int ms = 0; ms < 2; ++ms) xf[set][ms] = lds_r128(buf + ((xa ^ (ks * 32)) + ms * 4096));
  };
  auto rdw = [&](int set, unsigned buf, int ks, int ns) { wfr[set][ns] = lds_tr2(buf + wa[ns] + ks * 4096, buf + wa[ns] + ks * 4096 + 1024); };
  auto mm1 = [&](int set, int ms, int ns) {
    if (TYPE == 0 && ns == 1) acc[ms][1] = MFMA32(xf[set][ms], wfr[set][1], acc[ms][1]);
    else                      acc[ms][ns] = MFMA32(wfr[set][ns], xf[set][ms], acc[ms][ns]);
  };
  auto erd = [&](unsigned buf, int kk) {
    if (TYPE >= 2) { ex[kk] = lds_r128(buf + (exa ^ (kk * 64))); ew[kk] = lds_tr2(buf + ewa + kk * 2048, buf + ewa + kk * 2048 + 256); }
  };
  auto emm = [&](int kk) {
    if (TYPE == 2) eacc = MFMA16(ew[kk], ex[kk], eacc);
    if (TYPE == 3) eacc = MFMA16(ex[kk], ew[kk], eacc);
  };
  auto sub = [&](int set, unsigned buf, int ks) {
    SBAR(); mm1(set, 0, 0); SBAR(); rdx(set ^ 1, buf, ks);
    SBAR(); mm1(set, 1, 0); SBAR(); rdw(set ^ 1, buf, ks, 0);
    SBAR(); mm1(set, 0, 1); SBAR(); rdw(set ^ 1, buf, ks, 1);
    SBAR(); mm1(set, 1, 1); SBAR();
  };
  LDS_BAR();
  STAMP(0, 1);
  unsigned cur = lds0, nxt = lds0 + P1_STAGE;
  rdx(0, cur, 0); rdw(0, cur, 0, 0); rdw(0, cur, 0, 1);
  LGKM0();
#pragma unroll 1
  for (int t = 0; t < 16; ++t) {
    sub(0, cur, 1); erd(cur, 0);
    sub(1, cur, 2); erd(cur, 1); SBAR(); emm(0);
    sub(0, cur, 3); emm(1); SBAR();
    LDS_BAR();
    sub(1, nxt, 0);
    cur = nxt; nxt = (nxt == lds0 + 2 * P1_STAGE) ? lds0 : nxt + P1_STAGE;
  }
  STAMP(0, 2);
  bf16x8 wf[2][2];
  if constexpr (TYPE <= 1) {
    const unsigned a = lds0 + P1_PHI + (TYPE == 0 ? wn : 2 * wn) * 2048 + (4 * h + q4) * 64 + chalf * 32 + p4 * 8;
#pragma unroll
    for (int n = 0; n <= TYPE; ++n)
#pragma unroll
      for (int s = 0; s < 2; ++s) wf[n][s] = lds_tr2(a + n * 2048 + s * 1024, a + n * 2048 + s * 1024 + 512);
  }

  if (TYPE >= 2) {
#pragma unroll
    for (int ms = 0; ms < 2; ++ms)
#pragma unroll
      for (int ns = 0; ns < 2; ++ns) {
        const f32x16 v = acc[ms][ns] * QSCALE;
        const unsigned p = WS_QB + ((rb * 8 + wm * 2 + ms) * 32 + ct * 4 + wn * 2 + ns) * 2048 + lane * 16;
        st16(sws, p, pack8<0>(v)); st16(sws, p + 1024, pack8<1>(v));
      }
    const int g = lane >> 4, tok0 = rb * 256 + 64 * (ct >> 1) + 16 * wm;
    unsigned p = WS_KV + (tok0 >> 5) * 4096;
    if (TYPE == 3) p += 2048 + (wm & 1) * 1024 + (16 * wn + (lane & 15) + 32 * (g & 1)) * 16 + (g >> 1) * 8;
    else           p += wn * 1024 + (16 * (wm & 1) + (lane & 15) + 32 * (g & 1)) * 16 + (g >> 1) * 8;
    st8(sws, p, u32x2{pk2(eacc[0], eacc[1]), pk2(eacc[2], eacc[3])});
    WAIT_BAR(0);
  } else if (TYPE == 1) {
    f32x16 pq[2][2];
#pragma unroll
    for (int ms = 0; ms < 2; ++ms)
#pragma unroll
      for (int ns = 0; ns < 2; ++ns) {
        pq[ms][ns] = MFMA32(wf[ns][0], pack8<0>(acc[ms][ns]), f32x16{});
        pq[ms][ns] = MFMA32(wf[ns][1], pack8<1>(acc[ms][ns]), pq[ms][ns]);
      }
#pragma unroll
    for (int ms = 0; ms < 2; ++ms)
#pragma unroll
      for (int ns = 0; ns < 2; ++ns) {
        f32x16 ep, em;
        feat_map<false>(pq[ms][ns], ep, em);
        const unsigned p = WS_QA + ((rb * 8 + wm * 2 + ms) * 32 + ct * 4 + wn * 2 + ns) * 4096 + lane * 16;
        st16(sws, p, pack8<0>(ep)); st16(sws, p + 1024, pack8<1>(ep)); st16(sws, p + 2048, pack8<0>(em)); st16(sws, p + 3072, pack8<1>(em));
      }
  } else {
    const unsigned img = lds0 + w * 8192;
    const bf16x8 sel = row_sum_sel(lane);
    f32x16 pk[2], kvp = {}, kvm = {};
    f32x4 zp = {}, zm = {};
    bf16x8 vf[2][2], ap[2], am[2];
#pragma unroll
    for (int ms = 0; ms < 2; ++ms) {
      pk[ms] = MFMA32(wf[0][0], pack8<0>(acc[ms][0]), f32x16{});
      pk[ms] = MFMA32(wf[0][1], pack8<1>(acc[ms][0]), pk[ms]);
    }
#pragma unroll
    for (int ms = 0; ms < 2; ++ms) { vf[ms][0] = pack8<0>(acc[ms][1]); vf[ms][1] = pack8<1>(acc[ms][1]); }
    auto kvmm = [&](int ms) {
#pragma unroll
      for (int s = 0; s < 2; ++s) {
        kvp = MFMA32(ap[s], vf[ms][s], kvp); kvm = MFMA32(am[s], vf[ms][s], kvm);
        zp = MFMA16(sel, ap[s], zp);         zm = MFMA16(sel, am[s], zm);
      }
    };
#pragma unroll
    for (int ms = 0; ms < 2; ++ms) {
      f32x16 ep, em;
      feat_map<true>(pk[ms], ep, em);
      if (ms == 1) kvmm(0);
#pragma unroll
      for (int g = 0; g < 4; ++g) {
        const unsigned a = img + r32 * 64 + (((2 * g + h) ^ ((r32 >> 1) & 7)) << 3);
        *(AS3 u32x2*)a = u32x2{pk2(ep[4 * g], ep[4 * g + 1]), pk2(ep[4 * g + 2], ep[4 * g + 3])};
        *(AS3 u32x2*)(a + 2048) = u32x2{pk2(em[4 * g], em[4 * g + 1]), pk2(em[4 * g + 2], em[4 * g + 3])};
      }
      asm volatile("" ::: "memory");
#pragma unroll
      for (int s = 0; s < 2; ++s) {
        const int t0 = 16 * s + 4 * h + q4, t1 = t0 + 8, u = 4 * chalf + p4;
        const unsigned a0 = img + t0 * 64 + ((u ^ ((t0 >> 1) & 7)) << 3), a1 = img + t1 * 64 + ((u ^ ((t1 >> 1) & 7)) << 3);
        ap[s] = lds_tr2(a0, a1); am[s] = lds_tr2(a0 + 2048, a1 + 2048);
      }
      asm volatile("" ::: "memory");
    }
    kvmm(1);
#pragma unroll
    for (int g = 0; g < 4; ++g) {
      *(AS3 f32x4*)(img + g * 1024 + lane * 16) = f32x4{kvp[4 * g], kvp[4 * g + 1], kvp[4 * g + 2], kvp[4 * g + 3]};
      *(AS3 f32x4*)(img + 4096 + g * 1024 + lane * 16) = f32x4{kvm[4 * g], kvm[4 * g + 1], kvm[4 * g + 2], kvm[4 * g + 3]};
    }
    if (lane < 32) {
      const unsigned a = lds0 + P1_ZRED + w * 256 + ((lane >> 2) & 1) * 64 + (lane >> 3) * 16 + (lane & 3) * 4;
      *(AS3 float*)a = zp[0]; *(AS3 float*)(a + 128) = zm[0];
    }
    asm volatile("" ::: "memory"); LGKM0();
    if (lane == 0) __hip_atomic_fetch_add((AS3 unsigned*)(lds0 + P1_CNT), 1u, __ATOMIC_RELAXED, __HIP_MEMORY_SCOPE_WORKGROUP);
    while (__hip_atomic_load((AS3 unsigned*)(lds0 + P1_CNT), __ATOMIC_RELAXED, __HIP_MEMORY_SCOPE_WORKGROUP) < 8u) __builtin_amdgcn_s_sleep(1);
    asm volatile("" ::: "memory");
#pragma unroll
    for (int hb = 0; hb < 2; ++hb) {
      f32x4 s = *(AS3 f32x4*)(lds0 + (hb * 4) * 8192 + tid * 16);
#pragma unroll
      for (int m = 1; m < 4; ++m) s += *(AS3 f32x4*)(lds0 + (hb * 4 + m) * 8192 + tid * 16);
      st16(sws, WS_KVP + (((2 * ct + hb) * 8 + rb) * 512 + tid) * 16, s);
    }
    if (tid < 128) {
      const int hb = tid >> 6;
      float s = 0.f;
#pragma unroll
      for (int m = 0; m < 4; ++m) s += *(AS3 float*)(lds0 + P1_ZRED + (hb * 4 + m) * 256 + lane * 4);
      st4(sws, WS_ZP + (((2 * ct + hb) * 8 + rb) * 64 + lane) * 4, s);
    }
  }
  STAMP(0, 3);
}

DI void attn_pass_fast(unsigned lds0, const bf16x8 (&q)[2][2], const bf16x8 sel, int kh, int lane, f32x16 (&O)[2], f32x4 (&L)[2], unsigned& tv) {
  TS(0);
  typedef const AS3 char* lp;
  const lp l0 = (lp)(lds0 + lane * 16), la = l0 + kh * 16384;
#define RD(p) (*(const AS3 bf16x8*)(p))
  if (kh) __builtin_amdgcn_s_setprio(1);
  bf16x8 ka = RD(l0 + 98304), kb = RD(l0 + 98304 + 1024);
  f32x16 negm[2], S0, S1;
#pragma unroll
  for (int r = 0; r < 2; ++r) {
    f32x16 z = {};
    z = MFMA32(ka, q[r][0], z); z = MFMA32(kb, q[r][1], z);
    const float m = xhalf_max(max16(z));
#pragma unroll
    for (int i = 0; i < 16; ++i) negm[r][i] = -m;
    PIN(negm[r]);
    O[r] = f32x16{}; L[r] = f32x4{};
  }
  ka = RD(la + 98304); kb = RD(la + 98304 + 1024);
  bf16x8 va = RD(la + 98304 + 2048), vb = bf16x8{};
  S0 = MFMA32(ka, q[0][0], negm[0]); S0 = MFMA32(kb, q[0][1], S0);
  u32x4 p0 = {}, p1 = {};
  LGKM0();
#define EX4(SC, i) do { SC[i] = __builtin_amdgcn_exp2f(SC[i]); SC[i + 1] = __builtin_amdgcn_exp2f(SC[i + 1]); \
    SC[i + 2] = __builtin_amdgcn_exp2f(SC[i + 2]); SC[i + 3] = __builtin_amdgcn_exp2f(SC[i + 3]); PIN(SC); } while (0)
#define CV4(P, SC, i) do { P[0] = pk2(SC[i], SC[i + 1]); P[1] = pk2(SC[i + 2], SC[i + 3]); P[2] = pk2(SC[i + 4], SC[i + 5]); P[3] = pk2(SC[i + 6], SC[i + 7]); PIN(P); } while (0)
#define BF(P) __builtin_bit_cast(bf16x8, P)
#define TILE(TA, TN) do { \
    SBAR(); S1 = MFMA32(ka, q[1][0], negm[1]);  SBAR(); ka = RD(TN);          EX4(S0, 0);  \
    SBAR(); O[1] = MFMA32(vb, BF(p1), O[1]);    SBAR(); vb = RD((TA) + 3072); EX4(S0, 4);  \
    SBAR(); L[1] = MFMA16(sel, BF(p1), L[1]);   SBAR(); CV4(p0, S0, 0); \
    SBAR(); S1 = MFMA32(kb, q[1][1], S1);       SBAR(); kb = RD((TN) + 1024); EX4(S0, 8);  \
    SBAR(); O[0] = MFMA32(va, BF(p0), O[0]);    SBAR(); EX4(S0, 12); \
    SBAR(); L[0] = MFMA16(sel, BF(p0), L[0]);   SBAR(); CV4(p1, S0, 8); \
    SBAR(); S0 = MFMA32(ka, q[0][0], negm[0]);  SBAR(); EX4(S1, 0);  \
    SBAR(); O[0] = MFMA32(vb, BF(p1), O[0]);    SBAR(); EX4(S1, 4);  \
    SBAR(); L[0] = MFMA16(sel, BF(p1), L[0]);   SBAR(); CV4(p0, S1, 0); \
    SBAR(); S0 = MFMA32(kb, q[0][1], S0);       SBAR(); EX4(S1, 8);  \
    SBAR(); O[1] = MFMA32(va, BF(p0), O[1]);    SBAR(); va = RD((TN) + 2048); EX4(S1, 12); \
    SBAR(); L[1] = MFMA16(sel, BF(p0), L[1]);   SBAR(); CV4(p1, S1, 8); } while (0)
  TS(1);
#pragma unroll 1
  for (int c = 0; c < 8; ++c) {
    const lp cb = la + ((c + 3) & 3) * 32768, nb = la + (c & 3) * 32768;
    TILE(cb, cb + 4096);
    TILE(cb + 4096, cb + 8192);
    SBAR(); if (kh) __builtin_amdgcn_s_setprio(0);
    TILE(cb + 8192, cb + 12288);
    SBAR(); TS(2 + 2 * c);
    asm volatile("s_waitcnt vmcnt(0)\n\ts_barrier" ::: "memory"); TS(3 + 2 * c);
    if (kh) __builtin_amdgcn_s_setprio(1);
    TILE(cb + 12288, nb);
  }
  SBAR();
  O[1] = MFMA32(vb, BF(p1), O[1]); L[1] = MFMA16(sel, BF(p1), L[1]);
  __builtin_amdgcn_s_setprio(0);
  TS(18);
#undef TILE
#undef EX4
#undef CV4
#undef BF
#undef RD
}

DI void attn_pass_safe(rsrc_t sws, const bf16x8 q0, const bf16x8 q1, const bf16x8 sel, int lane, f32x16& O, f32x4& L) {
  O = f32x16{}; L = f32x4{};
  float m = -INFINITY;
#pragma unroll 1
  for (int t = 0; t < 64; ++t) {
    bf16x8 a[4];
#pragma unroll
    for (int j = 0; j < 4; ++j) a[j] = __builtin_bit_cast(bf16x8, ld16(sws, WS_KV + t * 4096 + j * 1024 + lane * 16));
    f32x16 Sc = {};
    Sc = MFMA32(a[0], q0, Sc); Sc = MFMA32(a[1], q1, Sc);
    const float mn = fmaxf(m, xhalf_max(max16(Sc)));
    const float al = __builtin_amdgcn_exp2f(m - mn);
    m = mn;
#pragma unroll
    for (int r = 0; r < 16; ++r) { O[r] *= al; Sc[r] = __builtin_amdgcn_exp2f(Sc[r] - mn); }
    L *= al;
    const bf16x8 p0 = pack8<0>(Sc), p1 = pack8<1>(Sc);
    O = MFMA32(a[2], p0, O); L = MFMA16(sel, p0, L);
    O = MFMA32(a[3], p1, O); L = MFMA16(sel, p1, L);
  }
}

DI void helper_program(unsigned lds0, const unsigned short* __restrict__ X, const unsigned short* __restrict__ W, const unsigned short* __restrict__ E,
                       const unsigned short* __restrict__ woa, const unsigned short* __restrict__ wob, char* __restrict__ ws, rsrc_t sws,
                       int type, int rb, int ct, int x, int idx, int lw, int lane) {
  __builtin_amdgcn_s_setprio(3);
  const u64 e0 = __hip_atomic_load((AS1 u64*)(ws + WS_FLG) + F_EPOCH, __ATOMIC_RELAXED, __HIP_MEMORY_SCOPE_AGENT);
  const unsigned vl = lane * 16;
  {
    const bool ext = type >= 2;
    const unsigned ldw = ext ? 2048u : 6144u;
    const rsrc_t sx = mk_rsrc(X + (size_t)rb * 256 * D, 256u * 2048u);
    const rsrc_t sw = mk_rsrc(W, 1024u * ldw);
    const rsrc_t se = mk_rsrc(E, 1024u * 64u);
    unsigned xv[8], wv[4];
#pragma unroll
    for (int j = 0; j < 8; ++j) {
      const int row = 8 * (8 * lw + j) + (lane >> 3), c = (lane & 7) ^ ((row >> 1) & 7);
      xv[j] = row * 2048 + c * 16 - (j & 3) * 1024;
    }
#pragma unroll
    for (int j = 0; j < 4; ++j) {
      const int k = 4 * (4 * lw + j) + (lane >> 4), c = (lane & 15) ^ (((lane >> 4) & 3) << 2);
      const int col = (type == 0) ? 1024 + ((c >> 2) & 1) * 1024 + (2 * ct + (c >> 3)) * 32 + (c & 3) * 8 : ct * 128 + c * 8;
      wv[j] = k * ldw + col * 2 - j * 1024;
    }
    auto stage = [&](int t0, unsigned buf) {
      const int t = (t0 + KROT) & 15;
      const unsigned sox = t * 128, sow = t * 64 * ldw;
      set_m0(buf + P1_WOFF + lw * 4096);
      dma16<0>(wv[0], sw, sow); dma16<1024>(wv[1], sw, sow); dma16<2048>(wv[2], sw, sow); dma16<3072>(wv[3], sw, sow);
      set_m0(buf + lw * 8192);
      dma16<0>(xv[0], sx, sox); dma16<1024>(xv[1], sx, sox); dma16<2048>(xv[2], sx, sox); dma16<3072>(xv[3], sx, sox);
      set_m0(buf + lw * 8192 + 4096);
      dma16<0>(xv[4], sx, sox); dma16<1024>(xv[5], sx, sox); dma16<2048>(xv[6], sx, sox); dma16<3072>(xv[7], sx, sox);
      if (ext) { set_m0(buf + P1_EOFF + lw * 1024); dma16<0>(vl, se, t * 4096 + lw * 1024); }
    };
    stage(0, lds0); stage(1, lds0 + P1_STAGE);
    if (ext) WAIT_BAR(13); else WAIT_BAR(12);
    stage(2, lds0 + 2 * P1_STAGE);
    unsigned buf = lds0;
#pragma unroll 1
    for (int t = 0; t < 13; ++t) {
      if (ext) WAIT_BAR(13); else WAIT_BAR(12);
      stage(t + 3, buf);
      buf = (buf == lds0 + 2 * P1_STAGE) ? lds0 : buf + P1_STAGE;
    }
    if (ext) WAIT_BAR(13); else WAIT_BAR(12);
    if (!ext && (type == 1 || lw < 2)) {
      set_m0(lds0 + P1_PHI + lw * 2048);
      const unsigned so = ((type == 0 ? 2 * ct : 4 * ct) + lw) * 2048;
      dma16<0>(vl, se, so); dma16<1024>(vl, se, so);
    }
    WAIT_BAR(0);
    WAIT_BAR(0);
  }
  const u64 tag = ((u64)(unsigned)__builtin_amdgcn_readfirstlane((unsigned)(e0 >> 32)) << 32 | (unsigned)__builtin_amdgcn_readfirstlane((unsigned)e0))
                  * 0x5851F42D4C957F2Dull + 0x14057B7EF767814Full;
  const bool lead = (lw == 0) && (lane == 0);
  if (type >= 2) { WAIT_BAR(0); if (lead) post(ws, F_KV + rb * 16 + ct, tag); }
  auto chunk = [&](int c) {
    const unsigned l = lds0 + ((c + 3) & 3) * 32768 + lw * 8192, so = WS_KV + ((c + x) & 7) * 32768 + lw * 8192;
    dma4k(l, vl, sws, so); dma4k(l + 4096, vl, sws, so + 4096);
  };
  STAMP(512, 10);
  if (lw == 0) {
    poll(ws, F_KV + x * 16 + (lane & 7), tag);
    __hip_atomic_store((AS3 unsigned*)(lds0 + P1_GO), 1u, __ATOMIC_RELAXED, __HIP_MEMORY_SCOPE_WORKGROUP);
  } else {
    while (__hip_atomic_load((AS3 unsigned*)(lds0 + P1_GO), __ATOMIC_RELAXED, __HIP_MEMORY_SCOPE_WORKGROUP) == 0u) __builtin_amdgcn_s_sleep(1);
    asm volatile("" ::: "memory");
  }
  STAMP(512, 11);
  dma4k(lds0 + P2_Q + lw * 4096, vl, sws, WS_QB + ((8 * x + (idx >> 2)) * 32 + 8 * (idx & 3)) * 2048 + lw * 4096);
  chunk(0);
  WAIT_BAR(0);
  poll(ws, F_KV + (lane >> 3) * 16 + (lane & 7), tag);
  chunk(1); chunk(2); chunk(3);
  WAIT_BAR(16);
  if (lead && type < 2) post(ws, type == 0 ? F_K + ct * 8 + rb : F_Q + rb * 8 + ct, tag);
  if (lw == 1) *(AS3 unsigned*)(lds0 + P2_ZB + 128 + lane * 4) = lane < 32 ? 0x3f803f80u : 0u;
  WAIT_BAR(0);
  const int head = 4 * x + (idx & 3), tb = idx >> 2;
  poll(ws, lane < 8 ? F_K + (head >> 1) * 8 + lane : F_Q + tb * 8 + x, tag);
  STAMP(512, 12);
  chunk(4);
  u32x4 kvp[2][8], fq[2][4];
  float zs[8];
#pragma unroll
  for (int e = 0; e < 2; ++e)
#pragma unroll
    for (int p = 0; p < 8; ++p) kvp[e][p] = ld16(sws, WS_KVP + ((head * 8 + p) * 512 + (2 * lw + e) * 64 + lane) * 16);
#pragma unroll
  for (int p = 0; p < 8; ++p) zs[p] = ld4(sws, WS_ZP + ((head * 8 + p) * 64 + lane) * 4);
  asm volatile("s_barrier" ::: "memory");
#pragma unroll
  for (int e = 0; e < 2; ++e) {
#pragma unroll
    for (int p = 0; p < 8; ++p) PIN(kvp[e][p]);
#define F4(v) __builtin_bit_cast(f32x4, v)
    const f32x4 sm = ((F4(kvp[e][0]) + F4(kvp[e][1])) + (F4(kvp[e][2]) + F4(kvp[e][3]))) + ((F4(kvp[e][4]) + F4(kvp[e][5])) + (F4(kvp[e][6]) + F4(kvp[e][7])));
#undef F4
    *(AS3 u32x2*)(lds0 + P2_KVF + lw * 1024 + vl + e * 8) = u32x2{pk2(sm[0], sm[1]), pk2(sm[2], sm[3])};
  }
  {
#pragma unroll
    for (int p = 0; p < 8; ++p) PIN(zs[p]);
    const float z = ((zs[0] + zs[1]) + (zs[2] + zs[3])) + ((zs[4] + zs[5]) + (zs[6] + zs[7]));
    if (lw == 0) *(AS3 unsigned short*)(lds0 + P2_ZB + lane * 2) = (unsigned short)(pk2(z, z) & 0xffffu);
  }
  chunk(5);
#pragma unroll
  for (int e = 0; e < 2; ++e)
#pragma unroll
    for (int j = 0; j < 4; ++j) fq[e][j] = ld16(sws, WS_QA + ((8 * tb + 2 * lw + e) * 32 + head) * 4096 + j * 1024 + vl);
  asm volatile("s_waitcnt lgkmcnt(0)\n\ts_barrier" ::: "memory");
#pragma unroll
  for (int e = 0; e < 2; ++e)
#pragma unroll
    for (int j = 0; j < 4; ++j) PIN(fq[e][j]);
  chunk(6);
  {
    const int row = lane & 15, kg = lane >> 4;
    const bool on = (((row >> 2) ^ kg) & 1) == 0;
    const unsigned za = lds0 + P2_ZB + ((on && (row & 3) == 0) ? (kg >> 1) * 32 : (on && (row & 3) == 1) ? 128 : 256);
    const unsigned ka = lds0 + P2_KVF + vl;
#pragma unroll
    for (int e = 0; e < 2; ++e) {
#define BF(v) __builtin_bit_cast(bf16x8, v)
      f32x16 np = MFMA32(lds_r128(ka), BF(fq[e][0]), f32x16{});        f32x4 dp = MFMA16(lds_r128(za), BF(fq[e][0]), f32x4{});
      np = MFMA32(lds_r128(ka + 1024), BF(fq[e][1]), np);              dp = MFMA16(lds_r128(za + 16), BF(fq[e][1]), dp);
      f32x16 nm = MFMA32(lds_r128(ka + 2048), BF(fq[e][2]), f32x16{}); f32x4 dm = MFMA16(lds_r128(za + 64), BF(fq[e][2]), f32x4{});
      nm = MFMA32(lds_r128(ka + 3072), BF(fq[e][3]), nm);              dm = MFMA16(lds_r128(za + 80), BF(fq[e][3]), dm);
#undef BF
      const float a = __builtin_amdgcn_rcpf(dp[1]), b = __builtin_amdgcn_rcpf(dm[1]);
      const float r = __builtin_amdgcn_rcpf(dp[0] * a + dm[0] * b + 1e-6f);
      const f32x16 o = np * (a * r) + nm * (b * r);
      const unsigned p = WS_OA + ((8 * tb + 2 * lw + e) * 32 + head) * 2048 + vl;
      st16(sws, p, pack8<0>(o)); st16(sws, p + 1024, pack8<1>(o));
    }
  }
  WAIT_BAR(0);
  if (lead) post(ws, F_OA + tb * 32 + head, tag);
  chunk(7);
  if (blockIdx.x == 0 && lw == 1) {
    poll(ws, lane, tag); poll(ws, 64 + lane, tag); poll(ws, 128 + lane, tag);
    if (lane == 0) post(ws, F_EPOCH, tag);
  }
  unsigned pfs = 0;
  pf_line(pfs, (const char*)woa + (size_t)(idx * 32 + lw * 8 + (lane >> 3)) * 2048 + (x & 1) * 1024 + (lane & 7) * 128);
  pf_line(pfs, (const char*)wob + (size_t)(idx * 32 + lw * 8 + (lane >> 3)) * 2048 + (x & 1) * 1024 + (lane & 7) * 128);
  const int br = idx >> 4, rb3 = 4 * (x >> 1) + (idx & 3), cb = 4 * (x & 1) + ((idx >> 2) & 3);
  const rsrc_t sw = mk_rsrc(br ? wob : woa, 1024u * 2048u);
  unsigned wv[4];
#pragma unroll
  for (int j = 0; j < 4; ++j) {
    const int k = 4 * (4 * lw + j) + (lane >> 4), c = (lane & 15) ^ (((lane >> 4) & 3) << 2);
    wv[j] = k * 2048 + (cb * 128 + c * 8) * 2 - j * 1024;
  }
  const unsigned osrc = (br ? WS_OB : WS_OA) + (rb3 * 4 + lw) * 65536;
  auto wst = [&](int t) {
    const unsigned sow = t * 64 * 2048;
    set_m0(lds0 + p3_wslot(t) + lw * 4096);
    dma16<0>(wv[0], sw, sow); dma16<1024>(wv[1], sw, sow); dma16<2048>(wv[2], sw, sow); dma16<3072>(wv[3], sw, sow);
  };
  auto ost = [&](int t) { dma4k(lds0 + P3_O + (t & 3) * 16384 + lw * 4096, vl, sws, osrc + t * 4096); };
  asm volatile("s_barrier" ::: "memory");
  wst(0); wst(1); wst(2); wst(3);
  WAIT_BAR(18);
  wst(4); wst(5);
  asm volatile("s_barrier" ::: "memory");
  asm volatile("s_barrier" ::: "memory");
  asm volatile("s_barrier" ::: "memory");
  if (lead) post(ws, F_OB + x * 32 + idx, tag);
  STAMP(512, 13);
  if (lw == 0) poll(ws, br ? F_OB + (rb3 >> 1) * 32 + (rb3 & 1) * 16 + (lane & 15) : F_OA + (rb3 >> 1) * 32 + (lane & 31), tag);
  asm volatile("s_barrier" ::: "memory");
  STAMP(512, 14);
  ost(0); ost(1); ost(2); ost(3);
  WAIT_BAR(12);
  PIN(pfs);
  WAIT_BAR(8);  ost(4); wst(6);
  WAIT_BAR(12); ost(5); wst(7);
  WAIT_BAR(16); ost(6); wst(8);
  WAIT_BAR(20); ost(7); wst(9);
  WAIT_BAR(20); ost(8); wst(10);
  WAIT_BAR(20); ost(9); wst(11);
  WAIT_BAR(20); ost(10); wst(12);
  WAIT_BAR(20); ost(11); wst(13);
  WAIT_BAR(20); ost(12); wst(14);
  WAIT_BAR(20); ost(13); wst(15);
  WAIT_BAR(20); ost(14);
  WAIT_BAR(16); ost(15);
  WAIT_BAR(12);
  WAIT_BAR(4);
  WAIT_BAR(0);
}

__global__ __launch_bounds__(768, 3) void fused_fwd(const unsigned short* __restrict__ xa, const unsigned short* __restrict__ wqkv,
                                                    const unsigned short* __restrict__ wphi, const unsigned short* __restrict__ woa,
                                                    const unsigned short* __restrict__ xb, const unsigned short* __restrict__ wq,
                                                    const unsigned short* __restrict__ wk, const unsigned short* __restrict__ wv,
                                                    const unsigned short* __restrict__ wob, char* __restrict__ ws, unsigned short* __restrict__ out) {
  __shared__ __attribute__((aligned(1024))) char smem[LDS_BYTES];
  const unsigned lds0 = (unsigned)(uintptr_t)(AS3 char*)smem;
  const int tid = threadIdx.x, lane = tid & 63, w = __builtin_amdgcn_readfirstlane(tid >> 6);
  const int b = blockIdx.x, x = b & 7, idx = b >> 3;
  const int rb = idx < 24 ? 4 * (x & 1) + (idx & 3) : 2 * (x >> 1) + (idx & 1);
  const int ct = idx < 16 ? 4 * (x >> 1) + (idx >> 2) : idx < 24 ? 2 * (x >> 1) + ((idx >> 2) & 1) : 4 * (x & 1) + ((idx >> 1) & 3);
  const int type = idx < 16 ? 0 : idx < 24 ? 1 : 2 + (ct & 1);
  const rsrc_t sws = mk_rsrc(ws, WS_END);
  STAMP(0, 0);
  if (w >= 8) {
    helper_program(lds0, type >= 2 ? xb : xa, type >= 2 ? wq : wqkv, type == 3 ? wv : type == 2 ? wk : wphi, woa, wob, ws, sws, type, rb, ct, x, idx, w - 8, lane);
    return;
  }
  if (type == 0)      p1_compute<0>(lds0, ws, sws, rb, ct, w, lane);
  else if (type == 1) p1_compute<1>(lds0, ws, sws, rb, ct, w, lane);
  else if (type == 2) p1_compute<2>(lds0, ws, sws, rb, ct, w, lane);
  else                p1_compute<3>(lds0, ws, sws, rb, ct, w, lane);

  const int kh = w >> 2, r32 = lane & 31, h = lane >> 5;
  {
    const bf16x8 sel = row_sum_sel(lane);
    LDS_BAR();
    STAMP(0, 4);
    const unsigned qa = lds0 + P2_Q + (2 * (w & 3) + kh) * 2048 + lane * 16;
    bf16x8 q[2][2];
    q[0][0] = lds_r128(qa);        q[0][1] = lds_r128(qa + 1024);
    q[1][0] = lds_r128(qa ^ 2048); q[1][1] = lds_r128((qa ^ 2048) + 1024);
    f32x16 O[2]; f32x4 L[2];
    unsigned tv = 0;
    attn_pass_fast(lds0, q, sel, kh, lane, O, L, tv);
    TSOUT();
    STAMP(0, 5);
    const unsigned xs = lds0 + P2_XO + w * 4096 + lane * 16, xr = lds0 + P2_XO + (w ^ 4) * 4096 + lane * 16;
#pragma unroll
    for (int g = 0; g < 4; ++g) *(AS3 f32x4*)(xs + g * 1024) = f32x4{O[1][4 * g], O[1][4 * g + 1], O[1][4 * g + 2], O[1][4 * g + 3]};
    *(AS3 float*)(lds0 + P2_XL + w * 256 + lane * 4) = L[1][0];
    LDS_BAR();
#pragma unroll
    for (int g = 0; g < 4; ++g) {
      const f32x4 o = *(AS3 f32x4*)(xr + g * 1024);
      O[0][4 * g] += o[0]; O[0][4 * g + 1] += o[1]; O[0][4 * g + 2] += o[2]; O[0][4 * g + 3] += o[3];
    }
    L[0][0] += *(AS3 float*)(lds0 + P2_XL + (w ^ 4) * 256 + lane * 4);
    bool bad = !(L[0][0] < 1.8446744e19f);
#pragma unroll
    for (int i = 0; i < 16; ++i) bad |= !(fabsf(O[0][i]) < 3.0e38f);
    if (__any(bad)) attn_pass_safe(sws, q[0][0], q[0][1], sel, lane, O[0], L[0]);
    const float r = __builtin_amdgcn_rcpf(xhalf_sum(L[0][0]));
    O[0] *= r;
    const unsigned p = WS_OB + ((8 * x + (idx >> 2)) * 32 + 8 * (idx & 3) + 2 * (w & 3) + kh) * 2048 + lane * 16;
    st16(sws, p, pack8<0>(O[0])); st16(sws, p + 1024, pack8<1>(O[0]));
    WAIT_BAR(0);
    STAMP(0, 6);
  }
  const int br = idx >> 4, rb3 = 4 * (x >> 1) + (idx & 3), cb = 4 * (x & 1) + ((idx >> 2) & 3);
  const int wm = w & 1, wn = (w >> 1) & 1;
  const int q4 = (lane & 15) >> 2, p4 = lane & 3, chalf = (lane >> 4) & 1;
  const unsigned oa = wm * 8192 + kh * (4096 + 2048) + lane * 16;
  unsigned wa[2];
#pragma unroll
  for (int ns = 0; ns < 2; ++ns)
    wa[ns] = (kh * 32 + 4 * h + q4) * 256 + ((((wn * 2 + ns) ^ q4) << 2) | (chalf * 2 + (p4 >> 1))) * 16 + (p4 & 1) * 8;
  f32x16 acc[2][2] = {};
  bf16x8 xf[2][2], wfr[2][2];
  auto rdx = [&](int set, unsigned buf, int ks) {
#pragma unroll
    for (int ms = 0; ms < 2; ++ms) xf[set][ms] = lds_r128(buf + (oa ^ (ms * 4096)) + ks * 1024);
  };
  auto rdw = [&](int set, unsigned buf, int ks, int ns) { wfr[set][ns] = lds_tr2(buf + wa[ns] + ks * 4096, buf + wa[ns] + ks * 4096 + 2048); };
  auto mm1 = [&](int set, int ms, int ns) { acc[ms][ns] = MFMA32(wfr[set][ns], xf[set][ms], acc[ms][ns]); };
  auto sub = [&](int set, bool rd, unsigned ob, unsigned wb, int ks) {
    SBAR(); mm1(set, 0, 0); SBAR(); if (rd) rdx(set ^ 1, ob, ks);
    SBAR(); mm1(set, 1, 0); SBAR(); if (rd) rdw(set ^ 1, wb, ks, 0);
    SBAR(); mm1(set, 0, 1); SBAR(); if (rd) rdw(set ^ 1, wb, ks, 1);
    SBAR(); mm1(set, 1, 1); SBAR();
  };
  asm volatile("s_barrier" ::: "memory");
  LDS_BAR();
  STAMP(0, 7);
  unsigned ob = lds0 + P3_O, wb = lds0 + p3_wslot(0);
  rdx(0, ob, 0); rdw(0, wb, 0, 0); rdw(0, wb, 0, 1);
  LGKM0();
#pragma unroll 1
  for (int t = 0; t < 15; ++t) {
    sub(0, true, ob, wb, 1);
    ob = (ob == lds0 + P3_O + 3 * 16384) ? lds0 + P3_O : ob + 16384;
    wb = (wb == lds0 + 147456) ? lds0 : (wb == lds0 + 16384) ? lds0 + 98304 : wb + 16384;
    LDS_BAR();
    sub(1, true, ob, wb, 0);
  }
  sub(0, true, ob, wb, 1);
  sub(1, false, ob, wb, 0);
  STAMP(0, 8);
  const unsigned xs = lds0 + w * 8192 + lane * 16, xr = lds0 + (w ^ 4) * 8192 + lane * 16;
#pragma unroll
  for (int ns = 0; ns < 2; ++ns)
#pragma unroll
    for (int g = 0; g < 4; ++g) {
      const f32x16& a = acc[1][ns];
      *(AS3 f32x4*)(xs + ns * 4096 + g * 1024) = f32x4{a[4 * g], a[4 * g + 1], a[4 * g + 2], a[4 * g + 3]};
    }
  LDS_BAR();
  const unsigned img = lds0 + 98304 + w * 4096;
#pragma unroll
  for (int ns = 0; ns < 2; ++ns)
#pragma unroll
    for (int g = 0; g < 4; ++g) {
      const f32x16& a = acc[0][ns];
      const f32x4 o = *(AS3 f32x4*)(xr + ns * 4096 + g * 1024);
      *(AS3 u32x2*)(img + r32 * 128 + (((ns * 4 + g) ^ (r32 & 7)) << 4) + h * 8) =
          u32x2{pk2(a[4 * g] + o[0], a[4 * g + 1] + o[1]), pk2(a[4 * g + 2] + o[2], a[4 * g + 3] + o[3])};
    }
  asm volatile("" ::: "memory");
  const int tok0 = rb3 * 128 + wm * 64 + kh * 32;
  unsigned short* op = out + (size_t)tok0 * 2048 + br * 1024 + cb * 128 + wn * 64;
#pragma unroll
  for (int i = 0; i < 4; ++i) {
    const int row = 8 * i + (lane >> 3), ch = lane & 7;
    const u32x4 v = *(AS3 u32x4*)(img + row * 128 + ((ch ^ (row & 7)) << 4));
    *(u32x4*)(op + (size_t)row * 2048 + ch * 8) = v;
  }
  STAMP(0, 9);
  if (threadIdx.x == 0 && blockIdx.x == 0) {
    asm volatile("s_waitcnt vmcnt(0)" ::: "memory");
    const u64 t0 = *(volatile u64*)(ws + (100u << 20));
    for (int it = 0; it < 100000 && __builtin_amdgcn_s_memrealtime() - t0 < 10000; ++it) __builtin_amdgcn_s_sleep(1);
  }
}

__global__ void tiny(char* ws) { if (threadIdx.x == 0) *(u64*)(ws + (100u << 20) + blockIdx.x * 128) = __builtin_amdgcn_s_memrealtime(); }
extern "C" void kernel_launch(void* const* d_in, const int* in_sizes, int n_in, void* d_out, int out_size, void* d_ws, size_t ws_size,
                              hipStream_t stream) {
  (void)in_sizes; (void)n_in; (void)out_size; (void)ws_size;
  typedef const unsigned short* cp;
  tiny<<<256, 64, 0, stream>>>((char*)d_ws);
  fused_fwd<<<256, 768, 0, stream>>>((cp)d_in[0], (cp)d_in[1], (cp)d_in[2], (cp)d_in[3], (cp)d_in[4], (cp)d_in[5], (cp)d_in[6], (cp)d_in[7],
                                     (cp)d_in[8], (char*)d_ws, (unsigned short*)d_out);
}
```

```cpp
#define STAMPS
#include <hip/hip_runtime.h>
#include <hip/hip_bf16.h>
#include <stdint.h>
#pragma clang diagnostic ignored "-Wint-to-pointer-cast"

typedef __bf16   bf16x8 __attribute__((ext_vector_type(8)));
typedef __bf16   bf16x2 __attribute__((ext_vector_type(2)));
typedef short    s16x4  __attribute__((ext_vector_type(4)));
typedef short    s16x8  __attribute__((ext_vector_type(8)));
typedef float    f32x16 __attribute__((ext_vector_type(16)));
typedef float    f32x4  __attribute__((ext_vector_type(4)));
typedef float    f32x2  __attribute__((ext_vector_type(2)));
typedef unsigned u32x4  __attribute__((ext_vector_type(4)));
typedef unsigned u32x2  __attribute__((ext_vector_type(2)));
typedef unsigned long long u64;
typedef __amdgpu_buffer_rsrc_t rsrc_t;

#define AS1 __attribute__((address_space(1)))
#define AS3 __attribute__((address_space(3)))
#define DI __device__ __forceinline__
#define MFMA32(a, b, c) __builtin_amdgcn_mfma_f32_32x32x16_bf16((a), (b), (c), 0, 0, 0)
#define MFMA16(a, b, c) __builtin_amdgcn_mfma_f32_16x16x32_bf16((a), (b), (c), 0, 0, 0)
#define SBAR() __builtin_amdgcn_sched_barrier(0)
#define WAIT_BAR(N) asm volatile("s_waitcnt vmcnt(" #N ") lgkmcnt(0)\n\ts_barrier" ::: "memory")
#define LGKM0() __builtin_amdgcn_s_waitcnt(0xC07F)
#define LDS_BAR() do { asm volatile("" ::: "memory"); LGKM0(); asm volatile("s_barrier" ::: "memory"); } while (0)

#define STAMP(th, i) do { if (threadIdx.x == (th)) { u64* sp_ = (u64*)(ws + (64u << 20)) + (size_t)blockIdx.x * 32 + 2 * (i); \
    sp_[0] = __builtin_amdgcn_s_memtime(); sp_[1] = __builtin_amdgcn_s_memrealtime(); } } while (0)
#define TS(i) do {} while (0)
#define TSOUT() do {} while (0)
#define ZREF 0
#define EPF 1
#define EMV 1
#define PFB 0
#define PF0 3
#define BROT (8 * (((x >> 1) ^ (x >> 2)) & 1))
#define NTX 0
#define NTW 0
#define KROT (2 * x)
#define MAPV 1
constexpr int D = 1024;
constexpr float LOG2E = 1.4426950408889634f;
constexpr float QSCALE = 0.17677669529663687f * LOG2E;

constexpr unsigned WS_QA  = 0;
constexpr unsigned WS_QB  = WS_QA + (8u << 20);
constexpr unsigned WS_OA  = WS_QB + (4u << 20);
constexpr unsigned WS_OB  = WS_OA + (4u << 20);
constexpr unsigned WS_KV  = WS_OB + (4u << 20);
constexpr unsigned WS_KVP = WS_KV + (256u << 10);
constexpr unsigned WS_ZP  = WS_KVP + (2u << 20);
constexpr unsigned WS_FLG = WS_ZP + (64u << 10);
constexpr unsigned WS_END = WS_FLG + (16u << 10);
constexpr int F_K = 0, F_Q = 128, F_OA = 256, F_OB = 512, F_KV = 768, F_EPOCH = 1024;

DI unsigned pk2(float lo, float hi) {
  f32x2 v = {lo, hi};
  return __builtin_bit_cast(unsigned, __builtin_convertvector(v, bf16x2));
}
template <int SS> DI bf16x8 pack8(const f32x16& x) {
  u32x4 p = {pk2(x[8 * SS + 0], x[8 * SS + 1]), pk2(x[8 * SS + 2], x[8 * SS + 3]), pk2(x[8 * SS + 4], x[8 * SS + 5]), pk2(x[8 * SS + 6], x[8 * SS + 7])};
  return __builtin_bit_cast(bf16x8, p);
}
DI bf16x8 lds_r128(unsigned a) { return *(const AS3 bf16x8*)a; }
DI bf16x8 lds_tr2(unsigned a0, unsigned a1) {
  s16x4 lo = __builtin_amdgcn_ds_read_tr16_b64_v4i16((AS3 s16x4*)a0);
  s16x4 hi = __builtin_amdgcn_ds_read_tr16_b64_v4i16((AS3 s16x4*)a1);
  s16x8 r = __builtin_shufflevector(lo, hi, 0, 1, 2, 3, 4, 5, 6, 7);
  return __builtin_bit_cast(bf16x8, r);
}
DI rsrc_t mk_rsrc(const void* p, unsigned bytes) { return __builtin_amdgcn_make_buffer_rsrc((void*)p, (short)0, (int)bytes, 0x00020000); }
DI void set_m0(unsigned v) { asm volatile("s_mov_b32 m0, %0\n\ts_nop 0" ::"s"(v) : "memory"); }
template <int IMM> DI void dma16(unsigned voff, rsrc_t srd, unsigned soff) {
  asm volatile("buffer_load_dwordx4 %0, %1, %2 offen offset:%3 lds" ::"v"(voff), "s"(srd), "s"(soff), "i"(IMM) : "memory");
}
template <int IMM> DI void dma16n(unsigned voff, rsrc_t srd, unsigned soff) {
  asm volatile("buffer_load_dwordx4 %0, %1, %2 offen offset:%3 nt lds" ::"v"(voff), "s"(srd), "s"(soff), "i"(IMM) : "memory");
}
#define DMAX dma16
#define DMAW dma16
template <int IMM> DI void dma16d(unsigned voff, rsrc_t srd, unsigned soff) {
  asm volatile("buffer_load_dwordx4 %0, %1, %2 offen offset:%3 sc1 lds" ::"v"(voff), "s"(srd), "s"(soff), "i"(IMM) : "memory");
}
DI void dma4k(unsigned lds, unsigned voff, rsrc_t srd, unsigned soff) {
  set_m0(lds); dma16d<0>(voff, srd, soff); dma16d<1024>(voff, srd, soff); dma16d<2048>(voff, srd, soff); dma16d<3072>(voff, srd, soff);
}
template <class T> DI void st16(rsrc_t r, unsigned voff, T v) { __builtin_amdgcn_raw_buffer_store_b128(__builtin_bit_cast(u32x4, v), r, (int)voff, 0, 16); }
DI void st8(rsrc_t r, unsigned voff, u32x2 v) { __builtin_amdgcn_raw_buffer_store_b64(v, r, (int)voff, 0, 16); }
DI void st4(rsrc_t r, unsigned voff, float v) { __builtin_amdgcn_raw_buffer_store_b32(__float_as_uint(v), r, (int)voff, 0, 16); }
DI u32x4 ld16(rsrc_t r, unsigned voff) { return __builtin_amdgcn_raw_buffer_load_b128(r, (int)voff, 0, 16); }
DI float ld4(rsrc_t r, unsigned voff) { return __uint_as_float(__builtin_amdgcn_raw_buffer_load_b32(r, (int)voff, 0, 16)); }
DI void post(char* ws, int f, u64 tag) { __hip_atomic_store((AS1 u64*)(ws + WS_FLG) + f, tag, __ATOMIC_RELAXED, __HIP_MEMORY_SCOPE_AGENT); }
DI void poll(char* ws, int f, u64 tag) {
#pragma unroll 1
  for (int it = 0; it < (1 << 17); ++it) {
    const u64 v = __hip_atomic_load((AS1 u64*)(ws + WS_FLG) + f, __ATOMIC_RELAXED, __HIP_MEMORY_SCOPE_AGENT);
    if (__all(v == tag)) break;
    __builtin_amdgcn_s_sleep(2);
  }
  asm volatile("" ::: "memory");
}
DI void pf_line(unsigned& sink, const void* p) { asm volatile("global_load_dword %0, %1, off" : "+v"(sink) : "v"(p) : "memory"); }
#define PIN(x) asm volatile("" : "+v"(x))
DI float xhalf_max(float v) {
  auto r = __builtin_amdgcn_permlane32_swap(__float_as_uint(v), __float_as_uint(v), false, false);
  return fmaxf(__uint_as_float(r[0]), __uint_as_float(r[1]));
}
DI float xhalf_min(float v) {
  auto r = __builtin_amdgcn_permlane32_swap(__float_as_uint(v), __float_as_uint(v), false, false);
  return fminf(__uint_as_float(r[0]), __uint_as_float(r[1]));
}
DI float xhalf_sum(float v) {
  auto r = __builtin_amdgcn_permlane32_swap(__float_as_uint(v), __float_as_uint(v), false, false);
  return __uint_as_float(r[0]) + __uint_as_float(r[1]);
}
DI float max16(const f32x16& x) {
  float a = fmaxf(fmaxf(x[0], x[1]), x[2]), b = fmaxf(fmaxf(x[3], x[4]), x[5]);
  a = fmaxf(fmaxf(a, x[6]), x[7]);   b = fmaxf(fmaxf(b, x[8]), x[9]);
  a = fmaxf(fmaxf(a, x[10]), x[11]); b = fmaxf(fmaxf(b, x[12]), x[13]);
  a = fmaxf(fmaxf(a, x[14]), x[15]);
  return fmaxf(a, b);
}
DI float min16(const f32x16& x) {
  float a = fminf(fminf(x[0], x[1]), x[2]), b = fminf(fminf(x[3], x[4]), x[5]);
  a = fminf(fminf(a, x[6]), x[7]);   b = fminf(fminf(b, x[8]), x[9]);
  a = fminf(fminf(a, x[10]), x[11]); b = fminf(fminf(b, x[12]), x[13]);
  a = fminf(fminf(a, x[14]), x[15]);
  return fminf(a, b);
}
template <bool NORM> DI void feat_map(const f32x16& p, f32x16& ep, f32x16& em) {
  const float mx = xhalf_max(max16(p)), mn = xhalf_min(min16(p));
  const f32x2 a = {-mx * LOG2E, -mx * LOG2E}, b = {mn * LOG2E, mn * LOG2E}, c = {LOG2E, LOG2E};
  f32x2 sp = {0.f, 0.f}, sm = {0.f, 0.f};
#pragma unroll
  for (int i = 0; i < 16; i += 2) {
    const f32x2 x = {p[i], p[i + 1]};
    const f32x2 u = __builtin_elementwise_fma(x, c, a), v = __builtin_elementwise_fma(x, -c, b);
    const f32x2 eu = {__builtin_amdgcn_exp2f(u[0]), __builtin_amdgcn_exp2f(u[1])}, ev = {__builtin_amdgcn_exp2f(v[0]), __builtin_amdgcn_exp2f(v[1])};
    ep[i] = eu[0]; ep[i + 1] = eu[1]; em[i] = ev[0]; em[i + 1] = ev[1];
    if (i == 0) { sp = eu; sm = ev; } else { sp += eu; sm += ev; }
  }
  if (NORM) {
    const float rp = __builtin_amdgcn_rcpf(xhalf_sum(sp[0] + sp[1])), rm = __builtin_amdgcn_rcpf(xhalf_sum(sm[0] + sm[1]));
    ep *= rp; em *= rm;
  }
}
DI bf16x8 row_sum_sel(int lane) {
  const unsigned u = ((lane & 11) == 0 && (((lane >> 2) ^ (lane >> 4)) & 1) == 0) ? 0x3f803f80u : 0u;
  return __builtin_bit_cast(bf16x8, u32x4{u, u, u, u});
}

constexpr int LDS_BYTES = 163840;
constexpr int P1_WOFF = 32768, P1_EOFF = 49152, P1_STAGE = 53248, P1_ZRED = 3 * P1_STAGE, P1_CNT = P1_ZRED + 2048, P1_GO = P1_CNT + 16, P1_PHI = 65536;
constexpr int P2_Q = 131072, P2_KVF = 147456, P2_ZB = 151552;
constexpr int P2_XO = 32768, P2_XL = 0;
constexpr int P3_O = 0;
DI constexpr int p3_wslot(int t) { return 65536 + ((t + 2) % 6) * 16384; }

template <int TYPE>
DI void p1_compute(unsigned lds0, const char* pfp, unsigned& pfs, char* __restrict__ ws, rsrc_t sws, int rb, int ct, int w, int lane) {
  const int tid = threadIdx.x;
  const int wm = w & 3, wn = w >> 2, r32 = lane & 31, h = lane >> 5;
  if (tid < 8) *(AS3 unsigned*)(lds0 + P1_CNT + tid * 4) = 0u;

  const int xrow = wm * 64 + r32;
  const unsigned xa = xrow * 128 + ((h ^ ((xrow >> 1) & 7)) << 4);
  const int q4 = (lane & 15) >> 2, p4 = lane & 3, chalf = (lane >> 4) & 1;
  unsigned wa[2];
#pragma unroll
  for (int ns = 0; ns < 2; ++ns)
    wa[ns] = P1_WOFF + (8 * h + q4) * 256 + ((((wn * 2 + ns) ^ q4) << 2) | (chalf * 2 + (p4 >> 1))) * 16 + (p4 & 1) * 8;
  const int erow = 64 * (ct >> 1) + 16 * wm + (lane & 15);
  const unsigned exa = erow * 128 + (((lane >> 4) ^ ((erow >> 1) & 7)) << 4);
  const unsigned ewa = P1_EOFF + (8 * (lane >> 4) + q4) * 64 + wn * 32 + p4 * 8;

  f32x16 acc[2][2] = {};
  f32x4 eacc = {};
  bf16x8 xf[2][2], wfr[2][2], ex[2], ew[2];
  auto rdx = [&](int set, unsigned buf, int ks) {
#pragma unroll
    for (int ms = 0; ms < 2; ++ms) xf[set][ms] = lds_r128(buf + ((xa ^ (ks * 32)) + ms * 4096));
  };
  auto rdw = [&](int set, unsigned buf, int ks, int ns) { wfr[set][ns] = lds_tr2(buf + wa[ns] + ks * 4096, buf + wa[ns] + ks * 4096 + 1024); };
  auto mm1 = [&](int set, int ms, int ns) {
    if (TYPE == 0 && ns == 1) acc[ms][1] = MFMA32(xf[set][ms], wfr[set][1], acc[ms][1]);
    else                      acc[ms][ns] = MFMA32(wfr[set][ns], xf[set][ms], acc[ms][ns]);
  };
  auto erd = [&](unsigned buf, int kk) {
    if (TYPE >= 2) { ex[kk] = lds_r128(buf + (exa ^ (kk * 64))); ew[kk] = lds_tr2(buf + ewa + kk * 2048, buf + ewa + kk * 2048 + 256); }
  };
  auto emm = [&](int kk) {
    if (TYPE == 2) eacc = MFMA16(ew[kk], ex[kk], eacc);
    if (TYPE == 3) eacc = MFMA16(ex[kk], ew[kk], eacc);
  };
  auto sub = [&](int set, unsigned buf, int ks) {
    SBAR(); mm1(set, 0, 0); SBAR(); rdx(set ^ 1, buf, ks);
    SBAR(); mm1(set, 1, 0); SBAR(); rdw(set ^ 1, buf, ks, 0);
    SBAR(); mm1(set, 0, 1); SBAR(); rdw(set ^ 1, buf, ks, 1);
    SBAR(); mm1(set, 1, 1); SBAR();
  };
  if (PFB == 1 && TYPE < 2 && pfp) pf_line(pfs, pfp);
  LDS_BAR();
  if (PFB == 2 && TYPE < 2 && pfp) pf_line(pfs, pfp);
  STAMP(0, 1);
  unsigned cur = lds0, nxt = lds0 + P1_STAGE;
  rdx(0, cur, 0); rdw(0, cur, 0, 0); rdw(0, cur, 0, 1);
  LGKM0();
#pragma unroll 1
  for (int t = 0; t < 16; ++t) {
    sub(0, cur, 1); erd(cur, 0);
    sub(1, cur, 2); erd(cur, 1);
    sub(0, cur, 3); emm(0); emm(1); SBAR();
    LDS_BAR();
    sub(1, nxt, 0);
    cur = nxt; nxt = (nxt == lds0 + 2 * P1_STAGE) ? lds0 : nxt + P1_STAGE;
  }
  STAMP(0, 2);
  bf16x8 wf[2][2];
  if constexpr (TYPE <= 1) {
    const unsigned a = lds0 + P1_PHI + (TYPE == 0 ? wn : 2 * wn) * 2048 + (4 * h + q4) * 64 + chalf * 32 + p4 * 8;
#pragma unroll
    for (int n = 0; n <= TYPE; ++n)
#pragma unroll
      for (int s = 0; s < 2; ++s) wf[n][s] = lds_tr2(a + n * 2048 + s * 1024, a + n * 2048 + s * 1024 + 512);
  }

  if (TYPE >= 2) {
#pragma unroll
    for (int ms = 0; ms < 2; ++ms)
#pragma unroll
      for (int ns = 0; ns < 2; ++ns) {
        const f32x16 v = acc[ms][ns] * QSCALE;
        const unsigned p = WS_QB + ((rb * 8 + wm * 2 + ms) * 32 + ct * 4 + wn * 2 + ns) * 2048 + lane * 16;
        st16(sws, p, pack8<0>(v)); st16(sws, p + 1024, pack8<1>(v));
      }
    const int g = lane >> 4, tok0 = rb * 256 + 64 * (ct >> 1) + 16 * wm;
    unsigned p = WS_KV + (tok0 >> 5) * 4096;
    if (TYPE == 3) p += 2048 + (wm & 1) * 1024 + (16 * wn + (lane & 15) + 32 * (g & 1)) * 16 + (g >> 1) * 8;
    else           p += wn * 1024 + (16 * (wm & 1) + (lane & 15) + 32 * (g & 1)) * 16 + (g >> 1) * 8;
    st8(sws, p, u32x2{pk2(eacc[0], eacc[1]), pk2(eacc[2], eacc[3])});
    WAIT_BAR(0);
  } else if (TYPE == 1) {
    f32x16 pq[2][2];
#pragma unroll
    for (int ms = 0; ms < 2; ++ms)
#pragma unroll
      for (int ns = 0; ns < 2; ++ns) {
        pq[ms][ns] = MFMA32(wf[ns][0], pack8<0>(acc[ms][ns]), f32x16{});
        pq[ms][ns] = MFMA32(wf[ns][1], pack8<1>(acc[ms][ns]), pq[ms][ns]);
      }
#pragma unroll
    for (int ms = 0; ms < 2; ++ms)
#pragma unroll
      for (int ns = 0; ns < 2; ++ns) {
        f32x16 ep, em;
        feat_map<false>(pq[ms][ns], ep, em);
        const unsigned p = WS_QA + ((rb * 8 + wm * 2 + ms) * 32 + ct * 4 + wn * 2 + ns) * 4096 + lane * 16;
        st16(sws, p, pack8<0>(ep)); st16(sws, p + 1024, pack8<1>(ep)); st16(sws, p + 2048, pack8<0>(em)); st16(sws, p + 3072, pack8<1>(em));
      }
  } else {
    const unsigned img = lds0 + w * 8192;
    const bf16x8 sel = row_sum_sel(lane);
    f32x16 pk[2], kv[2] = {};
    f32x4 zz[2] = {};
    bf16x8 vf[2][2], af[2][2];
#pragma unroll
    for (int ms = 0; ms < 2; ++ms) {
      pk[ms] = MFMA32(wf[0][0], pack8<0>(acc[ms][0]), f32x16{});
      pk[ms] = MFMA32(wf[0][1], pack8<1>(acc[ms][0]), pk[ms]);
    }
#pragma unroll
    for (int ms = 0; ms < 2; ++ms) { vf[ms][0] = pack8<0>(acc[ms][1]); vf[ms][1] = pack8<1>(acc[ms][1]); PIN(vf[ms][0]); PIN(vf[ms][1]); }
    float off[4];
#pragma unroll
    for (int ms = 0; ms < 2; ++ms) { off[2 * ms] = -LOG2E * xhalf_max(max16(pk[ms])); off[2 * ms + 1] = LOG2E * xhalf_min(min16(pk[ms])); }
    const unsigned wr = img + 4096 + r32 * 64, sw = (r32 >> 1) & 7;
    const int t0 = 4 * h + q4, u8 = 4 * chalf + p4;
    const unsigned rd0 = img + 4096 + t0 * 64 + ((u8 ^ (t0 >> 1)) << 3), rd1 = img + 4096 + (t0 + 8) * 64 + ((u8 ^ ((t0 >> 1) + 4)) << 3);
    auto put = [&](int sg) {
#pragma unroll
      for (int g = 0; g < 4; ++g) *(AS3 f32x4*)(img + sg * 4096 + g * 1024 + lane * 16) = f32x4{kv[sg][4 * g], kv[sg][4 * g + 1], kv[sg][4 * g + 2], kv[sg][4 * g + 3]};
    };
    f32x16 e[2];
    float rs[2];
    auto mm = [&](int u, int j) {
      if (j & 1) zz[u & 1] = MFMA16(sel, af[u & 1][j >> 1], zz[u & 1]); else kv[u & 1] = MFMA32(af[u & 1][j >> 1], vf[u >> 1][j >> 1], kv[u & 1]);
    };
    auto exps = [&](int u, int mu) {
      const f32x16& p = pk[u >> 1];
      const float c = (u & 1) ? -LOG2E : LOG2E;
      f32x2 sm;
#pragma unroll
      for (int i = 0; i < 16; i += 2) {
        if (mu >= 0 && i >= 8) { SBAR(); mm(mu, (i - 8) >> 1); SBAR(); }
        const f32x2 v = __builtin_elementwise_fma(f32x2{p[i], p[i + 1]}, f32x2{c, c}, f32x2{off[u], off[u]});
        const f32x2 ev = {__builtin_amdgcn_exp2f(v[0]), __builtin_amdgcn_exp2f(v[1])};
        e[u & 1][i] = ev[0]; e[u & 1][i + 1] = ev[1];
        if (i == 0) sm = ev; else sm += ev;
      }
      rs[u & 1] = __builtin_amdgcn_rcpf(xhalf_sum(sm[0] + sm[1]));
    };
    auto rest = [&](int u) {
      const f32x16 n = e[u & 1] * rs[u & 1];
#pragma unroll
      for (int g = 0; g < 4; ++g) *(AS3 u32x2*)(wr + (u & 1) * 2048 + (((2 * g + h) ^ sw) << 3)) = u32x2{pk2(n[4 * g], n[4 * g + 1]), pk2(n[4 * g + 2], n[4 * g + 3])};
      asm volatile("" ::: "memory");
#pragma unroll
      for (int s = 0; s < 2; ++s) af[u & 1][s] = lds_tr2(rd0 + (u & 1) * 2048 + s * 1024, rd1 + (u & 1) * 2048 + s * 1024);
      asm volatile("" ::: "memory");
      SBAR();
    };
    exps(0, -1); exps(1, -1); rest(0);
    exps(2, 0);  rest(1);
    exps(3, 1);  rest(2);
    rest(3);
    mm(2, 0); mm(2, 2); mm(3, 0); mm(3, 2); SBAR(); mm(2, 1); mm(2, 3); mm(3, 1); mm(3, 3);
    put(0); put(1);
    if (lane < 32) {
      const unsigned a = lds0 + P1_ZRED + w * 256 + ((lane >> 2) & 1) * 64 + (lane >> 3) * 16 + (lane & 3) * 4;
      *(AS3 float*)a = zz[0][0]; *(AS3 float*)(a + 128) = zz[1][0];
    }
  }
  STAMP(0, 3);
}

DI void attn_pass_fast(unsigned lds0, const bf16x8 (&q)[2][2], const bf16x8 sel, int kh, int lane, f32x16 (&O)[2], f32x4 (&L)[2], unsigned& tv) {
  TS(0);
  typedef const AS3 char* lp;
  const lp l0 = (lp)(lds0 + lane * 16), la = l0 + kh * 16384;
#define RD(p) (*(const AS3 bf16x8*)(p))
  if (kh) __builtin_amdgcn_s_setprio(1);
  bf16x8 ka = RD(l0 + 98304), kb = RD(l0 + 98304 + 1024);
  f32x16 negm[2], S0, S1;
#pragma unroll
  for (int r = 0; r < 2; ++r) {
    f32x16 z = {};
    z = MFMA32(ka, q[r][0], z); z = MFMA32(kb, q[r][1], z);
    const float m = xhalf_max(max16(z));
#pragma unroll
    for (int i = 0; i < 16; ++i) negm[r][i] = -m;
    PIN(negm[r]);
    O[r] = f32x16{}; L[r] = f32x4{};
  }
  ka = RD(la + 98304); kb = RD(la + 98304 + 1024);
  bf16x8 va = RD(la + 98304 + 2048), vb = bf16x8{};
  S0 = MFMA32(ka, q[0][0], negm[0]); S0 = MFMA32(kb, q[0][1], S0);
  u32x4 p0 = {}, p1 = {};
  LGKM0();
#define EX4(SC, i) do { SC[i] = __builtin_amdgcn_exp2f(SC[i]); SC[i + 1] = __builtin_amdgcn_exp2f(SC[i + 1]); \
    SC[i + 2] = __builtin_amdgcn_exp2f(SC[i + 2]); SC[i + 3] = __builtin_amdgcn_exp2f(SC[i + 3]); PIN(SC); } while (0)
#define CV4(P, SC, i) do { P[0] = pk2(SC[i], SC[i + 1]); P[1] = pk2(SC[i + 2], SC[i + 3]); P[2] = pk2(SC[i + 4], SC[i + 5]); P[3] = pk2(SC[i + 6], SC[i + 7]); PIN(P); } while (0)
#define BF(P) __builtin_bit_cast(bf16x8, P)
#define TILE(TA, TN) do { \
    SBAR(); S1 = MFMA32(ka, q[1][0], negm[1]);  SBAR(); ka = RD(TN);          EX4(S0, 0);  \
    SBAR(); O[1] = MFMA32(vb, BF(p1), O[1]);    SBAR(); vb = RD((TA) + 3072); EX4(S0, 4);  \
    SBAR(); L[1] = MFMA16(sel, BF(p1), L[1]);   SBAR(); CV4(p0, S0, 0); \
    SBAR(); S1 = MFMA32(kb, q[1][1], S1);       SBAR(); kb = RD((TN) + 1024); EX4(S0, 8);  \
    SBAR(); O[0] = MFMA32(va, BF(p0), O[0]);    SBAR(); EX4(S0, 12); \
    SBAR(); L[0] = MFMA16(sel, BF(p0), L[0]);   SBAR(); CV4(p1, S0, 8); \
    SBAR(); S0 = MFMA32(ka, q[0][0], negm[0]);  SBAR(); EX4(S1, 0);  \
    SBAR(); O[0] = MFMA32(vb, BF(p1), O[0]);    SBAR(); EX4(S1, 4);  \
    SBAR(); L[0] = MFMA16(sel, BF(p1), L[0]);   SBAR(); CV4(p0, S1, 0); \
    SBAR(); S0 = MFMA32(kb, q[0][1], S0);       SBAR(); EX4(S1, 8);  \
    SBAR(); O[1] = MFMA32(va, BF(p0), O[1]);    SBAR(); va = RD((TN) + 2048); EX4(S1, 12); \
    SBAR(); L[1] = MFMA16(sel, BF(p0), L[1]);   SBAR(); CV4(p1, S1, 8); } while (0)
  TS(1);
#pragma unroll 1
  for (int c = 0; c < 8; ++c) {
    const lp cb = la + ((3 - c) & 3) * 32768, nb = la + ((2 - c) & 3) * 32768;
    TILE(cb, cb + 4096);
    TILE(cb + 4096, cb + 8192);
    SBAR(); if (kh) __builtin_amdgcn_s_setprio(0);
    TILE(cb + 8192, cb + 12288);
    SBAR(); TS(2 + 2 * c);
    asm volatile("s_waitcnt vmcnt(0)\n\ts_barrier" ::: "memory"); TS(3 + 2 * c);
    if (kh) __builtin_amdgcn_s_setprio(1);
    TILE(cb + 12288, nb);
  }
  SBAR();
  O[1] = MFMA32(vb, BF(p1), O[1]); L[1] = MFMA16(sel, BF(p1), L[1]);
  __builtin_amdgcn_s_setprio(0);
  TS(18);
#undef TILE
#undef EX4
#undef CV4
#undef BF
#undef RD
}

DI void attn_pass_safe(rsrc_t sws, const bf16x8 q0, const bf16x8 q1, const bf16x8 sel, int lane, f32x16& O, f32x4& L) {
  O = f32x16{}; L = f32x4{};
  float m = -INFINITY;
#pragma unroll 1
  for (int t = 0; t < 64; ++t) {
    bf16x8 a[4];
#pragma unroll
    for (int j = 0; j < 4; ++j) a[j] = __builtin_bit_cast(bf16x8, ld16(sws, WS_KV + t * 4096 + j * 1024 + lane * 16));
    f32x16 Sc = {};
    Sc = MFMA32(a[0], q0, Sc); Sc = MFMA32(a[1], q1, Sc);
    const float mn = fmaxf(m, xhalf_max(max16(Sc)));
    const float al = __builtin_amdgcn_exp2f(m - mn);
    m = mn;
#pragma unroll
    for (int r = 0; r < 16; ++r) { O[r] *= al; Sc[r] = __builtin_amdgcn_exp2f(Sc[r] - mn); }
    L *= al;
    const bf16x8 p0 = pack8<0>(Sc), p1 = pack8<1>(Sc);
    O = MFMA32(a[2], p0, O); L = MFMA16(sel, p0, L);
    O = MFMA32(a[3], p1, O); L = MFMA16(sel, p1, L);
  }
}

DI void helper_program(unsigned lds0, const unsigned short* __restrict__ X, const unsigned short* __restrict__ W, const unsigned short* __restrict__ E,
                       const unsigned short* __restrict__ woa, const unsigned short* __restrict__ wob, char* __restrict__ ws, rsrc_t sws,
                       int type, int rb, int ct, int x, int idx, int lw, int lane) {
  __builtin_amdgcn_s_setprio(3);
  const u64 e0 = __hip_atomic_load((AS1 u64*)(ws + WS_FLG) + F_EPOCH, __ATOMIC_RELAXED, __HIP_MEMORY_SCOPE_AGENT);
  const unsigned vl = lane * 16;
  {
    const bool ext = type >= 2;
    const unsigned ldw = ext ? 2048u : 6144u;
    const rsrc_t sx = mk_rsrc(X + (size_t)rb * 256 * D, 256u * 2048u);
    const rsrc_t sw = mk_rsrc(W, 1024u * ldw);
    const rsrc_t se = mk_rsrc(E, 1024u * 64u);
    unsigned xv[8], wv[4];
#pragma unroll
    for (int j = 0; j < 8; ++j) {
      const int row = 8 * (8 * lw + j) + (lane >> 3), c = (lane & 7) ^ ((row >> 1) & 7);
      xv[j] = row * 2048 + c * 16 - (j & 3) * 1024;
    }
#pragma unroll
    for (int j = 0; j < 4; ++j) {
      const int k = 4 * (4 * lw + j) + (lane >> 4), c = (lane & 15) ^ (((lane >> 4) & 3) << 2);
      const int col = (type == 0) ? 1024 + ((c >> 2) & 1) * 1024 + (2 * ct + (c >> 3)) * 32 + (c & 3) * 8 : ct * 128 + c * 8;
      wv[j] = k * ldw + col * 2 - j * 1024;
    }
    auto stage = [&](int t0, unsigned buf) {
      const int t = (t0 + (ext ? BROT : KROT)) & 15;
      const unsigned sox = t * 128, sow = t * 64 * ldw;
      set_m0(buf + P1_WOFF + lw * 4096);
      DMAW<0>(wv[0], sw, sow); DMAW<1024>(wv[1], sw, sow); DMAW<2048>(wv[2], sw, sow); DMAW<3072>(wv[3], sw, sow);
      if (ext ? (NTX & 2) : (NTX & 1)) {
        set_m0(buf + lw * 8192);
        dma16n<0>(xv[0], sx, sox); dma16n<1024>(xv[1], sx, sox); dma16n<2048>(xv[2], sx, sox); dma16n<3072>(xv[3], sx, sox);
        set_m0(buf + lw * 8192 + 4096);
        dma16n<0>(xv[4], sx, sox); dma16n<1024>(xv[5], sx, sox); dma16n<2048>(xv[6], sx, sox); dma16n<3072>(xv[7], sx, sox);
      } else {
        set_m0(buf + lw * 8192);
        dma16<0>(xv[0], sx, sox); dma16<1024>(xv[1], sx, sox); dma16<2048>(xv[2], sx, sox); dma16<3072>(xv[3], sx, sox);
        set_m0(buf + lw * 8192 + 4096);
        dma16<0>(xv[4], sx, sox); dma16<1024>(xv[5], sx, sox); dma16<2048>(xv[6], sx, sox); dma16<3072>(xv[7], sx, sox);
      }
      if (ext) { set_m0(buf + P1_EOFF + lw * 1024); dma16<0>(vl, se, t * 4096 + lw * 1024); }
    };
    stage(0, lds0); stage(1, lds0 + P1_STAGE);
    if (ext) WAIT_BAR(13); else WAIT_BAR(12);
    stage(2, lds0 + 2 * P1_STAGE);
    unsigned buf = lds0;
#pragma unroll 1
    for (int t = 0; t < 13; ++t) {
      if (ext) WAIT_BAR(13); else WAIT_BAR(12);
      stage(t + 3, buf);
      buf = (buf == lds0 + 2 * P1_STAGE) ? lds0 : buf + P1_STAGE;
    }
    if (ext) WAIT_BAR(13); else WAIT_BAR(12);
    if (!ext && (type == 1 || lw < 2)) {
      set_m0(lds0 + P1_PHI + lw * 2048);
      const unsigned so = ((type == 0 ? 2 * ct : 4 * ct) + lw) * 2048;
      dma16<0>(vl, se, so); dma16<1024>(vl, se, so);
    }
    WAIT_BAR(0);
    WAIT_BAR(0);
  }
  const u64 tag = ((u64)(unsigned)__builtin_amdgcn_readfirstlane((unsigned)(e0 >> 32)) << 32 | (unsigned)__builtin_amdgcn_readfirstlane((unsigned)e0))
                  * 0x5851F42D4C957F2Dull + 0x14057B7EF767814Full;
  const bool lead = (lw == 0) && (lane == 0);
  if (type >= 2) { WAIT_BAR(0); if (lead) post(ws, F_KV + rb * 16 + ct, tag); }
  auto chunk = [&](int c) {
    const unsigned l = lds0 + ((3 - c) & 3) * 32768 + lw * 8192, so = WS_KV + ((c + x) & 7) * 32768 + lw * 8192;
    dma4k(l, vl, sws, so); dma4k(l + 4096, vl, sws, so + 4096);
  };
  STAMP(512, 10);
  if (lw == 0) {
    poll(ws, F_KV + x * 16 + (lane & 7), tag);
    __hip_atomic_store((AS3 unsigned*)(lds0 + P1_GO), 1u, __ATOMIC_RELAXED, __HIP_MEMORY_SCOPE_WORKGROUP);
  } else {
    while (__hip_atomic_load((AS3 unsigned*)(lds0 + P1_GO), __ATOMIC_RELAXED, __HIP_MEMORY_SCOPE_WORKGROUP) == 0u) __builtin_amdgcn_s_sleep(1);
    asm volatile("" ::: "memory");
  }
  STAMP(512, 11);
  dma4k(lds0 + P2_Q + lw * 4096, vl, sws, WS_QB + ((8 * x + (idx >> 2)) * 32 + 8 * (idx & 3)) * 2048 + lw * 4096);
  chunk(0);
  WAIT_BAR(0);
  poll(ws, F_KV + (lane >> 3) * 16 + (lane & 7), tag);
  chunk(1);
  if (type == 0) {
#pragma unroll
    for (int j = 0; j < 4; ++j) {
      const unsigned a = lds0 + (lw >> 1) * 32768 + ((lw & 1) * 4 + j) * 1024 + vl;
      const f32x4 sm = (*(AS3 f32x4*)a + *(AS3 f32x4*)(a + 8192)) + (*(AS3 f32x4*)(a + 16384) + *(AS3 f32x4*)(a + 24576));
      st16(sws, WS_KVP + ((2 * ct + (lw >> 1)) * 8 + rb) * 8192 + ((lw & 1) * 4 + j) * 1024 + vl, sm);
    }
    if (lw < 2) {
      const unsigned a = lds0 + P1_ZRED + lw * 1024 + lane * 4;
      st4(sws, WS_ZP + (((2 * ct + lw) * 8 + rb) * 64 + lane) * 4, (*(AS3 float*)a + *(AS3 float*)(a + 256)) + (*(AS3 float*)(a + 512) + *(AS3 float*)(a + 768)));
    }
  }
  WAIT_BAR(0);
  chunk(2); chunk(3);
  if (lead && type < 2) post(ws, type == 0 ? F_K + ct * 8 + rb : F_Q + rb * 8 + ct, tag);
  const int br = !(x & 1), rb3 = 4 * (x >> 1) + (idx & 3), cb = idx >> 2;
  const unsigned short* wo = br ? wob : woa;
  unsigned pfs = 0;
  auto warm_wo = [&]() {
    pf_line(pfs, (const char*)wo + idx * 65536 + lw * 16384 + lane * 128);
    pf_line(pfs, (const char*)wo + idx * 65536 + lw * 16384 + 8192 + lane * 128);
  };
  if (lw == 1) *(AS3 unsigned*)(lds0 + P2_ZB + 128 + lane * 4) = lane < 32 ? 0x3f803f80u : 0u;
  WAIT_BAR(8);
  if (br) {
    const int head = 4 * x + (idx & 7), tp = idx >> 3;
    poll(ws, lane < 8 ? F_K + (head >> 1) * 8 + lane : F_Q + (2 * tp + (lane & 1)) * 8 + (head >> 2), tag);
    STAMP(512, 12);
    chunk(4);
    u32x4 kvp[2][8], fq[4][4];
    float zs[8];
    auto ldq = [&](int e) {
#pragma unroll
      for (int j = 0; j < 4; ++j) fq[e][j] = ld16(sws, WS_QA + ((16 * tp + 4 * lw + e) * 32 + head) * 4096 + j * 1024 + vl);
    };
#pragma unroll
    for (int e = 0; e < 2; ++e)
#pragma unroll
      for (int p = 0; p < 8; ++p) kvp[e][p] = ld16(sws, WS_KVP + ((head * 8 + p) * 512 + (2 * lw + e) * 64 + lane) * 16);
#pragma unroll
    for (int p = 0; p < 8; ++p) zs[p] = ld4(sws, WS_ZP + ((head * 8 + p) * 64 + lane) * 4);
    ldq(0); ldq(1);
    asm volatile("s_barrier" ::: "memory");
#pragma unroll
    for (int e = 0; e < 2; ++e) {
#pragma unroll
      for (int p = 0; p < 8; ++p) PIN(kvp[e][p]);
#define F4(v) __builtin_bit_cast(f32x4, v)
      const f32x4 sm = ((F4(kvp[e][0]) + F4(kvp[e][1])) + (F4(kvp[e][2]) + F4(kvp[e][3]))) + ((F4(kvp[e][4]) + F4(kvp[e][5])) + (F4(kvp[e][6]) + F4(kvp[e][7])));
#undef F4
      *(AS3 u32x2*)(lds0 + P2_KVF + lw * 1024 + vl + e * 8) = u32x2{pk2(sm[0], sm[1]), pk2(sm[2], sm[3])};
    }
    {
#pragma unroll
      for (int p = 0; p < 8; ++p) PIN(zs[p]);
      const float z = ((zs[0] + zs[1]) + (zs[2] + zs[3])) + ((zs[4] + zs[5]) + (zs[6] + zs[7]));
      if (lw == 0) *(AS3 unsigned short*)(lds0 + P2_ZB + lane * 2) = (unsigned short)(pk2(z, z) & 0xffffu);
    }
    chunk(5);
    ldq(2); ldq(3);
    const int row = lane & 15, kg = lane >> 4;
    const bool on = (((row >> 2) ^ kg) & 1) == 0;
    const unsigned za = lds0 + P2_ZB + ((on && (row & 3) == 0) ? (kg >> 1) * 32 : (on && (row & 3) == 1) ? 128 : 256);
    const unsigned ka = lds0 + P2_KVF + vl;
    auto otile = [&](int e) {
#define BF(v) __builtin_bit_cast(bf16x8, v)
      f32x16 np = MFMA32(lds_r128(ka), BF(fq[e][0]), f32x16{});        f32x4 dp = MFMA16(lds_r128(za), BF(fq[e][0]), f32x4{});
      np = MFMA32(lds_r128(ka + 1024), BF(fq[e][1]), np);              dp = MFMA16(lds_r128(za + 16), BF(fq[e][1]), dp);
      f32x16 nm = MFMA32(lds_r128(ka + 2048), BF(fq[e][2]), f32x16{}); f32x4 dm = MFMA16(lds_r128(za + 64), BF(fq[e][2]), f32x4{});
      nm = MFMA32(lds_r128(ka + 3072), BF(fq[e][3]), nm);              dm = MFMA16(lds_r128(za + 80), BF(fq[e][3]), dm);
#undef BF
      const float a = __builtin_amdgcn_rcpf(dp[1]), b = __builtin_amdgcn_rcpf(dm[1]);
      const float r = __builtin_amdgcn_rcpf(dp[0] * a + dm[0] * b + 1e-6f);
      const f32x16 o = np * (a * r) + nm * (b * r);
      const unsigned p = WS_OA + ((16 * tp + 4 * lw + e) * 32 + head) * 2048 + vl;
      st16(sws, p, pack8<0>(o)); st16(sws, p + 1024, pack8<1>(o));
    };
    WAIT_BAR(16);
#pragma unroll
    for (int j = 0; j < 4; ++j) { PIN(fq[0][j]); PIN(fq[1][j]); }
    chunk(6);
    otile(0); otile(1);
    WAIT_BAR(12);
#pragma unroll
    for (int j = 0; j < 4; ++j) { PIN(fq[2][j]); PIN(fq[3][j]); }
    chunk(7);
    otile(2); otile(3);
    WAIT_BAR(0);
    if (lead) post(ws, F_OA + tp * 32 + head, tag);
    warm_wo();
  } else {
    chunk(4); warm_wo(); WAIT_BAR(10);
    chunk(5); WAIT_BAR(10);
    chunk(6); WAIT_BAR(8);
    chunk(7);
    if (blockIdx.x == 1 && lw == 1) {
      poll(ws, lane, tag); poll(ws, 64 + lane, tag); poll(ws, 128 + lane, tag);
      if (lane == 0) post(ws, F_EPOCH, tag);
    }
    WAIT_BAR(8);
  }
  const rsrc_t sw = mk_rsrc(wo, 1024u * 2048u);
  unsigned wv[4];
#pragma unroll
  for (int j = 0; j < 4; ++j) {
    const int k = 4 * (4 * lw + j) + (lane >> 4), c = (lane & 15) ^ (((lane >> 4) & 3) << 2);
    wv[j] = k * 2048 + (cb * 128 + c * 8) * 2 - j * 1024;
  }
  const unsigned osrc = (br ? WS_OB : WS_OA) + (rb3 * 4 + lw) * 65536;
  auto wst = [&](int t) {
    const unsigned sow = t * 64 * 2048;
    set_m0(lds0 + p3_wslot(t) + lw * 4096);
    dma16<0>(wv[0], sw, sow); dma16<1024>(wv[1], sw, sow); dma16<2048>(wv[2], sw, sow); dma16<3072>(wv[3], sw, sow);
  };
  auto ost = [&](int t) { dma4k(lds0 + P3_O + (t & 3) * 16384 + lw * 4096, vl, sws, osrc + t * 4096); };
  wst(0); wst(1); wst(2); wst(3);
  if (br) asm volatile("s_barrier" ::: "memory"); else WAIT_BAR(16);
  wst(4); wst(5);
  if (!br && lw == 0) poll(ws, F_OA + (rb3 >> 2) * 32 + (lane & 31), tag);
  asm volatile("s_barrier" ::: "memory");
  asm volatile("s_barrier" ::: "memory");
  asm volatile("s_barrier" ::: "memory");
  if (lead) post(ws, F_OB + x * 32 + idx, tag);
  STAMP(512, 13);
  if (br && lw == 0) poll(ws, F_OB + (rb3 >> 1) * 32 + (rb3 & 1) * 16 + (lane & 15), tag);
  asm volatile("s_barrier" ::: "memory");
  STAMP(512, 14);
  ost(0); ost(1); ost(2); ost(3);
  WAIT_BAR(12);
  PIN(pfs);
  WAIT_BAR(8);  ost(4); wst(6);
  WAIT_BAR(12); ost(5); wst(7);
  WAIT_BAR(16); ost(6); wst(8);
  WAIT_BAR(20); ost(7); wst(9);
  WAIT_BAR(20); ost(8); wst(10);
  WAIT_BAR(20); ost(9); wst(11);
  WAIT_BAR(20); ost(10); wst(12);
  WAIT_BAR(20); ost(11); wst(13);
  WAIT_BAR(20); ost(12); wst(14);
  WAIT_BAR(20); ost(13); wst(15);
  WAIT_BAR(20); ost(14);
  WAIT_BAR(16); ost(15);
  WAIT_BAR(12);
  WAIT_BAR(4);
  WAIT_BAR(0);
}

__global__ __launch_bounds__(768, 3) void fused_fwd(const unsigned short* __restrict__ xa, const unsigned short* __restrict__ wqkv,
                                                    const unsigned short* __restrict__ wphi, const unsigned short* __restrict__ woa,
                                                    const unsigned short* __restrict__ xb, const unsigned short* __restrict__ wq,
                                                    const unsigned short* __restrict__ wk, const unsigned short* __restrict__ wv,
                                                    const unsigned short* __restrict__ wob, char* __restrict__ ws, unsigned short* __restrict__ out) {
  __shared__ __attribute__((aligned(1024))) char smem[LDS_BYTES];
  const unsigned lds0 = (unsigned)(uintptr_t)(AS3 char*)smem;
  const int tid = threadIdx.x, lane = tid & 63, w = __builtin_amdgcn_readfirstlane(tid >> 6);
  const int b = blockIdx.x, x = b & 7, idx = b >> 3;
  const int rb = 4 * ((x >> 1) & 1) + (idx & 3);
  const int ct = idx < 16 ? 8 * (x >> 2) + 4 * (x & 1) + (idx >> 2) : 4 * (x >> 2) + ((idx >> 2) & 3);
  const int type = idx < 16 ? 0 : (x & 1) ? 1 : 2 + (ct & 1);
  const rsrc_t sws = mk_rsrc(ws, WS_END);
  STAMP(0, 0);
  if (tid == 0) ((u64*)(ws + (64u << 20)))[(size_t)blockIdx.x * 32 + 30] = (unsigned)__builtin_amdgcn_s_getreg(20 | (3 << 11)) & 0xfu;
  if (w >= 8) {
    helper_program(lds0, type >= 2 ? xb : xa, type >= 2 ? wq : wqkv, type == 3 ? wv : type == 2 ? wk : wphi, woa, wob, ws, sws, type, rb, ct, x, idx, w - 8, lane);
    return;
  }
  unsigned pfs = 0;
  if (type == 0 && !(x & 1) && w == 0) pf_line(pfs, (const char*)(lane < 32 ? wv : wk) + (idx * 32 + (lane & 31)) * 128);
  const char* pfp = nullptr;
  if (tid < 32 * (8 - PF0)) {
    const int id = ((x & 1) * 16 + idx) * (32 * (8 - PF0)) + tid, st = id / 1536, r = id - st * 1536, k = (PF0 + st + BROT) & 15;
    pfp = r < 1024 ? (const char*)xb + (((x >> 1) & 1) * 1024 + r) * 2048 + k * 128 : (const char*)wq + (64 * k + ((r - 1024) >> 3)) * 2048 + (x >> 2) * 1024 + (r & 7) * 128;
  }
  if (type == 0)      p1_compute<0>(lds0, pfp, pfs, ws, sws, rb, ct, w, lane);
  else if (type == 1) p1_compute<1>(lds0, pfp, pfs, ws, sws, rb, ct, w, lane);
  else if (type == 2) p1_compute<2>(lds0, pfp, pfs, ws, sws, rb, ct, w, lane);
  else                p1_compute<3>(lds0, pfp, pfs, ws, sws, rb, ct, w, lane);

  const int kh = w >> 2, r32 = lane & 31, h = lane >> 5;
  {
    const bf16x8 sel = row_sum_sel(lane);
    LDS_BAR();
    STAMP(0, 4);
    const unsigned qa = lds0 + P2_Q + (2 * (w & 3) + kh) * 2048 + lane * 16;
    bf16x8 q[2][2];
    q[0][0] = lds_r128(qa);        q[0][1] = lds_r128(qa + 1024);
    q[1][0] = lds_r128(qa ^ 2048); q[1][1] = lds_r128((qa ^ 2048) + 1024);
    f32x16 O[2]; f32x4 L[2];
    unsigned tv = 0;
    attn_pass_fast(lds0, q, sel, kh, lane, O, L, tv);
    TSOUT();
    STAMP(0, 5);
    const unsigned xs = lds0 + P2_XO + w * 4096 + lane * 16, xr = lds0 + P2_XO + (w ^ 4) * 4096 + lane * 16;
#pragma unroll
    for (int g = 0; g < 4; ++g) *(AS3 f32x4*)(xs + g * 1024) = f32x4{O[1][4 * g], O[1][4 * g + 1], O[1][4 * g + 2], O[1][4 * g + 3]};
    *(AS3 float*)(lds0 + P2_XL + w * 256 + lane * 4) = L[1][0];
    LDS_BAR();
#pragma unroll
    for (int g = 0; g < 4; ++g) {
      const f32x4 o = *(AS3 f32x4*)(xr + g * 1024);
      O[0][4 * g] += o[0]; O[0][4 * g + 1] += o[1]; O[0][4 * g + 2] += o[2]; O[0][4 * g + 3] += o[3];
    }
    L[0][0] += *(AS3 float*)(lds0 + P2_XL + (w ^ 4) * 256 + lane * 4);
    bool bad = !(L[0][0] < 1.8446744e19f);
#pragma unroll
    for (int i = 0; i < 16; ++i) bad |= !(fabsf(O[0][i]) < 3.0e38f);
    if (__any(bad)) attn_pass_safe(sws, q[0][0], q[0][1], sel, lane, O[0], L[0]);
    const float r = __builtin_amdgcn_rcpf(xhalf_sum(L[0][0]));
    O[0] *= r;
    const unsigned p = WS_OB + ((8 * x + (idx >> 2)) * 32 + 8 * (idx & 3) + 2 * (w & 3) + kh) * 2048 + lane * 16;
    st16(sws, p, pack8<0>(O[0])); st16(sws, p + 1024, pack8<1>(O[0]));
    WAIT_BAR(0);
    PIN(pfs);
    STAMP(0, 6);
  }
  const int br = !(x & 1), rb3 = 4 * (x >> 1) + (idx & 3), cb = idx >> 2;
  const int wm = w & 1, wn = (w >> 1) & 1;
  const int q4 = (lane & 15) >> 2, p4 = lane & 3, chalf = (lane >> 4) & 1;
  const unsigned oa = wm * 8192 + kh * (4096 + 2048) + lane * 16;
  unsigned wa[2];
#pragma unroll
  for (int ns = 0; ns < 2; ++ns)
    wa[ns] = (kh * 32 + 4 * h + q4) * 256 + ((((wn * 2 + ns) ^ q4) << 2) | (chalf * 2 + (p4 >> 1))) * 16 + (p4 & 1) * 8;
  f32x16 acc[2][2] = {};
  bf16x8 xf[2][2], wfr[2][2];
  auto rdx = [&](int set, unsigned buf, int ks) {
#pragma unroll
    for (int ms = 0; ms < 2; ++ms) xf[set][ms] = lds_r128(buf + (oa ^ (ms * 4096)) + ks * 1024);
  };
  auto rdw = [&](int set, unsigned buf, int ks, int ns) { wfr[set][ns] = lds_tr2(buf + wa[ns] + ks * 4096, buf + wa[ns] + ks * 4096 + 2048); };
  auto mm1 = [&](int set, int ms, int ns) { acc[ms][ns] = MFMA32(wfr[set][ns], xf[set][ms], acc[ms][ns]); };
  auto sub = [&](int set, bool rd, unsigned ob, unsigned wb, int ks) {
    SBAR(); mm1(set, 0, 0); SBAR(); if (rd) rdx(set ^ 1, ob, ks);
    SBAR(); mm1(set, 1, 0); SBAR(); if (rd) rdw(set ^ 1, wb, ks, 0);
    SBAR(); mm1(set, 0, 1); SBAR(); if (rd) rdw(set ^ 1, wb, ks, 1);
    SBAR(); mm1(set, 1, 1); SBAR();
  };
  asm volatile("s_barrier" ::: "memory");
  LDS_BAR();
  STAMP(0, 7);
  unsigned ob = lds0 + P3_O, wb = lds0 + p3_wslot(0);
  rdx(0, ob, 0); rdw(0, wb, 0, 0); rdw(0, wb, 0, 1);
  LGKM0();
#pragma unroll 1
  for (int t = 0; t < 15; ++t) {
    sub(0, true, ob, wb, 1);
    ob = (ob == lds0 + P3_O + 3 * 16384) ? lds0 + P3_O : ob + 16384;
    wb = (wb == lds0 + 147456) ? lds0 + 65536 : wb + 16384;
    LDS_BAR();
    sub(1, true, ob, wb, 0);
  }
  sub(0, true, ob, wb, 1);
  sub(1, false, ob, wb, 0);
  STAMP(0, 8);
  const unsigned xs = lds0 + 65536 + w * 8192 + lane * 16, xr = lds0 + 65536 + (w ^ 4) * 8192 + lane * 16;
#pragma unroll
  for (int ns = 0; ns < 2; ++ns)
#pragma unroll
    for (int g = 0; g < 4; ++g) {
      const f32x16& a = acc[1][ns];
      *(AS3 f32x4*)(xs + ns * 4096 + g * 1024) = f32x4{a[4 * g], a[4 * g + 1], a[4 * g + 2], a[4 * g + 3]};
    }
  LDS_BAR();
  const unsigned img = lds0 + w * 4096;
#pragma unroll
  for (int ns = 0; ns < 2; ++ns)
#pragma unroll
    for (int g = 0; g < 4; ++g) {
      const f32x16& a = acc[0][ns];
      const f32x4 o = *(AS3 f32x4*)(xr + ns * 4096 + g * 1024);
      *(AS3 u32x2*)(img + r32 * 128 + (((ns * 4 + g) ^ (r32 & 7)) << 4) + h * 8) =
          u32x2{pk2(a[4 * g] + o[0], a[4 * g + 1] + o[1]), pk2(a[4 * g + 2] + o[2], a[4 * g + 3] + o[3])};
    }
  asm volatile("" ::: "memory");
  const int tok0 = rb3 * 128 + wm * 64 + kh * 32;
  const rsrc_t so = mk_rsrc(out, 2048u * 4096u);
  const unsigned op = tok0 * 4096 + br * 2048 + cb * 256 + wn * 128;
#pragma unroll
  for (int i = 0; i < 4; ++i) {
    const int row = 8 * i + (lane >> 3), ch = lane & 7;
    const u32x4 v = *(AS3 u32x4*)(img + row * 128 + ((ch ^ (row & 7)) << 4));
    st16(so, op + row * 4096 + ch * 16, v);
  }
  STAMP(0, 9);
  if (threadIdx.x == 0 && blockIdx.x == 128) {
    asm volatile("s_waitcnt vmcnt(0)" ::: "memory");
    volatile u64* sp_ = (volatile u64*)(ws + (64u << 20)) + (size_t)blockIdx.x * 32;
    u64 d = 10000 + (sp_[2 * 1 + 1] - sp_[2 * 0 + 1]) * 10;
    d = d < 100000 ? d : 100000;
    const u64 t0 = sp_[1];
    for (int it = 0; it < 1000000 && __builtin_amdgcn_s_memrealtime() - t0 < d; ++it) __builtin_amdgcn_s_sleep(1);
  }
}

extern "C" void kernel_launch(void* const* d_in, const int* in_sizes, int n_in, void* d_out, int out_size, void* d_ws, size_t ws_size,
                              hipStream_t stream) {
  (void)in_sizes; (void)n_in; (void)out_size; (void)ws_size;
  typedef const unsigned short* cp;
  fused_fwd<<<256, 768, 0, stream>>>((cp)d_in[0], (cp)d_in[1], (cp)d_in[2], (cp)d_in[3], (cp)d_in[4], (cp)d_in[5], (cp)d_in[6], (cp)d_in[7],
                                     (cp)d_in[8], (char*)d_ws, (unsigned short*)d_out);
}
```

```cpp
#include <hip/hip_runtime.h>
#include <hip/hip_bf16.h>
#include <stdint.h>
#pragma clang diagnostic ignored "-Wint-to-pointer-cast"

typedef __bf16   bf16x8 __attribute__((ext_vector_type(8)));
typedef __bf16   bf16x2 __attribute__((ext_vector_type(2)));
typedef short    s16x4  __attribute__((ext_vector_type(4)));
typedef short    s16x8  __attribute__((ext_vector_type(8)));
typedef float    f32x16 __attribute__((ext_vector_type(16)));
typedef float    f32x4  __attribute__((ext_vector_type(4)));
typedef float    f32x2  __attribute__((ext_vector_type(2)));
typedef unsigned u32x4  __attribute__((ext_vector_type(4)));
typedef unsigned u32x2  __attribute__((ext_vector_type(2)));
typedef unsigned long long u64;
typedef __amdgpu_buffer_rsrc_t rsrc_t;

#define AS1 __attribute__((address_space(1)))
#define AS3 __attribute__((address_space(3)))
#define DI __device__ __forceinline__
#define MFMA32(a, b, c) __builtin_amdgcn_mfma_f32_32x32x16_bf16((a), (b), (c), 0, 0, 0)
#define MFMA16(a, b, c) __builtin_amdgcn_mfma_f32_16x16x32_bf16((a), (b), (c), 0, 0, 0)
#define SBAR() __builtin_amdgcn_sched_barrier(0)
#define WAIT_BAR(N) asm volatile("s_waitcnt vmcnt(" #N ") lgkmcnt(0)\n\ts_barrier" ::: "memory")
#define LGKM0() __builtin_amdgcn_s_waitcnt(0xC07F)
#define LDS_BAR() do { asm volatile("" ::: "memory"); LGKM0(); asm volatile("s_barrier" ::: "memory"); } while (0)

#define STAMP(th, i) do {} while (0)
#define TS(i) do {} while (0)
#define TSOUT() do {} while (0)
#define ZREF 0
#define EPF 1
#define EMV 1
#define PFB 0
#define PF0 3
#define BROT (8 * (((x >> 1) ^ (x >> 2)) & 1))
#define NTX 0
#define NTW 0
#define KROT (2 * x)
#define MAPV 1
constexpr int D = 1024;
constexpr float LOG2E = 1.4426950408889634f;
constexpr float QSCALE = 0.17677669529663687f * LOG2E;

constexpr unsigned WS_QA  = 0;
constexpr unsigned WS_QB  = WS_QA + (8u << 20);
constexpr unsigned WS_OA  = WS_QB + (4u << 20);
constexpr unsigned WS_OB  = WS_OA + (4u << 20);
constexpr unsigned WS_KV  = WS_OB + (4u << 20);
constexpr unsigned WS_KVP = WS_KV + (256u << 10);
constexpr unsigned WS_ZP  = WS_KVP + (2u << 20);
constexpr unsigned WS_FLG = WS_ZP + (64u << 10);
constexpr unsigned WS_END = WS_FLG + (16u << 10);
constexpr int F_K = 0, F_Q = 128, F_OA = 256, F_OB = 512, F_KV = 768, F_EPOCH = 1024;

DI unsigned pk2(float lo, float hi) {
  f32x2 v = {lo, hi};
  return __builtin_bit_cast(unsigned, __builtin_convertvector(v, bf16x2));
}
template <int SS> DI bf16x8 pack8(const f32x16& x) {
  u32x4 p = {pk2(x[8 * SS + 0], x[8 * SS + 1]), pk2(x[8 * SS + 2], x[8 * SS + 3]), pk2(x[8 * SS + 4], x[8 * SS + 5]), pk2(x[8 * SS + 6], x[8 * SS + 7])};
  return __builtin_bit_cast(bf16x8, p);
}
DI bf16x8 lds_r128(unsigned a) { return *(const AS3 bf16x8*)a; }
DI bf16x8 lds_tr2(unsigned a0, unsigned a1) {
  s16x4 lo = __builtin_amdgcn_ds_read_tr16_b64_v4i16((AS3 s16x4*)a0);
  s16x4 hi = __builtin_amdgcn_ds_read_tr16_b64_v4i16((AS3 s16x4*)a1);
  s16x8 r = __builtin_shufflevector(lo, hi, 0, 1, 2, 3, 4, 5, 6, 7);
  return __builtin_bit_cast(bf16x8, r);
}
DI rsrc_t mk_rsrc(const void* p, unsigned bytes) { return __builtin_amdgcn_make_buffer_rsrc((void*)p, (short)0, (int)bytes, 0x00020000); }
DI void set_m0(unsigned v) { asm volatile("s_mov_b32 m0, %0\n\ts_nop 0" ::"s"(v) : "memory"); }
template <int IMM> DI void dma16(unsigned voff, rsrc_t srd, unsigned soff) {
  asm volatile("buffer_load_dwordx4 %0, %1, %2 offen offset:%3 lds" ::"v"(voff), "s"(srd), "s"(soff), "i"(IMM) : "memory");
}
template <int IMM> DI void dma16n(unsigned voff, rsrc_t srd, unsigned soff) {
  asm volatile("buffer_load_dwordx4 %0, %1, %2 offen offset:%3 nt lds" ::"v"(voff), "s"(srd), "s"(soff), "i"(IMM) : "memory");
}
#define DMAX dma16
#define DMAW dma16
template <int IMM> DI void dma16d(unsigned voff, rsrc_t srd, unsigned soff) {
  asm volatile("buffer_load_dwordx4 %0, %1, %2 offen offset:%3 sc1 lds" ::"v"(voff), "s"(srd), "s"(soff), "i"(IMM) : "memory");
}
DI void dma4k(unsigned lds, unsigned voff, rsrc_t srd, unsigned soff) {
  set_m0(lds); dma16d<0>(voff, srd, soff); dma16d<1024>(voff, srd, soff); dma16d<2048>(voff, srd, soff); dma16d<3072>(voff, srd, soff);
}
template <class T> DI void st16(rsrc_t r, unsigned voff, T v) { __builtin_amdgcn_raw_buffer_store_b128(__builtin_bit_cast(u32x4, v), r, (int)voff, 0, 16); }
DI void st8(rsrc_t r, unsigned voff, u32x2 v) { __builtin_amdgcn_raw_buffer_store_b64(v, r, (int)voff, 0, 16); }
DI void st4(rsrc_t r, unsigned voff, float v) { __builtin_amdgcn_raw_buffer_store_b32(__float_as_uint(v), r, (int)voff, 0, 16); }
DI u32x4 ld16(rsrc_t r, unsigned voff) { return __builtin_amdgcn_raw_buffer_load_b128(r, (int)voff, 0, 16); }
DI float ld4(rsrc_t r, unsigned voff) { return __uint_as_float(__builtin_amdgcn_raw_buffer_load_b32(r, (int)voff, 0, 16)); }
DI void post(char* ws, int f, u64 tag) { __hip_atomic_store((AS1 u64*)(ws + WS_FLG) + f, tag, __ATOMIC_RELAXED, __HIP_MEMORY_SCOPE_AGENT); }
DI void poll(char* ws, int f, u64 tag) {
#pragma unroll 1
  for (int it = 0; it < (1 << 17); ++it) {
    const u64 v = __hip_atomic_load((AS1 u64*)(ws + WS_FLG) + f, __ATOMIC_RELAXED, __HIP_MEMORY_SCOPE_AGENT);
    if (__all(v == tag)) break;
    __builtin_amdgcn_s_sleep(2);
  }
  asm volatile("" ::: "memory");
}
DI void pf_line(unsigned& sink, const void* p) { asm volatile("global_load_dword %0, %1, off" : "+v"(sink) : "v"(p) : "memory"); }
#define PIN(x) asm volatile("" : "+v"(x))
DI float xhalf_max(float v) {
  auto r = __builtin_amdgcn_permlane32_swap(__float_as_uint(v), __float_as_uint(v), false, false);
  return fmaxf(__uint_as_float(r[0]), __uint_as_float(r[1]));
}
DI float xhalf_min(float v) {
  auto r = __builtin_amdgcn_permlane32_swap(__float_as_uint(v), __float_as_uint(v), false, false);
  return fminf(__uint_as_float(r[0]), __uint_as_float(r[1]));
}
DI float xhalf_sum(float v) {
  auto r = __builtin_amdgcn_permlane32_swap(__float_as_uint(v), __float_as_uint(v), false, false);
  return __uint_as_float(r[0]) + __uint_as_float(r[1]);
}
DI float max16(const f32x16& x) {
  float a = fmaxf(fmaxf(x[0], x[1]), x[2]), b = fmaxf(fmaxf(x[3], x[4]), x[5]);
  a = fmaxf(fmaxf(a, x[6]), x[7]);   b = fmaxf(fmaxf(b, x[8]), x[9]);
  a = fmaxf(fmaxf(a, x[10]), x[11]); b = fmaxf(fmaxf(b, x[12]), x[13]);
  a = fmaxf(fmaxf(a, x[14]), x[15]);
  return fmaxf(a, b);
}
DI float min16(const f32x16& x) {
  float a = fminf(fminf(x[0], x[1]), x[2]), b = fminf(fminf(x[3], x[4]), x[5]);
  a = fminf(fminf(a, x[6]), x[7]);   b = fminf(fminf(b, x[8]), x[9]);
  a = fminf(fminf(a, x[10]), x[11]); b = fminf(fminf(b, x[12]), x[13]);
  a = fminf(fminf(a, x[14]), x[15]);
  return fminf(a, b);
}
template <bool NORM> DI void feat_map(const f32x16& p, f32x16& ep, f32x16& em) {
  const float mx = xhalf_max(max16(p)), mn = xhalf_min(min16(p));
  const f32x2 a = {-mx * LOG2E, -mx * LOG2E}, b = {mn * LOG2E, mn * LOG2E}, c = {LOG2E, LOG2E};
  f32x2 sp = {0.f, 0.f}, sm = {0.f, 0.f};
#pragma unroll
  for (int i = 0; i < 16; i += 2) {
    const f32x2 x = {p[i], p[i + 1]};
    const f32x2 u = __builtin_elementwise_fma(x, c, a), v = __builtin_elementwise_fma(x, -c, b);
    const f32x2 eu = {__builtin_amdgcn_exp2f(u[0]), __builtin_amdgcn_exp2f(u[1])}, ev = {__builtin_amdgcn_exp2f(v[0]), __builtin_amdgcn_exp2f(v[1])};
    ep[i] = eu[0]; ep[i + 1] = eu[1]; em[i] = ev[0]; em[i + 1] = ev[1];
    if (i == 0) { sp = eu; sm = ev; } else { sp += eu; sm += ev; }
  }
  if (NORM) {
    const float rp = __builtin_amdgcn_rcpf(xhalf_sum(sp[0] + sp[1])), rm = __builtin_amdgcn_rcpf(xhalf_sum(sm[0] + sm[1]));
    ep *= rp; em *= rm;
  }
}
DI bf16x8 row_sum_sel(int lane) {
  const unsigned u = ((lane & 11) == 0 && (((lane >> 2) ^ (lane >> 4)) & 1) == 0) ? 0x3f803f80u : 0u;
  return __builtin_bit_cast(bf16x8, u32x4{u, u, u, u});
}

constexpr int LDS_BYTES = 163840;
constexpr int P1_WOFF = 32768, P1_EOFF = 49152, P1_STAGE = 53248, P1_ZRED = 3 * P1_STAGE, P1_CNT = P1_ZRED + 2048, P1_GO = P1_CNT + 16, P1_PHI = 65536;
constexpr int P2_Q = 131072, P2_KVF = 147456, P2_ZB = 151552;
constexpr int P2_XO = 32768, P2_XL = 0;
constexpr int P3_O = 0;
DI constexpr int p3_wslot(int t) { return 65536 + ((t + 2) % 6) * 16384; }

template <int TYPE>
DI void p1_compute(unsigned lds0, const char* pfp, unsigned& pfs, char* __restrict__ ws, rsrc_t sws, int rb, int ct, int w, int lane) {
  const int tid = threadIdx.x;
  const int wm = w & 3, wn = w >> 2, r32 = lane & 31, h = lane >> 5;
  if (tid < 8) *(AS3 unsigned*)(lds0 + P1_CNT + tid * 4) = 0u;

  const int xrow = wm * 64 + r32;
  const unsigned xa = xrow * 128 + ((h ^ ((xrow >> 1) & 7)) << 4);
  const int q4 = (lane & 15) >> 2, p4 = lane & 3, chalf = (lane >> 4) & 1;
  unsigned wa[2];
#pragma unroll
  for (int ns = 0; ns < 2; ++ns)
    wa[ns] = P1_WOFF + (8 * h + q4) * 256 + ((((wn * 2 + ns) ^ q4) << 2) | (chalf * 2 + (p4 >> 1))) * 16 + (p4 & 1) * 8;
  const int erow = 64 * (ct >> 1) + 16 * wm + (lane & 15);
  const unsigned exa = erow * 128 + (((lane >> 4) ^ ((erow >> 1) & 7)) << 4);
  const unsigned ewa = P1_EOFF + (8 * (lane >> 4) + q4) * 64 + wn * 32 + p4 * 8;

  f32x16 acc[2][2] = {};
  f32x4 eacc = {};
  bf16x8 xf[2][2], wfr[2][2], ex[2], ew[2];
  auto rdx = [&](int set, unsigned buf, int ks) {
#pragma unroll
    for (int ms = 0; ms < 2; ++ms) xf[set][ms] = lds_r128(buf + ((xa ^ (ks * 32)) + ms * 4096));
  };
  auto rdw = [&](int set, unsigned buf, int ks, int ns) { wfr[set][ns] = lds_tr2(buf + wa[ns] + ks * 4096, buf + wa[ns] + ks * 4096 + 1024); };
  auto mm1 = [&](int set, int ms, int ns) {
    if (TYPE == 0 && ns == 1) acc[ms][1] = MFMA32(xf[set][ms], wfr[set][1], acc[ms][1]);
    else                      acc[ms][ns] = MFMA32(wfr[set][ns], xf[set][ms], acc[ms][ns]);
  };
  auto erd = [&](unsigned buf, int kk) {
    if (TYPE >= 2) { ex[kk] = lds_r128(buf + (exa ^ (kk * 64))); ew[kk] = lds_tr2(buf + ewa + kk * 2048, buf + ewa + kk * 2048 + 256); }
  };
  auto emm = [&](int kk) {
    if (TYPE == 2) eacc = MFMA16(ew[kk], ex[kk], eacc);
    if (TYPE == 3) eacc = MFMA16(ex[kk], ew[kk], eacc);
  };
  auto sub = [&](int set, unsigned buf, int ks) {
    SBAR(); mm1(set, 0, 0); SBAR(); rdx(set ^ 1, buf, ks);
    SBAR(); mm1(set, 1, 0); SBAR(); rdw(set ^ 1, buf, ks, 0);
    SBAR(); mm1(set, 0, 1); SBAR(); rdw(set ^ 1, buf, ks, 1);
    SBAR(); mm1(set, 1, 1); SBAR();
  };
  if (PFB == 1 && TYPE < 2 && pfp) pf_line(pfs, pfp);
  LDS_BAR();
  if (PFB == 2 && TYPE < 2 && pfp) pf_line(pfs, pfp);
  STAMP(0, 1);
  unsigned cur = lds0, nxt = lds0 + P1_STAGE;
  rdx(0, cur, 0); rdw(0, cur, 0, 0); rdw(0, cur, 0, 1);
  LGKM0();
#pragma unroll 1
  for (int t = 0; t < 16; ++t) {
    sub(0, cur, 1); erd(cur, 0);
    sub(1, cur, 2); erd(cur, 1);
    sub(0, cur, 3); emm(0); emm(1); SBAR();
    LDS_BAR();
    sub(1, nxt, 0);
    cur = nxt; nxt = (nxt == lds0 + 2 * P1_STAGE) ? lds0 : nxt + P1_STAGE;
  }
  STAMP(0, 2);
  bf16x8 wf[2][2];
  if constexpr (TYPE <= 1) {
    const unsigned a = lds0 + P1_PHI + (TYPE == 0 ? wn : 2 * wn) * 2048 + (4 * h + q4) * 64 + chalf * 32 + p4 * 8;
#pragma unroll
    for (int n = 0; n <= TYPE; ++n)
#pragma unroll
      for (int s = 0; s < 2; ++s) wf[n][s] = lds_tr2(a + n * 2048 + s * 1024, a + n * 2048 + s * 1024 + 512);
  }

  if (TYPE >= 2) {
#pragma unroll
    for (int ms = 0; ms < 2; ++ms)
#pragma unroll
      for (int ns = 0; ns < 2; ++ns) {
        const f32x16 v = acc[ms][ns] * QSCALE;
        const unsigned p = WS_QB + ((rb * 8 + wm * 2 + ms) * 32 + ct * 4 + wn * 2 + ns) * 2048 + lane * 16;
        st16(sws, p, pack8<0>(v)); st16(sws, p + 1024, pack8<1>(v));
      }
    const int g = lane >> 4, tok0 = rb * 256 + 64 * (ct >> 1) + 16 * wm;
    unsigned p = WS_KV + (tok0 >> 5) * 4096;
    if (TYPE == 3) p += 2048 + (wm & 1) * 1024 + (16 * wn + (lane & 15) + 32 * (g & 1)) * 16 + (g >> 1) * 8;
    else           p += wn * 1024 + (16 * (wm & 1) + (lane & 15) + 32 * (g & 1)) * 16 + (g >> 1) * 8;
    st8(sws, p, u32x2{pk2(eacc[0], eacc[1]), pk2(eacc[2], eacc[3])});
    WAIT_BAR(0);
  } else if (TYPE == 1) {
    f32x16 pq[2][2];
#pragma unroll
    for (int ms = 0; ms < 2; ++ms)
#pragma unroll
      for (int ns = 0; ns < 2; ++ns) {
        pq[ms][ns] = MFMA32(wf[ns][0], pack8<0>(acc[ms][ns]), f32x16{});
        pq[ms][ns] = MFMA32(wf[ns][1], pack8<1>(acc[ms][ns]), pq[ms][ns]);
      }
#pragma unroll
    for (int ms = 0; ms < 2; ++ms)
#pragma unroll
      for (int ns = 0; ns < 2; ++ns) {
        f32x16 ep, em;
        feat_map<false>(pq[ms][ns], ep, em);
        const unsigned p = WS_QA + ((rb * 8 + wm * 2 + ms) * 32 + ct * 4 + wn * 2 + ns) * 4096 + lane * 16;
        st16(sws, p, pack8<0>(ep)); st16(sws, p + 1024, pack8<1>(ep)); st16(sws, p + 2048, pack8<0>(em)); st16(sws, p + 3072, pack8<1>(em));
      }
  } else {
    const unsigned img = lds0 + w * 8192;
    const bf16x8 sel = row_sum_sel(lane);
    f32x16 pk[2], kv[2] = {};
    f32x4 zz[2] = {};
    bf16x8 vf[2][2], af[2][2];
#pragma unroll
    for (int ms = 0; ms < 2; ++ms) {
      pk[ms] = MFMA32(wf[0][0], pack8<0>(acc[ms][0]), f32x16{});
      pk[ms] = MFMA32(wf[0][1], pack8<1>(acc[ms][0]), pk[ms]);
    }
#pragma unroll
    for (int ms = 0; ms < 2; ++ms) { vf[ms][0] = pack8<0>(acc[ms][1]); vf[ms][1] = pack8<1>(acc[ms][1]); PIN(vf[ms][0]); PIN(vf[ms][1]); }
    float off[4];
#pragma unroll
    for (int ms = 0; ms < 2; ++ms) { off[2 * ms] = -LOG2E * xhalf_max(max16(pk[ms])); off[2 * ms + 1] = LOG2E * xhalf_min(min16(pk[ms])); }
    const unsigned wr = img + 4096 + r32 * 64, sw = (r32 >> 1) & 7;
    const int t0 = 4 * h + q4, u8 = 4 * chalf + p4;
    const unsigned rd0 = img + 4096 + t0 * 64 + ((u8 ^ (t0 >> 1)) << 3), rd1 = img + 4096 + (t0 + 8) * 64 + ((u8 ^ ((t0 >> 1) + 4)) << 3);
    auto put = [&](int sg) {
#pragma unroll
      for (int g = 0; g < 4; ++g) *(AS3 f32x4*)(img + sg * 4096 + g * 1024 + lane * 16) = f32x4{kv[sg][4 * g], kv[sg][4 * g + 1], kv[sg][4 * g + 2], kv[sg][4 * g + 3]};
    };
    f32x16 e[2];
    float rs[2];
    auto mm = [&](int u, int j) {
      if (j & 1) zz[u & 1] = MFMA16(sel, af[u & 1][j >> 1], zz[u & 1]); else kv[u & 1] = MFMA32(af[u & 1][j >> 1], vf[u >> 1][j >> 1], kv[u & 1]);
    };
    auto exps = [&](int u, int mu) {
      const f32x16& p = pk[u >> 1];
      const float c = (u & 1) ? -LOG2E : LOG2E;
      f32x2 sm;
#pragma unroll
      for (int i = 0; i < 16; i += 2) {
        if (mu >= 0 && i >= 8) { SBAR(); mm(mu, (i - 8) >> 1); SBAR(); }
        const f32x2 v = __builtin_elementwise_fma(f32x2{p[i], p[i + 1]}, f32x2{c, c}, f32x2{off[u], off[u]});
        const f32x2 ev = {__builtin_amdgcn_exp2f(v[0]), __builtin_amdgcn_exp2f(v[1])};
        e[u & 1][i] = ev[0]; e[u & 1][i + 1] = ev[1];
        if (i == 0) sm = ev; else sm += ev;
      }
      rs[u & 1] = __builtin_amdgcn_rcpf(xhalf_sum(sm[0] + sm[1]));
    };
    auto rest = [&](int u) {
      const f32x16 n = e[u & 1] * rs[u & 1];
#pragma unroll
      for (int g = 0; g < 4; ++g) *(AS3 u32x2*)(wr + (u & 1) * 2048 + (((2 * g + h) ^ sw) << 3)) = u32x2{pk2(n[4 * g], n[4 * g + 1]), pk2(n[4 * g + 2], n[4 * g + 3])};
      asm volatile("" ::: "memory");
#pragma unroll
      for (int s = 0; s < 2; ++s) af[u & 1][s] = lds_tr2(rd0 + (u & 1) * 2048 + s * 1024, rd1 + (u & 1) * 2048 + s * 1024);
      asm volatile("" ::: "memory");
      SBAR();
    };
    exps(0, -1); exps(1, -1); rest(0);
    exps(2, 0);  rest(1);
    exps(3, 1);  rest(2);
    rest(3);
    mm(2, 0); mm(2, 2); mm(3, 0); mm(3, 2); SBAR(); mm(2, 1); mm(2, 3); mm(3, 1); mm(3, 3);
    put(0); put(1);
    if (lane < 32) {
      const unsigned a = lds0 + P1_ZRED + w * 256 + ((lane >> 2) & 1) * 64 + (lane >> 3) * 16 + (lane & 3) * 4;
      *(AS3 float*)a = zz[0][0]; *(AS3 float*)(a + 128) = zz[1][0];
    }
  }
  STAMP(0, 3);
}

DI void attn_pass_fast(unsigned lds0, const bf16x8 (&q)[2][2], const bf16x8 sel, int kh, int lane, f32x16 (&O)[2], f32x4 (&L)[2], unsigned& tv) {
  TS(0);
  typedef const AS3 char* lp;
  const lp l0 = (lp)(lds0 + lane * 16), la = l0 + kh * 16384;
#define RD(p) (*(const AS3 bf16x8*)(p))
  if (kh) __builtin_amdgcn_s_setprio(1);
  bf16x8 ka = RD(l0 + 98304), kb = RD(l0 + 98304 + 1024);
  f32x16 negm[2], S0, S1;
#pragma unroll
  for (int r = 0; r < 2; ++r) {
    f32x16 z = {};
    z = MFMA32(ka, q[r][0], z); z = MFMA32(kb, q[r][1], z);
    const float m = xhalf_max(max16(z));
#pragma unroll
    for (int i = 0; i < 16; ++i) negm[r][i] = -m;
    PIN(negm[r]);
    O[r] = f32x16{}; L[r] = f32x4{};
  }
  ka = RD(la + 98304); kb = RD(la + 98304 + 1024);
  bf16x8 va = RD(la + 98304 + 2048), vb = bf16x8{};
  S0 = MFMA32(ka, q[0][0], negm[0]); S0 = MFMA32(kb, q[0][1], S0);
  u32x4 p0 = {}, p1 = {};
  LGKM0();
#define EX4(SC, i) do { SC[i] = __builtin_amdgcn_exp2f(SC[i]); SC[i + 1] = __builtin_amdgcn_exp2f(SC[i + 1]); \
    SC[i + 2] = __builtin_amdgcn_exp2f(SC[i + 2]); SC[i + 3] = __builtin_amdgcn_exp2f(SC[i + 3]); PIN(SC); } while (0)
#define CV4(P, SC, i) do { P[0] = pk2(SC[i], SC[i + 1]); P[1] = pk2(SC[i + 2], SC[i + 3]); P[2] = pk2(SC[i + 4], SC[i + 5]); P[3] = pk2(SC[i + 6], SC[i + 7]); PIN(P); } while (0)
#define BF(P) __builtin_bit_cast(bf16x8, P)
#define TILE(TA, TN) do { \
    SBAR(); S1 = MFMA32(ka, q[1][0], negm[1]);  SBAR(); ka = RD(TN);          EX4(S0, 0);  \
    SBAR(); O[1] = MFMA32(vb, BF(p1), O[1]);    SBAR(); vb = RD((TA) + 3072); EX4(S0, 4);  \
    SBAR(); L[1] = MFMA16(sel, BF(p1), L[1]);   SBAR(); CV4(p0, S0, 0); \
    SBAR(); S1 = MFMA32(kb, q[1][1], S1);       SBAR(); kb = RD((TN) + 1024); EX4(S0, 8);  \
    SBAR(); O[0] = MFMA32(va, BF(p0), O[0]);    SBAR(); EX4(S0, 12); \
    SBAR(); L[0] = MFMA16(sel, BF(p0), L[0]);   SBAR(); CV4(p1, S0, 8); \
    SBAR(); S0 = MFMA32(ka, q[0][0], negm[0]);  SBAR(); EX4(S1, 0);  \
    SBAR(); O[0] = MFMA32(vb, BF(p1), O[0]);    SBAR(); EX4(S1, 4);  \
    SBAR(); L[0] = MFMA16(sel, BF(p1), L[0]);   SBAR(); CV4(p0, S1, 0); \
    SBAR(); S0 = MFMA32(kb, q[0][1], S0);       SBAR(); EX4(S1, 8);  \
    SBAR(); O[1] = MFMA32(va, BF(p0), O[1]);    SBAR(); va = RD((TN) + 2048); EX4(S1, 12); \
    SBAR(); L[1] = MFMA16(sel, BF(p0), L[1]);   SBAR(); CV4(p1, S1, 8); } while (0)
  TS(1);
#pragma unroll 1
  for (int c = 0; c < 8; ++c) {
    const lp cb = la + ((3 - c) & 3) * 32768, nb = la + ((2 - c) & 3) * 32768;
    TILE(cb, cb + 4096);
    TILE(cb + 4096, cb + 8192);
    SBAR(); if (kh) __builtin_amdgcn_s_setprio(0);
    TILE(cb + 8192, cb + 12288);
    SBAR(); TS(2 + 2 * c);
    asm volatile("s_waitcnt vmcnt(0)\n\ts_barrier" ::: "memory"); TS(3 + 2 * c);
    if (kh) __builtin_amdgcn_s_setprio(1);
    TILE(cb + 12288, nb);
  }
  SBAR();
  O[1] = MFMA32(vb, BF(p1), O[1]); L[1] = MFMA16(sel, BF(p1), L[1]);
  __builtin_amdgcn_s_setprio(0);
  TS(18);
#undef TILE
#undef EX4
#undef CV4
#undef BF
#undef RD
}

DI void attn_pass_safe(rsrc_t sws, const bf16x8 q0, const bf16x8 q1, const bf16x8 sel, int lane, f32x16& O, f32x4& L) {
  O = f32x16{}; L = f32x4{};
  float m = -INFINITY;
#pragma unroll 1
  for (int t = 0; t < 64; ++t) {
    bf16x8 a[4];
#pragma unroll
    for (int j = 0; j < 4; ++j) a[j] = __builtin_bit_cast(bf16x8, ld16(sws, WS_KV + t * 4096 + j * 1024 + lane * 16));
    f32x16 Sc = {};
    Sc = MFMA32(a[0], q0, Sc); Sc = MFMA32(a[1], q1, Sc);
    const float mn = fmaxf(m, xhalf_max(max16(Sc)));
    const float al = __builtin_amdgcn_exp2f(m - mn);
    m = mn;
#pragma unroll
    for (int r = 0; r < 16; ++r) { O[r] *= al; Sc[r] = __builtin_amdgcn_exp2f(Sc[r] - mn); }
    L *= al;
    const bf16x8 p0 = pack8<0>(Sc), p1 = pack8<1>(Sc);
    O = MFMA32(a[2], p0, O); L = MFMA16(sel, p0, L);
    O = MFMA32(a[3], p1, O); L = MFMA16(sel, p1, L);
  }
}

DI void helper_program(unsigned lds0, const unsigned short* __restrict__ X, const unsigned short* __restrict__ W, const unsigned short* __restrict__ E,
                       const unsigned short* __restrict__ woa, const unsigned short* __restrict__ wob, char* __restrict__ ws, rsrc_t sws,
                       int type, int rb, int ct, int x, int idx, int lw, int lane) {
  __builtin_amdgcn_s_setprio(3);
  const u64 e0 = __hip_atomic_load((AS1 u64*)(ws + WS_FLG) + F_EPOCH, __ATOMIC_RELAXED, __HIP_MEMORY_SCOPE_AGENT);
  const unsigned vl = lane * 16;
  {
    const bool ext = type >= 2;
    const unsigned ldw = ext ? 2048u : 6144u;
    const rsrc_t sx = mk_rsrc(X + (size_t)rb * 256 * D, 256u * 2048u);
    const rsrc_t sw = mk_rsrc(W, 1024u * ldw);
    const rsrc_t se = mk_rsrc(E, 1024u * 64u);
    unsigned xv[8], wv[4];
#pragma unroll
    for (int j = 0; j < 8; ++j) {
      const int row = 8 * (8 * lw + j) + (lane >> 3), c = (lane & 7) ^ ((row >> 1) & 7);
      xv[j] = row * 2048 + c * 16 - (j & 3) * 1024;
    }
#pragma unroll
    for (int j = 0; j < 4; ++j) {
      const int k = 4 * (4 * lw + j) + (lane >> 4), c = (lane & 15) ^ (((lane >> 4) & 3) << 2);
      const int col = (type == 0) ? 1024 + ((c >> 2) & 1) * 1024 + (2 * ct + (c >> 3)) * 32 + (c & 3) * 8 : ct * 128 + c * 8;
      wv[j] = k * ldw + col * 2 - j * 1024;
    }
    auto stage = [&](int t0, unsigned buf) {
      const int t = (t0 + (ext ? BROT : KROT)) & 15;
      const unsigned sox = t * 128, sow = t * 64 * ldw;
      set_m0(buf + P1_WOFF + lw * 4096);
      DMAW<0>(wv[0], sw, sow); DMAW<1024>(wv[1], sw, sow); DMAW<2048>(wv[2], sw, sow); DMAW<3072>(wv[3], sw, sow);
      if (ext ? (NTX & 2) : (NTX & 1)) {
        set_m0(buf + lw * 8192);
        dma16n<0>(xv[0], sx, sox); dma16n<1024>(xv[1], sx, sox); dma16n<2048>(xv[2], sx, sox); dma16n<3072>(xv[3], sx, sox);
        set_m0(buf + lw * 8192 + 4096);
        dma16n<0>(xv[4], sx, sox); dma16n<1024>(xv[5], sx, sox); dma16n<2048>(xv[6], sx, sox); dma16n<3072>(xv[7], sx, sox);
      } else {
        set_m0(buf + lw * 8192);
        dma16<0>(xv[0], sx, sox); dma16<1024>(xv[1], sx, sox); dma16<2048>(xv[2], sx, sox); dma16<3072>(xv[3], sx, sox);
        set_m0(buf + lw * 8192 + 4096);
        dma16<0>(xv[4], sx, sox); dma16<1024>(xv[5], sx, sox); dma16<2048>(xv[6], sx, sox); dma16<3072>(xv[7], sx, sox);
      }
      if (ext) { set_m0(buf + P1_EOFF + lw * 1024); dma16<0>(vl, se, t * 4096 + lw * 1024); }
    };
    stage(0, lds0); stage(1, lds0 + P1_STAGE);
    if (ext) WAIT_BAR(13); else WAIT_BAR(12);
    stage(2, lds0 + 2 * P1_STAGE);
    unsigned buf = lds0;
#pragma unroll 1
    for (int t = 0; t < 13; ++t) {
      if (ext) WAIT_BAR(13); else WAIT_BAR(12);
      stage(t + 3, buf);
      buf = (buf == lds0 + 2 * P1_STAGE) ? lds0 : buf + P1_STAGE;
    }
    if (ext) WAIT_BAR(13); else WAIT_BAR(12);
    if (!ext && (type == 1 || lw < 2)) {
      set_m0(lds0 + P1_PHI + lw * 2048);
      const unsigned so = ((type == 0 ? 2 * ct : 4 * ct) + lw) * 2048;
      dma16<0>(vl, se, so); dma16<1024>(vl, se, so);
    }
    WAIT_BAR(0);
    WAIT_BAR(0);
  }
  const u64 tag = ((u64)(unsigned)__builtin_amdgcn_readfirstlane((unsigned)(e0 >> 32)) << 32 | (unsigned)__builtin_amdgcn_readfirstlane((unsigned)e0))
                  * 0x5851F42D4C957F2Dull + 0x14057B7EF767814Full;
  const bool lead = (lw == 0) && (lane == 0);
  if (type >= 2) { WAIT_BAR(0); if (lead) post(ws, F_KV + rb * 16 + ct, tag); }
  auto chunk = [&](int c) {
    const unsigned l = lds0 + ((3 - c) & 3) * 32768 + lw * 8192, so = WS_KV + ((c + x) & 7) * 32768 + lw * 8192;
    dma4k(l, vl, sws, so); dma4k(l + 4096, vl, sws, so + 4096);
  };
  STAMP(512, 10);
  if (lw == 0) {
    poll(ws, F_KV + x * 16 + (lane & 7), tag);
    __hip_atomic_store((AS3 unsigned*)(lds0 + P1_GO), 1u, __ATOMIC_RELAXED, __HIP_MEMORY_SCOPE_WORKGROUP);
  } else {
    while (__hip_atomic_load((AS3 unsigned*)(lds0 + P1_GO), __ATOMIC_RELAXED, __HIP_MEMORY_SCOPE_WORKGROUP) == 0u) __builtin_amdgcn_s_sleep(1);
    asm volatile("" ::: "memory");
  }
  STAMP(512, 11);
  dma4k(lds0 + P2_Q + lw * 4096, vl, sws, WS_QB + ((8 * x + (idx >> 2)) * 32 + 8 * (idx & 3)) * 2048 + lw * 4096);
  chunk(0);
  WAIT_BAR(0);
  poll(ws, F_KV + (lane >> 3) * 16 + (lane & 7), tag);
  chunk(1);
  if (type == 0) {
#pragma unroll
    for (int j = 0; j < 4; ++j) {
      const unsigned a = lds0 + (lw >> 1) * 32768 + ((lw & 1) * 4 + j) * 1024 + vl;
      const f32x4 sm = (*(AS3 f32x4*)a + *(AS3 f32x4*)(a + 8192)) + (*(AS3 f32x4*)(a + 16384) + *(AS3 f32x4*)(a + 24576));
      st16(sws, WS_KVP + ((2 * ct + (lw >> 1)) * 8 + rb) * 8192 + ((lw & 1) * 4 + j) * 1024 + vl, sm);
    }
    if (lw < 2) {
      const unsigned a = lds0 + P1_ZRED + lw * 1024 + lane * 4;
      st4(sws, WS_ZP + (((2 * ct + lw) * 8 + rb) * 64 + lane) * 4, (*(AS3 float*)a + *(AS3 float*)(a + 256)) + (*(AS3 float*)(a + 512) + *(AS3 float*)(a + 768)));
    }
  }
  WAIT_BAR(0);
  chunk(2); chunk(3);
  if (lead && type < 2) post(ws, type == 0 ? F_K + ct * 8 + rb : F_Q + rb * 8 + ct, tag);
  const int br = !(x & 1), rb3 = 4 * (x >> 1) + (idx & 3), cb = idx >> 2;
  const unsigned short* wo = br ? wob : woa;
  unsigned pfs = 0;
  auto warm_wo = [&]() {
    pf_line(pfs, (const char*)wo + idx * 65536 + lw * 16384 + lane * 128);
    pf_line(pfs, (const char*)wo + idx * 65536 + lw * 16384 + 8192 + lane * 128);
  };
  if (lw == 1) *(AS3 unsigned*)(lds0 + P2_ZB + 128 + lane * 4) = lane < 32 ? 0x3f803f80u : 0u;
  WAIT_BAR(8);
  if (br) {
    const int head = 4 * x + (idx & 7), tp = idx >> 3;
    poll(ws, lane < 8 ? F_K + (head >> 1) * 8 + lane : F_Q + (2 * tp + (lane & 1)) * 8 + (head >> 2), tag);
    STAMP(512, 12);
    chunk(4);
    u32x4 kvp[2][8], fq[4][4];
    float zs[8];
    auto ldq = [&](int e) {
#pragma unroll
      for (int j = 0; j < 4; ++j) fq[e][j] = ld16(sws, WS_QA + ((16 * tp + 4 * lw + e) * 32 + head) * 4096 + j * 1024 + vl);
    };
#pragma unroll
    for (int e = 0; e < 2; ++e)
#pragma unroll
      for (int p = 0; p < 8; ++p) kvp[e][p] = ld16(sws, WS_KVP + ((head * 8 + p) * 512 + (2 * lw + e) * 64 + lane) * 16);
#pragma unroll
    for (int p = 0; p < 8; ++p) zs[p] = ld4(sws, WS_ZP + ((head * 8 + p) * 64 + lane) * 4);
    ldq(0); ldq(1);
    asm volatile("s_barrier" ::: "memory");
#pragma unroll
    for (int e = 0; e < 2; ++e) {
#pragma unroll
      for (int p = 0; p < 8; ++p) PIN(kvp[e][p]);
#define F4(v) __builtin_bit_cast(f32x4, v)
      const f32x4 sm = ((F4(kvp[e][0]) + F4(kvp[e][1])) + (F4(kvp[e][2]) + F4(kvp[e][3]))) + ((F4(kvp[e][4]) + F4(kvp[e][5])) + (F4(kvp[e][6]) + F4(kvp[e][7])));
#undef F4
      *(AS3 u32x2*)(lds0 + P2_KVF + lw * 1024 + vl + e * 8) = u32x2{pk2(sm[0], sm[1]), pk2(sm[2], sm[3])};
    }
    {
#pragma unroll
      for (int p = 0; p < 8; ++p) PIN(zs[p]);
      const float z = ((zs[0] + zs[1]) + (zs[2] + zs[3])) + ((zs[4] + zs[5]) + (zs[6] + zs[7]));
      if (lw == 0) *(AS3 unsigned short*)(lds0 + P2_ZB + lane * 2) = (unsigned short)(pk2(z, z) & 0xffffu);
    }
    chunk(5);
    ldq(2); ldq(3);
    const int row = lane & 15, kg = lane >> 4;
    const bool on = (((row >> 2) ^ kg) & 1) == 0;
    const unsigned za = lds0 + P2_ZB + ((on && (row & 3) == 0) ? (kg >> 1) * 32 : (on && (row & 3) == 1) ? 128 : 256);
    const unsigned ka = lds0 + P2_KVF + vl;
    auto otile = [&](int e) {
#define BF(v) __builtin_bit_cast(bf16x8, v)
      f32x16 np = MFMA32(lds_r128(ka), BF(fq[e][0]), f32x16{});        f32x4 dp = MFMA16(lds_r128(za), BF(fq[e][0]), f32x4{});
      np = MFMA32(lds_r128(ka + 1024), BF(fq[e][1]), np);              dp = MFMA16(lds_r128(za + 16), BF(fq[e][1]), dp);
      f32x16 nm = MFMA32(lds_r128(ka + 2048), BF(fq[e][2]), f32x16{}); f32x4 dm = MFMA16(lds_r128(za + 64), BF(fq[e][2]), f32x4{});
      nm = MFMA32(lds_r128(ka + 3072), BF(fq[e][3]), nm);              dm = MFMA16(lds_r128(za + 80), BF(fq[e][3]), dm);
#undef BF
      const float a = __builtin_amdgcn_rcpf(dp[1]), b = __builtin_amdgcn_rcpf(dm[1]);
      const float r = __builtin_amdgcn_rcpf(dp[0] * a + dm[0] * b + 1e-6f);
      const f32x16 o = np * (a * r) + nm * (b * r);
      const unsigned p = WS_OA + ((16 * tp + 4 * lw + e) * 32 + head) * 2048 + vl;
      st16(sws, p, pack8<0>(o)); st16(sws, p + 1024, pack8<1>(o));
    };
    WAIT_BAR(16);
#pragma unroll
    for (int j = 0; j < 4; ++j) { PIN(fq[0][j]); PIN(fq[1][j]); }
    chunk(6);
    otile(0); otile(1);
    WAIT_BAR(12);
#pragma unroll
    for (int j = 0; j < 4; ++j) { PIN(fq[2][j]); PIN(fq[3][j]); }
    chunk(7);
    otile(2); otile(3);
    WAIT_BAR(0);
    if (lead) post(ws, F_OA + tp * 32 + head, tag);
    warm_wo();
  } else {
    chunk(4); warm_wo(); WAIT_BAR(10);
    chunk(5); WAIT_BAR(10);
    chunk(6); WAIT_BAR(8);
    chunk(7);
    if (blockIdx.x == 1 && lw == 1) {
      poll(ws, lane, tag); poll(ws, 64 + lane, tag); poll(ws, 128 + lane, tag);
      if (lane == 0) post(ws, F_EPOCH, tag);
    }
    WAIT_BAR(8);
  }
  const rsrc_t sw = mk_rsrc(wo, 1024u * 2048u);
  unsigned wv[4];
#pragma unroll
  for (int j = 0; j < 4; ++j) {
    const int k = 4 * (4 * lw + j) + (lane >> 4), c = (lane & 15) ^ (((lane >> 4) & 3) << 2);
    wv[j] = k * 2048 + (cb * 128 + c * 8) * 2 - j * 1024;
  }
  const unsigned osrc = (br ? WS_OB : WS_OA) + (rb3 * 4 + lw) * 65536;
  auto wst = [&](int t) {
    const unsigned sow = t * 64 * 2048;
    set_m0(lds0 + p3_wslot(t) + lw * 4096);
    dma16<0>(wv[0], sw, sow); dma16<1024>(wv[1], sw, sow); dma16<2048>(wv[2], sw, sow); dma16<3072>(wv[3], sw, sow);
  };
  auto ost = [&](int t) { dma4k(lds0 + P3_O + (t & 3) * 16384 + lw * 4096, vl, sws, osrc + t * 4096); };
  wst(0); wst(1); wst(2); wst(3);
  if (br) asm volatile("s_barrier" ::: "memory"); else WAIT_BAR(16);
  wst(4); wst(5);
  if (!br && lw == 0) poll(ws, F_OA + (rb3 >> 2) * 32 + (lane & 31), tag);
  asm volatile("s_barrier" ::: "memory");
  asm volatile("s_barrier" ::: "memory");
  asm volatile("s_barrier" ::: "memory");
  if (lead) post(ws, F_OB + x * 32 + idx, tag);
  STAMP(512, 13);
  if (br && lw == 0) poll(ws, F_OB + (rb3 >> 1) * 32 + (rb3 & 1) * 16 + (lane & 15), tag);
  asm volatile("s_barrier" ::: "memory");
  STAMP(512, 14);
  ost(0); ost(1); ost(2); ost(3);
  WAIT_BAR(12);
  PIN(pfs);
  WAIT_BAR(8);  ost(4); wst(6);
  WAIT_BAR(12); ost(5); wst(7);
  WAIT_BAR(16); ost(6); wst(8);
  WAIT_BAR(20); ost(7); wst(9);
  WAIT_BAR(20); ost(8); wst(10);
  WAIT_BAR(20); ost(9); wst(11);
  WAIT_BAR(20); ost(10); wst(12);
  WAIT_BAR(20); ost(11); wst(13);
  WAIT_BAR(20); ost(12); wst(14);
  WAIT_BAR(20); ost(13); wst(15);
  WAIT_BAR(20); ost(14);
  WAIT_BAR(16); ost(15);
  WAIT_BAR(12);
  WAIT_BAR(4);
  WAIT_BAR(0);
}

__global__ __launch_bounds__(768, 3) void fused_fwd(const unsigned short* __restrict__ xa, const unsigned short* __restrict__ wqkv,
                                                    const unsigned short* __restrict__ wphi, const unsigned short* __restrict__ woa,
                                                    const unsigned short* __restrict__ xb, const unsigned short* __restrict__ wq,
                                                    const unsigned short* __restrict__ wk, const unsigned short* __restrict__ wv,
                                                    const unsigned short* __restrict__ wob, char* __restrict__ ws, unsigned short* __restrict__ out) {
  __shared__ __attribute__((aligned(1024))) char smem[LDS_BYTES];
  const unsigned lds0 = (unsigned)(uintptr_t)(AS3 char*)smem;
  const int tid = threadIdx.x, lane = tid & 63, w = __builtin_amdgcn_readfirstlane(tid >> 6);
  const int b = blockIdx.x, x = b & 7, idx = b >> 3;
  const int rb = 4 * ((x >> 1) & 1) + (idx & 3);
  const int ct = idx < 16 ? 8 * (x >> 2) + 4 * (x & 1) + (idx >> 2) : 4 * (x >> 2) + ((idx >> 2) & 3);
  const int type = idx < 16 ? 0 : (x & 1) ? 1 : 2 + (ct & 1);
  const rsrc_t sws = mk_rsrc(ws, WS_END);
  STAMP(0, 0);
  if (w >= 8) {
    helper_program(lds0, type >= 2 ? xb : xa, type >= 2 ? wq : wqkv, type == 3 ? wv : type == 2 ? wk : wphi, woa, wob, ws, sws, type, rb, ct, x, idx, w - 8, lane);
    return;
  }
  unsigned pfs = 0;
  if (type == 0 && !(x & 1) && w == 0) pf_line(pfs, (const char*)(lane < 32 ? wv : wk) + (idx * 32 + (lane & 31)) * 128);
  const char* pfp = nullptr;
  if (tid < 32 * (8 - PF0)) {
    const int id = ((x & 1) * 16 + idx) * (32 * (8 - PF0)) + tid, st = id / 1536, r = id - st * 1536, k = (PF0 + st + BROT) & 15;
    pfp = r < 1024 ? (const char*)xb + (((x >> 1) & 1) * 1024 + r) * 2048 + k * 128 : (const char*)wq + (64 * k + ((r - 1024) >> 3)) * 2048 + (x >> 2) * 1024 + (r & 7) * 128;
  }
  if (type == 0)      p1_compute<0>(lds0, pfp, pfs, ws, sws, rb, ct, w, lane);
  else if (type == 1) p1_compute<1>(lds0, pfp, pfs, ws, sws, rb, ct, w, lane);
  else if (type == 2) p1_compute<2>(lds0, pfp, pfs, ws, sws, rb, ct, w, lane);
  else                p1_compute<3>(lds0, pfp, pfs, ws, sws, rb, ct, w, lane);

  const int kh = w >> 2, r32 = lane & 31, h = lane >> 5;
  {
    const bf16x8 sel = row_sum_sel(lane);
    LDS_BAR();
    STAMP(0, 4);
    const unsigned qa = lds0 + P2_Q + (2 * (w & 3) + kh) * 2048 + lane * 16;
    bf16x8 q[2][2];
    q[0][0] = lds_r128(qa);        q[0][1] = lds_r128(qa + 1024);
    q[1][0] = lds_r128(qa ^ 2048); q[1][1] = lds_r128((qa ^ 2048) + 1024);
    f32x16 O[2]; f32x4 L[2];
    unsigned tv = 0;
    attn_pass_fast(lds0, q, sel, kh, lane, O, L, tv);
    TSOUT();
    STAMP(0, 5);
    const unsigned xs = lds0 + P2_XO + w * 4096 + lane * 16, xr = lds0 + P2_XO + (w ^ 4) * 4096 + lane * 16;
#pragma unroll
    for (int g = 0; g < 4; ++g) *(AS3 f32x4*)(xs + g * 1024) = f32x4{O[1][4 * g], O[1][4 * g + 1], O[1][4 * g + 2], O[1][4 * g + 3]};
    *(AS3 float*)(lds0 + P2_XL + w * 256 + lane * 4) = L[1][0];
    LDS_BAR();
#pragma unroll
    for (int g = 0; g < 4; ++g) {
      const f32x4 o = *(AS3 f32x4*)(xr + g * 1024);
      O[0][4 * g] += o[0]; O[0][4 * g + 1] += o[1]; O[0][4 * g + 2] += o[2]; O[0][4 * g + 3] += o[3];
    }
    L[0][0] += *(AS3 float*)(lds0 + P2_XL + (w ^ 4) * 256 + lane * 4);
    bool bad = !(L[0][0] < 1.8446744e19f);
#pragma unroll
    for (int i = 0; i < 16; ++i) bad |= !(fabsf(O[0][i]) < 3.0e38f);
    if (__any(bad)) attn_pass_safe(sws, q[0][0], q[0][1], sel, lane, O[0], L[0]);
    const float r = __builtin_amdgcn_rcpf(xhalf_sum(L[0][0]));
    O[0] *= r;
    const unsigned p = WS_OB + ((8 * x + (idx >> 2)) * 32 + 8 * (idx & 3) + 2 * (w & 3) + kh) * 2048 + lane * 16;
    st16(sws, p, pack8<0>(O[0])); st16(sws, p + 1024, pack8<1>(O[0]));
    WAIT_BAR(0);
    PIN(pfs);
    STAMP(0, 6);
  }
  const int br = !(x & 1), rb3 = 4 * (x >> 1) + (idx & 3), cb = idx >> 2;
  const int wm = w & 1, wn = (w >> 1) & 1;
  const int q4 = (lane & 15) >> 2, p4 = lane & 3, chalf = (lane >> 4) & 1;
  const unsigned oa = wm * 8192 + kh * (4096 + 2048) + lane * 16;
  unsigned wa[2];
#pragma unroll
  for (int ns = 0; ns < 2; ++ns)
    wa[ns] = (kh * 32 + 4 * h + q4) * 256 + ((((wn * 2 + ns) ^ q4) << 2) | (chalf * 2 + (p4 >> 1))) * 16 + (p4 & 1) * 8;
  f32x16 acc[2][2] = {};
  bf16x8 xf[2][2], wfr[2][2];
  auto rdx = [&](int set, unsigned buf, int ks) {
#pragma unroll
    for (int ms = 0; ms < 2; ++ms) xf[set][ms] = lds_r128(buf + (oa ^ (ms * 4096)) + ks * 1024);
  };
  auto rdw = [&](int set, unsigned buf, int ks, int ns) { wfr[set][ns] = lds_tr2(buf + wa[ns] + ks * 4096, buf + wa[ns] + ks * 4096 + 2048); };
  auto mm1 = [&](int set, int ms, int ns) { acc[ms][ns] = MFMA32(wfr[set][ns], xf[set][ms], acc[ms][ns]); };
  auto sub = [&](int set, bool rd, unsigned ob, unsigned wb, int ks) {
    SBAR(); mm1(set, 0, 0); SBAR(); if (rd) rdx(set ^ 1, ob, ks);
    SBAR(); mm1(set, 1, 0); SBAR(); if (rd) rdw(set ^ 1, wb, ks, 0);
    SBAR(); mm1(set, 0, 1); SBAR(); if (rd) rdw(set ^ 1, wb, ks, 1);
    SBAR(); mm1(set, 1, 1); SBAR();
  };
  asm volatile("s_barrier" ::: "memory");
  LDS_BAR();
  STAMP(0, 7);
  unsigned ob = lds0 + P3_O, wb = lds0 + p3_wslot(0);
  rdx(0, ob, 0); rdw(0, wb, 0, 0); rdw(0, wb, 0, 1);
  LGKM0();
#pragma unroll 1
  for (int t = 0; t < 15; ++t) {
    sub(0, true, ob, wb, 1);
    ob = (ob == lds0 + P3_O + 3 * 16384) ? lds0 + P3_O : ob + 16384;
    wb = (wb == lds0 + 147456) ? lds0 + 65536 : wb + 16384;
    LDS_BAR();
    sub(1, true, ob, wb, 0);
  }
  sub(0, true, ob, wb, 1);
  sub(1, false, ob, wb, 0);
  STAMP(0, 8);
  const unsigned xs = lds0 + 65536 + w * 8192 + lane * 16, xr = lds0 + 65536 + (w ^ 4) * 8192 + lane * 16;
#pragma unroll
  for (int ns = 0; ns < 2; ++ns)
#pragma unroll
    for (int g = 0; g < 4; ++g) {
      const f32x16& a = acc[1][ns];
      *(AS3 f32x4*)(xs + ns * 4096 + g * 1024) = f32x4{a[4 * g], a[4 * g + 1], a[4 * g + 2], a[4 * g + 3]};
    }
  LDS_BAR();
  const unsigned img = lds0 + w * 4096;
#pragma unroll
  for (int ns = 0; ns < 2; ++ns)
#pragma unroll
    for (int g = 0; g < 4; ++g) {
      const f32x16& a = acc[0][ns];
      const f32x4 o = *(AS3 f32x4*)(xr + ns * 4096 + g * 1024);
      *(AS3 u32x2*)(img + r32 * 128 + (((ns * 4 + g) ^ (r32 & 7)) << 4) + h * 8) =
          u32x2{pk2(a[4 * g] + o[0], a[4 * g + 1] + o[1]), pk2(a[4 * g + 2] + o[2], a[4 * g + 3] + o[3])};
    }
  asm volatile("" ::: "memory");
  const int tok0 = rb3 * 128 + wm * 64 + kh * 32;
  const rsrc_t so = mk_rsrc(out, 2048u * 4096u);
  const unsigned op = tok0 * 4096 + br * 2048 + cb * 256 + wn * 128;
#pragma unroll
  for (int i = 0; i < 4; ++i) {
    const int row = 8 * i + (lane >> 3), ch = lane & 7;
    const u32x4 v = *(AS3 u32x4*)(img + row * 128 + ((ch ^ (row & 7)) << 4));
    st16(so, op + row * 4096 + ch * 16, v);
  }
  STAMP(0, 9);
}

extern "C" void kernel_launch(void* const* d_in, const int* in_sizes, int n_in, void* d_out, int out_size, void* d_ws, size_t ws_size,
                              hipStream_t stream) {
  (void)in_sizes; (void)n_in; (void)out_size; (void)ws_size;
  typedef const unsigned short* cp;
  fused_fwd<<<256, 768, 0, stream>>>((cp)d_in[0], (cp)d_in[1], (cp)d_in[2], (cp)d_in[3], (cp)d_in[4], (cp)d_in[5], (cp)d_in[6], (cp)d_in[7],
                                     (cp)d_in[8], (char*)d_ws, (unsigned short*)d_out);
}
```

```cpp
#include <hip/hip_runtime.h>
#include <hip/hip_bf16.h>
#include <stdint.h>
#pragma clang diagnostic ignored "-Wint-to-pointer-cast"

typedef __bf16   bf16x8 __attribute__((ext_vector_type(8)));
typedef __bf16   bf16x16 __attribute__((ext_vector_type(16)));
typedef __bf16   bf16x2 __attribute__((ext_vector_type(2)));
typedef short    s16x4  __attribute__((ext_vector_type(4)));
typedef short    s16x8  __attribute__((ext_vector_type(8)));
typedef float    f32x16 __attribute__((ext_vector_type(16)));
typedef float    f32x4  __attribute__((ext_vector_type(4)));
typedef float    f32x2  __attribute__((ext_vector_type(2)));
typedef unsigned u32x4  __attribute__((ext_vector_type(4)));
typedef unsigned u32x2  __attribute__((ext_vector_type(2)));
typedef unsigned long long u64;
typedef __amdgpu_buffer_rsrc_t rsrc_t;

#define AS1 __attribute__((address_space(1)))
#define AS3 __attribute__((address_space(3)))
#define DI __device__ __forceinline__
#define MFMA32(a, b, c) __builtin_amdgcn_mfma_f32_32x32x16_bf16((a), (b), (c), 0, 0, 0)
#define MFMA16(a, b, c) __builtin_amdgcn_mfma_f32_16x16x32_bf16((a), (b), (c), 0, 0, 0)
#define SM32(a, b, c) __builtin_amdgcn_smfmac_f32_32x32x32_bf16((a), (b), (c), odd, 0, 0)
#define SM16(a, b, c) __builtin_amdgcn_smfmac_f32_16x16x64_bf16((a), (b), (c), odd, 0, 0)
#define LO(S) __builtin_bit_cast(bf16x16, __builtin_shufflevector(S, S, 0, 1, 2, 3, 4, 5, 6, 7))
#define HI(S) __builtin_bit_cast(bf16x16, __builtin_shufflevector(S, S, 8, 9, 10, 11, 12, 13, 14, 15))
#define SBAR() __builtin_amdgcn_sched_barrier(0)
#define WAIT_BAR(N) asm volatile("s_waitcnt vmcnt(" #N ") lgkmcnt(0)\n\ts_barrier" ::: "memory")
#define LGKM0() __builtin_amdgcn_s_waitcnt(0xC07F)
#define LDS_BAR() do { asm volatile("" ::: "memory"); LGKM0(); asm volatile("s_barrier" ::: "memory"); } while (0)

#define STAMP(th, i) do {} while (0)
#define TS(i) do {} while (0)
#define TSOUT() do {} while (0)
#define ZREF 0
#define EPF 1
#define EMV 1
#define PFB 0
#define PF0 3
#define BROT (8 * (((x >> 1) ^ (x >> 2)) & 1))
#define NTX 0
#define NTW 0
#define KROT (2 * x)
#define MAPV 1
constexpr int D = 1024;
constexpr float LOG2E = 1.4426950408889634f;
constexpr float QSCALE = 0.17677669529663687f * LOG2E;

constexpr unsigned WS_QA  = 0;
constexpr unsigned WS_QB  = WS_QA + (8u << 20);
constexpr unsigned WS_OA  = WS_QB + (4u << 20);
constexpr unsigned WS_OB  = WS_OA + (4u << 20);
constexpr unsigned WS_KV  = WS_OB + (4u << 20);
constexpr unsigned WS_KVP = WS_KV + (256u << 10);
constexpr unsigned WS_ZP  = WS_KVP + (2u << 20);
constexpr unsigned WS_FLG = WS_ZP + (64u << 10);
constexpr unsigned WS_END = WS_FLG + (16u << 10);
constexpr int F_K = 0, F_Q = 128, F_OA = 256, F_OB = 512, F_KV = 768, F_EPOCH = 1024;

DI unsigned pk2(float lo, float hi) {
  f32x2 v = {lo, hi};
  return __builtin_bit_cast(unsigned, __builtin_convertvector(v, bf16x2));
}
template <int SS> DI bf16x8 pack8(const f32x16& x) {
  u32x4 p = {pk2(x[8 * SS + 0], x[8 * SS + 1]), pk2(x[8 * SS + 2], x[8 * SS + 3]), pk2(x[8 * SS + 4], x[8 * SS + 5]), pk2(x[8 * SS + 6], x[8 * SS + 7])};
  return __builtin_bit_cast(bf16x8, p);
}
DI bf16x8 lds_r128(unsigned a) { return *(const AS3 bf16x8*)a; }
DI bf16x8 lds_tr2(unsigned a0, unsigned a1) {
  s16x4 lo = __builtin_amdgcn_ds_read_tr16_b64_v4i16((AS3 s16x4*)a0);
  s16x4 hi = __builtin_amdgcn_ds_read_tr16_b64_v4i16((AS3 s16x4*)a1);
  s16x8 r = __builtin_shufflevector(lo, hi, 0, 1, 2, 3, 4, 5, 6, 7);
  return __builtin_bit_cast(bf16x8, r);
}
DI rsrc_t mk_rsrc(const void* p, unsigned bytes) { return __builtin_amdgcn_make_buffer_rsrc((void*)p, (short)0, (int)bytes, 0x00020000); }
DI void set_m0(unsigned v) { asm volatile("s_mov_b32 m0, %0\n\ts_nop 0" ::"s"(v) : "memory"); }
template <int IMM> DI void dma16(unsigned voff, rsrc_t srd, unsigned soff) {
  asm volatile("buffer_load_dwordx4 %0, %1, %2 offen offset:%3 lds" ::"v"(voff), "s"(srd), "s"(soff), "i"(IMM) : "memory");
}
template <int IMM> DI void dma16n(unsigned voff, rsrc_t srd, unsigned soff) {
  asm volatile("buffer_load_dwordx4 %0, %1, %2 offen offset:%3 nt lds" ::"v"(voff), "s"(srd), "s"(soff), "i"(IMM) : "memory");
}
#define DMAX dma16
#define DMAW dma16
template <int IMM> DI void dma16d(unsigned voff, rsrc_t srd, unsigned soff) {
  asm volatile("buffer_load_dwordx4 %0, %1, %2 offen offset:%3 sc1 lds" ::"v"(voff), "s"(srd), "s"(soff), "i"(IMM) : "memory");
}
DI void dma4k(unsigned lds, unsigned voff, rsrc_t srd, unsigned soff) {
  set_m0(lds); dma16d<0>(voff, srd, soff); dma16d<1024>(voff, srd, soff); dma16d<2048>(voff, srd, soff); dma16d<3072>(voff, srd, soff);
}
template <class T> DI void st16(rsrc_t r, unsigned voff, T v) { __builtin_amdgcn_raw_buffer_store_b128(__builtin_bit_cast(u32x4, v), r, (int)voff, 0, 16); }
DI void st8(rsrc_t r, unsigned voff, u32x2 v) { __builtin_amdgcn_raw_buffer_store_b64(v, r, (int)voff, 0, 16); }
DI void st4(rsrc_t r, unsigned voff, float v) { __builtin_amdgcn_raw_buffer_store_b32(__float_as_uint(v), r, (int)voff, 0, 16); }
DI u32x4 ld16(rsrc_t r, unsigned voff) { return __builtin_amdgcn_raw_buffer_load_b128(r, (int)voff, 0, 16); }
DI float ld4(rsrc_t r, unsigned voff) { return __uint_as_float(__builtin_amdgcn_raw_buffer_load_b32(r, (int)voff, 0, 16)); }
DI void post(char* ws, int f, u64 tag) { __hip_atomic_store((AS1 u64*)(ws + WS_FLG) + f, tag, __ATOMIC_RELAXED, __HIP_MEMORY_SCOPE_AGENT); }
DI void poll(char* ws, int f, u64 tag) {
#pragma unroll 1
  for (int it = 0; it < (1 << 17); ++it) {
    const u64 v = __hip_atomic_load((AS1 u64*)(ws + WS_FLG) + f, __ATOMIC_RELAXED, __HIP_MEMORY_SCOPE_AGENT);
    if (__all(v == tag)) break;
    __builtin_amdgcn_s_sleep(2);
  }
  asm volatile("" ::: "memory");
}
DI void pf_line(unsigned& sink, const void* p) { asm volatile("global_load_dword %0, %1, off" : "+v"(sink) : "v"(p) : "memory"); }
#define PIN(x) asm volatile("" : "+v"(x))
DI float one(float v) { asm("" : "+v"(v)); return v; }
DI f32x4 add4(f32x4 a, f32x4 b) { return f32x4{one(a[0] + b[0]), one(a[1] + b[1]), one(a[2] + b[2]), one(a[3] + b[3])}; }
DI float xhalf_max(float v) {
  auto r = __builtin_amdgcn_permlane32_swap(__float_as_uint(v), __float_as_uint(v), false, false);
  return fmaxf(__uint_as_float(r[0]), __uint_as_float(r[1]));
}
DI float xhalf_min(float v) {
  auto r = __builtin_amdgcn_permlane32_swap(__float_as_uint(v), __float_as_uint(v), false, false);
  return fminf(__uint_as_float(r[0]), __uint_as_float(r[1]));
}
DI float xhalf_sum(float v) {
  auto r = __builtin_amdgcn_permlane32_swap(__float_as_uint(v), __float_as_uint(v), false, false);
  return __uint_as_float(r[0]) + __uint_as_float(r[1]);
}
DI float max16(const f32x16& x) {
  float a = fmaxf(fmaxf(x[0], x[1]), x[2]), b = fmaxf(fmaxf(x[3], x[4]), x[5]);
  a = fmaxf(fmaxf(a, x[6]), x[7]);   b = fmaxf(fmaxf(b, x[8]), x[9]);
  a = fmaxf(fmaxf(a, x[10]), x[11]); b = fmaxf(fmaxf(b, x[12]), x[13]);
  a = fmaxf(fmaxf(a, x[14]), x[15]);
  return fmaxf(a, b);
}
DI float min16(const f32x16& x) {
  float a = fminf(fminf(x[0], x[1]), x[2]), b = fminf(fminf(x[3], x[4]), x[5]);
  a = fminf(fminf(a, x[6]), x[7]);   b = fminf(fminf(b, x[8]), x[9]);
  a = fminf(fminf(a, x[10]), x[11]); b = fminf(fminf(b, x[12]), x[13]);
  a = fminf(fminf(a, x[14]), x[15]);
  return fminf(a, b);
}
template <bool NORM> DI void feat_map(const f32x16& p, f32x16& ep, f32x16& em) {
  const float mx = xhalf_max(max16(p)), mn = xhalf_min(min16(p));
  const f32x2 a = {-mx * LOG2E, -mx * LOG2E}, b = {mn * LOG2E, mn * LOG2E}, c = {LOG2E, LOG2E};
  f32x2 sp = {0.f, 0.f}, sm = {0.f, 0.f};
#pragma unroll
  for (int i = 0; i < 16; i += 2) {
    const f32x2 x = {p[i], p[i + 1]};
    const f32x2 u = __builtin_elementwise_fma(x, c, a), v = __builtin_elementwise_fma(x, -c, b);
    const f32x2 eu = {__builtin_amdgcn_exp2f(u[0]), __builtin_amdgcn_exp2f(u[1])}, ev = {__builtin_amdgcn_exp2f(v[0]), __builtin_amdgcn_exp2f(v[1])};
    ep[i] = eu[0]; ep[i + 1] = eu[1]; em[i] = ev[0]; em[i + 1] = ev[1];
    if (i == 0) { sp = eu; sm = ev; } else { sp += eu; sm += ev; }
  }
  if (NORM) {
    const float rp = __builtin_amdgcn_rcpf(xhalf_sum(sp[0] + sp[1])), rm = __builtin_amdgcn_rcpf(xhalf_sum(sm[0] + sm[1]));
    ep *= rp; em *= rm;
  }
}
DI bf16x8 row_sum_sel(int lane) {
  const unsigned u = ((lane & 11) == 0 && (((lane >> 2) ^ (lane >> 4)) & 1) == 0) ? 0x3f803f80u : 0u;
  return __builtin_bit_cast(bf16x8, u32x4{u, u, u, u});
}

constexpr int LDS_BYTES = 163840;
constexpr int P1_WOFF = 32768, P1_EOFF = 49152, P1_STAGE = 53248, P1_ZRED = 3 * P1_STAGE, P1_CNT = P1_ZRED + 2048, P1_GO = P1_CNT + 16, P1_PHI = 65536;
constexpr int P2_Q = 131072, P2_KVF = 147456, P2_ZB = 151552;
constexpr int P2_XO = 32768, P2_XL = 0;
constexpr int P3_O = 0;
DI constexpr int p3_wslot(int t) { return 65536 + ((t + 2) % 6) * 16384; }

template <int TYPE>
DI void p1_compute(unsigned lds0, const char* pfp, unsigned& pfs, char* __restrict__ ws, rsrc_t sws, int rb, int ct, int w, int lane) {
  const int tid = threadIdx.x;
  const int wm = w & 3, wn = w >> 2, r32 = lane & 31, h = lane >> 5;
  if (tid < 8) *(AS3 unsigned*)(lds0 + P1_CNT + tid * 4) = 0u;

  const int xrow = wm * 64 + r32;
  const unsigned xa = xrow * 128 + ((h ^ ((xrow >> 1) & 7)) << 4);
  const int q4 = (lane & 15) >> 2, p4 = lane & 3, chalf = (lane >> 4) & 1;
  unsigned wa[2];
#pragma unroll
  for (int ns = 0; ns < 2; ++ns)
    wa[ns] = P1_WOFF + (8 * h + q4) * 256 + ((((wn * 2 + ns) ^ q4) << 2) | (chalf * 2 + (p4 >> 1))) * 16 + (p4 & 1) * 8;
  const int erow = 64 * (ct >> 1) + 16 * wm + (lane & 15);
  const unsigned exa = erow * 128 + (((lane >> 4) ^ ((erow >> 1) & 7)) << 4);
  const unsigned ewa = P1_EOFF + (8 * (lane >> 4) + q4) * 64 + wn * 32 + p4 * 8;

  f32x16 acc[2][2] = {};
  f32x4 eacc = {};
  bf16x8 xf[2][2], wfr[2][2], ex[2], ew[2];
  auto rdx = [&](int set, unsigned buf, int ks) {
#pragma unroll
    for (int ms = 0; ms < 2; ++ms) xf[set][ms] = lds_r128(buf + ((xa ^ (ks * 32)) + ms * 4096));
  };
  auto rdw = [&](int set, unsigned buf, int ks, int ns) { wfr[set][ns] = lds_tr2(buf + wa[ns] + ks * 4096, buf + wa[ns] + ks * 4096 + 1024); };
  auto mm1 = [&](int set, int ms, int ns) {
    if (TYPE == 0 && ns == 1) acc[ms][1] = MFMA32(xf[set][ms], wfr[set][1], acc[ms][1]);
    else                      acc[ms][ns] = MFMA32(wfr[set][ns], xf[set][ms], acc[ms][ns]);
  };
  auto erd = [&](unsigned buf, int kk) {
    if (TYPE >= 2) { ex[kk] = lds_r128(buf + (exa ^ (kk * 64))); ew[kk] = lds_tr2(buf + ewa + kk * 2048, buf + ewa + kk * 2048 + 256); }
  };
  auto emm = [&](int kk) {
    if (TYPE == 2) eacc = MFMA16(ew[kk], ex[kk], eacc);
    if (TYPE == 3) eacc = MFMA16(ex[kk], ew[kk], eacc);
  };
  auto sub = [&](int set, unsigned buf, int ks) {
    SBAR(); mm1(set, 0, 0); SBAR(); rdx(set ^ 1, buf, ks);
    SBAR(); mm1(set, 1, 0); SBAR(); rdw(set ^ 1, buf, ks, 0);
    SBAR(); mm1(set, 0, 1); SBAR(); rdw(set ^ 1, buf, ks, 1);
    SBAR(); mm1(set, 1, 1); SBAR();
  };
  if (PFB == 1 && TYPE < 2 && pfp) pf_line(pfs, pfp);
  LDS_BAR();
  if (PFB == 2 && TYPE < 2 && pfp) pf_line(pfs, pfp);
  STAMP(0, 1);
  unsigned cur = lds0, nxt = lds0 + P1_STAGE;
  rdx(0, cur, 0); rdw(0, cur, 0, 0); rdw(0, cur, 0, 1);
  LGKM0();
#pragma unroll 1
  for (int t = 0; t < 16; ++t) {
    sub(0, cur, 1); erd(cur, 0);
    sub(1, cur, 2); erd(cur, 1);
    sub(0, cur, 3); emm(0); emm(1); SBAR();
    LDS_BAR();
    sub(1, nxt, 0);
    cur = nxt; nxt = (nxt == lds0 + 2 * P1_STAGE) ? lds0 : nxt + P1_STAGE;
  }
  STAMP(0, 2);
  bf16x8 wf[2][2];
  if constexpr (TYPE <= 1) {
    const unsigned a = lds0 + P1_PHI + (TYPE == 0 ? wn : 2 * wn) * 2048 + (4 * h + q4) * 64 + chalf * 32 + p4 * 8;
#pragma unroll
    for (int n = 0; n <= TYPE; ++n)
#pragma unroll
      for (int s = 0; s < 2; ++s) wf[n][s] = lds_tr2(a + n * 2048 + s * 1024, a + n * 2048 + s * 1024 + 512);
  }

  if (TYPE >= 2) {
#pragma unroll
    for (int ms = 0; ms < 2; ++ms)
#pragma unroll
      for (int ns = 0; ns < 2; ++ns) {
        const f32x16 v = acc[ms][ns] * QSCALE;
        const unsigned p = WS_QB + ((rb * 8 + wm * 2 + ms) * 32 + ct * 4 + wn * 2 + ns) * 2048 + lane * 16;
        st16(sws, p, pack8<0>(v)); st16(sws, p + 1024, pack8<1>(v));
      }
    const int g = lane >> 4, tok0 = rb * 256 + 64 * (ct >> 1) + 16 * wm;
    unsigned p = WS_KV + (tok0 >> 5) * 4096;
    if (TYPE == 3) p += 2048 + (wm & 1) * 1024 + (16 * wn + (lane & 15) + 32 * (g >> 1)) * 16 + (g & 1) * 8;
    else           p += wn * 1024 + (16 * (wm & 1) + (lane & 15) + 32 * (g & 1)) * 16 + (g >> 1) * 8;
    st8(sws, p, u32x2{pk2(eacc[0], eacc[1]), pk2(eacc[2], eacc[3])});
    WAIT_BAR(0);
  } else if (TYPE == 1) {
    f32x16 pq[2][2];
#pragma unroll
    for (int ms = 0; ms < 2; ++ms)
#pragma unroll
      for (int ns = 0; ns < 2; ++ns) {
        pq[ms][ns] = MFMA32(wf[ns][0], pack8<0>(acc[ms][ns]), f32x16{});
        pq[ms][ns] = MFMA32(wf[ns][1], pack8<1>(acc[ms][ns]), pq[ms][ns]);
      }
#pragma unroll
    for (int ms = 0; ms < 2; ++ms)
#pragma unroll
      for (int ns = 0; ns < 2; ++ns) {
        f32x16 ep, em;
        feat_map<false>(pq[ms][ns], ep, em);
        const unsigned p = WS_QA + ((rb * 8 + wm * 2 + ms) * 32 + ct * 4 + wn * 2 + ns) * 4096 + lane * 16;
        st16(sws, p, pack8<0>(ep)); st16(sws, p + 1024, pack8<1>(ep)); st16(sws, p + 2048, pack8<0>(em)); st16(sws, p + 3072, pack8<1>(em));
      }
  } else {
    const unsigned img = lds0 + w * 8192;
    const bf16x8 sel = row_sum_sel(lane);
    f32x16 pk[2], kv[2] = {};
    f32x4 zz[2] = {};
    bf16x8 vf[2][2], af[2][2];
#pragma unroll
    for (int ms = 0; ms < 2; ++ms) {
      pk[ms] = MFMA32(wf[0][0], pack8<0>(acc[ms][0]), f32x16{});
      pk[ms] = MFMA32(wf[0][1], pack8<1>(acc[ms][0]), pk[ms]);
    }
#pragma unroll
    for (int ms = 0; ms < 2; ++ms) { vf[ms][0] = pack8<0>(acc[ms][1]); vf[ms][1] = pack8<1>(acc[ms][1]); PIN(vf[ms][0]); PIN(vf[ms][1]); }
    float off[4];
#pragma unroll
    for (int ms = 0; ms < 2; ++ms) { off[2 * ms] = -LOG2E * xhalf_max(max16(pk[ms])); off[2 * ms + 1] = LOG2E * xhalf_min(min16(pk[ms])); }
    const unsigned wr = img + 4096 + r32 * 64, sw = (r32 >> 1) & 7;
    const int t0 = 4 * h + q4, u8 = 4 * chalf + p4;
    const unsigned rd0 = img + 4096 + t0 * 64 + ((u8 ^ (t0 >> 1)) << 3), rd1 = img + 4096 + (t0 + 8) * 64 + ((u8 ^ ((t0 >> 1) + 4)) << 3);
    auto put = [&](int sg) {
#pragma unroll
      for (int g = 0; g < 4; ++g) *(AS3 f32x4*)(img + sg * 4096 + g * 1024 + lane * 16) = f32x4{kv[sg][4 * g], kv[sg][4 * g + 1], kv[sg][4 * g + 2], kv[sg][4 * g + 3]};
    };
    f32x16 e[2];
    float rs[2];
    auto mm = [&](int u, int j) {
      if (j & 1) zz[u & 1] = MFMA16(sel, af[u & 1][j >> 1], zz[u & 1]); else kv[u & 1] = MFMA32(af[u & 1][j >> 1], vf[u >> 1][j >> 1], kv[u & 1]);
    };
    auto exps = [&](int u, int mu) {
      const f32x16& p = pk[u >> 1];
      const float c = (u & 1) ? -LOG2E : LOG2E;
      f32x2 sm;
#pragma unroll
      for (int i = 0; i < 16; i += 2) {
        if (mu >= 0 && i >= 8) { SBAR(); mm(mu, (i - 8) >> 1); SBAR(); }
        const f32x2 v = __builtin_elementwise_fma(f32x2{p[i], p[i + 1]}, f32x2{c, c}, f32x2{off[u], off[u]});
        const f32x2 ev = {__builtin_amdgcn_exp2f(v[0]), __builtin_amdgcn_exp2f(v[1])};
        e[u & 1][i] = ev[0]; e[u & 1][i + 1] = ev[1];
        if (i == 0) sm = ev; else sm += ev;
      }
      rs[u & 1] = __builtin_amdgcn_rcpf(xhalf_sum(sm[0] + sm[1]));
    };
    auto rest = [&](int u) {
      const f32x16 n = e[u & 1] * rs[u & 1];
#pragma unroll
      for (int g = 0; g < 4; ++g) *(AS3 u32x2*)(wr + (u & 1) * 2048 + (((2 * g + h) ^ sw) << 3)) = u32x2{pk2(n[4 * g], n[4 * g + 1]), pk2(n[4 * g + 2], n[4 * g + 3])};
      asm volatile("" ::: "memory");
#pragma unroll
      for (int s = 0; s < 2; ++s) af[u & 1][s] = lds_tr2(rd0 + (u & 1) * 2048 + s * 1024, rd1 + (u & 1) * 2048 + s * 1024);
      asm volatile("" ::: "memory");
      SBAR();
    };
    exps(0, -1); exps(1, -1); rest(0);
    exps(2, 0);  rest(1);
    exps(3, 1);  rest(2);
    rest(3);
    mm(2, 0); mm(2, 2); mm(3, 0); mm(3, 2); SBAR(); mm(2, 1); mm(2, 3); mm(3, 1); mm(3, 3);
    put(0); put(1);
    if (lane < 32) {
      const unsigned a = lds0 + P1_ZRED + w * 256 + ((lane >> 2) & 1) * 64 + (lane >> 3) * 16 + (lane & 3) * 4;
      *(AS3 float*)a = zz[0][0]; *(AS3 float*)(a + 128) = zz[1][0];
    }
  }
  STAMP(0, 3);
}

DI void attn_pass_fast(unsigned lds0, const bf16x8 (&q)[2][2], const bf16x8 sel, int kh, int lane, f32x16 (&O)[2], f32x4 (&L)[2], unsigned& tv) {
  TS(0);
  typedef const AS3 char* lp;
  const lp l0 = (lp)(lds0 + lane * 16), la = l0 + kh * 16384;
#define RD(p) (*(const AS3 bf16x8*)(p))
  if (kh) __builtin_amdgcn_s_setprio(1);
  bf16x8 ka = RD(l0 + 98304), kb = RD(l0 + 98304 + 1024);
  f32x16 negm[2], S0, S1;
#pragma unroll
  for (int r = 0; r < 2; ++r) {
    f32x16 z = {};
    z = MFMA32(ka, q[r][0], z); z = MFMA32(kb, q[r][1], z);
    const float m = xhalf_max(max16(z));
#pragma unroll
    for (int i = 0; i < 16; ++i) negm[r][i] = -m;
    PIN(negm[r]);
    O[r] = f32x16{}; L[r] = f32x4{};
  }
  ka = RD(la + 98304); kb = RD(la + 98304 + 1024);
  bf16x8 va = RD(la + 98304 + 2048), vb = bf16x8{};
  S0 = MFMA32(ka, q[0][0], negm[0]); S0 = MFMA32(kb, q[0][1], S0);
  S1 = f32x16{};
  int odd = 0xDDDD;
  PIN(odd);
  LGKM0();
#define EXN(SC, i, n) do { _Pragma("unroll") for (int k_ = 0; k_ < n; ++k_) SC[i + k_] = __builtin_amdgcn_exp2f(SC[i + k_]); PIN(SC); } while (0)
#define MM(D, E) do { SBAR(); D = E; PIN(D); SBAR(); } while (0)
#define TILE(TA, TN) do { \
    SBAR();                                                             EXN(S0, 0, 2);  \
    MM(O[1], SM32(vb, HI(S1), O[1]));       vb = RD((TA) + 3072);       EXN(S0, 2, 3);  \
    MM(L[1], SM16(sel, HI(S1), L[1]));                                  EXN(S0, 5, 2);  \
    MM(S1, MFMA32(ka, q[1][0], negm[1]));   ka = RD(TN);                EXN(S0, 7, 3);  \
    MM(S1, MFMA32(kb, q[1][1], S1));        kb = RD((TN) + 1024);       EXN(S0, 10, 3); \
    MM(O[0], SM32(va, LO(S0), O[0]));                                   EXN(S0, 13, 3); \
    MM(L[0], SM16(sel, LO(S0), L[0]));                                  EXN(S1, 0, 2);  \
    MM(O[0], SM32(vb, HI(S0), O[0]));                                   EXN(S1, 2, 3);  \
    MM(L[0], SM16(sel, HI(S0), L[0]));                                  EXN(S1, 5, 2);  \
    MM(S0, MFMA32(ka, q[0][0], negm[0]));                               EXN(S1, 7, 3);  \
    MM(S0, MFMA32(kb, q[0][1], S0));                                    EXN(S1, 10, 3); \
    MM(O[1], SM32(va, LO(S1), O[1]));       va = RD((TN) + 2048);       EXN(S1, 13, 3); \
    MM(L[1], SM16(sel, LO(S1), L[1])); } while (0)
  TS(1);
#pragma unroll 1
  for (int c = 0; c < 8; ++c) {
    const lp cb = la + ((3 - c) & 3) * 32768, nb = la + ((2 - c) & 3) * 32768;
    TILE(cb, cb + 4096);
    TILE(cb + 4096, cb + 8192);
    SBAR(); if (kh) __builtin_amdgcn_s_setprio(0);
    TILE(cb + 8192, cb + 12288);
    SBAR(); TS(2 + 2 * c);
    asm volatile("s_waitcnt vmcnt(0)\n\ts_barrier" ::: "memory"); TS(3 + 2 * c);
    if (kh) __builtin_amdgcn_s_setprio(1);
    TILE(cb + 12288, nb);
  }
  SBAR();
  O[1] = SM32(vb, HI(S1), O[1]); L[1] = SM16(sel, HI(S1), L[1]);
  __builtin_amdgcn_s_setprio(0);
  TS(18);
#undef TILE
#undef EXN
#undef MM
#undef RD
}

DI void attn_pass_safe(rsrc_t sws, const bf16x8 q0, const bf16x8 q1, const bf16x8 sel, int lane, f32x16& O, f32x4& L) {
  O = f32x16{}; L = f32x4{};
  float m = -INFINITY;
  const int odd = 0xDDDD;
#pragma unroll 1
  for (int t = 0; t < 64; ++t) {
    bf16x8 a[4];
#pragma unroll
    for (int j = 0; j < 4; ++j) a[j] = __builtin_bit_cast(bf16x8, ld16(sws, WS_KV + t * 4096 + j * 1024 + lane * 16));
    f32x16 Sc = {};
    Sc = MFMA32(a[0], q0, Sc); Sc = MFMA32(a[1], q1, Sc);
    const float mn = fmaxf(m, xhalf_max(max16(Sc)));
    const float al = __builtin_amdgcn_exp2f(m - mn);
    m = mn;
#pragma unroll
    for (int r = 0; r < 16; ++r) { O[r] *= al; Sc[r] = __builtin_amdgcn_exp2f(Sc[r] - mn); }
    L *= al;
    O = SM32(a[2], LO(Sc), O); L = SM16(sel, LO(Sc), L);
    O = SM32(a[3], HI(Sc), O); L = SM16(sel, HI(Sc), L);
  }
}

DI void helper_program(unsigned lds0, const unsigned short* __restrict__ X, const unsigned short* __restrict__ W, const unsigned short* __restrict__ E,
                       const unsigned short* __restrict__ woa, const unsigned short* __restrict__ wob, char* __restrict__ ws, rsrc_t sws,
                       int type, int rb, int ct, int x, int idx, int lw, int lane) {
  __builtin_amdgcn_s_setprio(3);
  const u64 e0 = __hip_atomic_load((AS1 u64*)(ws + WS_FLG) + F_EPOCH, __ATOMIC_RELAXED, __HIP_MEMORY_SCOPE_AGENT);
  const unsigned vl = lane * 16;
  {
    const bool ext = type >= 2;
    const unsigned ldw = ext ? 2048u : 6144u;
    const rsrc_t sx = mk_rsrc(X + (size_t)rb * 256 * D, 256u * 2048u);
    const rsrc_t sw = mk_rsrc(W, 1024u * ldw);
    const rsrc_t se = mk_rsrc(E, 1024u * 64u);
    unsigned xv[8], wv[4];
#pragma unroll
    for (int j = 0; j < 8; ++j) {
      const int row = 8 * (8 * lw + j) + (lane >> 3), c = (lane & 7) ^ ((row >> 1) & 7);
      xv[j] = row * 2048 + c * 16 - (j & 3) * 1024;
    }
#pragma unroll
    for (int j = 0; j < 4; ++j) {
      const int k = 4 * (4 * lw + j) + (lane >> 4), c = (lane & 15) ^ (((lane >> 4) & 3) << 2);
      const int col = (type == 0) ? 1024 + ((c >> 2) & 1) * 1024 + (2 * ct + (c >> 3)) * 32 + (c & 3) * 8 : ct * 128 + c * 8;
      wv[j] = k * ldw + col * 2 - j * 1024;
    }
    auto stage = [&](int t0, unsigned buf) {
      const int t = (t0 + (ext ? BROT : KROT)) & 15;
      const unsigned sox = t * 128, sow = t * 64 * ldw;
      set_m0(buf + P1_WOFF + lw * 4096);
      DMAW<0>(wv[0], sw, sow); DMAW<1024>(wv[1], sw, sow); DMAW<2048>(wv[2], sw, sow); DMAW<3072>(wv[3], sw, sow);
      if (ext ? (NTX & 2) : (NTX & 1)) {
        set_m0(buf + lw * 8192);
        dma16n<0>(xv[0], sx, sox); dma16n<1024>(xv[1], sx, sox); dma16n<2048>(xv[2], sx, sox); dma16n<3072>(xv[3], sx, sox);
        set_m0(buf + lw * 8192 + 4096);
        dma16n<0>(xv[4], sx, sox); dma16n<1024>(xv[5], sx, sox); dma16n<2048>(xv[6], sx, sox); dma16n<3072>(xv[7], sx, sox);
      } else {
        set_m0(buf + lw * 8192);
        dma16<0>(xv[0], sx, sox); dma16<1024>(xv[1], sx, sox); dma16<2048>(xv[2], sx, sox); dma16<3072>(xv[3], sx, sox);
        set_m0(buf + lw * 8192 + 4096);
        dma16<0>(xv[4], sx, sox); dma16<1024>(xv[5], sx, sox); dma16<2048>(xv[6], sx, sox); dma16<3072>(xv[7], sx, sox);
      }
      if (ext) { set_m0(buf + P1_EOFF + lw * 1024); dma16<0>(vl, se, t * 4096 + lw * 1024); }
    };
    stage(0, lds0); stage(1, lds0 + P1_STAGE);
    if (ext) WAIT_BAR(13); else WAIT_BAR(12);
    stage(2, lds0 + 2 * P1_STAGE);
    unsigned buf = lds0;
#pragma unroll 1
    for (int t = 0; t < 13; ++t) {
      if (ext) WAIT_BAR(13); else WAIT_BAR(12);
      stage(t + 3, buf);
      buf = (buf == lds0 + 2 * P1_STAGE) ? lds0 : buf + P1_STAGE;
    }
    if (ext) WAIT_BAR(13); else WAIT_BAR(12);
    if (!ext && (type == 1 || lw < 2)) {
      set_m0(lds0 + P1_PHI + lw * 2048);
      const unsigned so = ((type == 0 ? 2 * ct : 4 * ct) + lw) * 2048;
      dma16<0>(vl, se, so); dma16<1024>(vl, se, so);
    }
    WAIT_BAR(0);
    WAIT_BAR(0);
  }
  const u64 tag = ((u64)(unsigned)__builtin_amdgcn_readfirstlane((unsigned)(e0 >> 32)) << 32 | (unsigned)__builtin_amdgcn_readfirstlane((unsigned)e0))
                  * 0x5851F42D4C957F2Dull + 0x14057B7EF767814Full;
  const bool lead = (lw == 0) && (lane == 0);
  if (type >= 2) { WAIT_BAR(0); if (lead) post(ws, F_KV + rb * 16 + ct, tag); }
  auto chunk = [&](int c) {
    const unsigned l = lds0 + ((3 - c) & 3) * 32768 + lw * 8192, so = WS_KV + ((c + x) & 7) * 32768 + lw * 8192;
    dma4k(l, vl, sws, so); dma4k(l + 4096, vl, sws, so + 4096);
  };
  STAMP(512, 10);
  if (lw == 0) {
    poll(ws, F_KV + x * 16 + (lane & 7), tag);
    __hip_atomic_store((AS3 unsigned*)(lds0 + P1_GO), 1u, __ATOMIC_RELAXED, __HIP_MEMORY_SCOPE_WORKGROUP);
  } else {
    while (__hip_atomic_load((AS3 unsigned*)(lds0 + P1_GO), __ATOMIC_RELAXED, __HIP_MEMORY_SCOPE_WORKGROUP) == 0u) __builtin_amdgcn_s_sleep(1);
    asm volatile("" ::: "memory");
  }
  STAMP(512, 11);
  dma4k(lds0 + P2_Q + lw * 4096, vl, sws, WS_QB + ((8 * x + (idx >> 2)) * 32 + 8 * (idx & 3)) * 2048 + lw * 4096);
  chunk(0);
  WAIT_BAR(0);
  poll(ws, F_KV + (lane >> 3) * 16 + (lane & 7), tag);
  chunk(1);
  if (type == 0) {
#pragma unroll
    for (int j = 0; j < 4; ++j) {
      const unsigned a = lds0 + (lw >> 1) * 32768 + ((lw & 1) * 4 + j) * 1024 + vl;
      const f32x4 sm = add4(add4(*(AS3 f32x4*)a, *(AS3 f32x4*)(a + 8192)), add4(*(AS3 f32x4*)(a + 16384), *(AS3 f32x4*)(a + 24576)));
      st16(sws, WS_KVP + ((2 * ct + (lw >> 1)) * 8 + rb) * 8192 + ((lw & 1) * 4 + j) * 1024 + vl, sm);
    }
    if (lw < 2) {
      const unsigned a = lds0 + P1_ZRED + lw * 1024 + lane * 4;
      st4(sws, WS_ZP + (((2 * ct + lw) * 8 + rb) * 64 + lane) * 4, one(*(AS3 float*)a + *(AS3 float*)(a + 256)) + one(*(AS3 float*)(a + 512) + *(AS3 float*)(a + 768)));
    }
  }
  WAIT_BAR(0);
  chunk(2); chunk(3);
  if (lead && type < 2) post(ws, type == 0 ? F_K + ct * 8 + rb : F_Q + rb * 8 + ct, tag);
  const int br = !(x & 1), rb3 = 4 * (x >> 1) + (idx & 3), cb = idx >> 2;
  const unsigned short* wo = br ? wob : woa;
  unsigned pfs = 0;
  auto warm_wo = [&]() {
    pf_line(pfs, (const char*)wo + idx * 65536 + lw * 16384 + lane * 128);
    pf_line(pfs, (const char*)wo + idx * 65536 + lw * 16384 + 8192 + lane * 128);
  };
  if (lw == 1) *(AS3 unsigned*)(lds0 + P2_ZB + 128 + lane * 4) = lane < 32 ? 0x3f803f80u : 0u;
  WAIT_BAR(8);
  if (br) {
    const int head = 4 * x + (idx & 7), tp = idx >> 3;
    poll(ws, lane < 8 ? F_K + (head >> 1) * 8 + lane : F_Q + (2 * tp + (lane & 1)) * 8 + (head >> 2), tag);
    STAMP(512, 12);
    chunk(4);
    u32x4 kvp[2][8], fq[4][4];
    float zs[8];
    auto ldq = [&](int e) {
#pragma unroll
      for (int j = 0; j < 4; ++j) fq[e][j] = ld16(sws, WS_QA + ((16 * tp + 4 * lw + e) * 32 + head) * 4096 + j * 1024 + vl);
    };
#pragma unroll
    for (int e = 0; e < 2; ++e)
#pragma unroll
      for (int p = 0; p < 8; ++p) kvp[e][p] = ld16(sws, WS_KVP + ((head * 8 + p) * 512 + (2 * lw + e) * 64 + lane) * 16);
#pragma unroll
    for (int p = 0; p < 8; ++p) zs[p] = ld4(sws, WS_ZP + ((head * 8 + p) * 64 + lane) * 4);
    ldq(0); ldq(1);
    asm volatile("s_barrier" ::: "memory");
#pragma unroll
    for (int e = 0; e < 2; ++e) {
#pragma unroll
      for (int p = 0; p < 8; ++p) PIN(kvp[e][p]);
#define F4(v) __builtin_bit_cast(f32x4, v)
      const f32x4 sm = add4(add4(add4(F4(kvp[e][0]), F4(kvp[e][1])), add4(F4(kvp[e][2]), F4(kvp[e][3]))), add4(add4(F4(kvp[e][4]), F4(kvp[e][5])), add4(F4(kvp[e][6]), F4(kvp[e][7]))));
#undef F4
      *(AS3 u32x2*)(lds0 + P2_KVF + lw * 1024 + vl + e * 8) = u32x2{pk2(sm[0], sm[1]), pk2(sm[2], sm[3])};
    }
    {
#pragma unroll
      for (int p = 0; p < 8; ++p) PIN(zs[p]);
      const float z = one(one(zs[0] + zs[1]) + one(zs[2] + zs[3])) + one(one(zs[4] + zs[5]) + one(zs[6] + zs[7]));
      if (lw == 0) *(AS3 unsigned short*)(lds0 + P2_ZB + lane * 2) = (unsigned short)(pk2(z, z) & 0xffffu);
    }
    chunk(5);
    ldq(2); ldq(3);
    const int row = lane & 15, kg = lane >> 4;
    const bool on = (((row >> 2) ^ kg) & 1) == 0;
    const unsigned za = lds0 + P2_ZB + ((on && (row & 3) == 0) ? (kg >> 1) * 32 : (on && (row & 3) == 1) ? 128 : 256);
    const unsigned ka = lds0 + P2_KVF + vl;
    auto otile = [&](int e) {
#define BF(v) __builtin_bit_cast(bf16x8, v)
      f32x16 np = MFMA32(lds_r128(ka), BF(fq[e][0]), f32x16{});        f32x4 dp = MFMA16(lds_r128(za), BF(fq[e][0]), f32x4{});
      np = MFMA32(lds_r128(ka + 1024), BF(fq[e][1]), np);              dp = MFMA16(lds_r128(za + 16), BF(fq[e][1]), dp);
      f32x16 nm = MFMA32(lds_r128(ka + 2048), BF(fq[e][2]), f32x16{}); f32x4 dm = MFMA16(lds_r128(za + 64), BF(fq[e][2]), f32x4{});
      nm = MFMA32(lds_r128(ka + 3072), BF(fq[e][3]), nm);              dm = MFMA16(lds_r128(za + 80), BF(fq[e][3]), dm);
#undef BF
      const float a = __builtin_amdgcn_rcpf(dp[1]), b = __builtin_amdgcn_rcpf(dm[1]);
      const float r = __builtin_amdgcn_rcpf(one(dp[0] * a) + one(dm[0] * b) + 1e-6f), ar = one(a * r), br = one(b * r);
      f32x16 o;
#pragma unroll
      for (int i = 0; i < 16; ++i) o[i] = one(__builtin_fmaf(nm[i], br, one(np[i] * ar)));
      const unsigned p = WS_OA + ((16 * tp + 4 * lw + e) * 32 + head) * 2048 + vl;
      st16(sws, p, pack8<0>(o)); st16(sws, p + 1024, pack8<1>(o));
    };
    WAIT_BAR(16);
#pragma unroll
    for (int j = 0; j < 4; ++j) { PIN(fq[0][j]); PIN(fq[1][j]); }
    chunk(6);
    otile(0); otile(1);
    WAIT_BAR(12);
#pragma unroll
    for (int j = 0; j < 4; ++j) { PIN(fq[2][j]); PIN(fq[3][j]); }
    chunk(7);
    otile(2); otile(3);
    WAIT_BAR(0);
    if (lead) post(ws, F_OA + tp * 32 + head, tag);
    warm_wo();
  } else {
    chunk(4); warm_wo(); WAIT_BAR(10);
    chunk(5); WAIT_BAR(10);
    chunk(6); WAIT_BAR(8);
    chunk(7);
    if (blockIdx.x == 1 && lw == 1) {
      poll(ws, lane, tag); poll(ws, 64 + lane, tag); poll(ws, 128 + lane, tag);
      if (lane == 0) post(ws, F_EPOCH, tag);
    }
    WAIT_BAR(8);
  }
  const rsrc_t sw = mk_rsrc(wo, 1024u * 2048u);
  unsigned wv[4];
#pragma unroll
  for (int j = 0; j < 4; ++j) {
    const int k = 4 * (4 * lw + j) + (lane >> 4), c = (lane & 15) ^ (((lane >> 4) & 3) << 2);
    wv[j] = k * 2048 + (cb * 128 + c * 8) * 2 - j * 1024;
  }
  const unsigned osrc = (br ? WS_OB : WS_OA) + (rb3 * 4 + lw) * 65536;
  auto wst = [&](int t) {
    const unsigned sow = t * 64 * 2048;
    set_m0(lds0 + p3_wslot(t) + lw * 4096);
    dma16<0>(wv[0], sw, sow); dma16<1024>(wv[1], sw, sow); dma16<2048>(wv[2], sw, sow); dma16<3072>(wv[3], sw, sow);
  };
  auto ost = [&](int t) { dma4k(lds0 + P3_O + (t & 3) * 16384 + lw * 4096, vl, sws, osrc + t * 4096); };
  wst(0); wst(1); wst(2); wst(3);
  if (br) asm volatile("s_barrier" ::: "memory"); else WAIT_BAR(16);
  wst(4); wst(5);
  if (!br && lw == 0) poll(ws, F_OA + (rb3 >> 2) * 32 + (lane & 31), tag);
  asm volatile("s_barrier" ::: "memory");
  asm volatile("s_barrier" ::: "memory");
  asm volatile("s_barrier" ::: "memory");
  if (lead) post(ws, F_OB + x * 32 + idx, tag);
  STAMP(512, 13);
  if (br && lw == 0) poll(ws, F_OB + (rb3 >> 1) * 32 + (rb3 & 1) * 16 + (lane & 15), tag);
  asm volatile("s_barrier" ::: "memory");
  STAMP(512, 14);
  ost(0); ost(1); ost(2); ost(3);
  WAIT_BAR(12);
  PIN(pfs);
  WAIT_BAR(8);  ost(4); wst(6);
  WAIT_BAR(12); ost(5); wst(7);
  WAIT_BAR(16); ost(6); wst(8);
  WAIT_BAR(20); ost(7); wst(9);
  WAIT_BAR(20); ost(8); wst(10);
  WAIT_BAR(20); ost(9); wst(11);
  WAIT_BAR(20); ost(10); wst(12);
  WAIT_BAR(20); ost(11); wst(13);
  WAIT_BAR(20); ost(12); wst(14);
  WAIT_BAR(20); ost(13); wst(15);
  WAIT_BAR(20); ost(14);
  WAIT_BAR(16); ost(15);
  WAIT_BAR(12);
  WAIT_BAR(4);
  WAIT_BAR(0);
}

__global__ __launch_bounds__(768, 3) void fused_fwd(const unsigned short* __restrict__ xa, const unsigned short* __restrict__ wqkv,
                                                    const unsigned short* __restrict__ wphi, const unsigned short* __restrict__ woa,
                                                    const unsigned short* __restrict__ xb, const unsigned short* __restrict__ wq,
                                                    const unsigned short* __restrict__ wk, const unsigned short* __restrict__ wv,
                                                    const unsigned short* __restrict__ wob, char* __restrict__ ws, unsigned short* __restrict__ out) {
  __shared__ __attribute__((aligned(1024))) char smem[LDS_BYTES];
  const unsigned lds0 = (unsigned)(uintptr_t)(AS3 char*)smem;
  const int tid = threadIdx.x, lane = tid & 63, w = __builtin_amdgcn_readfirstlane(tid >> 6);
  const int b = blockIdx.x, x = b & 7, idx = b >> 3;
  const int rb = 4 * ((x >> 1) & 1) + (idx & 3);
  const int ct = idx < 16 ? 8 * (x >> 2) + 4 * (x & 1) + (idx >> 2) : 4 * (x >> 2) + ((idx >> 2) & 3);
  const int type = idx < 16 ? 0 : (x & 1) ? 1 : 2 + (ct & 1);
  const rsrc_t sws = mk_rsrc(ws, WS_END);
  STAMP(0, 0);
  if (w >= 8) {
    helper_program(lds0, type >= 2 ? xb : xa, type >= 2 ? wq : wqkv, type == 3 ? wv : type == 2 ? wk : wphi, woa, wob, ws, sws, type, rb, ct, x, idx, w - 8, lane);
    return;
  }
  unsigned pfs = 0;
  if (type == 0 && !(x & 1) && w == 0) pf_line(pfs, (const char*)(lane < 32 ? wv : wk) + (idx * 32 + (lane & 31)) * 128);
  const char* pfp = nullptr;
  if (tid < 32 * (8 - PF0)) {
    const int id = ((x & 1) * 16 + idx) * (32 * (8 - PF0)) + tid, st = id / 1536, r = id - st * 1536, k = (PF0 + st + BROT) & 15;
    pfp = r < 1024 ? (const char*)xb + (((x >> 1) & 1) * 1024 + r) * 2048 + k * 128 : (const char*)wq + (64 * k + ((r - 1024) >> 3)) * 2048 + (x >> 2) * 1024 + (r & 7) * 128;
  }
  if (type == 0)      p1_compute<0>(lds0, pfp, pfs, ws, sws, rb, ct, w, lane);
  else if (type == 1) p1_compute<1>(lds0, pfp, pfs, ws, sws, rb, ct, w, lane);
  else if (type == 2) p1_compute<2>(lds0, pfp, pfs, ws, sws, rb, ct, w, lane);
  else                p1_compute<3>(lds0, pfp, pfs, ws, sws, rb, ct, w, lane);

  const int kh = w >> 2, r32 = lane & 31, h = lane >> 5;
  {
    const unsigned s0 = (lane & 15) == 0 ? 0x3f803f80u : 0u, s4 = (lane & 15) == 4 ? 0x3f803f80u : 0u;
    const bf16x8 sel = __builtin_bit_cast(bf16x8, u32x4{s0, s0, s4, s4});
    LDS_BAR();
    STAMP(0, 4);
    const unsigned qa = lds0 + P2_Q + (2 * (w & 3) + kh) * 2048 + lane * 16;
    bf16x8 q[2][2];
    q[0][0] = lds_r128(qa);        q[0][1] = lds_r128(qa + 1024);
    q[1][0] = lds_r128(qa ^ 2048); q[1][1] = lds_r128((qa ^ 2048) + 1024);
    f32x16 O[2]; f32x4 L[2];
    unsigned tv = 0;
    attn_pass_fast(lds0, q, sel, kh, lane, O, L, tv);
    TSOUT();
    STAMP(0, 5);
    const unsigned xs = lds0 + P2_XO + w * 4096 + lane * 16, xr = lds0 + P2_XO + (w ^ 4) * 4096 + lane * 16;
#pragma unroll
    for (int g = 0; g < 4; ++g) *(AS3 f32x4*)(xs + g * 1024) = f32x4{O[1][4 * g], O[1][4 * g + 1], O[1][4 * g + 2], O[1][4 * g + 3]};
    *(AS3 float*)(lds0 + P2_XL + w * 256 + lane * 4) = L[1][0];
    LDS_BAR();
#pragma unroll
    for (int g = 0; g < 4; ++g) {
      const f32x4 o = *(AS3 f32x4*)(xr + g * 1024);
      O[0][4 * g] += o[0]; O[0][4 * g + 1] += o[1]; O[0][4 * g + 2] += o[2]; O[0][4 * g + 3] += o[3];
    }
    L[0][0] += *(AS3 float*)(lds0 + P2_XL + (w ^ 4) * 256 + lane * 4);
    bool bad = !(L[0][0] < 1.8446744e19f);
#pragma unroll
    for (int i = 0; i < 16; ++i) bad |= !(fabsf(O[0][i]) < 3.0e38f);
    if (__any(bad)) attn_pass_safe(sws, q[0][0], q[0][1], sel, lane, O[0], L[0]);
    const float r = __builtin_amdgcn_rcpf(xhalf_sum(L[0][0]));
    O[0] *= r;
    const unsigned p = WS_OB + ((8 * x + (idx >> 2)) * 32 + 8 * (idx & 3) + 2 * (w & 3) + kh) * 2048 + lane * 16;
    st16(sws, p, pack8<0>(O[0])); st16(sws, p + 1024, pack8<1>(O[0]));
    WAIT_BAR(0);
    PIN(pfs);
    STAMP(0, 6);
  }
  const int br = !(x & 1), rb3 = 4 * (x >> 1) + (idx & 3), cb = idx >> 2;
  const int wm = w & 1, wn = (w >> 1) & 1;
  const int q4 = (lane & 15) >> 2, p4 = lane & 3, chalf = (lane >> 4) & 1;
  const unsigned oa = wm * 8192 + kh * (4096 + 2048) + lane * 16;
  unsigned wa[2];
#pragma unroll
  for (int ns = 0; ns < 2; ++ns)
    wa[ns] = (kh * 32 + 4 * h + q4) * 256 + ((((wn * 2 + ns) ^ q4) << 2) | (chalf * 2 + (p4 >> 1))) * 16 + (p4 & 1) * 8;
  f32x16 acc[2][2] = {};
  bf16x8 xf[2][2], wfr[2][2];
  auto rdx = [&](int set, unsigned buf, int ks) {
#pragma unroll
    for (int ms = 0; ms < 2; ++ms) xf[set][ms] = lds_r128(buf + (oa ^ (ms * 4096)) + ks * 1024);
  };
  auto rdw = [&](int set, unsigned buf, int ks, int ns) { wfr[set][ns] = lds_tr2(buf + wa[ns] + ks * 4096, buf + wa[ns] + ks * 4096 + 2048); };
  auto mm1 = [&](int set, int ms, int ns) { acc[ms][ns] = MFMA32(wfr[set][ns], xf[set][ms], acc[ms][ns]); };
  auto sub = [&](int set, bool rd, unsigned ob, unsigned wb, int ks) {
    SBAR(); mm1(set, 0, 0); SBAR(); if (rd) rdx(set ^ 1, ob, ks);
    SBAR(); mm1(set, 1, 0); SBAR(); if (rd) rdw(set ^ 1, wb, ks, 0);
    SBAR(); mm1(set, 0, 1); SBAR(); if (rd) rdw(set ^ 1, wb, ks, 1);
    SBAR(); mm1(set, 1, 1); SBAR();
  };
  asm volatile("s_barrier" ::: "memory");
  LDS_BAR();
  STAMP(0, 7);
  unsigned ob = lds0 + P3_O, wb = lds0 + p3_wslot(0);
  rdx(0, ob, 0); rdw(0, wb, 0, 0); rdw(0, wb, 0, 1);
  LGKM0();
#pragma unroll 1
  for (int t = 0; t < 15; ++t) {
    sub(0, true, ob, wb, 1);
    ob = (ob == lds0 + P3_O + 3 * 16384) ? lds0 + P3_O : ob + 16384;
    wb = (wb == lds0 + 147456) ? lds0 + 65536 : wb + 16384;
    LDS_BAR();
    sub(1, true, ob, wb, 0);
  }
  sub(0, true, ob, wb, 1);
  sub(1, false, ob, wb, 0);
  STAMP(0, 8);
  const unsigned xs = lds0 + 65536 + w * 8192 + lane * 16, xr = lds0 + 65536 + (w ^ 4) * 8192 + lane * 16;
#pragma unroll
  for (int ns = 0; ns < 2; ++ns)
#pragma unroll
    for (int g = 0; g < 4; ++g) {
      const f32x16& a = acc[1][ns];
      *(AS3 f32x4*)(xs + ns * 4096 + g * 1024) = f32x4{a[4 * g], a[4 * g + 1], a[4 * g + 2], a[4 * g + 3]};
    }
  LDS_BAR();
  const unsigned img = lds0 + w * 4096;
#pragma unroll
  for (int ns = 0; ns < 2; ++ns)
#pragma unroll
    for (int g = 0; g < 4; ++g) {
      const f32x16& a = acc[0][ns];
      const f32x4 o = *(AS3 f32x4*)(xr + ns * 4096 + g * 1024);
      *(AS3 u32x2*)(img + r32 * 128 + (((ns * 4 + g) ^ (r32 & 7)) << 4) + h * 8) =
          u32x2{pk2(a[4 * g] + o[0], a[4 * g + 1] + o[1]), pk2(a[4 * g + 2] + o[2], a[4 * g + 3] + o[3])};
    }
  asm volatile("" ::: "memory");
  const int tok0 = rb3 * 128 + wm * 64 + kh * 32;
  const rsrc_t so = mk_rsrc(out, 2048u * 4096u);
  const unsigned op = tok0 * 4096 + br * 2048 + cb * 256 + wn * 128;
#pragma unroll
  for (int i = 0; i < 4; ++i) {
    const int row = 8 * i + (lane >> 3), ch = lane & 7;
    const u32x4 v = *(AS3 u32x4*)(img + row * 128 + ((ch ^ (row & 7)) << 4));
    st16(so, op + row * 4096 + ch * 16, v);
  }
  STAMP(0, 9);
}

extern "C" void kernel_launch(void* const* d_in, const int* in_sizes, int n_in, void* d_out, int out_size, void* d_ws, size_t ws_size,
                              hipStream_t stream) {
  (void)in_sizes; (void)n_in; (void)out_size; (void)ws_size;
  typedef const unsigned short* cp;
  fused_fwd<<<256, 768, 0, stream>>>((cp)d_in[0], (cp)d_in[1], (cp)d_in[2], (cp)d_in[3], (cp)d_in[4], (cp)d_in[5], (cp)d_in[6], (cp)d_in[7],
                                     (cp)d_in[8], (char*)d_ws, (unsigned short*)d_out);
}
```

```cpp
#include <hip/hip_runtime.h>
#include <hip/hip_bf16.h>
#include <stdint.h>
#pragma clang diagnostic ignored "-Wint-to-pointer-cast"

typedef __bf16   bf16x8 __attribute__((ext_vector_type(8)));
typedef __bf16   bf16x16 __attribute__((ext_vector_type(16)));
typedef __bf16   bf16x2 __attribute__((ext_vector_type(2)));
typedef short    s16x4  __attribute__((ext_vector_type(4)));
typedef short    s16x8  __attribute__((ext_vector_type(8)));
typedef float    f32x16 __attribute__((ext_vector_type(16)));
typedef float    f32x4  __attribute__((ext_vector_type(4)));
typedef float    f32x2  __attribute__((ext_vector_type(2)));
typedef unsigned u32x4  __attribute__((ext_vector_type(4)));
typedef unsigned u32x2  __attribute__((ext_vector_type(2)));
typedef unsigned long long u64;
typedef __amdgpu_buffer_rsrc_t rsrc_t;

#define AS1 __attribute__((address_space(1)))
#define AS3 __attribute__((address_space(3)))
#define DI __device__ __forceinline__
#define MFMA32(a, b, c) __builtin_amdgcn_mfma_f32_32x32x16_bf16((a), (b), (c), 0, 0, 0)
#define MFMA16(a, b, c) __builtin_amdgcn_mfma_f32_16x16x32_bf16((a), (b), (c), 0, 0, 0)
#define SM32(a, b, c) __builtin_amdgcn_smfmac_f32_32x32x32_bf16((a), (b), (c), odd, 0, 0)
#define SM16(a, b, c) __builtin_amdgcn_smfmac_f32_16x16x64_bf16((a), (b), (c), odd, 0, 0)
#define LO(S) __builtin_bit_cast(bf16x16, __builtin_shufflevector(S, S, 0, 1, 2, 3, 4, 5, 6, 7))
#define HI(S) __builtin_bit_cast(bf16x16, __builtin_shufflevector(S, S, 8, 9, 10, 11, 12, 13, 14, 15))
#define SBAR() __builtin_amdgcn_sched_barrier(0)
#define WAIT_BAR(N) asm volatile("s_waitcnt vmcnt(" #N ") lgkmcnt(0)\n\ts_barrier" ::: "memory")
#define LGKM0() __builtin_amdgcn_s_waitcnt(0xC07F)
#define LDS_BAR() do { asm volatile("" ::: "memory"); LGKM0(); asm volatile("s_barrier" ::: "memory"); } while (0)

#define STAMP(th, i) do {} while (0)
#define TS(i) do {} while (0)
#define TSOUT() do {} while (0)
#define HF 1
#define VT ((HF) ? (threadIdx.x + 512) % 768 : threadIdx.x)
#define KA 1
#define HX(c) __builtin_expect(c, 1)
#define EL 1
#define ZREF 0
#define EPF 1
#define EMV 1
#define PFB 0
#define PF0 3
#define BROT (8 * (((x >> 1) ^ (x >> 2)) & 1))
#define NTX 0
#define NTW 0
#define KROT (2 * x)
#define MAPV 1
constexpr int D = 1024;
constexpr float LOG2E = 1.4426950408889634f;
constexpr float QSCALE = 0.17677669529663687f * LOG2E;

constexpr unsigned WS_QA  = 0;
constexpr unsigned WS_QB  = WS_QA + (8u << 20);
constexpr unsigned WS_OA  = WS_QB + (4u << 20);
constexpr unsigned WS_OB  = WS_OA + (4u << 20);
constexpr unsigned WS_KV  = WS_OB + (4u << 20);
constexpr unsigned WS_KVP = WS_KV + (256u << 10);
constexpr unsigned WS_ZP  = WS_KVP + (2u << 20);
constexpr unsigned WS_FLG = WS_ZP + (64u << 10);
constexpr unsigned WS_END = WS_FLG + (16u << 10);
constexpr int F_K = 0, F_Q = 128, F_OA = 256, F_OB = 512, F_KV = 768, F_EPOCH = 1024;

DI unsigned pk2(float lo, float hi) {
  f32x2 v = {lo, hi};
  return __builtin_bit_cast(unsigned, __builtin_convertvector(v, bf16x2));
}
template <int SS> DI bf16x8 pack8(const f32x16& x) {
  u32x4 p = {pk2(x[8 * SS + 0], x[8 * SS + 1]), pk2(x[8 * SS + 2], x[8 * SS + 3]), pk2(x[8 * SS + 4], x[8 * SS + 5]), pk2(x[8 * SS + 6], x[8 * SS + 7])};
  return __builtin_bit_cast(bf16x8, p);
}
DI bf16x8 lds_r128(unsigned a) { return *(const AS3 bf16x8*)a; }
DI bf16x8 lds_tr2(unsigned a0, unsigned a1) {
  s16x4 lo = __builtin_amdgcn_ds_read_tr16_b64_v4i16((AS3 s16x4*)a0);
  s16x4 hi = __builtin_amdgcn_ds_read_tr16_b64_v4i16((AS3 s16x4*)a1);
  s16x8 r = __builtin_shufflevector(lo, hi, 0, 1, 2, 3, 4, 5, 6, 7);
  return __builtin_bit_cast(bf16x8, r);
}
DI rsrc_t mk_rsrc(const void* p, unsigned bytes) { return __builtin_amdgcn_make_buffer_rsrc((void*)p, (short)0, (int)bytes, 0x00020000); }
DI void set_m0(unsigned v) { asm volatile("s_mov_b32 m0, %0\n\ts_nop 0" ::"s"(v) : "memory"); }
template <int IMM> DI void dma16(unsigned voff, rsrc_t srd, unsigned soff) {
  asm volatile("buffer_load_dwordx4 %0, %1, %2 offen offset:%3 lds" ::"v"(voff), "s"(srd), "s"(soff), "i"(IMM) : "memory");
}
template <int IMM> DI void dma16n(unsigned voff, rsrc_t srd, unsigned soff) {
  asm volatile("buffer_load_dwordx4 %0, %1, %2 offen offset:%3 nt lds" ::"v"(voff), "s"(srd), "s"(soff), "i"(IMM) : "memory");
}
#define DMAX dma16
#define DMAW dma16
template <int IMM> DI void dma16d(unsigned voff, rsrc_t srd, unsigned soff) {
  asm volatile("buffer_load_dwordx4 %0, %1, %2 offen offset:%3 sc1 lds" ::"v"(voff), "s"(srd), "s"(soff), "i"(IMM) : "memory");
}
DI void dma4k(unsigned lds, unsigned voff, rsrc_t srd, unsigned soff) {
  set_m0(lds); dma16d<0>(voff, srd, soff); dma16d<1024>(voff, srd, soff); dma16d<2048>(voff, srd, soff); dma16d<3072>(voff, srd, soff);
}
template <class T> DI void st16(rsrc_t r, unsigned voff, T v) { __builtin_amdgcn_raw_buffer_store_b128(__builtin_bit_cast(u32x4, v), r, (int)voff, 0, 16); }
DI void st8(rsrc_t r, unsigned voff, u32x2 v) { __builtin_amdgcn_raw_buffer_store_b64(v, r, (int)voff, 0, 16); }
DI void st4(rsrc_t r, unsigned voff, float v) { __builtin_amdgcn_raw_buffer_store_b32(__float_as_uint(v), r, (int)voff, 0, 16); }
DI u32x4 ld16(rsrc_t r, unsigned voff) { return __builtin_amdgcn_raw_buffer_load_b128(r, (int)voff, 0, 16); }
DI float ld4(rsrc_t r, unsigned voff) { return __uint_as_float(__builtin_amdgcn_raw_buffer_load_b32(r, (int)voff, 0, 16)); }
DI void post(char* ws, int f, u64 tag) { __hip_atomic_store((AS1 u64*)(ws + WS_FLG) + f, tag, __ATOMIC_RELAXED, __HIP_MEMORY_SCOPE_AGENT); }
DI void poll(char* ws, int f, u64 tag) {
#pragma unroll 1
  for (int it = 0; it < (1 << 17); ++it) {
    const u64 v = __hip_atomic_load((AS1 u64*)(ws + WS_FLG) + f, __ATOMIC_RELAXED, __HIP_MEMORY_SCOPE_AGENT);
    if (__all(v == tag)) break;
    __builtin_amdgcn_s_sleep(2);
  }
  asm volatile("" ::: "memory");
}
DI void pf_line(unsigned& sink, const void* p) { asm volatile("global_load_dword %0, %1, off" : "+v"(sink) : "v"(p) : "memory"); }
#define PIN(x) asm volatile("" : "+v"(x))
DI float one(float v) { asm("" : "+v"(v)); return v; }
DI f32x4 add4(f32x4 a, f32x4 b) { return f32x4{one(a[0] + b[0]), one(a[1] + b[1]), one(a[2] + b[2]), one(a[3] + b[3])}; }
DI float xhalf_max(float v) {
  auto r = __builtin_amdgcn_permlane32_swap(__float_as_uint(v), __float_as_uint(v), false, false);
  return fmaxf(__uint_as_float(r[0]), __uint_as_float(r[1]));
}
DI float xhalf_min(float v) {
  auto r = __builtin_amdgcn_permlane32_swap(__float_as_uint(v), __float_as_uint(v), false, false);
  return fminf(__uint_as_float(r[0]), __uint_as_float(r[1]));
}
DI float xhalf_sum(float v) {
  auto r = __builtin_amdgcn_permlane32_swap(__float_as_uint(v), __float_as_uint(v), false, false);
  return __uint_as_float(r[0]) + __uint_as_float(r[1]);
}
DI float max16(const f32x16& x) {
  float a = fmaxf(fmaxf(x[0], x[1]), x[2]), b = fmaxf(fmaxf(x[3], x[4]), x[5]);
  a = fmaxf(fmaxf(a, x[6]), x[7]);   b = fmaxf(fmaxf(b, x[8]), x[9]);
  a = fmaxf(fmaxf(a, x[10]), x[11]); b = fmaxf(fmaxf(b, x[12]), x[13]);
  a = fmaxf(fmaxf(a, x[14]), x[15]);
  return fmaxf(a, b);
}
DI float min16(const f32x16& x) {
  float a = fminf(fminf(x[0], x[1]), x[2]), b = fminf(fminf(x[3], x[4]), x[5]);
  a = fminf(fminf(a, x[6]), x[7]);   b = fminf(fminf(b, x[8]), x[9]);
  a = fminf(fminf(a, x[10]), x[11]); b = fminf(fminf(b, x[12]), x[13]);
  a = fminf(fminf(a, x[14]), x[15]);
  return fminf(a, b);
}
template <bool NORM> DI void feat_map(const f32x16& p, f32x16& ep, f32x16& em) {
  const float mx = xhalf_max(max16(p)), mn = xhalf_min(min16(p));
  const f32x2 a = {-mx * LOG2E, -mx * LOG2E}, b = {mn * LOG2E, mn * LOG2E}, c = {LOG2E, LOG2E};
  f32x2 sp = {0.f, 0.f}, sm = {0.f, 0.f};
#pragma unroll
  for (int i = 0; i < 16; i += 2) {
    const f32x2 x = {p[i], p[i + 1]};
    const f32x2 u = __builtin_elementwise_fma(x, c, a), v = __builtin_elementwise_fma(x, -c, b);
    const f32x2 eu = {__builtin_amdgcn_exp2f(u[0]), __builtin_amdgcn_exp2f(u[1])}, ev = {__builtin_amdgcn_exp2f(v[0]), __builtin_amdgcn_exp2f(v[1])};
    ep[i] = eu[0]; ep[i + 1] = eu[1]; em[i] = ev[0]; em[i + 1] = ev[1];
    if (i == 0) { sp = eu; sm = ev; } else { sp += eu; sm += ev; }
  }
  if (NORM) {
    const float rp = __builtin_amdgcn_rcpf(xhalf_sum(sp[0] + sp[1])), rm = __builtin_amdgcn_rcpf(xhalf_sum(sm[0] + sm[1]));
    ep *= rp; em *= rm;
  }
}
DI bf16x8 row_sum_sel(int lane) {
  const unsigned u = ((lane & 11) == 0 && (((lane >> 2) ^ (lane >> 4)) & 1) == 0) ? 0x3f803f80u : 0u;
  return __builtin_bit_cast(bf16x8, u32x4{u, u, u, u});
}

constexpr int LDS_BYTES = 163840;
constexpr int P1_WOFF = 32768, P1_EOFF = 49152, P1_STAGE = 53248, P1_ZRED = 3 * P1_STAGE, P1_CNT = P1_ZRED + 2048, P1_GO = P1_CNT + 16, P1_PHI = 65536;
constexpr int P2_Q = 131072, P2_KVF = 147456, P2_ZB = 151552;
constexpr int P2_XO = 32768, P2_XL = 0;
#define NO 4
constexpr int NW = 10 - NO;
constexpr int P3_O = 0, P3_W = NO * 16384;
DI constexpr int p3_wslot(int t) { return P3_W + ((t + NW - 4) % NW) * 16384; }
DI constexpr int p3_inflight(int t) {
  int n = 0, after = 0;
  for (int j = 0; j < NO; ++j) { n += 4; if (j == t + 1) after = n; }
  for (int u = 0; u < t; ++u) {
    if (u + NW < 16) { n += 4; if (u + NW == t + 1) after = n; }
    if (u + NO < 16) { n += 4; if (u + NO == t + 1) after = n; }
  }
  return n - after;
}
template <int N> DI void wait_bar() { asm volatile("s_waitcnt vmcnt(%0) lgkmcnt(0)\n\ts_barrier" ::"i"(N) : "memory"); }

template <int TYPE>
DI void p1_compute(unsigned lds0, char* __restrict__ ws, rsrc_t sws, int rb, int ct, int w, int lane) {
  const int wm = w & 3, wn = w >> 2, r32 = lane & 31, h = lane >> 5;
  if (w == 0 && lane == 0) *(AS3 unsigned*)(lds0 + P1_GO) = 0u;

  const int xrow = wm * 64 + r32;
  const unsigned xa = xrow * 128 + ((h ^ ((xrow >> 1) & 7)) << 4);
  const int q4 = (lane & 15) >> 2, p4 = lane & 3, chalf = (lane >> 4) & 1;
  unsigned wa[2];
#pragma unroll
  for (int ns = 0; ns < 2; ++ns)
    wa[ns] = P1_WOFF + (8 * h + q4) * 256 + ((((wn * 2 + ns) ^ q4) << 2) | (chalf * 2 + (p4 >> 1))) * 16 + (p4 & 1) * 8;
  const int erow = 64 * (ct >> 1) + 16 * wm + (lane & 15);
  const unsigned exa = erow * 128 + (((lane >> 4) ^ ((erow >> 1) & 7)) << 4);
  const unsigned ewa = P1_EOFF + (8 * (lane >> 4) + q4) * 64 + wn * 32 + p4 * 8;

  f32x16 acc[2][2] = {};
  f32x4 eacc = {};
  bf16x8 xf[2][2], wfr[2][2], ex[2], ew[2];
  auto rdx = [&](int set, unsigned buf, int ks) {
#pragma unroll
    for (int ms = 0; ms < 2; ++ms) xf[set][ms] = lds_r128(buf + ((xa ^ (ks * 32)) + ms * 4096));
  };
  auto rdw = [&](int set, unsigned buf, int ks, int ns) { wfr[set][ns] = lds_tr2(buf + wa[ns] + ks * 4096, buf + wa[ns] + ks * 4096 + 1024); };
  auto mm1 = [&](int set, int ms, int ns) {
    if (TYPE == 0 && ns == 1) acc[ms][1] = MFMA32(xf[set][ms], wfr[set][1], acc[ms][1]);
    else                      acc[ms][ns] = MFMA32(wfr[set][ns], xf[set][ms], acc[ms][ns]);
  };
  auto erd = [&](unsigned buf, int kk) {
    if (TYPE >= 2) { ex[kk] = lds_r128(buf + (exa ^ (kk * 64))); ew[kk] = lds_tr2(buf + ewa + kk * 2048, buf + ewa + kk * 2048 + 256); }
  };
  auto emm = [&](int kk) {
    if (TYPE == 2) eacc = MFMA16(ew[kk], ex[kk], eacc);
    if (TYPE == 3) eacc = MFMA16(ex[kk], ew[kk], eacc);
  };
  auto sub = [&](int set, unsigned buf, int ks) {
    SBAR(); mm1(set, 0, 0); SBAR(); rdx(set ^ 1, buf, ks);
    SBAR(); mm1(set, 1, 0); SBAR(); rdw(set ^ 1, buf, ks, 0);
    SBAR(); mm1(set, 0, 1); SBAR(); rdw(set ^ 1, buf, ks, 1);
    SBAR(); mm1(set, 1, 1); SBAR();
  };
  LDS_BAR();
  STAMP(0, 1);
  unsigned cur = lds0, nxt = lds0 + P1_STAGE;
  rdx(0, cur, 0); rdw(0, cur, 0, 0); rdw(0, cur, 0, 1);
  LGKM0();
#pragma unroll 1
  for (int t = 0; t < 16; ++t) {
    sub(0, cur, 1); erd(cur, 0);
    sub(1, cur, 2); erd(cur, 1);
    sub(0, cur, 3); emm(0); emm(1); SBAR();
    LDS_BAR();
    sub(1, nxt, 0);
    cur = nxt; nxt = (nxt == lds0 + 2 * P1_STAGE) ? lds0 : nxt + P1_STAGE;
  }
  STAMP(0, 2);
  bf16x8 wf[2][2];
  if constexpr (TYPE <= 1) {
    const unsigned a = lds0 + P1_PHI + (TYPE == 0 ? wn : 2 * wn) * 2048 + (4 * h + q4) * 64 + chalf * 32 + p4 * 8;
#pragma unroll
    for (int n = 0; n <= TYPE; ++n)
#pragma unroll
      for (int s = 0; s < 2; ++s) wf[n][s] = lds_tr2(a + n * 2048 + s * 1024, a + n * 2048 + s * 1024 + 512);
  }

  if (TYPE >= 2) {
#pragma unroll
    for (int ms = 0; ms < 2; ++ms)
#pragma unroll
      for (int ns = 0; ns < 2; ++ns) {
        const f32x16 v = acc[ms][ns] * QSCALE;
        const unsigned p = WS_QB + ((rb * 8 + wm * 2 + ms) * 32 + ct * 4 + wn * 2 + ns) * 2048 + lane * 16;
        st16(sws, p, pack8<0>(v)); st16(sws, p + 1024, pack8<1>(v));
      }
    const int g = lane >> 4, tok0 = rb * 256 + 64 * (ct >> 1) + 16 * wm;
    unsigned p = WS_KV + (tok0 >> 5) * 4096;
    if (TYPE == 3) p += 2048 + (wm & 1) * 1024 + (16 * wn + (lane & 15) + 32 * (g >> 1)) * 16 + (g & 1) * 8;
    else           p += wn * 1024 + (16 * (wm & 1) + (lane & 15) + 32 * (g & 1)) * 16 + (g >> 1) * 8;
    st8(sws, p, u32x2{pk2(eacc[0], eacc[1]), pk2(eacc[2], eacc[3])});
    WAIT_BAR(0);
  } else if (TYPE == 1) {
    f32x16 pq[2][2];
#pragma unroll
    for (int ms = 0; ms < 2; ++ms)
#pragma unroll
      for (int ns = 0; ns < 2; ++ns) {
        pq[ms][ns] = MFMA32(wf[ns][0], pack8<0>(acc[ms][ns]), f32x16{});
        pq[ms][ns] = MFMA32(wf[ns][1], pack8<1>(acc[ms][ns]), pq[ms][ns]);
      }
#pragma unroll
    for (int ms = 0; ms < 2; ++ms)
#pragma unroll
      for (int ns = 0; ns < 2; ++ns) {
        f32x16 ep, em;
        feat_map<false>(pq[ms][ns], ep, em);
        const unsigned p = WS_QA + ((rb * 8 + wm * 2 + ms) * 32 + ct * 4 + wn * 2 + ns) * 4096 + lane * 16;
        st16(sws, p, pack8<0>(ep)); st16(sws, p + 1024, pack8<1>(ep)); st16(sws, p + 2048, pack8<0>(em)); st16(sws, p + 3072, pack8<1>(em));
      }
  } else {
    const unsigned img = lds0 + w * 8192;
    const bf16x8 sel = row_sum_sel(lane);
    f32x16 pk[2], kv[2] = {};
    f32x4 zz[2] = {};
    bf16x8 vf[2][2], af[2][2];
#pragma unroll
    for (int ms = 0; ms < 2; ++ms) {
      pk[ms] = MFMA32(wf[0][0], pack8<0>(acc[ms][0]), f32x16{});
      pk[ms] = MFMA32(wf[0][1], pack8<1>(acc[ms][0]), pk[ms]);
    }
#pragma unroll
    for (int ms = 0; ms < 2; ++ms) { vf[ms][0] = pack8<0>(acc[ms][1]); vf[ms][1] = pack8<1>(acc[ms][1]); PIN(vf[ms][0]); PIN(vf[ms][1]); }
    float off[4];
#pragma unroll
    for (int ms = 0; ms < 2; ++ms) { off[2 * ms] = -LOG2E * xhalf_max(max16(pk[ms])); off[2 * ms + 1] = LOG2E * xhalf_min(min16(pk[ms])); }
    const unsigned wr = img + 4096 + r32 * 64, sw = (r32 >> 1) & 7;
    const int t0 = 4 * h + q4, u8 = 4 * chalf + p4;
    const unsigned rd0 = img + 4096 + t0 * 64 + ((u8 ^ (t0 >> 1)) << 3), rd1 = img + 4096 + (t0 + 8) * 64 + ((u8 ^ ((t0 >> 1) + 4)) << 3);
    auto put = [&](int sg) {
#pragma unroll
      for (int g = 0; g < 4; ++g) *(AS3 f32x4*)(img + sg * 4096 + g * 1024 + lane * 16) = f32x4{kv[sg][4 * g], kv[sg][4 * g + 1], kv[sg][4 * g + 2], kv[sg][4 * g + 3]};
    };
    f32x16 e[2];
    float rs[2];
    auto mm = [&](int u, int j) {
      if (j & 1) zz[u & 1] = MFMA16(sel, af[u & 1][j >> 1], zz[u & 1]); else kv[u & 1] = MFMA32(af[u & 1][j >> 1], vf[u >> 1][j >> 1], kv[u & 1]);
    };
    auto exps = [&](int u, int mu) {
      const f32x16& p = pk[u >> 1];
      const float c = (u & 1) ? -LOG2E : LOG2E;
      f32x2 sm;
#pragma unroll
      for (int i = 0; i < 16; i += 2) {
        if (mu >= 0 && i >= 8) { SBAR(); mm(mu, (i - 8) >> 1); SBAR(); }
        const f32x2 v = __builtin_elementwise_fma(f32x2{p[i], p[i + 1]}, f32x2{c, c}, f32x2{off[u], off[u]});
        const f32x2 ev = {__builtin_amdgcn_exp2f(v[0]), __builtin_amdgcn_exp2f(v[1])};
        e[u & 1][i] = ev[0]; e[u & 1][i + 1] = ev[1];
        if (i == 0) sm = ev; else sm += ev;
      }
      rs[u & 1] = __builtin_amdgcn_rcpf(xhalf_sum(sm[0] + sm[1]));
    };
    auto rest = [&](int u) {
      const f32x16 n = e[u & 1] * rs[u & 1];
#pragma unroll
      for (int g = 0; g < 4; ++g) *(AS3 u32x2*)(wr + (u & 1) * 2048 + (((2 * g + h) ^ sw) << 3)) = u32x2{pk2(n[4 * g], n[4 * g + 1]), pk2(n[4 * g + 2], n[4 * g + 3])};
      asm volatile("" ::: "memory");
#pragma unroll
      for (int s = 0; s < 2; ++s) af[u & 1][s] = lds_tr2(rd0 + (u & 1) * 2048 + s * 1024, rd1 + (u & 1) * 2048 + s * 1024);
      asm volatile("" ::: "memory");
      SBAR();
    };
    exps(0, -1); exps(1, -1); rest(0);
    exps(2, 0);  rest(1);
    exps(3, 1);  rest(2);
    rest(3);
    mm(2, 0); mm(2, 2); mm(3, 0); mm(3, 2); SBAR(); mm(2, 1); mm(2, 3); mm(3, 1); mm(3, 3);
    put(0); put(1);
    if (lane < 32) {
      const unsigned a = lds0 + P1_ZRED + w * 256 + ((lane >> 2) & 1) * 64 + (lane >> 3) * 16 + (lane & 3) * 4;
      *(AS3 float*)a = zz[0][0]; *(AS3 float*)(a + 128) = zz[1][0];
    }
  }
  STAMP(0, 3);
}

DI void attn_pass_fast(unsigned lds0, unsigned qa, bf16x8 (&q)[2][2], bf16x8 sel, int kh, int lane, f32x16 (&O)[2], f32x4 (&L)[2], unsigned& tv, char* ws) {
  typedef const AS3 char* lp;
  lp l0 = (lp)(lds0 + lane * 16), la = l0 + kh * 16384;
#define RD(p) (*(const AS3 bf16x8*)(p))
  f32x16 negm[2], S0, S1 = {};
  bf16x8 vb = bf16x8{};
  int odd = 0xDDDD;
  O[0] = f32x16{}; O[1] = f32x16{}; L[0] = f32x4{}; L[1] = f32x4{};
  unsigned a0 = lds0 + lane * 16 + 98304, a1 = a0 + kh * 16384, qb = qa ^ 2048;
  PIN(O[0]); PIN(O[1]); PIN(L[0]); PIN(L[1]); PIN(S1); PIN(vb); PIN(odd); PIN(sel); PIN(a0); PIN(a1); PIN(qa); PIN(qb);
  LDS_BAR();
  STAMP(0, 4);
  TS(0);
  if (kh) __builtin_amdgcn_s_setprio(1);
  const bf16x8 ra = lds_r128(a0);
  q[0][0] = lds_r128(qa); q[1][0] = lds_r128(qb);
  const bf16x8 rb = lds_r128(a0 + 1024);
  q[0][1] = lds_r128(qa + 1024); q[1][1] = lds_r128(qb + 1024);
  bf16x8 ka = lds_r128(a1), kb = lds_r128(a1 + 1024), va = lds_r128(a1 + 2048);
  SBAR();
  negm[0] = MFMA32(ra, q[0][0], f32x16{}); negm[1] = MFMA32(ra, q[1][0], f32x16{});
  negm[0] = MFMA32(rb, q[0][1], negm[0]);  negm[1] = MFMA32(rb, q[1][1], negm[1]);
#pragma unroll
  for (int r = 0; r < 2; ++r) {
    const float m = xhalf_max(max16(negm[r]));
#pragma unroll
    for (int i = 0; i < 16; ++i) negm[r][i] = -m;
    PIN(negm[r]);
    SBAR();
    if (r == 0) { S0 = MFMA32(ka, q[0][0], negm[0]); S0 = MFMA32(kb, q[0][1], S0); PIN(S0); SBAR(); }
  }
#define EXN(SC, i, n) do { _Pragma("unroll") for (int k_ = 0; k_ < n; ++k_) SC[i + k_] = __builtin_amdgcn_exp2f(SC[i + k_]); PIN(SC); } while (0)
#define MM(D, E) do { SBAR(); D = E; PIN(D); SBAR(); } while (0)
#define TILE(TA, TN) do { \
    SBAR();                                                             EXN(S0, 0, 2);  \
    MM(O[1], SM32(vb, HI(S1), O[1]));       vb = RD((TA) + 3072);       EXN(S0, 2, 3);  \
    MM(L[1], SM16(sel, HI(S1), L[1]));                                  EXN(S0, 5, 2);  \
    MM(S1, MFMA32(ka, q[1][0], negm[1]));   ka = RD(TN);                EXN(S0, 7, 3);  \
    MM(S1, MFMA32(kb, q[1][1], S1));        kb = RD((TN) + 1024);       EXN(S0, 10, 3); \
    MM(O[0], SM32(va, LO(S0), O[0]));                                   EXN(S0, 13, 3); \
    MM(L[0], SM16(sel, LO(S0), L[0]));                                  EXN(S1, 0, 2);  \
    MM(O[0], SM32(vb, HI(S0), O[0]));                                   EXN(S1, 2, 3);  \
    MM(L[0], SM16(sel, HI(S0), L[0]));                                  EXN(S1, 5, 2);  \
    MM(S0, MFMA32(ka, q[0][0], negm[0]));                               EXN(S1, 7, 3);  \
    MM(S0, MFMA32(kb, q[0][1], S0));                                    EXN(S1, 10, 3); \
    MM(O[1], SM32(va, LO(S1), O[1]));       va = RD((TN) + 2048);       EXN(S1, 13, 3); \
    MM(L[1], SM16(sel, LO(S1), L[1])); } while (0)
  TS(1);
#pragma unroll 1
  for (int c = 0; c < 8; ++c) {
    const lp cb = la + ((3 - c) & 3) * 32768, nb = la + ((2 - c) & 3) * 32768;
    TILE(cb, cb + 4096);
    TILE(cb + 4096, cb + 8192);
    SBAR(); if (kh) __builtin_amdgcn_s_setprio(0);
    TILE(cb + 8192, cb + 12288);
    SBAR(); TS(2 + 2 * c);
    asm volatile("s_waitcnt vmcnt(0)\n\ts_barrier" ::: "memory"); TS(3 + 2 * c);
    if (kh) __builtin_amdgcn_s_setprio(1);
    TILE(cb + 12288, nb);
  }
  SBAR();
  O[1] = SM32(vb, HI(S1), O[1]); L[1] = SM16(sel, HI(S1), L[1]);
  __builtin_amdgcn_s_setprio(0);
  TS(18);
#undef TILE
#undef EXN
#undef MM
#undef RD
}

DI void attn_pass_safe(rsrc_t sws, const bf16x8 q0, const bf16x8 q1, const bf16x8 sel, int lane, f32x16& O, f32x4& L) {
  O = f32x16{}; L = f32x4{};
  float m = -INFINITY;
  const int odd = 0xDDDD;
#pragma unroll 1
  for (int t = 0; t < 64; ++t) {
    bf16x8 a[4];
#pragma unroll
    for (int j = 0; j < 4; ++j) a[j] = __builtin_bit_cast(bf16x8, ld16(sws, WS_KV + t * 4096 + j * 1024 + lane * 16));
    f32x16 Sc = {};
    Sc = MFMA32(a[0], q0, Sc); Sc = MFMA32(a[1], q1, Sc);
    const float mn = fmaxf(m, xhalf_max(max16(Sc)));
    const float al = __builtin_amdgcn_exp2f(m - mn);
    m = mn;
#pragma unroll
    for (int r = 0; r < 16; ++r) { O[r] *= al; Sc[r] = __builtin_amdgcn_exp2f(Sc[r] - mn); }
    L *= al;
    O = SM32(a[2], LO(Sc), O); L = SM16(sel, LO(Sc), L);
    O = SM32(a[3], HI(Sc), O); L = SM16(sel, HI(Sc), L);
  }
}

DI void helper_program(unsigned lds0, const unsigned short* __restrict__ X, const unsigned short* __restrict__ W, const unsigned short* __restrict__ E,
                       const unsigned short* __restrict__ woa, const unsigned short* __restrict__ wob, char* __restrict__ ws, rsrc_t sws,
                       int type, int rb, int ct, int x, int idx, int lw, int lane) {
  __builtin_amdgcn_s_setprio(3);
  u64 e0;
  const unsigned vl = lane * 16;
  {
    const bool ext = type >= 2;
    const unsigned ldw = ext ? 2048u : 6144u;
    const rsrc_t sx = mk_rsrc(X + (size_t)rb * 256 * D, 256u * 2048u);
    const rsrc_t sw = mk_rsrc(W, 1024u * ldw);
    const rsrc_t se = mk_rsrc(E, 1024u * 64u);
    unsigned xv[8], wv[4];
#pragma unroll
    for (int j = 0; j < 8; ++j) {
      const int row = 8 * (8 * lw + j) + (lane >> 3), c = (lane & 7) ^ ((row >> 1) & 7);
      xv[j] = row * 2048 + c * 16 - (j & 3) * 1024;
    }
#pragma unroll
    for (int j = 0; j < 4; ++j) {
      const int k = 4 * (4 * lw + j) + (lane >> 4), c = (lane & 15) ^ (((lane >> 4) & 3) << 2);
      const int col = (type == 0) ? 1024 + ((c >> 2) & 1) * 1024 + (2 * ct + (c >> 3)) * 32 + (c & 3) * 8 : ct * 128 + c * 8;
      wv[j] = k * ldw + col * 2 - j * 1024;
    }
    auto stage = [&](int t0, unsigned buf) {
      const int t = (t0 + (ext ? BROT : KROT)) & 15;
      const unsigned sox = t * 128, sow = t * 64 * ldw;
      set_m0(buf + P1_WOFF + lw * 4096);
      DMAW<0>(wv[0], sw, sow); DMAW<1024>(wv[1], sw, sow); DMAW<2048>(wv[2], sw, sow); DMAW<3072>(wv[3], sw, sow);
      if (ext ? (NTX & 2) : (NTX & 1)) {
        set_m0(buf + lw * 8192);
        dma16n<0>(xv[0], sx, sox); dma16n<1024>(xv[1], sx, sox); dma16n<2048>(xv[2], sx, sox); dma16n<3072>(xv[3], sx, sox);
        set_m0(buf + lw * 8192 + 4096);
        dma16n<0>(xv[4], sx, sox); dma16n<1024>(xv[5], sx, sox); dma16n<2048>(xv[6], sx, sox); dma16n<3072>(xv[7], sx, sox);
      } else {
        set_m0(buf + lw * 8192);
        dma16<0>(xv[0], sx, sox); dma16<1024>(xv[1], sx, sox); dma16<2048>(xv[2], sx, sox); dma16<3072>(xv[3], sx, sox);
        set_m0(buf + lw * 8192 + 4096);
        dma16<0>(xv[4], sx, sox); dma16<1024>(xv[5], sx, sox); dma16<2048>(xv[6], sx, sox); dma16<3072>(xv[7], sx, sox);
      }
      if (ext) { set_m0(buf + P1_EOFF + lw * 1024); dma16<0>(vl, se, t * 4096 + lw * 1024); }
    };
    stage(0, lds0); stage(1, lds0 + P1_STAGE);
    if (ext) WAIT_BAR(13); else WAIT_BAR(12);
    stage(2, lds0 + 2 * P1_STAGE);
    unsigned buf = lds0;
#pragma unroll 1
    for (int t = 0; t < 13; ++t) {
      if (ext) WAIT_BAR(13); else WAIT_BAR(12);
      stage(t + 3, buf);
      buf = (buf == lds0 + 2 * P1_STAGE) ? lds0 : buf + P1_STAGE;
    }
    if (ext) WAIT_BAR(13); else WAIT_BAR(12);
    e0 = __hip_atomic_load((AS1 u64*)(ws + WS_FLG) + F_EPOCH, __ATOMIC_RELAXED, __HIP_MEMORY_SCOPE_AGENT);
    if (!ext && (type == 1 || lw < 2)) {
      set_m0(lds0 + P1_PHI + lw * 2048);
      const unsigned so = ((type == 0 ? 2 * ct : 4 * ct) + lw) * 2048;
      dma16<0>(vl, se, so); dma16<1024>(vl, se, so);
    }
    WAIT_BAR(0);
    WAIT_BAR(0);
  }
  const u64 tag = ((u64)(unsigned)__builtin_amdgcn_readfirstlane((unsigned)(e0 >> 32)) << 32 | (unsigned)__builtin_amdgcn_readfirstlane((unsigned)e0))
                  * 0x5851F42D4C957F2Dull + 0x14057B7EF767814Full;
  const bool lead = (lw == 0) && (lane == 0);
  if (type >= 2) { WAIT_BAR(0); if (lead) post(ws, F_KV + rb * 16 + ct, tag); }
  auto chunk = [&](int c) {
    const unsigned l = lds0 + ((3 - c) & 3) * 32768 + lw * 8192, so = WS_KV + ((c + x) & 7) * 32768 + lw * 8192;
    dma4k(l, vl, sws, so); dma4k(l + 4096, vl, sws, so + 4096);
  };
  STAMP(512, 10);
  if (lw == 0) {
    poll(ws, F_KV + x * 16 + (lane & 7), tag);
    __hip_atomic_store((AS3 unsigned*)(lds0 + P1_GO), 1u, __ATOMIC_RELAXED, __HIP_MEMORY_SCOPE_WORKGROUP);
  } else {
    while (__hip_atomic_load((AS3 unsigned*)(lds0 + P1_GO), __ATOMIC_RELAXED, __HIP_MEMORY_SCOPE_WORKGROUP) == 0u) __builtin_amdgcn_s_sleep(1);
    asm volatile("" ::: "memory");
  }
  STAMP(512, 11);
  dma4k(lds0 + P2_Q + lw * 4096, vl, sws, WS_QB + ((8 * x + (idx >> 2)) * 32 + 8 * (idx & 3)) * 2048 + lw * 4096);
  chunk(0);
  WAIT_BAR(0);
  poll(ws, F_KV + (lane >> 3) * 16 + (lane & 7), tag);
  chunk(1);
  if (type == 0) {
#pragma unroll
    for (int j = 0; j < 4; ++j) {
      const unsigned a = lds0 + (lw >> 1) * 32768 + ((lw & 1) * 4 + j) * 1024 + vl;
      const f32x4 sm = add4(add4(*(AS3 f32x4*)a, *(AS3 f32x4*)(a + 8192)), add4(*(AS3 f32x4*)(a + 16384), *(AS3 f32x4*)(a + 24576)));
      st16(sws, WS_KVP + ((2 * ct + (lw >> 1)) * 8 + rb) * 8192 + ((lw & 1) * 4 + j) * 1024 + vl, sm);
    }
    if (lw < 2) {
      const unsigned a = lds0 + P1_ZRED + lw * 1024 + lane * 4;
      st4(sws, WS_ZP + (((2 * ct + lw) * 8 + rb) * 64 + lane) * 4, one(*(AS3 float*)a + *(AS3 float*)(a + 256)) + one(*(AS3 float*)(a + 512) + *(AS3 float*)(a + 768)));
    }
  }
  WAIT_BAR(0);
  chunk(2); chunk(3);
  if (lead && type < 2) post(ws, type == 0 ? F_K + ct * 8 + rb : F_Q + rb * 8 + ct, tag);
  const int br = !(x & 1), rb3 = 4 * (x >> 1) + (idx & 3), cb = idx >> 2;
  const unsigned short* wo = br ? wob : woa;
  unsigned pfs = 0;
  auto warm_wo = [&]() {
    pf_line(pfs, (const char*)wo + idx * 65536 + lw * 16384 + lane * 128);
    pf_line(pfs, (const char*)wo + idx * 65536 + lw * 16384 + 8192 + lane * 128);
  };
  if (lw == 1) *(AS3 unsigned*)(lds0 + P2_ZB + 128 + lane * 4) = lane < 32 ? 0x3f803f80u : 0u;
  WAIT_BAR(8);
  if (br) {
    const int head = 4 * x + (idx & 7), tp = idx >> 3;
    poll(ws, lane < 8 ? F_K + (head >> 1) * 8 + lane : F_Q + (2 * tp + (lane & 1)) * 8 + (head >> 2), tag);
    STAMP(512, 12);
    chunk(4);
    u32x4 kvp[2][8], fq[4][4];
    float zs[8];
    auto ldq = [&](int e) {
#pragma unroll
      for (int j = 0; j < 4; ++j) fq[e][j] = ld16(sws, WS_QA + ((16 * tp + 4 * lw + e) * 32 + head) * 4096 + j * 1024 + vl);
    };
#pragma unroll
    for (int e = 0; e < 2; ++e)
#pragma unroll
      for (int p = 0; p < 8; ++p) kvp[e][p] = ld16(sws, WS_KVP + ((head * 8 + p) * 512 + (2 * lw + e) * 64 + lane) * 16);
#pragma unroll
    for (int p = 0; p < 8; ++p) zs[p] = ld4(sws, WS_ZP + ((head * 8 + p) * 64 + lane) * 4);
    ldq(0); ldq(1);
    asm volatile("s_barrier" ::: "memory");
#pragma unroll
    for (int e = 0; e < 2; ++e) {
#pragma unroll
      for (int p = 0; p < 8; ++p) PIN(kvp[e][p]);
#define F4(v) __builtin_bit_cast(f32x4, v)
      const f32x4 sm = add4(add4(add4(F4(kvp[e][0]), F4(kvp[e][1])), add4(F4(kvp[e][2]), F4(kvp[e][3]))), add4(add4(F4(kvp[e][4]), F4(kvp[e][5])), add4(F4(kvp[e][6]), F4(kvp[e][7]))));
#undef F4
      *(AS3 u32x2*)(lds0 + P2_KVF + lw * 1024 + vl + e * 8) = u32x2{pk2(sm[0], sm[1]), pk2(sm[2], sm[3])};
    }
    {
#pragma unroll
      for (int p = 0; p < 8; ++p) PIN(zs[p]);
      const float z = one(one(zs[0] + zs[1]) + one(zs[2] + zs[3])) + one(one(zs[4] + zs[5]) + one(zs[6] + zs[7]));
      if (lw == 0) *(AS3 unsigned short*)(lds0 + P2_ZB + lane * 2) = (unsigned short)(pk2(z, z) & 0xffffu);
    }
    chunk(5);
    ldq(2); ldq(3);
    const int row = lane & 15, kg = lane >> 4;
    const bool on = (((row >> 2) ^ kg) & 1) == 0;
    const unsigned za = lds0 + P2_ZB + ((on && (row & 3) == 0) ? (kg >> 1) * 32 : (on && (row & 3) == 1) ? 128 : 256);
    const unsigned ka = lds0 + P2_KVF + vl;
    auto otile = [&](int e) {
#define BF(v) __builtin_bit_cast(bf16x8, v)
      f32x16 np = MFMA32(lds_r128(ka), BF(fq[e][0]), f32x16{});        f32x4 dp = MFMA16(lds_r128(za), BF(fq[e][0]), f32x4{});
      np = MFMA32(lds_r128(ka + 1024), BF(fq[e][1]), np);              dp = MFMA16(lds_r128(za + 16), BF(fq[e][1]), dp);
      f32x16 nm = MFMA32(lds_r128(ka + 2048), BF(fq[e][2]), f32x16{}); f32x4 dm = MFMA16(lds_r128(za + 64), BF(fq[e][2]), f32x4{});
      nm = MFMA32(lds_r128(ka + 3072), BF(fq[e][3]), nm);              dm = MFMA16(lds_r128(za + 80), BF(fq[e][3]), dm);
#undef BF
      const float a = __builtin_amdgcn_rcpf(dp[1]), b = __builtin_amdgcn_rcpf(dm[1]);
      const float r = __builtin_amdgcn_rcpf(one(dp[0] * a) + one(dm[0] * b) + 1e-6f), ar = one(a * r), br = one(b * r);
      f32x16 o;
#pragma unroll
      for (int i = 0; i < 16; ++i) o[i] = one(__builtin_fmaf(nm[i], br, one(np[i] * ar)));
      const unsigned p = WS_OA + ((16 * tp + 4 * lw + e) * 32 + head) * 2048 + vl;
      st16(sws, p, pack8<0>(o)); st16(sws, p + 1024, pack8<1>(o));
    };
    WAIT_BAR(16);
#pragma unroll
    for (int j = 0; j < 4; ++j) { PIN(fq[0][j]); PIN(fq[1][j]); }
    chunk(6);
    otile(0); otile(1);
    WAIT_BAR(12);
#pragma unroll
    for (int j = 0; j < 4; ++j) { PIN(fq[2][j]); PIN(fq[3][j]); }
    chunk(7);
    otile(2); otile(3);
    WAIT_BAR(0);
    if (lead) post(ws, F_OA + tp * 32 + head, tag);
    warm_wo();
  } else {
    chunk(4); warm_wo(); WAIT_BAR(10);
    chunk(5); WAIT_BAR(10);
    chunk(6); WAIT_BAR(8);
    chunk(7);
    if (blockIdx.x == 1 && lw == 1) {
      poll(ws, lane, tag); poll(ws, 64 + lane, tag); poll(ws, 128 + lane, tag);
      if (lane == 0) post(ws, F_EPOCH, tag);
    }
    WAIT_BAR(8);
  }
  const rsrc_t sw = mk_rsrc(wo, 1024u * 2048u);
  unsigned wv[4];
#pragma unroll
  for (int j = 0; j < 4; ++j) {
    const int k = 4 * (4 * lw + j) + (lane >> 4), c = (lane & 15) ^ (((lane >> 4) & 3) << 2);
    wv[j] = k * 2048 + (cb * 128 + c * 8) * 2 - j * 1024;
  }
  const unsigned osrc = (br ? WS_OB : WS_OA) + (rb3 * 4 + lw) * 65536;
  auto wst = [&](int t) {
    const unsigned sow = t * 64 * 2048;
    set_m0(lds0 + p3_wslot(t) + lw * 4096);
    dma16<0>(wv[0], sw, sow); dma16<1024>(wv[1], sw, sow); dma16<2048>(wv[2], sw, sow); dma16<3072>(wv[3], sw, sow);
  };
  auto ost = [&](int t) { dma4k(lds0 + P3_O + (t % NO) * 16384 + lw * 4096, vl, sws, osrc + t * 4096); };
  wst(0); wst(1); wst(2); wst(3);
  if (br) asm volatile("s_barrier" ::: "memory"); else WAIT_BAR(16);
  if (NW > 4) wst(4);
  if (NW > 5) wst(5);
  if (!br && lw == 0) poll(ws, F_OA + (rb3 >> 2) * 32 + (lane & 31), tag);
  asm volatile("s_barrier" ::: "memory");
  asm volatile("s_barrier" ::: "memory");
  asm volatile("s_barrier" ::: "memory");
  if (lead) post(ws, F_OB + x * 32 + idx, tag);
  STAMP(512, 13);
  if (br && lw == 0) poll(ws, F_OB + (rb3 >> 1) * 32 + (rb3 & 1) * 16 + (lane & 15), tag);
  asm volatile("s_barrier" ::: "memory");
  STAMP(512, 14);
#pragma unroll
  for (int t = 0; t < NO; ++t) ost(t);
  wait_bar<4 * (NO - 1)>();
  PIN(pfs);
#define RND(t) do { wait_bar<p3_inflight(t)>(); if (t + NW < 16) wst(t + NW); if (t + NO < 16) ost(t + NO); } while (0)
  RND(0); RND(1); RND(2); RND(3); RND(4); RND(5); RND(6); RND(7); RND(8); RND(9); RND(10); RND(11); RND(12); RND(13); RND(14);
#undef RND
}

__global__ __launch_bounds__(768, 3) void fused_fwd(const unsigned short* __restrict__ xa, const unsigned short* __restrict__ wqkv,
                                                    const unsigned short* __restrict__ wphi, const unsigned short* __restrict__ woa,
                                                    const unsigned short* __restrict__ xb, const unsigned short* __restrict__ wq,
                                                    const unsigned short* __restrict__ wk, const unsigned short* __restrict__ wv,
                                                    const unsigned short* __restrict__ wob, char* __restrict__ ws, unsigned short* __restrict__ out) {
  __shared__ __attribute__((aligned(1024))) char smem[LDS_BYTES];
  const unsigned lds0 = (unsigned)(uintptr_t)(AS3 char*)smem;
  const int tid = threadIdx.x, lane = tid & 63, w0 = __builtin_amdgcn_readfirstlane(tid >> 6), w = HF ? (w0 < 4 ? w0 + 8 : w0 - 4) : w0;
  asm volatile("" : "+s"(xa), "+s"(wqkv), "+s"(wphi), "+s"(xb), "+s"(wq), "+s"(wk), "+s"(wv), "+s"(ws));
  const int b = blockIdx.x, x = b & 7, idx = b >> 3;
  const int rb = 4 * ((x >> 1) & 1) + (idx & 3);
  const int ct = idx < 16 ? 8 * (x >> 2) + 4 * (x & 1) + (idx >> 2) : 4 * (x >> 2) + ((idx >> 2) & 3);
  const int type = idx < 16 ? 0 : (x & 1) ? 1 : 2 + (ct & 1);
  const rsrc_t sws = mk_rsrc(ws, WS_END);
  STAMP(0, 0);
  if (HX(w >= 8)) {
    helper_program(lds0, type >= 2 ? xb : xa, type >= 2 ? wq : wqkv, type == 3 ? wv : type == 2 ? wk : wphi, woa, wob, ws, sws, type, rb, ct, x, idx, w - 8, lane);
    return;
  }
  unsigned pfs = 0;
  if (type == 0 && !(x & 1) && w == 0) pf_line(pfs, (const char*)(lane < 32 ? wv : wk) + (idx * 32 + (lane & 31)) * 128);
  if (type == 0)      p1_compute<0>(lds0, ws, sws, rb, ct, w, lane);
  else if (type == 1) p1_compute<1>(lds0, ws, sws, rb, ct, w, lane);
  else if (type == 2) p1_compute<2>(lds0, ws, sws, rb, ct, w, lane);
  else                p1_compute<3>(lds0, ws, sws, rb, ct, w, lane);

  const int kh = w >> 2, r32 = lane & 31, h = lane >> 5;
  {
    const unsigned s0 = (lane & 15) == 0 ? 0x3f803f80u : 0u, s4 = (lane & 15) == 4 ? 0x3f803f80u : 0u;
    const bf16x8 sel = __builtin_bit_cast(bf16x8, u32x4{s0, s0, s4, s4});
    bf16x8 q[2][2];
    f32x16 O[2]; f32x4 L[2];
    unsigned tv = 0;
    attn_pass_fast(lds0, lds0 + P2_Q + (2 * (w & 3) + (kh ^ 1)) * 2048 + lane * 16, q, sel, kh, lane, O, L, tv, ws);
    TSOUT();
    STAMP(0, 5);
    const unsigned xs = lds0 + P2_XO + w * 4096 + lane * 16, xr = lds0 + P2_XO + (w ^ 4) * 4096 + lane * 16;
#pragma unroll
    for (int g = 0; g < 4; ++g) *(AS3 f32x4*)(xs + g * 1024) = f32x4{O[0][4 * g], O[0][4 * g + 1], O[0][4 * g + 2], O[0][4 * g + 3]};
    *(AS3 float*)(lds0 + P2_XL + w * 256 + lane * 4) = L[0][0];
    LDS_BAR();
#pragma unroll
    for (int g = 0; g < 4; ++g) {
      const f32x4 o = *(AS3 f32x4*)(xr + g * 1024);
      O[1][4 * g] += o[0]; O[1][4 * g + 1] += o[1]; O[1][4 * g + 2] += o[2]; O[1][4 * g + 3] += o[3];
    }
    L[1][0] += *(AS3 float*)(lds0 + P2_XL + (w ^ 4) * 256 + lane * 4);
    bool bad = !(L[1][0] < 1.8446744e19f);
#pragma unroll
    for (int i = 0; i < 16; ++i) bad |= !(fabsf(O[1][i]) < 3.0e38f);
    if (__any(bad)) attn_pass_safe(sws, q[1][0], q[1][1], sel, lane, O[1], L[1]);
    const float r = __builtin_amdgcn_rcpf(xhalf_sum(L[1][0]));
    O[1] *= r;
    const unsigned p = WS_OB + ((8 * x + (idx >> 2)) * 32 + 8 * (idx & 3) + 2 * (w & 3) + kh) * 2048 + lane * 16;
    st16(sws, p, pack8<0>(O[1])); st16(sws, p + 1024, pack8<1>(O[1]));
    WAIT_BAR(0);
    PIN(pfs);
    STAMP(0, 6);
  }
  const int br = !(x & 1), rb3 = 4 * (x >> 1) + (idx & 3), cb = idx >> 2;
  const int wm = w & 1, wn = (w >> 1) & 1;
  const int q4 = (lane & 15) >> 2, p4 = lane & 3, chalf = (lane >> 4) & 1;
  const unsigned oa = wm * 8192 + kh * (4096 + 2048) + lane * 16;
  unsigned wa[2];
#pragma unroll
  for (int ns = 0; ns < 2; ++ns)
    wa[ns] = (kh * 32 + 4 * h + q4) * 256 + ((((wn * 2 + ns) ^ q4) << 2) | (chalf * 2 + (p4 >> 1))) * 16 + (p4 & 1) * 8;
  f32x16 acc[2][2] = {};
  bf16x8 xf[2][2], wfr[2][2];
  auto rdx = [&](int set, unsigned buf, int ks) {
#pragma unroll
    for (int ms = 0; ms < 2; ++ms) xf[set][ms] = lds_r128(buf + (oa ^ (ms * 4096)) + ks * 1024);
  };
  auto rdw = [&](int set, unsigned buf, int ks, int ns) { wfr[set][ns] = lds_tr2(buf + wa[ns] + ks * 4096, buf + wa[ns] + ks * 4096 + 2048); };
  auto mm1 = [&](int set, int ms, int ns) { acc[ms][ns] = MFMA32(wfr[set][ns], xf[set][ms], acc[ms][ns]); };
  auto sub = [&](int set, bool rd, unsigned ob, unsigned wb, int ks) {
    SBAR(); mm1(set, 0, 0); SBAR(); if (rd) rdx(set ^ 1, ob, ks);
    SBAR(); mm1(set, 1, 0); SBAR(); if (rd) rdw(set ^ 1, wb, ks, 0);
    SBAR(); mm1(set, 0, 1); SBAR(); if (rd) rdw(set ^ 1, wb, ks, 1);
    SBAR(); mm1(set, 1, 1); SBAR();
  };
  asm volatile("s_barrier" ::: "memory");
  LDS_BAR();
  STAMP(0, 7);
  unsigned ob = lds0 + P3_O, wb = lds0 + p3_wslot(0);
  rdx(0, ob, 0); rdw(0, wb, 0, 0); rdw(0, wb, 0, 1);
  LGKM0();
#pragma unroll 1
  for (int t = 0; t < 15; ++t) {
    sub(0, true, ob, wb, 1);
    ob = (ob == lds0 + P3_O + (NO - 1) * 16384) ? lds0 + P3_O : ob + 16384;
    wb = (wb == lds0 + 147456) ? lds0 + P3_W : wb + 16384;
    LDS_BAR();
    sub(1, true, ob, wb, 0);
  }
  sub(0, true, ob, wb, 1);
  sub(1, false, ob, wb, 0);
  STAMP(0, 8);
  constexpr int XB = NO == 5 ? 16384 : 65536, IB = NO == 5 ? 131072 : 0;
  static_assert(NO == 4 || NO == 5 || NO == 6, "");
  const unsigned xs = lds0 + XB + w * 8192 + lane * 16, xr = lds0 + XB + (w ^ 4) * 8192 + lane * 16;
#pragma unroll
  for (int ns = 0; ns < 2; ++ns)
#pragma unroll
    for (int g = 0; g < 4; ++g) {
      const f32x16& a = acc[1][ns];
      *(AS3 f32x4*)(xs + ns * 4096 + g * 1024) = f32x4{a[4 * g], a[4 * g + 1], a[4 * g + 2], a[4 * g + 3]};
    }
  LDS_BAR();
  const unsigned img = lds0 + IB + w * 4096;
#pragma unroll
  for (int ns = 0; ns < 2; ++ns)
#pragma unroll
    for (int g = 0; g < 4; ++g) {
      const f32x16& a = acc[0][ns];
      const f32x4 o = *(AS3 f32x4*)(xr + ns * 4096 + g * 1024);
      *(AS3 u32x2*)(img + r32 * 128 + (((ns * 4 + g) ^ (r32 & 7)) << 4) + h * 8) =
          u32x2{pk2(a[4 * g] + o[0], a[4 * g + 1] + o[1]), pk2(a[4 * g + 2] + o[2], a[4 * g + 3] + o[3])};
    }
  asm volatile("" ::: "memory");
  const int tok0 = rb3 * 128 + wm * 64 + kh * 32;
  const rsrc_t so = mk_rsrc(out, 2048u * 4096u);
  const unsigned op = tok0 * 4096 + br * 2048 + cb * 256 + wn * 128;
#pragma unroll
  for (int i = 0; i < 4; ++i) {
    const int row = 8 * i + (lane >> 3), ch = lane & 7;
    const u32x4 v = *(AS3 u32x4*)(img + row * 128 + ((ch ^ (row & 7)) << 4));
    st16(so, op + row * 4096 + ch * 16, v);
  }
  STAMP(0, 9);
}

extern "C" void kernel_launch(void* const* d_in, const int* in_sizes, int n_in, void* d_out, int out_size, void* d_ws, size_t ws_size,
                              hipStream_t stream) {
  (void)in_sizes; (void)n_in; (void)out_size; (void)ws_size;
  typedef const unsigned short* cp;
  fused_fwd<<<256, 768, 0, stream>>>((cp)d_in[0], (cp)d_in[1], (cp)d_in[2], (cp)d_in[3], (cp)d_in[4], (cp)d_in[5], (cp)d_in[6], (cp)d_in[7],
                                     (cp)d_in[8], (char*)d_ws, (unsigned short*)d_out);
}
```

```cpp
#include <hip/hip_runtime.h>
#include <hip/hip_bf16.h>
#include <stdint.h>
#pragma clang diagnostic ignored "-Wint-to-pointer-cast"

typedef __bf16   bf16x8 __attribute__((ext_vector_type(8)));
typedef __bf16   bf16x16 __attribute__((ext_vector_type(16)));
typedef __bf16   bf16x2 __attribute__((ext_vector_type(2)));
typedef short    s16x4  __attribute__((ext_vector_type(4)));
typedef short    s16x8  __attribute__((ext_vector_type(8)));
typedef float    f32x16 __attribute__((ext_vector_type(16)));
typedef float    f32x4  __attribute__((ext_vector_type(4)));
typedef float    f32x2  __attribute__((ext_vector_type(2)));
typedef unsigned u32x4  __attribute__((ext_vector_type(4)));
typedef unsigned u32x2  __attribute__((ext_vector_type(2)));
typedef unsigned long long u64;
typedef __amdgpu_buffer_rsrc_t rsrc_t;

#define AS1 __attribute__((address_space(1)))
#define AS3 __attribute__((address_space(3)))
#define DI __device__ __forceinline__
#define MFMA32(a, b, c) __builtin_amdgcn_mfma_f32_32x32x16_bf16((a), (b), (c), 0, 0, 0)
#define MFMA16(a, b, c) __builtin_amdgcn_mfma_f32_16x16x32_bf16((a), (b), (c), 0, 0, 0)
#define SM32(a, b, c) __builtin_amdgcn_smfmac_f32_32x32x32_bf16((a), (b), (c), odd, 0, 0)
#define SM16(a, b, c) __builtin_amdgcn_smfmac_f32_16x16x64_bf16((a), (b), (c), odd, 0, 0)
#define LO(S) __builtin_bit_cast(bf16x16, __builtin_shufflevector(S, S, 0, 1, 2, 3, 4, 5, 6, 7))
#define HI(S) __builtin_bit_cast(bf16x16, __builtin_shufflevector(S, S, 8, 9, 10, 11, 12, 13, 14, 15))
#define SBAR() __builtin_amdgcn_sched_barrier(0)
#define WAIT_BAR(N) asm volatile("s_waitcnt vmcnt(" #N ") lgkmcnt(0)\n\ts_barrier" ::: "memory")
#define LGKM0() __builtin_amdgcn_s_waitcnt(0xC07F)
#define LDS_BAR() do { asm volatile("" ::: "memory"); LGKM0(); asm volatile("s_barrier" ::: "memory"); } while (0)

#define STAMP(th, i) do {} while (0)
#define TS(i) do {} while (0)
#define TSOUT() do {} while (0)
#define HF 1
#define VT ((HF) ? (threadIdx.x + 512) % 768 : threadIdx.x)
#define KA 1
#define HX(c) __builtin_expect(c, 1)
#define EL 1
#define CPF 0
#define TPF 1
#define ZREF 0
#define EPF 1
#define EMV 1
#define PFB 0
#define PF0 3
#define BROT (8 * (((x >> 1) ^ (x >> 2)) & 1))
#define NTX 0
#define NTW 0
#define KROT (2 * x)
#define MAPV 1
constexpr int D = 1024;
constexpr float LOG2E = 1.4426950408889634f;
constexpr float QSCALE = 0.17677669529663687f * LOG2E;

constexpr unsigned WS_QA  = 0;
constexpr unsigned WS_QB  = WS_QA + (8u << 20);
constexpr unsigned WS_OA  = WS_QB + (4u << 20);
constexpr unsigned WS_OB  = WS_OA + (4u << 20);
constexpr unsigned WS_KV  = WS_OB + (4u << 20);
constexpr unsigned WS_KVP = WS_KV + (256u << 10);
constexpr unsigned WS_ZP  = WS_KVP + (2u << 20);
constexpr unsigned WS_FLG = WS_ZP + (64u << 10);
constexpr unsigned WS_END = WS_FLG + (16u << 10);
constexpr int F_K = 0, F_Q = 128, F_OA = 256, F_OB = 512, F_KV = 768, F_EPOCH = 1024;

DI unsigned pk2(float lo, float hi) {
  f32x2 v = {lo, hi};
  return __builtin_bit_cast(unsigned, __builtin_convertvector(v, bf16x2));
}
template <int SS> DI bf16x8 pack8(const f32x16& x) {
  u32x4 p = {pk2(x[8 * SS + 0], x[8 * SS + 1]), pk2(x[8 * SS + 2], x[8 * SS + 3]), pk2(x[8 * SS + 4], x[8 * SS + 5]), pk2(x[8 * SS + 6], x[8 * SS + 7])};
  return __builtin_bit_cast(bf16x8, p);
}
DI bf16x8 lds_r128(unsigned a) { return *(const AS3 bf16x8*)a; }
DI bf16x8 lds_tr2(unsigned a0, unsigned a1) {
  s16x4 lo = __builtin_amdgcn_ds_read_tr16_b64_v4i16((AS3 s16x4*)a0);
  s16x4 hi = __builtin_amdgcn_ds_read_tr16_b64_v4i16((AS3 s16x4*)a1);
  s16x8 r = __builtin_shufflevector(lo, hi, 0, 1, 2, 3, 4, 5, 6, 7);
  return __builtin_bit_cast(bf16x8, r);
}
DI rsrc_t mk_rsrc(const void* p, unsigned bytes) { return __builtin_amdgcn_make_buffer_rsrc((void*)p, (short)0, (int)bytes, 0x00020000); }
DI void set_m0(unsigned v) { asm volatile("s_mov_b32 m0, %0\n\ts_nop 0" ::"s"(v) : "memory"); }
template <int IMM> DI void dma16(unsigned voff, rsrc_t srd, unsigned soff) {
  asm volatile("buffer_load_dwordx4 %0, %1, %2 offen offset:%3 lds" ::"v"(voff), "s"(srd), "s"(soff), "i"(IMM) : "memory");
}
template <int IMM> DI void dma16n(unsigned voff, rsrc_t srd, unsigned soff) {
  asm volatile("buffer_load_dwordx4 %0, %1, %2 offen offset:%3 nt lds" ::"v"(voff), "s"(srd), "s"(soff), "i"(IMM) : "memory");
}
#define DMAX dma16
#define DMAW dma16
template <int IMM> DI void dma16d(unsigned voff, rsrc_t srd, unsigned soff) {
  asm volatile("buffer_load_dwordx4 %0, %1, %2 offen offset:%3 sc1 lds" ::"v"(voff), "s"(srd), "s"(soff), "i"(IMM) : "memory");
}
DI void dma4k(unsigned lds, unsigned voff, rsrc_t srd, unsigned soff) {
  set_m0(lds); dma16d<0>(voff, srd, soff); dma16d<1024>(voff, srd, soff); dma16d<2048>(voff, srd, soff); dma16d<3072>(voff, srd, soff);
}
template <class T> DI void st16(rsrc_t r, unsigned voff, T v) { __builtin_amdgcn_raw_buffer_store_b128(__builtin_bit_cast(u32x4, v), r, (int)voff, 0, 16); }
DI void st8(rsrc_t r, unsigned voff, u32x2 v) { __builtin_amdgcn_raw_buffer_store_b64(v, r, (int)voff, 0, 16); }
DI void st4(rsrc_t r, unsigned voff, float v) { __builtin_amdgcn_raw_buffer_store_b32(__float_as_uint(v), r, (int)voff, 0, 16); }
DI u32x4 ld16(rsrc_t r, unsigned voff) { return __builtin_amdgcn_raw_buffer_load_b128(r, (int)voff, 0, 16); }
DI float ld4(rsrc_t r, unsigned voff) { return __uint_as_float(__builtin_amdgcn_raw_buffer_load_b32(r, (int)voff, 0, 16)); }
DI void post(char* ws, int f, u64 tag) { __hip_atomic_store((AS1 u64*)(ws + WS_FLG) + f, tag, __ATOMIC_RELAXED, __HIP_MEMORY_SCOPE_AGENT); }
DI void poll(char* ws, int f, u64 tag) {
#pragma unroll 1
  for (int it = 0; it < (1 << 17); ++it) {
    const u64 v = __hip_atomic_load((AS1 u64*)(ws + WS_FLG) + f, __ATOMIC_RELAXED, __HIP_MEMORY_SCOPE_AGENT);
    if (__all(v == tag)) break;
    __builtin_amdgcn_s_sleep(2);
  }
  asm volatile("" ::: "memory");
}
DI void pf_line(unsigned& sink, const void* p) { asm volatile("global_load_dword %0, %1, off" : "+v"(sink) : "v"(p) : "memory"); }
#define PIN(x) asm volatile("" : "+v"(x))
DI float one(float v) { asm("" : "+v"(v)); return v; }
DI f32x4 add4(f32x4 a, f32x4 b) { return f32x4{one(a[0] + b[0]), one(a[1] + b[1]), one(a[2] + b[2]), one(a[3] + b[3])}; }
DI float xhalf_max(float v) {
  auto r = __builtin_amdgcn_permlane32_swap(__float_as_uint(v), __float_as_uint(v), false, false);
  return fmaxf(__uint_as_float(r[0]), __uint_as_float(r[1]));
}
DI float xhalf_min(float v) {
  auto r = __builtin_amdgcn_permlane32_swap(__float_as_uint(v), __float_as_uint(v), false, false);
  return fminf(__uint_as_float(r[0]), __uint_as_float(r[1]));
}
DI float xhalf_sum(float v) {
  auto r = __builtin_amdgcn_permlane32_swap(__float_as_uint(v), __float_as_uint(v), false, false);
  return __uint_as_float(r[0]) + __uint_as_float(r[1]);
}
DI float max16(const f32x16& x) {
  float a = fmaxf(fmaxf(x[0], x[1]), x[2]), b = fmaxf(fmaxf(x[3], x[4]), x[5]);
  a = fmaxf(fmaxf(a, x[6]), x[7]);   b = fmaxf(fmaxf(b, x[8]), x[9]);
  a = fmaxf(fmaxf(a, x[10]), x[11]); b = fmaxf(fmaxf(b, x[12]), x[13]);
  a = fmaxf(fmaxf(a, x[14]), x[15]);
  return fmaxf(a, b);
}
DI float min16(const f32x16& x) {
  float a = fminf(fminf(x[0], x[1]), x[2]), b = fminf(fminf(x[3], x[4]), x[5]);
  a = fminf(fminf(a, x[6]), x[7]);   b = fminf(fminf(b, x[8]), x[9]);
  a = fminf(fminf(a, x[10]), x[11]); b = fminf(fminf(b, x[12]), x[13]);
  a = fminf(fminf(a, x[14]), x[15]);
  return fminf(a, b);
}
template <bool NORM> DI void feat_map(const f32x16& p, f32x16& ep, f32x16& em) {
  const float mx = xhalf_max(max16(p)), mn = xhalf_min(min16(p));
  const f32x2 a = {-mx * LOG2E, -mx * LOG2E}, b = {mn * LOG2E, mn * LOG2E}, c = {LOG2E, LOG2E};
  f32x2 sp = {0.f, 0.f}, sm = {0.f, 0.f};
#pragma unroll
  for (int i = 0; i < 16; i += 2) {
    const f32x2 x = {p[i], p[i + 1]};
    const f32x2 u = __builtin_elementwise_fma(x, c, a), v = __builtin_elementwise_fma(x, -c, b);
    const f32x2 eu = {__builtin_amdgcn_exp2f(u[0]), __builtin_amdgcn_exp2f(u[1])}, ev = {__builtin_amdgcn_exp2f(v[0]), __builtin_amdgcn_exp2f(v[1])};
    ep[i] = eu[0]; ep[i + 1] = eu[1]; em[i] = ev[0]; em[i + 1] = ev[1];
    if (i == 0) { sp = eu; sm = ev; } else { sp += eu; sm += ev; }
  }
  if (NORM) {
    const float rp = __builtin_amdgcn_rcpf(xhalf_sum(sp[0] + sp[1])), rm = __builtin_amdgcn_rcpf(xhalf_sum(sm[0] + sm[1]));
    ep *= rp; em *= rm;
  }
}
DI bf16x8 row_sum_sel(int lane) {
  const unsigned u = ((lane & 11) == 0 && (((lane >> 2) ^ (lane >> 4)) & 1) == 0) ? 0x3f803f80u : 0u;
  return __builtin_bit_cast(bf16x8, u32x4{u, u, u, u});
}

constexpr int LDS_BYTES = 163840;
constexpr int P1_WOFF = 32768, P1_EOFF = 49152, P1_STAGE = 53248, P1_ZRED = 3 * P1_STAGE, P1_CNT = P1_ZRED + 2048, P1_GO = P1_CNT + 16, P1_PHI = 65536;
constexpr int P2_Q = 131072, P2_KVF = 147456, P2_ZB = 151552;
constexpr int P2_XO = 32768, P2_XL = 0;
#define NO 4
constexpr int NW = 10 - NO;
constexpr int P3_O = 0, P3_W = NO * 16384;
DI constexpr int p3_wslot(int t) { return P3_W + ((t + NW - 4) % NW) * 16384; }
DI constexpr int p3_inflight(int t) {
  int n = 0, after = 0;
  for (int j = 0; j < NO; ++j) { n += 4; if (j == t + 1) after = n; }
  for (int u = 0; u < t; ++u) {
    if (u + NW < 16) { n += 4; if (u + NW == t + 1) after = n; }
    if (u + NO < 16) { n += 4; if (u + NO == t + 1) after = n; }
  }
  return n - after;
}
template <int N> DI void wait_bar() { asm volatile("s_waitcnt vmcnt(%0) lgkmcnt(0)\n\ts_barrier" ::"i"(N) : "memory"); }

template <int TYPE>
DI void p1_compute(unsigned lds0, char* __restrict__ ws, rsrc_t sws, int rb, int ct, int w, int lane) {
  const int wm = w & 3, wn = w >> 2, r32 = lane & 31, h = lane >> 5;
  if (w == 0 && lane == 0) *(AS3 unsigned*)(lds0 + P1_GO) = 0u;

  const int xrow = wm * 64 + r32;
  const unsigned xa = xrow * 128 + ((h ^ ((xrow >> 1) & 7)) << 4);
  const int q4 = (lane & 15) >> 2, p4 = lane & 3, chalf = (lane >> 4) & 1;
  unsigned wa[2];
#pragma unroll
  for (int ns = 0; ns < 2; ++ns)
    wa[ns] = P1_WOFF + (8 * h + q4) * 256 + ((((wn * 2 + ns) ^ q4) << 2) | (chalf * 2 + (p4 >> 1))) * 16 + (p4 & 1) * 8;
  const int erow = 64 * (ct >> 1) + 16 * wm + (lane & 15);
  const unsigned exa = erow * 128 + (((lane >> 4) ^ ((erow >> 1) & 7)) << 4);
  const unsigned ewa = P1_EOFF + (8 * (lane >> 4) + q4) * 64 + wn * 32 + p4 * 8;

  f32x16 acc[2][2] = {};
  f32x4 eacc = {};
  bf16x8 xf[2][2], wfr[2][2], ex[2], ew[2];
  auto rdx = [&](int set, unsigned buf, int ks) {
#pragma unroll
    for (int ms = 0; ms < 2; ++ms) xf[set][ms] = lds_r128(buf + ((xa ^ (ks * 32)) + ms * 4096));
  };
  auto rdw = [&](int set, unsigned buf, int ks, int ns) { wfr[set][ns] = lds_tr2(buf + wa[ns] + ks * 4096, buf + wa[ns] + ks * 4096 + 1024); };
  auto mm1 = [&](int set, int ms, int ns) {
    if (TYPE == 0 && ns == 1) acc[ms][1] = MFMA32(xf[set][ms], wfr[set][1], acc[ms][1]);
    else                      acc[ms][ns] = MFMA32(wfr[set][ns], xf[set][ms], acc[ms][ns]);
  };
  auto erd = [&](unsigned buf, int kk) {
    if (TYPE >= 2) { ex[kk] = lds_r128(buf + (exa ^ (kk * 64))); ew[kk] = lds_tr2(buf + ewa + kk * 2048, buf + ewa + kk * 2048 + 256); }
  };
  auto emm = [&](int kk) {
    if (TYPE == 2) eacc = MFMA16(ew[kk], ex[kk], eacc);
    if (TYPE == 3) eacc = MFMA16(ex[kk], ew[kk], eacc);
  };
  auto sub = [&](int set, unsigned buf, int ks) {
    SBAR(); mm1(set, 0, 0); SBAR(); rdx(set ^ 1, buf, ks);
    SBAR(); mm1(set, 1, 0); SBAR(); rdw(set ^ 1, buf, ks, 0);
    SBAR(); mm1(set, 0, 1); SBAR(); rdw(set ^ 1, buf, ks, 1);
    SBAR(); mm1(set, 1, 1); SBAR();
  };
  LDS_BAR();
  STAMP(0, 1);
  unsigned cur = lds0, nxt = lds0 + P1_STAGE;
  rdx(0, cur, 0); rdw(0, cur, 0, 0); rdw(0, cur, 0, 1);
  LGKM0();
#pragma unroll 1
  for (int t = 0; t < 16; ++t) {
    sub(0, cur, 1); erd(cur, 0);
    sub(1, cur, 2); erd(cur, 1);
    sub(0, cur, 3); emm(0); emm(1); SBAR();
    LDS_BAR();
    sub(1, nxt, 0);
    cur = nxt; nxt = (nxt == lds0 + 2 * P1_STAGE) ? lds0 : nxt + P1_STAGE;
  }
  STAMP(0, 2);
  bf16x8 wf[2][2];
  if constexpr (TYPE <= 1) {
    const unsigned a = lds0 + P1_PHI + (TYPE == 0 ? wn : 2 * wn) * 2048 + (4 * h + q4) * 64 + chalf * 32 + p4 * 8;
#pragma unroll
    for (int n = 0; n <= TYPE; ++n)
#pragma unroll
      for (int s = 0; s < 2; ++s) wf[n][s] = lds_tr2(a + n * 2048 + s * 1024, a + n * 2048 + s * 1024 + 512);
  }

  if (TYPE >= 2) {
#pragma unroll
    for (int ms = 0; ms < 2; ++ms)
#pragma unroll
      for (int ns = 0; ns < 2; ++ns) {
        const f32x16 v = acc[ms][ns] * QSCALE;
        const unsigned p = WS_QB + ((rb * 8 + wm * 2 + ms) * 32 + ct * 4 + wn * 2 + ns) * 2048 + lane * 16;
        st16(sws, p, pack8<0>(v)); st16(sws, p + 1024, pack8<1>(v));
      }
    const int g = lane >> 4, tok0 = rb * 256 + 64 * (ct >> 1) + 16 * wm;
    unsigned p = WS_KV + (tok0 >> 5) * 4096;
    if (TYPE == 3) p += 2048 + (wm & 1) * 1024 + (16 * wn + (lane & 15) + 32 * (g >> 1)) * 16 + (g & 1) * 8;
    else           p += wn * 1024 + (16 * (wm & 1) + (lane & 15) + 32 * (g & 1)) * 16 + (g >> 1) * 8;
    st8(sws, p, u32x2{pk2(eacc[0], eacc[1]), pk2(eacc[2], eacc[3])});
    WAIT_BAR(0);
  } else if (TYPE == 1) {
    f32x16 pq[2][2];
#pragma unroll
    for (int ms = 0; ms < 2; ++ms)
#pragma unroll
      for (int ns = 0; ns < 2; ++ns) {
        pq[ms][ns] = MFMA32(wf[ns][0], pack8<0>(acc[ms][ns]), f32x16{});
        pq[ms][ns] = MFMA32(wf[ns][1], pack8<1>(acc[ms][ns]), pq[ms][ns]);
      }
#pragma unroll
    for (int ms = 0; ms < 2; ++ms)
#pragma unroll
      for (int ns = 0; ns < 2; ++ns) {
        f32x16 ep, em;
        feat_map<false>(pq[ms][ns], ep, em);
        const unsigned p = WS_QA + ((rb * 8 + wm * 2 + ms) * 32 + ct * 4 + wn * 2 + ns) * 4096 + lane * 16;
        st16(sws, p, pack8<0>(ep)); st16(sws, p + 1024, pack8<1>(ep)); st16(sws, p + 2048, pack8<0>(em)); st16(sws, p + 3072, pack8<1>(em));
      }
  } else {
    const unsigned img = lds0 + w * 8192;
    const bf16x8 sel = row_sum_sel(lane);
    f32x16 pk[2], kv[2] = {};
    f32x4 zz[2] = {};
    bf16x8 vf[2][2], af[2][2];
#pragma unroll
    for (int ms = 0; ms < 2; ++ms) {
      pk[ms] = MFMA32(wf[0][0], pack8<0>(acc[ms][0]), f32x16{});
      pk[ms] = MFMA32(wf[0][1], pack8<1>(acc[ms][0]), pk[ms]);
    }
#pragma unroll
    for (int ms = 0; ms < 2; ++ms) { vf[ms][0] = pack8<0>(acc[ms][1]); vf[ms][1] = pack8<1>(acc[ms][1]); PIN(vf[ms][0]); PIN(vf[ms][1]); }
    float off[4];
#pragma unroll
    for (int ms = 0; ms < 2; ++ms) { off[2 * ms] = -LOG2E * xhalf_max(max16(pk[ms])); off[2 * ms + 1] = LOG2E * xhalf_min(min16(pk[ms])); }
    const unsigned wr = img + 4096 + r32 * 64, sw = (r32 >> 1) & 7;
    const int t0 = 4 * h + q4, u8 = 4 * chalf + p4;
    const unsigned rd0 = img + 4096 + t0 * 64 + ((u8 ^ (t0 >> 1)) << 3), rd1 = img + 4096 + (t0 + 8) * 64 + ((u8 ^ ((t0 >> 1) + 4)) << 3);
    auto put = [&](int sg) {
#pragma unroll
      for (int g = 0; g < 4; ++g) *(AS3 f32x4*)(img + sg * 4096 + g * 1024 + lane * 16) = f32x4{kv[sg][4 * g], kv[sg][4 * g + 1], kv[sg][4 * g + 2], kv[sg][4 * g + 3]};
    };
    f32x16 e[2];
    float rs[2];
    auto mm = [&](int u, int j) {
      if (j & 1) zz[u & 1] = MFMA16(sel, af[u & 1][j >> 1], zz[u & 1]); else kv[u & 1] = MFMA32(af[u & 1][j >> 1], vf[u >> 1][j >> 1], kv[u & 1]);
    };
    auto exps = [&](int u, int mu) {
      const f32x16& p = pk[u >> 1];
      const float c = (u & 1) ? -LOG2E : LOG2E;
      f32x2 sm;
#pragma unroll
      for (int i = 0; i < 16; i += 2) {
        if (mu >= 0 && i >= 8) { SBAR(); mm(mu, (i - 8) >> 1); SBAR(); }
        const f32x2 v = __builtin_elementwise_fma(f32x2{p[i], p[i + 1]}, f32x2{c, c}, f32x2{off[u], off[u]});
        const f32x2 ev = {__builtin_amdgcn_exp2f(v[0]), __builtin_amdgcn_exp2f(v[1])};
        e[u & 1][i] = ev[0]; e[u & 1][i + 1] = ev[1];
        if (i == 0) sm = ev; else sm += ev;
      }
      rs[u & 1] = __builtin_amdgcn_rcpf(xhalf_sum(sm[0] + sm[1]));
    };
    auto rest = [&](int u) {
      const f32x16 n = e[u & 1] * rs[u & 1];
#pragma unroll
      for (int g = 0; g < 4; ++g) *(AS3 u32x2*)(wr + (u & 1) * 2048 + (((2 * g + h) ^ sw) << 3)) = u32x2{pk2(n[4 * g], n[4 * g + 1]), pk2(n[4 * g + 2], n[4 * g + 3])};
      asm volatile("" ::: "memory");
#pragma unroll
      for (int s = 0; s < 2; ++s) af[u & 1][s] = lds_tr2(rd0 + (u & 1) * 2048 + s * 1024, rd1 + (u & 1) * 2048 + s * 1024);
      asm volatile("" ::: "memory");
      SBAR();
    };
    exps(0, -1); exps(1, -1); rest(0);
    exps(2, 0);  rest(1);
    exps(3, 1);  rest(2);
    rest(3);
    mm(2, 0); mm(2, 2); mm(3, 0); mm(3, 2); SBAR(); mm(2, 1); mm(2, 3); mm(3, 1); mm(3, 3);
    put(0); put(1);
    if (lane < 32) {
      const unsigned a = lds0 + P1_ZRED + w * 256 + ((lane >> 2) & 1) * 64 + (lane >> 3) * 16 + (lane & 3) * 4;
      *(AS3 float*)a = zz[0][0]; *(AS3 float*)(a + 128) = zz[1][0];
    }
  }
  STAMP(0, 3);
}

DI void attn_pass_fast(unsigned lds0, unsigned qa, bf16x8 (&q)[2][2], bf16x8 sel, int kh, int lane, f32x16 (&O)[2], f32x4 (&L)[2], unsigned& tv, char* ws) {
  typedef const AS3 char* lp;
  lp l0 = (lp)(lds0 + lane * 16), la = l0 + kh * 16384;
#define RD(p) (*(const AS3 bf16x8*)(p))
  f32x16 negm[2], S0, S1 = {};
  bf16x8 vb = bf16x8{};
  int odd = 0xDDDD;
  O[0] = f32x16{}; O[1] = f32x16{}; L[0] = f32x4{}; L[1] = f32x4{};
  unsigned a0 = lds0 + lane * 16 + 98304, a1 = a0 + kh * 16384, qb = qa ^ 2048;
  PIN(O[0]); PIN(O[1]); PIN(L[0]); PIN(L[1]); PIN(S1); PIN(vb); PIN(odd); PIN(sel); PIN(a0); PIN(a1); PIN(qa); PIN(qb);
  LDS_BAR();
  STAMP(0, 4);
  TS(0);
  if (kh) __builtin_amdgcn_s_setprio(1);
  const bf16x8 ra = lds_r128(a0);
  q[0][0] = lds_r128(qa); q[1][0] = lds_r128(qb);
  const bf16x8 rb = lds_r128(a0 + 1024);
  q[0][1] = lds_r128(qa + 1024); q[1][1] = lds_r128(qb + 1024);
  bf16x8 ka = lds_r128(a1), kb = lds_r128(a1 + 1024), va = lds_r128(a1 + 2048);
  SBAR();
  negm[0] = MFMA32(ra, q[0][0], f32x16{}); negm[1] = MFMA32(ra, q[1][0], f32x16{});
  negm[0] = MFMA32(rb, q[0][1], negm[0]);  negm[1] = MFMA32(rb, q[1][1], negm[1]);
#pragma unroll
  for (int r = 0; r < 2; ++r) {
    const float m = xhalf_max(max16(negm[r]));
#pragma unroll
    for (int i = 0; i < 16; ++i) negm[r][i] = -m;
    PIN(negm[r]);
    SBAR();
    if (r == 0) { S0 = MFMA32(ka, q[0][0], negm[0]); S0 = MFMA32(kb, q[0][1], S0); PIN(S0); SBAR(); }
  }
#define EXN(SC, i, n) do { _Pragma("unroll") for (int k_ = 0; k_ < n; ++k_) SC[i + k_] = __builtin_amdgcn_exp2f(SC[i + k_]); PIN(SC); } while (0)
#define MM(D, E) do { SBAR(); D = E; PIN(D); SBAR(); } while (0)
#define TILE(TA, TN) do { \
    SBAR();                                                             EXN(S0, 0, 2);  \
    MM(O[1], SM32(vb, HI(S1), O[1]));       vb = RD((TA) + 3072);       EXN(S0, 2, 3);  \
    MM(L[1], SM16(sel, HI(S1), L[1]));                                  EXN(S0, 5, 2);  \
    MM(S1, MFMA32(ka, q[1][0], negm[1]));   ka = RD(TN);                EXN(S0, 7, 3);  \
    MM(S1, MFMA32(kb, q[1][1], S1));        kb = RD((TN) + 1024);       EXN(S0, 10, 3); \
    MM(O[0], SM32(va, LO(S0), O[0]));                                   EXN(S0, 13, 3); \
    MM(L[0], SM16(sel, LO(S0), L[0]));                                  EXN(S1, 0, 2);  \
    MM(O[0], SM32(vb, HI(S0), O[0]));                                   EXN(S1, 2, 3);  \
    MM(L[0], SM16(sel, HI(S0), L[0]));                                  EXN(S1, 5, 2);  \
    MM(S0, MFMA32(ka, q[0][0], negm[0]));                               EXN(S1, 7, 3);  \
    MM(S0, MFMA32(kb, q[0][1], S0));                                    EXN(S1, 10, 3); \
    MM(O[1], SM32(va, LO(S1), O[1]));       va = RD((TN) + 2048);       EXN(S1, 13, 3); \
    MM(L[1], SM16(sel, LO(S1), L[1])); } while (0)
  TS(1);
#pragma unroll 1
  for (int c = 0; c < 8; ++c) {
    const lp cb = la + ((3 - c) & 3) * 32768, nb = la + ((2 - c) & 3) * 32768;
    TILE(cb, cb + 4096);
    TILE(cb + 4096, cb + 8192);
    SBAR(); if (kh) __builtin_amdgcn_s_setprio(0);
    TILE(cb + 8192, cb + 12288);
    SBAR(); TS(2 + 2 * c);
    asm volatile("s_waitcnt vmcnt(0)\n\ts_barrier" ::: "memory"); TS(3 + 2 * c);
    if (kh) __builtin_amdgcn_s_setprio(1);
    TILE(cb + 12288, nb);
  }
  SBAR();
  O[1] = SM32(vb, HI(S1), O[1]); L[1] = SM16(sel, HI(S1), L[1]);
  __builtin_amdgcn_s_setprio(0);
  TS(18);
#undef TILE
#undef EXN
#undef MM
#undef RD
}

DI void attn_pass_safe(rsrc_t sws, const bf16x8 q0, const bf16x8 q1, const bf16x8 sel, int lane, f32x16& O, f32x4& L) {
  O = f32x16{}; L = f32x4{};
  float m = -INFINITY;
  const int odd = 0xDDDD;
#pragma unroll 1
  for (int t = 0; t < 64; ++t) {
    bf16x8 a[4];
#pragma unroll
    for (int j = 0; j < 4; ++j) a[j] = __builtin_bit_cast(bf16x8, ld16(sws, WS_KV + t * 4096 + j * 1024 + lane * 16));
    f32x16 Sc = {};
    Sc = MFMA32(a[0], q0, Sc); Sc = MFMA32(a[1], q1, Sc);
    const float mn = fmaxf(m, xhalf_max(max16(Sc)));
    const float al = __builtin_amdgcn_exp2f(m - mn);
    m = mn;
#pragma unroll
    for (int r = 0; r < 16; ++r) { O[r] *= al; Sc[r] = __builtin_amdgcn_exp2f(Sc[r] - mn); }
    L *= al;
    O = SM32(a[2], LO(Sc), O); L = SM16(sel, LO(Sc), L);
    O = SM32(a[3], HI(Sc), O); L = SM16(sel, HI(Sc), L);
  }
}

DI void helper_program(unsigned lds0, const unsigned short* __restrict__ X, const unsigned short* __restrict__ W, const unsigned short* __restrict__ E,
                       const unsigned short* __restrict__ woa, const unsigned short* __restrict__ wob, char* __restrict__ ws, rsrc_t sws,
                       int type, int rb, int ct, int x, int idx, int lw, int lane) {
  __builtin_amdgcn_s_setprio(3);
  u64 e0;
  const unsigned vl = lane * 16;
  {
    const bool ext = type >= 2;
    const unsigned ldw = ext ? 2048u : 6144u;
    const rsrc_t sx = mk_rsrc(X + (size_t)rb * 256 * D, 256u * 2048u);
    const rsrc_t sw = mk_rsrc(W, 1024u * ldw);
    const rsrc_t se = mk_rsrc(E, 1024u * 64u);
    unsigned xv[8], wv[4];
#pragma unroll
    for (int j = 0; j < 8; ++j) {
      const int row = 8 * (8 * lw + j) + (lane >> 3), c = (lane & 7) ^ ((row >> 1) & 7);
      xv[j] = row * 2048 + c * 16 - (j & 3) * 1024;
    }
#pragma unroll
    for (int j = 0; j < 4; ++j) {
      const int k = 4 * (4 * lw + j) + (lane >> 4), c = (lane & 15) ^ (((lane >> 4) & 3) << 2);
      const int col = (type == 0) ? 1024 + ((c >> 2) & 1) * 1024 + (2 * ct + (c >> 3)) * 32 + (c & 3) * 8 : ct * 128 + c * 8;
      wv[j] = k * ldw + col * 2 - j * 1024;
    }
    auto stage = [&](int t0, unsigned buf) {
      const int t = (t0 + (ext ? BROT : KROT)) & 15;
      const unsigned sox = t * 128, sow = t * 64 * ldw;
      set_m0(buf + P1_WOFF + lw * 4096);
      DMAW<0>(wv[0], sw, sow); DMAW<1024>(wv[1], sw, sow); DMAW<2048>(wv[2], sw, sow); DMAW<3072>(wv[3], sw, sow);
      if (ext ? (NTX & 2) : (NTX & 1)) {
        set_m0(buf + lw * 8192);
        dma16n<0>(xv[0], sx, sox); dma16n<1024>(xv[1], sx, sox); dma16n<2048>(xv[2], sx, sox); dma16n<3072>(xv[3], sx, sox);
        set_m0(buf + lw * 8192 + 4096);
        dma16n<0>(xv[4], sx, sox); dma16n<1024>(xv[5], sx, sox); dma16n<2048>(xv[6], sx, sox); dma16n<3072>(xv[7], sx, sox);
      } else {
        set_m0(buf + lw * 8192);
        dma16<0>(xv[0], sx, sox); dma16<1024>(xv[1], sx, sox); dma16<2048>(xv[2], sx, sox); dma16<3072>(xv[3], sx, sox);
        set_m0(buf + lw * 8192 + 4096);
        dma16<0>(xv[4], sx, sox); dma16<1024>(xv[5], sx, sox); dma16<2048>(xv[6], sx, sox); dma16<3072>(xv[7], sx, sox);
      }
      if (ext) { set_m0(buf + P1_EOFF + lw * 1024); dma16<0>(vl, se, t * 4096 + lw * 1024); }
    };
    stage(0, lds0); stage(1, lds0 + P1_STAGE);
    if (ext) WAIT_BAR(13); else WAIT_BAR(12);
    stage(2, lds0 + 2 * P1_STAGE);
    unsigned buf = lds0;
#pragma unroll 1
    for (int t = 0; t < 13; ++t) {
      if (ext) WAIT_BAR(13); else WAIT_BAR(12);
      stage(t + 3, buf);
      buf = (buf == lds0 + 2 * P1_STAGE) ? lds0 : buf + P1_STAGE;
    }
    if (ext) WAIT_BAR(13); else WAIT_BAR(12);
    e0 = __hip_atomic_load((AS1 u64*)(ws + WS_FLG) + F_EPOCH, __ATOMIC_RELAXED, __HIP_MEMORY_SCOPE_AGENT);
    if (!ext && (type == 1 || lw < 2)) {
      set_m0(lds0 + P1_PHI + lw * 2048);
      const unsigned so = ((type == 0 ? 2 * ct : 4 * ct) + lw) * 2048;
      dma16<0>(vl, se, so); dma16<1024>(vl, se, so);
    }
    WAIT_BAR(0);
    WAIT_BAR(0);
  }
  const u64 tag = ((u64)(unsigned)__builtin_amdgcn_readfirstlane((unsigned)(e0 >> 32)) << 32 | (unsigned)__builtin_amdgcn_readfirstlane((unsigned)e0))
                  * 0x5851F42D4C957F2Dull + 0x14057B7EF767814Full;
  const bool lead = (lw == 0) && (lane == 0);
  if (type >= 2) { WAIT_BAR(0); if (lead) post(ws, F_KV + rb * 16 + ct, tag); }
  auto chunk = [&](int c) {
    const unsigned l = lds0 + ((3 - c) & 3) * 32768 + lw * 8192, so = WS_KV + ((c + x) & 7) * 32768 + lw * 8192;
    dma4k(l, vl, sws, so); dma4k(l + 4096, vl, sws, so + 4096);
  };
  STAMP(512, 10);
  if (lw == 0) {
    poll(ws, F_KV + x * 16 + (lane & 7), tag);
    __hip_atomic_store((AS3 unsigned*)(lds0 + P1_GO), 1u, __ATOMIC_RELAXED, __HIP_MEMORY_SCOPE_WORKGROUP);
  } else {
    while (__hip_atomic_load((AS3 unsigned*)(lds0 + P1_GO), __ATOMIC_RELAXED, __HIP_MEMORY_SCOPE_WORKGROUP) == 0u) __builtin_amdgcn_s_sleep(1);
    asm volatile("" ::: "memory");
  }
  STAMP(512, 11);
  dma4k(lds0 + P2_Q + lw * 4096, vl, sws, WS_QB + ((8 * x + (idx >> 2)) * 32 + 8 * (idx & 3)) * 2048 + lw * 4096);
  chunk(0);
  WAIT_BAR(0);
  poll(ws, F_KV + (lane >> 3) * 16 + (lane & 7), tag);
  chunk(1);
  if (type == 0) {
#pragma unroll
    for (int j = 0; j < 4; ++j) {
      const unsigned a = lds0 + (lw >> 1) * 32768 + ((lw & 1) * 4 + j) * 1024 + vl;
      const f32x4 sm = add4(add4(*(AS3 f32x4*)a, *(AS3 f32x4*)(a + 8192)), add4(*(AS3 f32x4*)(a + 16384), *(AS3 f32x4*)(a + 24576)));
      st16(sws, WS_KVP + ((2 * ct + (lw >> 1)) * 8 + rb) * 8192 + ((lw & 1) * 4 + j) * 1024 + vl, sm);
    }
    if (lw < 2) {
      const unsigned a = lds0 + P1_ZRED + lw * 1024 + lane * 4;
      st4(sws, WS_ZP + (((2 * ct + lw) * 8 + rb) * 64 + lane) * 4, one(*(AS3 float*)a + *(AS3 float*)(a + 256)) + one(*(AS3 float*)(a + 512) + *(AS3 float*)(a + 768)));
    }
  }
  WAIT_BAR(0);
  chunk(2); chunk(3);
  if (lead && type < 2) post(ws, type == 0 ? F_K + ct * 8 + rb : F_Q + rb * 8 + ct, tag);
  const int br = !(x & 1), rb3 = 4 * (x >> 1) + (idx & 3), cb = idx >> 2;
  const unsigned short* wo = br ? wob : woa;
  unsigned pfs = 0;
  auto warm_wo = [&]() {
    pf_line(pfs, (const char*)wo + idx * 65536 + lw * 16384 + lane * 128);
    pf_line(pfs, (const char*)wo + idx * 65536 + lw * 16384 + 8192 + lane * 128);
  };
  if (lw == 1) *(AS3 unsigned*)(lds0 + P2_ZB + 128 + lane * 4) = lane < 32 ? 0x3f803f80u : 0u;
  WAIT_BAR(8);
  if (br) {
    const int head = 4 * x + (idx & 7), tp = idx >> 3;
    poll(ws, lane < 8 ? F_K + (head >> 1) * 8 + lane : F_Q + (2 * tp + (lane & 1)) * 8 + (head >> 2), tag);
    STAMP(512, 12);
    chunk(4);
    u32x4 kvp[2][8], fq[4][4];
    float zs[8];
    auto ldq = [&](int e) {
#pragma unroll
      for (int j = 0; j < 4; ++j) fq[e][j] = ld16(sws, WS_QA + ((16 * tp + 4 * lw + e) * 32 + head) * 4096 + j * 1024 + vl);
    };
#pragma unroll
    for (int e = 0; e < 2; ++e)
#pragma unroll
      for (int p = 0; p < 8; ++p) kvp[e][p] = ld16(sws, WS_KVP + ((head * 8 + p) * 512 + (2 * lw + e) * 64 + lane) * 16);
#pragma unroll
    for (int p = 0; p < 8; ++p) zs[p] = ld4(sws, WS_ZP + ((head * 8 + p) * 64 + lane) * 4);
    ldq(0); ldq(1);
    asm volatile("s_barrier" ::: "memory");
#pragma unroll
    for (int e = 0; e < 2; ++e) {
#pragma unroll
      for (int p = 0; p < 8; ++p) PIN(kvp[e][p]);
#define F4(v) __builtin_bit_cast(f32x4, v)
      const f32x4 sm = add4(add4(add4(F4(kvp[e][0]), F4(kvp[e][1])), add4(F4(kvp[e][2]), F4(kvp[e][3]))), add4(add4(F4(kvp[e][4]), F4(kvp[e][5])), add4(F4(kvp[e][6]), F4(kvp[e][7]))));
#undef F4
      *(AS3 u32x2*)(lds0 + P2_KVF + lw * 1024 + vl + e * 8) = u32x2{pk2(sm[0], sm[1]), pk2(sm[2], sm[3])};
    }
    {
#pragma unroll
      for (int p = 0; p < 8; ++p) PIN(zs[p]);
      const float z = one(one(zs[0] + zs[1]) + one(zs[2] + zs[3])) + one(one(zs[4] + zs[5]) + one(zs[6] + zs[7]));
      if (lw == 0) *(AS3 unsigned short*)(lds0 + P2_ZB + lane * 2) = (unsigned short)(pk2(z, z) & 0xffffu);
    }
    chunk(5);
    ldq(2); ldq(3);
    const int row = lane & 15, kg = lane >> 4;
    const bool on = (((row >> 2) ^ kg) & 1) == 0;
    const unsigned za = lds0 + P2_ZB + ((on && (row & 3) == 0) ? (kg >> 1) * 32 : (on && (row & 3) == 1) ? 128 : 256);
    const unsigned ka = lds0 + P2_KVF + vl;
    auto otile = [&](int e) {
#define BF(v) __builtin_bit_cast(bf16x8, v)
      f32x16 np = MFMA32(lds_r128(ka), BF(fq[e][0]), f32x16{});        f32x4 dp = MFMA16(lds_r128(za), BF(fq[e][0]), f32x4{});
      np = MFMA32(lds_r128(ka + 1024), BF(fq[e][1]), np);              dp = MFMA16(lds_r128(za + 16), BF(fq[e][1]), dp);
      f32x16 nm = MFMA32(lds_r128(ka + 2048), BF(fq[e][2]), f32x16{}); f32x4 dm = MFMA16(lds_r128(za + 64), BF(fq[e][2]), f32x4{});
      nm = MFMA32(lds_r128(ka + 3072), BF(fq[e][3]), nm);              dm = MFMA16(lds_r128(za + 80), BF(fq[e][3]), dm);
#undef BF
      const float a = __builtin_amdgcn_rcpf(dp[1]), b = __builtin_amdgcn_rcpf(dm[1]);
      const float r = __builtin_amdgcn_rcpf(one(dp[0] * a) + one(dm[0] * b) + 1e-6f), ar = one(a * r), br = one(b * r);
      f32x16 o;
#pragma unroll
      for (int i = 0; i < 16; ++i) o[i] = one(__builtin_fmaf(nm[i], br, one(np[i] * ar)));
      const unsigned p = WS_OA + ((16 * tp + 4 * lw + e) * 32 + head) * 2048 + vl;
      st16(sws, p, pack8<0>(o)); st16(sws, p + 1024, pack8<1>(o));
    };
    WAIT_BAR(16);
#pragma unroll
    for (int j = 0; j < 4; ++j) { PIN(fq[0][j]); PIN(fq[1][j]); }
    chunk(6);
    otile(0); otile(1);
    WAIT_BAR(12);
#pragma unroll
    for (int j = 0; j < 4; ++j) { PIN(fq[2][j]); PIN(fq[3][j]); }
    chunk(7);
    otile(2); otile(3);
    WAIT_BAR(0);
    if (lead) post(ws, F_OA + tp * 32 + head, tag);
    warm_wo();
  } else {
    chunk(4); warm_wo(); WAIT_BAR(10);
    chunk(5); WAIT_BAR(10);
    chunk(6); WAIT_BAR(8);
    chunk(7);
    if (blockIdx.x == 1 && lw == 1) {
      poll(ws, lane, tag); poll(ws, 64 + lane, tag); poll(ws, 128 + lane, tag);
      if (lane == 0) post(ws, F_EPOCH, tag);
    }
    WAIT_BAR(8);
  }
  const rsrc_t sw = mk_rsrc(wo, 1024u * 2048u);
  unsigned wv[4];
#pragma unroll
  for (int j = 0; j < 4; ++j) {
    const int k = 4 * (4 * lw + j) + (lane >> 4), c = (lane & 15) ^ (((lane >> 4) & 3) << 2);
    wv[j] = k * 2048 + (cb * 128 + c * 8) * 2 - j * 1024;
  }
  const unsigned osrc = (br ? WS_OB : WS_OA) + (rb3 * 4 + lw) * 65536;
  auto wst = [&](int t) {
    const unsigned sow = t * 64 * 2048;
    set_m0(lds0 + p3_wslot(t) + lw * 4096);
    dma16<0>(wv[0], sw, sow); dma16<1024>(wv[1], sw, sow); dma16<2048>(wv[2], sw, sow); dma16<3072>(wv[3], sw, sow);
  };
  auto ost = [&](int t) { dma4k(lds0 + P3_O + (t % NO) * 16384 + lw * 4096, vl, sws, osrc + t * 4096); };
  wst(0); wst(1); wst(2); wst(3);
  if (br) asm volatile("s_barrier" ::: "memory"); else WAIT_BAR(16);
  if (NW > 4) wst(4);
  if (NW > 5) wst(5);
  if (!br && lw == 0) poll(ws, F_OA + (rb3 >> 2) * 32 + (lane & 31), tag);
  asm volatile("s_barrier" ::: "memory");
  asm volatile("s_barrier" ::: "memory");
  asm volatile("s_barrier" ::: "memory");
  if (lead) post(ws, F_OB + x * 32 + idx, tag);
  STAMP(512, 13);
  if (br && lw == 0) poll(ws, F_OB + (rb3 >> 1) * 32 + (rb3 & 1) * 16 + (lane & 15), tag);
  asm volatile("s_barrier" ::: "memory");
  STAMP(512, 14);
#pragma unroll
  for (int t = 0; t < NO; ++t) ost(t);
  wait_bar<4 * (NO - 1)>();
  PIN(pfs);
#define RND(t) do { wait_bar<p3_inflight(t)>(); if (t + NW < 16) wst(t + NW); if (t + NO < 16) ost(t + NO); } while (0)
  RND(0); RND(1); RND(2); RND(3); RND(4); RND(5); RND(6); RND(7); RND(8); RND(9); RND(10); RND(11); RND(12); RND(13); RND(14);
#undef RND
}

__global__ __launch_bounds__(768, 3) void fused_fwd(const unsigned short* __restrict__ xa, const unsigned short* __restrict__ wqkv,
                                                    const unsigned short* __restrict__ wphi, const unsigned short* __restrict__ woa,
                                                    const unsigned short* __restrict__ xb, const unsigned short* __restrict__ wq,
                                                    const unsigned short* __restrict__ wk, const unsigned short* __restrict__ wv,
                                                    const unsigned short* __restrict__ wob, char* __restrict__ ws, unsigned short* __restrict__ out) {
  __shared__ __attribute__((aligned(1024))) char smem[LDS_BYTES];
  const unsigned lds0 = (unsigned)(uintptr_t)(AS3 char*)smem;
  const int tid = threadIdx.x, lane = tid & 63, w0 = __builtin_amdgcn_readfirstlane(tid >> 6), w = HF ? (w0 < 4 ? w0 + 8 : w0 - 4) : w0;
  asm volatile("" : "+s"(xa), "+s"(wqkv), "+s"(wphi), "+s"(xb), "+s"(wq), "+s"(wk), "+s"(wv), "+s"(ws));
  const int b = blockIdx.x, x = b & 7, idx = b >> 3;
  const int rb = 4 * ((x >> 1) & 1) + (idx & 3);
  const int ct = idx < 16 ? 8 * (x >> 2) + 4 * (x & 1) + (idx >> 2) : 4 * (x >> 2) + ((idx >> 2) & 3);
  const int type = idx < 16 ? 0 : (x & 1) ? 1 : 2 + (ct & 1);
  const rsrc_t sws = mk_rsrc(ws, WS_END);
  STAMP(0, 0);
  if (HX(w >= 8)) {
    helper_program(lds0, type >= 2 ? xb : xa, type >= 2 ? wq : wqkv, type == 3 ? wv : type == 2 ? wk : wphi, woa, wob, ws, sws, type, rb, ct, x, idx, w - 8, lane);
    return;
  }
  unsigned pfs = 0;
  if (w == 2) {
    const char* p = lane < 24 ? ws + lane * (WS_END / 24) : lane < 32 ? (const char*)out + (lane - 24) * (1u << 20) + (1u << 19)
                  : (const char*)((lane & 4) ? wob : woa) + (lane & 3) * (1u << 19);
    if (lane < 40) asm volatile("global_load_dword %0, %1, off sc1" : "+v"(pfs) : "v"(p) : "memory");
  }
  if (type == 0 && !(x & 1) && w == 0) pf_line(pfs, (const char*)(lane < 32 ? wv : wk) + (idx * 32 + (lane & 31)) * 128);
  if (type == 0)      p1_compute<0>(lds0, ws, sws, rb, ct, w, lane);
  else if (type == 1) p1_compute<1>(lds0, ws, sws, rb, ct, w, lane);
  else if (type == 2) p1_compute<2>(lds0, ws, sws, rb, ct, w, lane);
  else                p1_compute<3>(lds0, ws, sws, rb, ct, w, lane);

  const int kh = w >> 2, r32 = lane & 31, h = lane >> 5;
  {
    const unsigned s0 = (lane & 15) == 0 ? 0x3f803f80u : 0u, s4 = (lane & 15) == 4 ? 0x3f803f80u : 0u;
    const bf16x8 sel = __builtin_bit_cast(bf16x8, u32x4{s0, s0, s4, s4});
    bf16x8 q[2][2];
    f32x16 O[2]; f32x4 L[2];
    unsigned tv = 0;
    attn_pass_fast(lds0, lds0 + P2_Q + (2 * (w & 3) + (kh ^ 1)) * 2048 + lane * 16, q, sel, kh, lane, O, L, tv, ws);
    TSOUT();
    STAMP(0, 5);
    const unsigned xs = lds0 + P2_XO + w * 4096 + lane * 16, xr = lds0 + P2_XO + (w ^ 4) * 4096 + lane * 16;
#pragma unroll
    for (int g = 0; g < 4; ++g) *(AS3 f32x4*)(xs + g * 1024) = f32x4{O[0][4 * g], O[0][4 * g + 1], O[0][4 * g + 2], O[0][4 * g + 3]};
    *(AS3 float*)(lds0 + P2_XL + w * 256 + lane * 4) = L[0][0];
    LDS_BAR();
#pragma unroll
    for (int g = 0; g < 4; ++g) {
      const f32x4 o = *(AS3 f32x4*)(xr + g * 1024);
      O[1][4 * g] += o[0]; O[1][4 * g + 1] += o[1]; O[1][4 * g + 2] += o[2]; O[1][4 * g + 3] += o[3];
    }
    L[1][0] += *(AS3 float*)(lds0 + P2_XL + (w ^ 4) * 256 + lane * 4);
    bool bad = !(L[1][0] < 1.8446744e19f);
#pragma unroll
    for (int i = 0; i < 16; ++i) bad |= !(fabsf(O[1][i]) < 3.0e38f);
    if (__any(bad)) attn_pass_safe(sws, q[1][0], q[1][1], sel, lane, O[1], L[1]);
    const float r = __builtin_amdgcn_rcpf(xhalf_sum(L[1][0]));
    O[1] *= r;
    const unsigned p = WS_OB + ((8 * x + (idx >> 2)) * 32 + 8 * (idx & 3) + 2 * (w & 3) + kh) * 2048 + lane * 16;
    st16(sws, p, pack8<0>(O[1])); st16(sws, p + 1024, pack8<1>(O[1]));
    WAIT_BAR(0);
    PIN(pfs);
    STAMP(0, 6);
  }
  const int br = !(x & 1), rb3 = 4 * (x >> 1) + (idx & 3), cb = idx >> 2;
  const int wm = w & 1, wn = (w >> 1) & 1;
  const int q4 = (lane & 15) >> 2, p4 = lane & 3, chalf = (lane >> 4) & 1;
  const unsigned oa = wm * 8192 + kh * (4096 + 2048) + lane * 16;
  unsigned wa[2];
#pragma unroll
  for (int ns = 0; ns < 2; ++ns)
    wa[ns] = (kh * 32 + 4 * h + q4) * 256 + ((((wn * 2 + ns) ^ q4) << 2) | (chalf * 2 + (p4 >> 1))) * 16 + (p4 & 1) * 8;
  f32x16 acc[2][2] = {};
  bf16x8 xf[2][2], wfr[2][2];
  auto rdx = [&](int set, unsigned buf, int ks) {
#pragma unroll
    for (int ms = 0; ms < 2; ++ms) xf[set][ms] = lds_r128(buf + (oa ^ (ms * 4096)) + ks * 1024);
  };
  auto rdw = [&](int set, unsigned buf, int ks, int ns) { wfr[set][ns] = lds_tr2(buf + wa[ns] + ks * 4096, buf + wa[ns] + ks * 4096 + 2048); };
  auto mm1 = [&](int set, int ms, int ns) { acc[ms][ns] = MFMA32(wfr[set][ns], xf[set][ms], acc[ms][ns]); };
  auto sub = [&](int set, bool rd, unsigned ob, unsigned wb, int ks) {
    SBAR(); mm1(set, 0, 0); SBAR(); if (rd) rdx(set ^ 1, ob, ks);
    SBAR(); mm1(set, 1, 0); SBAR(); if (rd) rdw(set ^ 1, wb, ks, 0);
    SBAR(); mm1(set, 0, 1); SBAR(); if (rd) rdw(set ^ 1, wb, ks, 1);
    SBAR(); mm1(set, 1, 1); SBAR();
  };
  asm volatile("s_barrier" ::: "memory");
  LDS_BAR();
  STAMP(0, 7);
  unsigned ob = lds0 + P3_O, wb = lds0 + p3_wslot(0);
  rdx(0, ob, 0); rdw(0, wb, 0, 0); rdw(0, wb, 0, 1);
  LGKM0();
#pragma unroll 1
  for (int t = 0; t < 15; ++t) {
    sub(0, true, ob, wb, 1);
    ob = (ob == lds0 + P3_O + (NO - 1) * 16384) ? lds0 + P3_O : ob + 16384;
    wb = (wb == lds0 + 147456) ? lds0 + P3_W : wb + 16384;
    LDS_BAR();
    sub(1, true, ob, wb, 0);
  }
  sub(0, true, ob, wb, 1);
  sub(1, false, ob, wb, 0);
  STAMP(0, 8);
  constexpr int XB = NO == 5 ? 16384 : 65536, IB = NO == 5 ? 131072 : 0;
  static_assert(NO == 4 || NO == 5 || NO == 6, "");
  const unsigned xs = lds0 + XB + w * 8192 + lane * 16, xr = lds0 + XB + (w ^ 4) * 8192 + lane * 16;
#pragma unroll
  for (int ns = 0; ns < 2; ++ns)
#pragma unroll
    for (int g = 0; g < 4; ++g) {
      const f32x16& a = acc[1][ns];
      *(AS3 f32x4*)(xs + ns * 4096 + g * 1024) = f32x4{a[4 * g], a[4 * g + 1], a[4 * g + 2], a[4 * g + 3]};
    }
  LDS_BAR();
  const unsigned img = lds0 + IB + w * 4096;
#pragma unroll
  for (int ns = 0; ns < 2; ++ns)
#pragma unroll
    for (int g = 0; g < 4; ++g) {
      const f32x16& a = acc[0][ns];
      const f32x4 o = *(AS3 f32x4*)(xr + ns * 4096 + g * 1024);
      *(AS3 u32x2*)(img + r32 * 128 + (((ns * 4 + g) ^ (r32 & 7)) << 4) + h * 8) =
          u32x2{pk2(a[4 * g] + o[0], a[4 * g + 1] + o[1]), pk2(a[4 * g + 2] + o[2], a[4 * g + 3] + o[3])};
    }
  asm volatile("" ::: "memory");
  const int tok0 = rb3 * 128 + wm * 64 + kh * 32;
  const rsrc_t so = mk_rsrc(out, 2048u * 4096u);
  const unsigned op = tok0 * 4096 + br * 2048 + cb * 256 + wn * 128;
#pragma unroll
  for (int i = 0; i < 4; ++i) {
    const int row = 8 * i + (lane >> 3), ch = lane & 7;
    const u32x4 v = *(AS3 u32x4*)(img + row * 128 + ((ch ^ (row & 7)) << 4));
    st16(so, op + row * 4096 + ch * 16, v);
  }
  STAMP(0, 9);
}

extern "C" void kernel_launch(void* const* d_in, const int* in_sizes, int n_in, void* d_out, int out_size, void* d_ws, size_t ws_size,
                              hipStream_t stream) {
  (void)in_sizes; (void)n_in; (void)out_size; (void)ws_size;
  typedef const unsigned short* cp;
  fused_fwd<<<256, 768, 0, stream>>>((cp)d_in[0], (cp)d_in[1], (cp)d_in[2], (cp)d_in[3], (cp)d_in[4], (cp)d_in[5], (cp)d_in[6], (cp)d_in[7],
                                     (cp)d_in[8], (char*)d_ws, (unsigned short*)d_out);
}
```

```cpp
#include <hip/hip_runtime.h>
#include <hip/hip_bf16.h>
#include <stdint.h>
#pragma clang diagnostic ignored "-Wint-to-pointer-cast"

typedef __bf16   bf16x8 __attribute__((ext_vector_type(8)));
typedef __bf16   bf16x16 __attribute__((ext_vector_type(16)));
typedef __bf16   bf16x2 __attribute__((ext_vector_type(2)));
typedef short    s16x4  __attribute__((ext_vector_type(4)));
typedef short    s16x8  __attribute__((ext_vector_type(8)));
typedef float    f32x16 __attribute__((ext_vector_type(16)));
typedef float    f32x4  __attribute__((ext_vector_type(4)));
typedef float    f32x2  __attribute__((ext_vector_type(2)));
typedef unsigned u32x4  __attribute__((ext_vector_type(4)));
typedef unsigned u32x2  __attribute__((ext_vector_type(2)));
typedef unsigned long long u64;
typedef __amdgpu_buffer_rsrc_t rsrc_t;

#define AS1 __attribute__((address_space(1)))
#define AS3 __attribute__((address_space(3)))
#define DI __device__ __forceinline__
#define MFMA32(a, b, c) __builtin_amdgcn_mfma_f32_32x32x16_bf16((a), (b), (c), 0, 0, 0)
#define MFMA16(a, b, c) __builtin_amdgcn_mfma_f32_16x16x32_bf16((a), (b), (c), 0, 0, 0)
#define SM32(a, b, c) __builtin_amdgcn_smfmac_f32_32x32x32_bf16((a), (b), (c), odd, 0, 0)
#define SM16(a, b, c) __builtin_amdgcn_smfmac_f32_16x16x64_bf16((a), (b), (c), odd, 0, 0)
#define LO(S) __builtin_bit_cast(bf16x16, __builtin_shufflevector(S, S, 0, 1, 2, 3, 4, 5, 6, 7))
#define HI(S) __builtin_bit_cast(bf16x16, __builtin_shufflevector(S, S, 8, 9, 10, 11, 12, 13, 14, 15))
#define SBAR() __builtin_amdgcn_sched_barrier(0)
#define WAIT_BAR(N) asm volatile("s_waitcnt vmcnt(" #N ") lgkmcnt(0)\n\ts_barrier" ::: "memory")
#define LGKM0() __builtin_amdgcn_s_waitcnt(0xC07F)
#define LDS_BAR() do { asm volatile("" ::: "memory"); LGKM0(); asm volatile("s_barrier" ::: "memory"); } while (0)

#define STAMP(th, i) do {} while (0)
#define TS(i) do {} while (0)
#define TSOUT() do {} while (0)
#define HF 1
#define VT ((HF) ? (threadIdx.x + 512) % 768 : threadIdx.x)
#define KA 1
#define HX(c) __builtin_expect(c, 1)
#define EL 1
#define CPF 10
#define TPF 0
#define ZREF 0
#define EPF 1
#define EMV 1
#define PFB 0
#define PF0 3
#define BROT (8 * (((x >> 1) ^ (x >> 2)) & 1))
#define NTX 0
#define NTW 0
#define KROT (2 * x)
#define MAPV 1
constexpr int D = 1024;
constexpr float LOG2E = 1.4426950408889634f;
constexpr float QSCALE = 0.17677669529663687f * LOG2E;

constexpr unsigned WS_QA  = 0;
constexpr unsigned WS_QB  = WS_QA + (8u << 20);
constexpr unsigned WS_OA  = WS_QB + (4u << 20);
constexpr unsigned WS_OB  = WS_OA + (4u << 20);
constexpr unsigned WS_KV  = WS_OB + (4u << 20);
constexpr unsigned WS_KVP = WS_KV + (256u << 10);
constexpr unsigned WS_ZP  = WS_KVP + (2u << 20);
constexpr unsigned WS_FLG = WS_ZP + (64u << 10);
constexpr unsigned WS_END = WS_FLG + (16u << 10);
constexpr int F_K = 0, F_Q = 128, F_OA = 256, F_OB = 512, F_KV = 768, F_EPOCH = 1024;

DI unsigned pk2(float lo, float hi) {
  f32x2 v = {lo, hi};
  return __builtin_bit_cast(unsigned, __builtin_convertvector(v, bf16x2));
}
template <int SS> DI bf16x8 pack8(const f32x16& x) {
  u32x4 p = {pk2(x[8 * SS + 0], x[8 * SS + 1]), pk2(x[8 * SS + 2], x[8 * SS + 3]), pk2(x[8 * SS + 4], x[8 * SS + 5]), pk2(x[8 * SS + 6], x[8 * SS + 7])};
  return __builtin_bit_cast(bf16x8, p);
}
DI bf16x8 lds_r128(unsigned a) { return *(const AS3 bf16x8*)a; }
DI bf16x8 lds_tr2(unsigned a0, unsigned a1) {
  s16x4 lo = __builtin_amdgcn_ds_read_tr16_b64_v4i16((AS3 s16x4*)a0);
  s16x4 hi = __builtin_amdgcn_ds_read_tr16_b64_v4i16((AS3 s16x4*)a1);
  s16x8 r = __builtin_shufflevector(lo, hi, 0, 1, 2, 3, 4, 5, 6, 7);
  return __builtin_bit_cast(bf16x8, r);
}
DI rsrc_t mk_rsrc(const void* p, unsigned bytes) { return __builtin_amdgcn_make_buffer_rsrc((void*)p, (short)0, (int)bytes, 0x00020000); }
DI void set_m0(unsigned v) { asm volatile("s_mov_b32 m0, %0\n\ts_nop 0" ::"s"(v) : "memory"); }
template <int IMM> DI void dma16(unsigned voff, rsrc_t srd, unsigned soff) {
  asm volatile("buffer_load_dwordx4 %0, %1, %2 offen offset:%3 lds" ::"v"(voff), "s"(srd), "s"(soff), "i"(IMM) : "memory");
}
template <int IMM> DI void dma16n(unsigned voff, rsrc_t srd, unsigned soff) {
  asm volatile("buffer_load_dwordx4 %0, %1, %2 offen offset:%3 nt lds" ::"v"(voff), "s"(srd), "s"(soff), "i"(IMM) : "memory");
}
#define DMAX dma16
#define DMAW dma16
template <int IMM> DI void dma16d(unsigned voff, rsrc_t srd, unsigned soff) {
  asm volatile("buffer_load_dwordx4 %0, %1, %2 offen offset:%3 sc1 lds" ::"v"(voff), "s"(srd), "s"(soff), "i"(IMM) : "memory");
}
DI void dma4k(unsigned lds, unsigned voff, rsrc_t srd, unsigned soff) {
  set_m0(lds); dma16d<0>(voff, srd, soff); dma16d<1024>(voff, srd, soff); dma16d<2048>(voff, srd, soff); dma16d<3072>(voff, srd, soff);
}
template <class T> DI void st16(rsrc_t r, unsigned voff, T v) { __builtin_amdgcn_raw_buffer_store_b128(__builtin_bit_cast(u32x4, v), r, (int)voff, 0, 16); }
DI void st8(rsrc_t r, unsigned voff, u32x2 v) { __builtin_amdgcn_raw_buffer_store_b64(v, r, (int)voff, 0, 16); }
DI void st4(rsrc_t r, unsigned voff, float v) { __builtin_amdgcn_raw_buffer_store_b32(__float_as_uint(v), r, (int)voff, 0, 16); }
DI u32x4 ld16(rsrc_t r, unsigned voff) { return __builtin_amdgcn_raw_buffer_load_b128(r, (int)voff, 0, 16); }
DI float ld4(rsrc_t r, unsigned voff) { return __uint_as_float(__builtin_amdgcn_raw_buffer_load_b32(r, (int)voff, 0, 16)); }
DI void post(char* ws, int f, u64 tag) { __hip_atomic_store((AS1 u64*)(ws + WS_FLG) + f, tag, __ATOMIC_RELAXED, __HIP_MEMORY_SCOPE_AGENT); }
DI void poll(char* ws, int f, u64 tag) {
#pragma unroll 1
  for (int it = 0; it < (1 << 17); ++it) {
    const u64 v = __hip_atomic_load((AS1 u64*)(ws + WS_FLG) + f, __ATOMIC_RELAXED, __HIP_MEMORY_SCOPE_AGENT);
    if (__all(v == tag)) break;
    __builtin_amdgcn_s_sleep(2);
  }
  asm volatile("" ::: "memory");
}
DI void pf_line(unsigned& sink, const void* p) { asm volatile("global_load_dword %0, %1, off" : "+v"(sink) : "v"(p) : "memory"); }
#define PIN(x) asm volatile("" : "+v"(x))
DI void pf_bin(unsigned bin, unsigned voff, const void* base) {
  asm volatile("s_mov_b32 m0, %0\n\ts_nop 0\n\tglobal_load_lds_dword %1, %2" ::"s"(bin), "v"(voff), "s"(base) : "memory");
}
DI float one(float v) { asm("" : "+v"(v)); return v; }
DI f32x4 add4(f32x4 a, f32x4 b) { return f32x4{one(a[0] + b[0]), one(a[1] + b[1]), one(a[2] + b[2]), one(a[3] + b[3])}; }
DI float xhalf_max(float v) {
  auto r = __builtin_amdgcn_permlane32_swap(__float_as_uint(v), __float_as_uint(v), false, false);
  return fmaxf(__uint_as_float(r[0]), __uint_as_float(r[1]));
}
DI float xhalf_min(float v) {
  auto r = __builtin_amdgcn_permlane32_swap(__float_as_uint(v), __float_as_uint(v), false, false);
  return fminf(__uint_as_float(r[0]), __uint_as_float(r[1]));
}
DI float xhalf_sum(float v) {
  auto r = __builtin_amdgcn_permlane32_swap(__float_as_uint(v), __float_as_uint(v), false, false);
  return __uint_as_float(r[0]) + __uint_as_float(r[1]);
}
DI float max16(const f32x16& x) {
  float a = fmaxf(fmaxf(x[0], x[1]), x[2]), b = fmaxf(fmaxf(x[3], x[4]), x[5]);
  a = fmaxf(fmaxf(a, x[6]), x[7]);   b = fmaxf(fmaxf(b, x[8]), x[9]);
  a = fmaxf(fmaxf(a, x[10]), x[11]); b = fmaxf(fmaxf(b, x[12]), x[13]);
  a = fmaxf(fmaxf(a, x[14]), x[15]);
  return fmaxf(a, b);
}
DI float min16(const f32x16& x) {
  float a = fminf(fminf(x[0], x[1]), x[2]), b = fminf(fminf(x[3], x[4]), x[5]);
  a = fminf(fminf(a, x[6]), x[7]);   b = fminf(fminf(b, x[8]), x[9]);
  a = fminf(fminf(a, x[10]), x[11]); b = fminf(fminf(b, x[12]), x[13]);
  a = fminf(fminf(a, x[14]), x[15]);
  return fminf(a, b);
}
template <bool NORM> DI void feat_map(const f32x16& p, f32x16& ep, f32x16& em) {
  const float mx = xhalf_max(max16(p)), mn = xhalf_min(min16(p));
  const f32x2 a = {-mx * LOG2E, -mx * LOG2E}, b = {mn * LOG2E, mn * LOG2E}, c = {LOG2E, LOG2E};
  f32x2 sp = {0.f, 0.f}, sm = {0.f, 0.f};
#pragma unroll
  for (int i = 0; i < 16; i += 2) {
    const f32x2 x = {p[i], p[i + 1]};
    const f32x2 u = __builtin_elementwise_fma(x, c, a), v = __builtin_elementwise_fma(x, -c, b);
    const f32x2 eu = {__builtin_amdgcn_exp2f(u[0]), __builtin_amdgcn_exp2f(u[1])}, ev = {__builtin_amdgcn_exp2f(v[0]), __builtin_amdgcn_exp2f(v[1])};
    ep[i] = eu[0]; ep[i + 1] = eu[1]; em[i] = ev[0]; em[i + 1] = ev[1];
    if (i == 0) { sp = eu; sm = ev; } else { sp += eu; sm += ev; }
  }
  if (NORM) {
    const float rp = __builtin_amdgcn_rcpf(xhalf_sum(sp[0] + sp[1])), rm = __builtin_amdgcn_rcpf(xhalf_sum(sm[0] + sm[1]));
    ep *= rp; em *= rm;
  }
}
DI bf16x8 row_sum_sel(int lane) {
  const unsigned u = ((lane & 11) == 0 && (((lane >> 2) ^ (lane >> 4)) & 1) == 0) ? 0x3f803f80u : 0u;
  return __builtin_bit_cast(bf16x8, u32x4{u, u, u, u});
}

constexpr int LDS_BYTES = 163840;
constexpr int P1_WOFF = 32768, P1_EOFF = 49152, P1_STAGE = 53248, P1_ZRED = 3 * P1_STAGE, P1_CNT = P1_ZRED + 2048, P1_GO = P1_CNT + 16, P1_BIN = P1_CNT + 256, P1_PHI = 65536;
constexpr int P2_Q = 131072, P2_KVF = 147456, P2_ZB = 151552;
constexpr int P2_XO = 32768, P2_XL = 0;
#define NO 4
constexpr int NW = 10 - NO;
constexpr int P3_O = 0, P3_W = NO * 16384;
DI constexpr int p3_wslot(int t) { return P3_W + ((t + NW - 4) % NW) * 16384; }
DI constexpr int p3_inflight(int t) {
  int n = 0, after = 0;
  for (int j = 0; j < NO; ++j) { n += 4; if (j == t + 1) after = n; }
  for (int u = 0; u < t; ++u) {
    if (u + NW < 16) { n += 4; if (u + NW == t + 1) after = n; }
    if (u + NO < 16) { n += 4; if (u + NO == t + 1) after = n; }
  }
  return n - after;
}
template <int N> DI void wait_bar() { asm volatile("s_waitcnt vmcnt(%0) lgkmcnt(0)\n\ts_barrier" ::"i"(N) : "memory"); }

template <int TYPE>
DI void p1_compute(unsigned lds0, char* __restrict__ ws, rsrc_t sws, int rb, int ct, int w, int lane) {
  const int wm = w & 3, wn = w >> 2, r32 = lane & 31, h = lane >> 5;
  if (w == 0 && lane == 0) *(AS3 unsigned*)(lds0 + P1_GO) = 0u;

  const int xrow = wm * 64 + r32;
  const unsigned xa = xrow * 128 + ((h ^ ((xrow >> 1) & 7)) << 4);
  const int q4 = (lane & 15) >> 2, p4 = lane & 3, chalf = (lane >> 4) & 1;
  unsigned wa[2];
#pragma unroll
  for (int ns = 0; ns < 2; ++ns)
    wa[ns] = P1_WOFF + (8 * h + q4) * 256 + ((((wn * 2 + ns) ^ q4) << 2) | (chalf * 2 + (p4 >> 1))) * 16 + (p4 & 1) * 8;
  const int erow = 64 * (ct >> 1) + 16 * wm + (lane & 15);
  const unsigned exa = erow * 128 + (((lane >> 4) ^ ((erow >> 1) & 7)) << 4);
  const unsigned ewa = P1_EOFF + (8 * (lane >> 4) + q4) * 64 + wn * 32 + p4 * 8;

  f32x16 acc[2][2] = {};
  f32x4 eacc = {};
  bf16x8 xf[2][2], wfr[2][2], ex[2], ew[2];
  auto rdx = [&](int set, unsigned buf, int ks) {
#pragma unroll
    for (int ms = 0; ms < 2; ++ms) xf[set][ms] = lds_r128(buf + ((xa ^ (ks * 32)) + ms * 4096));
  };
  auto rdw = [&](int set, unsigned buf, int ks, int ns) { wfr[set][ns] = lds_tr2(buf + wa[ns] + ks * 4096, buf + wa[ns] + ks * 4096 + 1024); };
  auto mm1 = [&](int set, int ms, int ns) {
    if (TYPE == 0 && ns == 1) acc[ms][1] = MFMA32(xf[set][ms], wfr[set][1], acc[ms][1]);
    else                      acc[ms][ns] = MFMA32(wfr[set][ns], xf[set][ms], acc[ms][ns]);
  };
  auto erd = [&](unsigned buf, int kk) {
    if (TYPE >= 2) { ex[kk] = lds_r128(buf + (exa ^ (kk * 64))); ew[kk] = lds_tr2(buf + ewa + kk * 2048, buf + ewa + kk * 2048 + 256); }
  };
  auto emm = [&](int kk) {
    if (TYPE == 2) eacc = MFMA16(ew[kk], ex[kk], eacc);
    if (TYPE == 3) eacc = MFMA16(ex[kk], ew[kk], eacc);
  };
  auto sub = [&](int set, unsigned buf, int ks) {
    SBAR(); mm1(set, 0, 0); SBAR(); rdx(set ^ 1, buf, ks);
    SBAR(); mm1(set, 1, 0); SBAR(); rdw(set ^ 1, buf, ks, 0);
    SBAR(); mm1(set, 0, 1); SBAR(); rdw(set ^ 1, buf, ks, 1);
    SBAR(); mm1(set, 1, 1); SBAR();
  };
  LDS_BAR();
  STAMP(0, 1);
  unsigned cur = lds0, nxt = lds0 + P1_STAGE;
  rdx(0, cur, 0); rdw(0, cur, 0, 0); rdw(0, cur, 0, 1);
  LGKM0();
#pragma unroll 1
  for (int t = 0; t < 16; ++t) {
    sub(0, cur, 1); erd(cur, 0);
    sub(1, cur, 2); erd(cur, 1);
    sub(0, cur, 3); emm(0); emm(1); SBAR();
    LDS_BAR();
    sub(1, nxt, 0);
    cur = nxt; nxt = (nxt == lds0 + 2 * P1_STAGE) ? lds0 : nxt + P1_STAGE;
  }
  STAMP(0, 2);
  bf16x8 wf[2][2];
  if constexpr (TYPE <= 1) {
    const unsigned a = lds0 + P1_PHI + (TYPE == 0 ? wn : 2 * wn) * 2048 + (4 * h + q4) * 64 + chalf * 32 + p4 * 8;
#pragma unroll
    for (int n = 0; n <= TYPE; ++n)
#pragma unroll
      for (int s = 0; s < 2; ++s) wf[n][s] = lds_tr2(a + n * 2048 + s * 1024, a + n * 2048 + s * 1024 + 512);
  }

  if (TYPE >= 2) {
#pragma unroll
    for (int ms = 0; ms < 2; ++ms)
#pragma unroll
      for (int ns = 0; ns < 2; ++ns) {
        const f32x16 v = acc[ms][ns] * QSCALE;
        const unsigned p = WS_QB + ((rb * 8 + wm * 2 + ms) * 32 + ct * 4 + wn * 2 + ns) * 2048 + lane * 16;
        st16(sws, p, pack8<0>(v)); st16(sws, p + 1024, pack8<1>(v));
      }
    const int g = lane >> 4, tok0 = rb * 256 + 64 * (ct >> 1) + 16 * wm;
    unsigned p = WS_KV + (tok0 >> 5) * 4096;
    if (TYPE == 3) p += 2048 + (wm & 1) * 1024 + (16 * wn + (lane & 15) + 32 * (g >> 1)) * 16 + (g & 1) * 8;
    else           p += wn * 1024 + (16 * (wm & 1) + (lane & 15) + 32 * (g & 1)) * 16 + (g >> 1) * 8;
    st8(sws, p, u32x2{pk2(eacc[0], eacc[1]), pk2(eacc[2], eacc[3])});
    WAIT_BAR(0);
  } else if (TYPE == 1) {
    f32x16 pq[2][2];
#pragma unroll
    for (int ms = 0; ms < 2; ++ms)
#pragma unroll
      for (int ns = 0; ns < 2; ++ns) {
        pq[ms][ns] = MFMA32(wf[ns][0], pack8<0>(acc[ms][ns]), f32x16{});
        pq[ms][ns] = MFMA32(wf[ns][1], pack8<1>(acc[ms][ns]), pq[ms][ns]);
      }
#pragma unroll
    for (int ms = 0; ms < 2; ++ms)
#pragma unroll
      for (int ns = 0; ns < 2; ++ns) {
        f32x16 ep, em;
        feat_map<false>(pq[ms][ns], ep, em);
        const unsigned p = WS_QA + ((rb * 8 + wm * 2 + ms) * 32 + ct * 4 + wn * 2 + ns) * 4096 + lane * 16;
        st16(sws, p, pack8<0>(ep)); st16(sws, p + 1024, pack8<1>(ep)); st16(sws, p + 2048, pack8<0>(em)); st16(sws, p + 3072, pack8<1>(em));
      }
  } else {
    const unsigned img = lds0 + w * 8192;
    const bf16x8 sel = row_sum_sel(lane);
    f32x16 pk[2], kv[2] = {};
    f32x4 zz[2] = {};
    bf16x8 vf[2][2], af[2][2];
#pragma unroll
    for (int ms = 0; ms < 2; ++ms) {
      pk[ms] = MFMA32(wf[0][0], pack8<0>(acc[ms][0]), f32x16{});
      pk[ms] = MFMA32(wf[0][1], pack8<1>(acc[ms][0]), pk[ms]);
    }
#pragma unroll
    for (int ms = 0; ms < 2; ++ms) { vf[ms][0] = pack8<0>(acc[ms][1]); vf[ms][1] = pack8<1>(acc[ms][1]); PIN(vf[ms][0]); PIN(vf[ms][1]); }
    float off[4];
#pragma unroll
    for (int ms = 0; ms < 2; ++ms) { off[2 * ms] = -LOG2E * xhalf_max(max16(pk[ms])); off[2 * ms + 1] = LOG2E * xhalf_min(min16(pk[ms])); }
    const unsigned wr = img + 4096 + r32 * 64, sw = (r32 >> 1) & 7;
    const int t0 = 4 * h + q4, u8 = 4 * chalf + p4;
    const unsigned rd0 = img + 4096 + t0 * 64 + ((u8 ^ (t0 >> 1)) << 3), rd1 = img + 4096 + (t0 + 8) * 64 + ((u8 ^ ((t0 >> 1) + 4)) << 3);
    auto put = [&](int sg) {
#pragma unroll
      for (int g = 0; g < 4; ++g) *(AS3 f32x4*)(img + sg * 4096 + g * 1024 + lane * 16) = f32x4{kv[sg][4 * g], kv[sg][4 * g + 1], kv[sg][4 * g + 2], kv[sg][4 * g + 3]};
    };
    f32x16 e[2];
    float rs[2];
    auto mm = [&](int u, int j) {
      if (j & 1) zz[u & 1] = MFMA16(sel, af[u & 1][j >> 1], zz[u & 1]); else kv[u & 1] = MFMA32(af[u & 1][j >> 1], vf[u >> 1][j >> 1], kv[u & 1]);
    };
    auto exps = [&](int u, int mu) {
      const f32x16& p = pk[u >> 1];
      const float c = (u & 1) ? -LOG2E : LOG2E;
      f32x2 sm;
#pragma unroll
      for (int i = 0; i < 16; i += 2) {
        if (mu >= 0 && i >= 8) { SBAR(); mm(mu, (i - 8) >> 1); SBAR(); }
        const f32x2 v = __builtin_elementwise_fma(f32x2{p[i], p[i + 1]}, f32x2{c, c}, f32x2{off[u], off[u]});
        const f32x2 ev = {__builtin_amdgcn_exp2f(v[0]), __builtin_amdgcn_exp2f(v[1])};
        e[u & 1][i] = ev[0]; e[u & 1][i + 1] = ev[1];
        if (i == 0) sm = ev; else sm += ev;
      }
      rs[u & 1] = __builtin_amdgcn_rcpf(xhalf_sum(sm[0] + sm[1]));
    };
    auto rest = [&](int u) {
      const f32x16 n = e[u & 1] * rs[u & 1];
#pragma unroll
      for (int g = 0; g < 4; ++g) *(AS3 u32x2*)(wr + (u & 1) * 2048 + (((2 * g + h) ^ sw) << 3)) = u32x2{pk2(n[4 * g], n[4 * g + 1]), pk2(n[4 * g + 2], n[4 * g + 3])};
      asm volatile("" ::: "memory");
#pragma unroll
      for (int s = 0; s < 2; ++s) af[u & 1][s] = lds_tr2(rd0 + (u & 1) * 2048 + s * 1024, rd1 + (u & 1) * 2048 + s * 1024);
      asm volatile("" ::: "memory");
      SBAR();
    };
    exps(0, -1); exps(1, -1); rest(0);
    exps(2, 0);  rest(1);
    exps(3, 1);  rest(2);
    rest(3);
    mm(2, 0); mm(2, 2); mm(3, 0); mm(3, 2); SBAR(); mm(2, 1); mm(2, 3); mm(3, 1); mm(3, 3);
    put(0); put(1);
    if (lane < 32) {
      const unsigned a = lds0 + P1_ZRED + w * 256 + ((lane >> 2) & 1) * 64 + (lane >> 3) * 16 + (lane & 3) * 4;
      *(AS3 float*)a = zz[0][0]; *(AS3 float*)(a + 128) = zz[1][0];
    }
  }
  STAMP(0, 3);
}

DI void attn_pass_fast(unsigned lds0, unsigned qa, bf16x8 (&q)[2][2], bf16x8 sel, int kh, int lane, f32x16 (&O)[2], f32x4 (&L)[2], unsigned& tv, char* ws) {
  typedef const AS3 char* lp;
  lp l0 = (lp)(lds0 + lane * 16), la = l0 + kh * 16384;
#define RD(p) (*(const AS3 bf16x8*)(p))
  f32x16 negm[2], S0, S1 = {};
  bf16x8 vb = bf16x8{};
  int odd = 0xDDDD;
  O[0] = f32x16{}; O[1] = f32x16{}; L[0] = f32x4{}; L[1] = f32x4{};
  unsigned a0 = lds0 + lane * 16 + 98304, a1 = a0 + kh * 16384, qb = qa ^ 2048;
  PIN(O[0]); PIN(O[1]); PIN(L[0]); PIN(L[1]); PIN(S1); PIN(vb); PIN(odd); PIN(sel); PIN(a0); PIN(a1); PIN(qa); PIN(qb);
  LDS_BAR();
  STAMP(0, 4);
  TS(0);
  if (kh) __builtin_amdgcn_s_setprio(1);
  const bf16x8 ra = lds_r128(a0);
  q[0][0] = lds_r128(qa); q[1][0] = lds_r128(qb);
  const bf16x8 rb = lds_r128(a0 + 1024);
  q[0][1] = lds_r128(qa + 1024); q[1][1] = lds_r128(qb + 1024);
  bf16x8 ka = lds_r128(a1), kb = lds_r128(a1 + 1024), va = lds_r128(a1 + 2048);
  SBAR();
  negm[0] = MFMA32(ra, q[0][0], f32x16{}); negm[1] = MFMA32(ra, q[1][0], f32x16{});
  negm[0] = MFMA32(rb, q[0][1], negm[0]);  negm[1] = MFMA32(rb, q[1][1], negm[1]);
#pragma unroll
  for (int r = 0; r < 2; ++r) {
    const float m = xhalf_max(max16(negm[r]));
#pragma unroll
    for (int i = 0; i < 16; ++i) negm[r][i] = -m;
    PIN(negm[r]);
    SBAR();
    if (r == 0) { S0 = MFMA32(ka, q[0][0], negm[0]); S0 = MFMA32(kb, q[0][1], S0); PIN(S0); SBAR(); }
  }
#define EXN(SC, i, n) do { _Pragma("unroll") for (int k_ = 0; k_ < n; ++k_) SC[i + k_] = __builtin_amdgcn_exp2f(SC[i + k_]); PIN(SC); } while (0)
#define MM(D, E) do { SBAR(); D = E; PIN(D); SBAR(); } while (0)
#define TILE(TA, TN) do { \
    SBAR();                                                             EXN(S0, 0, 2);  \
    MM(O[1], SM32(vb, HI(S1), O[1]));       vb = RD((TA) + 3072);       EXN(S0, 2, 3);  \
    MM(L[1], SM16(sel, HI(S1), L[1]));                                  EXN(S0, 5, 2);  \
    MM(S1, MFMA32(ka, q[1][0], negm[1]));   ka = RD(TN);                EXN(S0, 7, 3);  \
    MM(S1, MFMA32(kb, q[1][1], S1));        kb = RD((TN) + 1024);       EXN(S0, 10, 3); \
    MM(O[0], SM32(va, LO(S0), O[0]));                                   EXN(S0, 13, 3); \
    MM(L[0], SM16(sel, LO(S0), L[0]));                                  EXN(S1, 0, 2);  \
    MM(O[0], SM32(vb, HI(S0), O[0]));                                   EXN(S1, 2, 3);  \
    MM(L[0], SM16(sel, HI(S0), L[0]));                                  EXN(S1, 5, 2);  \
    MM(S0, MFMA32(ka, q[0][0], negm[0]));                               EXN(S1, 7, 3);  \
    MM(S0, MFMA32(kb, q[0][1], S0));                                    EXN(S1, 10, 3); \
    MM(O[1], SM32(va, LO(S1), O[1]));       va = RD((TN) + 2048);       EXN(S1, 13, 3); \
    MM(L[1], SM16(sel, LO(S1), L[1])); } while (0)
  TS(1);
#pragma unroll 1
  for (int c = 0; c < 8; ++c) {
    const lp cb = la + ((3 - c) & 3) * 32768, nb = la + ((2 - c) & 3) * 32768;
    TILE(cb, cb + 4096);
    TILE(cb + 4096, cb + 8192);
    SBAR(); if (kh) __builtin_amdgcn_s_setprio(0);
    TILE(cb + 8192, cb + 12288);
    SBAR(); TS(2 + 2 * c);
    asm volatile("s_waitcnt vmcnt(0)\n\ts_barrier" ::: "memory"); TS(3 + 2 * c);
    if (kh) __builtin_amdgcn_s_setprio(1);
    TILE(cb + 12288, nb);
  }
  SBAR();
  O[1] = SM32(vb, HI(S1), O[1]); L[1] = SM16(sel, HI(S1), L[1]);
  __builtin_amdgcn_s_setprio(0);
  TS(18);
#undef TILE
#undef EXN
#undef MM
#undef RD
}

DI void attn_pass_safe(rsrc_t sws, const bf16x8 q0, const bf16x8 q1, const bf16x8 sel, int lane, f32x16& O, f32x4& L) {
  O = f32x16{}; L = f32x4{};
  float m = -INFINITY;
  const int odd = 0xDDDD;
#pragma unroll 1
  for (int t = 0; t < 64; ++t) {
    bf16x8 a[4];
#pragma unroll
    for (int j = 0; j < 4; ++j) a[j] = __builtin_bit_cast(bf16x8, ld16(sws, WS_KV + t * 4096 + j * 1024 + lane * 16));
    f32x16 Sc = {};
    Sc = MFMA32(a[0], q0, Sc); Sc = MFMA32(a[1], q1, Sc);
    const float mn = fmaxf(m, xhalf_max(max16(Sc)));
    const float al = __builtin_amdgcn_exp2f(m - mn);
    m = mn;
#pragma unroll
    for (int r = 0; r < 16; ++r) { O[r] *= al; Sc[r] = __builtin_amdgcn_exp2f(Sc[r] - mn); }
    L *= al;
    O = SM32(a[2], LO(Sc), O); L = SM16(sel, LO(Sc), L);
    O = SM32(a[3], HI(Sc), O); L = SM16(sel, HI(Sc), L);
  }
}

DI void helper_program(unsigned lds0, const unsigned short* __restrict__ X, const unsigned short* __restrict__ W, const unsigned short* __restrict__ E,
                       const unsigned short* __restrict__ woa, const unsigned short* __restrict__ wob, char* __restrict__ ws, rsrc_t sws,
                       const char* pc0, unsigned co, int type, int rb, int ct, int x, int idx, int lw, int lane) {
  __builtin_amdgcn_s_setprio(3);
  u64 e0;
  const unsigned vl = lane * 16;
  {
    const bool ext = type >= 2;
    const unsigned ldw = ext ? 2048u : 6144u;
    const rsrc_t sx = mk_rsrc(X + (size_t)rb * 256 * D, 256u * 2048u);
    const rsrc_t sw = mk_rsrc(W, 1024u * ldw);
    const rsrc_t se = mk_rsrc(E, 1024u * 64u);
    unsigned xv[8], wv[4];
#pragma unroll
    for (int j = 0; j < 8; ++j) {
      const int row = 8 * (8 * lw + j) + (lane >> 3), c = (lane & 7) ^ ((row >> 1) & 7);
      xv[j] = row * 2048 + c * 16 - (j & 3) * 1024;
    }
#pragma unroll
    for (int j = 0; j < 4; ++j) {
      const int k = 4 * (4 * lw + j) + (lane >> 4), c = (lane & 15) ^ (((lane >> 4) & 3) << 2);
      const int col = (type == 0) ? 1024 + ((c >> 2) & 1) * 1024 + (2 * ct + (c >> 3)) * 32 + (c & 3) * 8 : ct * 128 + c * 8;
      wv[j] = k * ldw + col * 2 - j * 1024;
    }
    auto stage = [&](int t0, unsigned buf) {
      const int t = (t0 + (ext ? BROT : KROT)) & 15;
      const unsigned sox = t * 128, sow = t * 64 * ldw;
      set_m0(buf + P1_WOFF + lw * 4096);
      DMAW<0>(wv[0], sw, sow); DMAW<1024>(wv[1], sw, sow); DMAW<2048>(wv[2], sw, sow); DMAW<3072>(wv[3], sw, sow);
      if (ext ? (NTX & 2) : (NTX & 1)) {
        set_m0(buf + lw * 8192);
        dma16n<0>(xv[0], sx, sox); dma16n<1024>(xv[1], sx, sox); dma16n<2048>(xv[2], sx, sox); dma16n<3072>(xv[3], sx, sox);
        set_m0(buf + lw * 8192 + 4096);
        dma16n<0>(xv[4], sx, sox); dma16n<1024>(xv[5], sx, sox); dma16n<2048>(xv[6], sx, sox); dma16n<3072>(xv[7], sx, sox);
      } else {
        set_m0(buf + lw * 8192);
        dma16<0>(xv[0], sx, sox); dma16<1024>(xv[1], sx, sox); dma16<2048>(xv[2], sx, sox); dma16<3072>(xv[3], sx, sox);
        set_m0(buf + lw * 8192 + 4096);
        dma16<0>(xv[4], sx, sox); dma16<1024>(xv[5], sx, sox); dma16<2048>(xv[6], sx, sox); dma16<3072>(xv[7], sx, sox);
      }
      if (ext) { set_m0(buf + P1_EOFF + lw * 1024); dma16<0>(vl, se, t * 4096 + lw * 1024); }
    };
    stage(0, lds0); stage(1, lds0 + P1_STAGE);
    if (ext) WAIT_BAR(13); else WAIT_BAR(12);
    stage(2, lds0 + 2 * P1_STAGE);
    unsigned buf = lds0;
#pragma unroll 1
    for (int t = 0; t < 13; ++t) {
      if (ext) WAIT_BAR(13); else WAIT_BAR(12);
      stage(t + 3, buf);
      buf = (buf == lds0 + 2 * P1_STAGE) ? lds0 : buf + P1_STAGE;
    }
    if (ext) WAIT_BAR(13); else WAIT_BAR(12);
    e0 = __hip_atomic_load((AS1 u64*)(ws + WS_FLG) + F_EPOCH, __ATOMIC_RELAXED, __HIP_MEMORY_SCOPE_AGENT);
    if (lw == 3) pf_bin(lds0 + P1_BIN, co, pc0);
    if (!ext && (type == 1 || lw < 2)) {
      set_m0(lds0 + P1_PHI + lw * 2048);
      const unsigned so = ((type == 0 ? 2 * ct : 4 * ct) + lw) * 2048;
      dma16<0>(vl, se, so); dma16<1024>(vl, se, so);
    }
    WAIT_BAR(0);
    WAIT_BAR(0);
  }
  const u64 tag = ((u64)(unsigned)__builtin_amdgcn_readfirstlane((unsigned)(e0 >> 32)) << 32 | (unsigned)__builtin_amdgcn_readfirstlane((unsigned)e0))
                  * 0x5851F42D4C957F2Dull + 0x14057B7EF767814Full;
  const bool lead = (lw == 0) && (lane == 0);
  if (type >= 2) { WAIT_BAR(0); if (lead) post(ws, F_KV + rb * 16 + ct, tag); }
  auto chunk = [&](int c) {
    const unsigned l = lds0 + ((3 - c) & 3) * 32768 + lw * 8192, so = WS_KV + ((c + x) & 7) * 32768 + lw * 8192;
    dma4k(l, vl, sws, so); dma4k(l + 4096, vl, sws, so + 4096);
  };
  STAMP(512, 10);
  if (lw == 0) {
    poll(ws, F_KV + x * 16 + (lane & 7), tag);
    __hip_atomic_store((AS3 unsigned*)(lds0 + P1_GO), 1u, __ATOMIC_RELAXED, __HIP_MEMORY_SCOPE_WORKGROUP);
  } else {
    while (__hip_atomic_load((AS3 unsigned*)(lds0 + P1_GO), __ATOMIC_RELAXED, __HIP_MEMORY_SCOPE_WORKGROUP) == 0u) __builtin_amdgcn_s_sleep(1);
    asm volatile("" ::: "memory");
  }
  STAMP(512, 11);
  dma4k(lds0 + P2_Q + lw * 4096, vl, sws, WS_QB + ((8 * x + (idx >> 2)) * 32 + 8 * (idx & 3)) * 2048 + lw * 4096);
  chunk(0);
  WAIT_BAR(0);
  poll(ws, F_KV + (lane >> 3) * 16 + (lane & 7), tag);
  chunk(1);
  if (type == 0) {
#pragma unroll
    for (int j = 0; j < 4; ++j) {
      const unsigned a = lds0 + (lw >> 1) * 32768 + ((lw & 1) * 4 + j) * 1024 + vl;
      const f32x4 sm = add4(add4(*(AS3 f32x4*)a, *(AS3 f32x4*)(a + 8192)), add4(*(AS3 f32x4*)(a + 16384), *(AS3 f32x4*)(a + 24576)));
      st16(sws, WS_KVP + ((2 * ct + (lw >> 1)) * 8 + rb) * 8192 + ((lw & 1) * 4 + j) * 1024 + vl, sm);
    }
    if (lw < 2) {
      const unsigned a = lds0 + P1_ZRED + lw * 1024 + lane * 4;
      st4(sws, WS_ZP + (((2 * ct + lw) * 8 + rb) * 64 + lane) * 4, one(*(AS3 float*)a + *(AS3 float*)(a + 256)) + one(*(AS3 float*)(a + 512) + *(AS3 float*)(a + 768)));
    }
  }
  WAIT_BAR(0);
  chunk(2); chunk(3);
  if (lead && type < 2) post(ws, type == 0 ? F_K + ct * 8 + rb : F_Q + rb * 8 + ct, tag);
  const int br = !(x & 1), rb3 = 4 * (x >> 1) + (idx & 3), cb = idx >> 2;
  const unsigned short* wo = br ? wob : woa;
  unsigned pfs = 0;
  auto warm_wo = [&]() {
    pf_line(pfs, (const char*)wo + idx * 65536 + lw * 16384 + lane * 128);
    pf_line(pfs, (const char*)wo + idx * 65536 + lw * 16384 + 8192 + lane * 128);
  };
  if (lw == 1) *(AS3 unsigned*)(lds0 + P2_ZB + 128 + lane * 4) = lane < 32 ? 0x3f803f80u : 0u;
  WAIT_BAR(8);
  if (br) {
    const int head = 4 * x + (idx & 7), tp = idx >> 3;
    poll(ws, lane < 8 ? F_K + (head >> 1) * 8 + lane : F_Q + (2 * tp + (lane & 1)) * 8 + (head >> 2), tag);
    STAMP(512, 12);
    chunk(4);
    u32x4 kvp[2][8], fq[4][4];
    float zs[8];
    auto ldq = [&](int e) {
#pragma unroll
      for (int j = 0; j < 4; ++j) fq[e][j] = ld16(sws, WS_QA + ((16 * tp + 4 * lw + e) * 32 + head) * 4096 + j * 1024 + vl);
    };
#pragma unroll
    for (int e = 0; e < 2; ++e)
#pragma unroll
      for (int p = 0; p < 8; ++p) kvp[e][p] = ld16(sws, WS_KVP + ((head * 8 + p) * 512 + (2 * lw + e) * 64 + lane) * 16);
#pragma unroll
    for (int p = 0; p < 8; ++p) zs[p] = ld4(sws, WS_ZP + ((head * 8 + p) * 64 + lane) * 4);
    ldq(0); ldq(1);
    asm volatile("s_barrier" ::: "memory");
#pragma unroll
    for (int e = 0; e < 2; ++e) {
#pragma unroll
      for (int p = 0; p < 8; ++p) PIN(kvp[e][p]);
#define F4(v) __builtin_bit_cast(f32x4, v)
      const f32x4 sm = add4(add4(add4(F4(kvp[e][0]), F4(kvp[e][1])), add4(F4(kvp[e][2]), F4(kvp[e][3]))), add4(add4(F4(kvp[e][4]), F4(kvp[e][5])), add4(F4(kvp[e][6]), F4(kvp[e][7]))));
#undef F4
      *(AS3 u32x2*)(lds0 + P2_KVF + lw * 1024 + vl + e * 8) = u32x2{pk2(sm[0], sm[1]), pk2(sm[2], sm[3])};
    }
    {
#pragma unroll
      for (int p = 0; p < 8; ++p) PIN(zs[p]);
      const float z = one(one(zs[0] + zs[1]) + one(zs[2] + zs[3])) + one(one(zs[4] + zs[5]) + one(zs[6] + zs[7]));
      if (lw == 0) *(AS3 unsigned short*)(lds0 + P2_ZB + lane * 2) = (unsigned short)(pk2(z, z) & 0xffffu);
    }
    chunk(5);
    ldq(2); ldq(3);
    const int row = lane & 15, kg = lane >> 4;
    const bool on = (((row >> 2) ^ kg) & 1) == 0;
    const unsigned za = lds0 + P2_ZB + ((on && (row & 3) == 0) ? (kg >> 1) * 32 : (on && (row & 3) == 1) ? 128 : 256);
    const unsigned ka = lds0 + P2_KVF + vl;
    auto otile = [&](int e) {
#define BF(v) __builtin_bit_cast(bf16x8, v)
      f32x16 np = MFMA32(lds_r128(ka), BF(fq[e][0]), f32x16{});        f32x4 dp = MFMA16(lds_r128(za), BF(fq[e][0]), f32x4{});
      np = MFMA32(lds_r128(ka + 1024), BF(fq[e][1]), np);              dp = MFMA16(lds_r128(za + 16), BF(fq[e][1]), dp);
      f32x16 nm = MFMA32(lds_r128(ka + 2048), BF(fq[e][2]), f32x16{}); f32x4 dm = MFMA16(lds_r128(za + 64), BF(fq[e][2]), f32x4{});
      nm = MFMA32(lds_r128(ka + 3072), BF(fq[e][3]), nm);              dm = MFMA16(lds_r128(za + 80), BF(fq[e][3]), dm);
#undef BF
      const float a = __builtin_amdgcn_rcpf(dp[1]), b = __builtin_amdgcn_rcpf(dm[1]);
      const float r = __builtin_amdgcn_rcpf(one(dp[0] * a) + one(dm[0] * b) + 1e-6f), ar = one(a * r), br = one(b * r);
      f32x16 o;
#pragma unroll
      for (int i = 0; i < 16; ++i) o[i] = one(__builtin_fmaf(nm[i], br, one(np[i] * ar)));
      const unsigned p = WS_OA + ((16 * tp + 4 * lw + e) * 32 + head) * 2048 + vl;
      st16(sws, p, pack8<0>(o)); st16(sws, p + 1024, pack8<1>(o));
    };
    WAIT_BAR(16);
#pragma unroll
    for (int j = 0; j < 4; ++j) { PIN(fq[0][j]); PIN(fq[1][j]); }
    chunk(6);
    otile(0); otile(1);
    WAIT_BAR(12);
#pragma unroll
    for (int j = 0; j < 4; ++j) { PIN(fq[2][j]); PIN(fq[3][j]); }
    chunk(7);
    otile(2); otile(3);
    WAIT_BAR(0);
    if (lead) post(ws, F_OA + tp * 32 + head, tag);
    warm_wo();
  } else {
    chunk(4); warm_wo(); WAIT_BAR(10);
    chunk(5); WAIT_BAR(10);
    chunk(6); WAIT_BAR(8);
    chunk(7);
    if (blockIdx.x == 1 && lw == 1) {
      poll(ws, lane, tag); poll(ws, 64 + lane, tag); poll(ws, 128 + lane, tag);
      if (lane == 0) post(ws, F_EPOCH, tag);
    }
    WAIT_BAR(8);
  }
  const rsrc_t sw = mk_rsrc(wo, 1024u * 2048u);
  unsigned wv[4];
#pragma unroll
  for (int j = 0; j < 4; ++j) {
    const int k = 4 * (4 * lw + j) + (lane >> 4), c = (lane & 15) ^ (((lane >> 4) & 3) << 2);
    wv[j] = k * 2048 + (cb * 128 + c * 8) * 2 - j * 1024;
  }
  const unsigned osrc = (br ? WS_OB : WS_OA) + (rb3 * 4 + lw) * 65536;
  auto wst = [&](int t) {
    const unsigned sow = t * 64 * 2048;
    set_m0(lds0 + p3_wslot(t) + lw * 4096);
    dma16<0>(wv[0], sw, sow); dma16<1024>(wv[1], sw, sow); dma16<2048>(wv[2], sw, sow); dma16<3072>(wv[3], sw, sow);
  };
  auto ost = [&](int t) { dma4k(lds0 + P3_O + (t % NO) * 16384 + lw * 4096, vl, sws, osrc + t * 4096); };
  wst(0); wst(1); wst(2); wst(3);
  if (br) asm volatile("s_barrier" ::: "memory"); else WAIT_BAR(16);
  if (NW > 4) wst(4);
  if (NW > 5) wst(5);
  if (!br && lw == 0) poll(ws, F_OA + (rb3 >> 2) * 32 + (lane & 31), tag);
  asm volatile("s_barrier" ::: "memory");
  asm volatile("s_barrier" ::: "memory");
  asm volatile("s_barrier" ::: "memory");
  if (lead) post(ws, F_OB + x * 32 + idx, tag);
  STAMP(512, 13);
  if (br && lw == 0) poll(ws, F_OB + (rb3 >> 1) * 32 + (rb3 & 1) * 16 + (lane & 15), tag);
  asm volatile("s_barrier" ::: "memory");
  STAMP(512, 14);
#pragma unroll
  for (int t = 0; t < NO; ++t) ost(t);
  wait_bar<4 * (NO - 1)>();
  PIN(pfs);
#define RND(t) do { wait_bar<p3_inflight(t)>(); if (t + NW < 16) wst(t + NW); if (t + NO < 16) ost(t + NO); } while (0)
  RND(0); RND(1); RND(2); RND(3); RND(4); RND(5); RND(6); RND(7); RND(8); RND(9); RND(10); RND(11); RND(12); RND(13); RND(14);
#undef RND
}

__global__ __launch_bounds__(768, 3) void fused_fwd(const unsigned short* __restrict__ xa, const unsigned short* __restrict__ wqkv,
                                                    const unsigned short* __restrict__ wphi, const unsigned short* __restrict__ woa,
                                                    const unsigned short* __restrict__ xb, const unsigned short* __restrict__ wq,
                                                    const unsigned short* __restrict__ wk, const unsigned short* __restrict__ wv,
                                                    const unsigned short* __restrict__ wob, char* __restrict__ ws, unsigned short* __restrict__ out) {
  __shared__ __attribute__((aligned(1024))) char smem[LDS_BYTES];
  const unsigned lds0 = (unsigned)(uintptr_t)(AS3 char*)smem;
  const int tid = threadIdx.x, lane = tid & 63, w0 = __builtin_amdgcn_readfirstlane(tid >> 6), w = HF ? (w0 < 4 ? w0 + 8 : w0 - 4) : w0;
  const char* pc0; unsigned clen;
  asm volatile("s_getpc_b64 %0\n.Lfw_head:\n\ts_mov_b32 %1, .Lfw_tail-.Lfw_head" : "=s"(pc0), "=s"(clen));
  unsigned co = (((blockIdx.x >> 3) & 7) * 32 + lane) * 128;
  co = lane < 32 && co < clen ? co : 0u;
  if (CPF & 1) pf_bin(lds0 + P1_BIN, (tid >> 6) == 4 ? co : 0u, pc0);
  if (CPF & 4) asm volatile("s_cmp_lg_u32 %3, 4\n\ts_cbranch_scc1 1f\n\ts_mov_b32 m0, %0\n\ts_nop 0\n\tglobal_load_lds_dword %1, %2\n1:" ::"s"(lds0 + P1_BIN), "v"(co), "s"(pc0), "s"(w0) : "memory", "scc");
  SBAR();
  asm volatile("" : "+s"(xa), "+s"(wqkv), "+s"(wphi), "+s"(xb), "+s"(wq), "+s"(wk), "+s"(wv), "+s"(ws));
  const int b = blockIdx.x, x = b & 7, idx = b >> 3;
  const int rb = 4 * ((x >> 1) & 1) + (idx & 3);
  const int ct = idx < 16 ? 8 * (x >> 2) + 4 * (x & 1) + (idx >> 2) : 4 * (x >> 2) + ((idx >> 2) & 3);
  const int type = idx < 16 ? 0 : (x & 1) ? 1 : 2 + (ct & 1);
  const rsrc_t sws = mk_rsrc(ws, WS_END);
  STAMP(0, 0);
  if (HX(w >= 8)) {
    helper_program(lds0, type >= 2 ? xb : xa, type >= 2 ? wq : wqkv, type == 3 ? wv : type == 2 ? wk : wphi, woa, wob, ws, sws, pc0, co, type, rb, ct, x, idx, w - 8, lane);
    return;
  }
  if ((CPF & 8) && w == 2) pf_bin(lds0 + P1_BIN, co, pc0);
  if (type == 0 && !(x & 1) && w < 2) pf_bin(lds0 + P1_BIN, (idx * 32 + (lane & 31)) * 128, w ? wv : wk);
  if (type == 0)      p1_compute<0>(lds0, ws, sws, rb, ct, w, lane);
  else if (type == 1) p1_compute<1>(lds0, ws, sws, rb, ct, w, lane);
  else if (type == 2) p1_compute<2>(lds0, ws, sws, rb, ct, w, lane);
  else                p1_compute<3>(lds0, ws, sws, rb, ct, w, lane);

  const int kh = w >> 2, r32 = lane & 31, h = lane >> 5;
  {
    const unsigned s0 = (lane & 15) == 0 ? 0x3f803f80u : 0u, s4 = (lane & 15) == 4 ? 0x3f803f80u : 0u;
    const bf16x8 sel = __builtin_bit_cast(bf16x8, u32x4{s0, s0, s4, s4});
    bf16x8 q[2][2];
    f32x16 O[2]; f32x4 L[2];
    unsigned tv = 0;
    attn_pass_fast(lds0, lds0 + P2_Q + (2 * (w & 3) + (kh ^ 1)) * 2048 + lane * 16, q, sel, kh, lane, O, L, tv, ws);
    TSOUT();
    STAMP(0, 5);
    const unsigned xs = lds0 + P2_XO + w * 4096 + lane * 16, xr = lds0 + P2_XO + (w ^ 4) * 4096 + lane * 16;
#pragma unroll
    for (int g = 0; g < 4; ++g) *(AS3 f32x4*)(xs + g * 1024) = f32x4{O[0][4 * g], O[0][4 * g + 1], O[0][4 * g + 2], O[0][4 * g + 3]};
    *(AS3 float*)(lds0 + P2_XL + w * 256 + lane * 4) = L[0][0];
    LDS_BAR();
#pragma unroll
    for (int g = 0; g < 4; ++g) {
      const f32x4 o = *(AS3 f32x4*)(xr + g * 1024);
      O[1][4 * g] += o[0]; O[1][4 * g + 1] += o[1]; O[1][4 * g + 2] += o[2]; O[1][4 * g + 3] += o[3];
    }
    L[1][0] += *(AS3 float*)(lds0 + P2_XL + (w ^ 4) * 256 + lane * 4);
    bool bad = !(L[1][0] < 1.8446744e19f);
#pragma unroll
    for (int i = 0; i < 16; ++i) bad |= !(fabsf(O[1][i]) < 3.0e38f);
    if (__any(bad)) attn_pass_safe(sws, q[1][0], q[1][1], sel, lane, O[1], L[1]);
    const float r = __builtin_amdgcn_rcpf(xhalf_sum(L[1][0]));
    O[1] *= r;
    const unsigned p = WS_OB + ((8 * x + (idx >> 2)) * 32 + 8 * (idx & 3) + 2 * (w & 3) + kh) * 2048 + lane * 16;
    st16(sws, p, pack8<0>(O[1])); st16(sws, p + 1024, pack8<1>(O[1]));
    WAIT_BAR(0);
    STAMP(0, 6);
  }
  const int br = !(x & 1), rb3 = 4 * (x >> 1) + (idx & 3), cb = idx >> 2;
  const int wm = w & 1, wn = (w >> 1) & 1;
  const int q4 = (lane & 15) >> 2, p4 = lane & 3, chalf = (lane >> 4) & 1;
  const unsigned oa = wm * 8192 + kh * (4096 + 2048) + lane * 16;
  unsigned wa[2];
#pragma unroll
  for (int ns = 0; ns < 2; ++ns)
    wa[ns] = (kh * 32 + 4 * h + q4) * 256 + ((((wn * 2 + ns) ^ q4) << 2) | (chalf * 2 + (p4 >> 1))) * 16 + (p4 & 1) * 8;
  f32x16 acc[2][2] = {};
  bf16x8 xf[2][2], wfr[2][2];
  auto rdx = [&](int set, unsigned buf, int ks) {
#pragma unroll
    for (int ms = 0; ms < 2; ++ms) xf[set][ms] = lds_r128(buf + (oa ^ (ms * 4096)) + ks * 1024);
  };
  auto rdw = [&](int set, unsigned buf, int ks, int ns) { wfr[set][ns] = lds_tr2(buf + wa[ns] + ks * 4096, buf + wa[ns] + ks * 4096 + 2048); };
  auto mm1 = [&](int set, int ms, int ns) { acc[ms][ns] = MFMA32(wfr[set][ns], xf[set][ms], acc[ms][ns]); };
  auto sub = [&](int set, bool rd, unsigned ob, unsigned wb, int ks) {
    SBAR(); mm1(set, 0, 0); SBAR(); if (rd) rdx(set ^ 1, ob, ks);
    SBAR(); mm1(set, 1, 0); SBAR(); if (rd) rdw(set ^ 1, wb, ks, 0);
    SBAR(); mm1(set, 0, 1); SBAR(); if (rd) rdw(set ^ 1, wb, ks, 1);
    SBAR(); mm1(set, 1, 1); SBAR();
  };
  asm volatile("s_barrier" ::: "memory");
  LDS_BAR();
  STAMP(0, 7);
  unsigned ob = lds0 + P3_O, wb = lds0 + p3_wslot(0);
  rdx(0, ob, 0); rdw(0, wb, 0, 0); rdw(0, wb, 0, 1);
  LGKM0();
#pragma unroll 1
  for (int t = 0; t < 15; ++t) {
    sub(0, true, ob, wb, 1);
    ob = (ob == lds0 + P3_O + (NO - 1) * 16384) ? lds0 + P3_O : ob + 16384;
    wb = (wb == lds0 + 147456) ? lds0 + P3_W : wb + 16384;
    LDS_BAR();
    sub(1, true, ob, wb, 0);
  }
  sub(0, true, ob, wb, 1);
  sub(1, false, ob, wb, 0);
  STAMP(0, 8);
  constexpr int XB = NO == 5 ? 16384 : 65536, IB = NO == 5 ? 131072 : 0;
  static_assert(NO == 4 || NO == 5 || NO == 6, "");
  const unsigned xs = lds0 + XB + w * 8192 + lane * 16, xr = lds0 + XB + (w ^ 4) * 8192 + lane * 16;
#pragma unroll
  for (int ns = 0; ns < 2; ++ns)
#pragma unroll
    for (int g = 0; g < 4; ++g) {
      const f32x16& a = acc[1][ns];
      *(AS3 f32x4*)(xs + ns * 4096 + g * 1024) = f32x4{a[4 * g], a[4 * g + 1], a[4 * g + 2], a[4 * g + 3]};
    }
  LDS_BAR();
  const unsigned img = lds0 + IB + w * 4096;
#pragma unroll
  for (int ns = 0; ns < 2; ++ns)
#pragma unroll
    for (int g = 0; g < 4; ++g) {
      const f32x16& a = acc[0][ns];
      const f32x4 o = *(AS3 f32x4*)(xr + ns * 4096 + g * 1024);
      *(AS3 u32x2*)(img + r32 * 128 + (((ns * 4 + g) ^ (r32 & 7)) << 4) + h * 8) =
          u32x2{pk2(a[4 * g] + o[0], a[4 * g + 1] + o[1]), pk2(a[4 * g + 2] + o[2], a[4 * g + 3] + o[3])};
    }
  asm volatile("" ::: "memory");
  const int tok0 = rb3 * 128 + wm * 64 + kh * 32;
  const rsrc_t so = mk_rsrc(out, 2048u * 4096u);
  const unsigned op = tok0 * 4096 + br * 2048 + cb * 256 + wn * 128;
#pragma unroll
  for (int i = 0; i < 4; ++i) {
    const int row = 8 * i + (lane >> 3), ch = lane & 7;
    const u32x4 v = *(AS3 u32x4*)(img + row * 128 + ((ch ^ (row & 7)) << 4));
    st16(so, op + row * 4096 + ch * 16, v);
  }
  STAMP(0, 9);
  asm volatile(".Lfw_tail:");
}

extern "C" void kernel_launch(void* const* d_in, const int* in_sizes, int n_in, void* d_out, int out_size, void* d_ws, size_t ws_size,
                              hipStream_t stream) {
  (void)in_sizes; (void)n_in; (void)out_size; (void)ws_size;
  typedef const unsigned short* cp;
  fused_fwd<<<256, 768, 0, stream>>>((cp)d_in[0], (cp)d_in[1], (cp)d_in[2], (cp)d_in[3], (cp)d_in[4], (cp)d_in[5], (cp)d_in[6], (cp)d_in[7],
                                     (cp)d_in[8], (char*)d_ws, (unsigned short*)d_out);
}
```

```cpp
#define STAMPS
#include <hip/hip_runtime.h>
#include <hip/hip_bf16.h>
#include <stdint.h>
#pragma clang diagnostic ignored "-Wint-to-pointer-cast"

typedef __bf16   bf16x8 __attribute__((ext_vector_type(8)));
typedef __bf16   bf16x16 __attribute__((ext_vector_type(16)));
typedef __bf16   bf16x2 __attribute__((ext_vector_type(2)));
typedef short    s16x4  __attribute__((ext_vector_type(4)));
typedef short    s16x8  __attribute__((ext_vector_type(8)));
typedef float    f32x16 __attribute__((ext_vector_type(16)));
typedef float    f32x4  __attribute__((ext_vector_type(4)));
typedef float    f32x2  __attribute__((ext_vector_type(2)));
typedef unsigned u32x4  __attribute__((ext_vector_type(4)));
typedef unsigned u32x2  __attribute__((ext_vector_type(2)));
typedef unsigned long long u64;
typedef __amdgpu_buffer_rsrc_t rsrc_t;

#define AS1 __attribute__((address_space(1)))
#define AS3 __attribute__((address_space(3)))
#define DI __device__ __forceinline__
#define MFMA32(a, b, c) __builtin_amdgcn_mfma_f32_32x32x16_bf16((a), (b), (c), 0, 0, 0)
#define MFMA16(a, b, c) __builtin_amdgcn_mfma_f32_16x16x32_bf16((a), (b), (c), 0, 0, 0)
#define SM32(a, b, c) __builtin_amdgcn_smfmac_f32_32x32x32_bf16((a), (b), (c), odd, 0, 0)
#define SM16(a, b, c) __builtin_amdgcn_smfmac_f32_16x16x64_bf16((a), (b), (c), odd, 0, 0)
#define LO(S) __builtin_bit_cast(bf16x16, __builtin_shufflevector(S, S, 0, 1, 2, 3, 4, 5, 6, 7))
#define HI(S) __builtin_bit_cast(bf16x16, __builtin_shufflevector(S, S, 8, 9, 10, 11, 12, 13, 14, 15))
#define SBAR() __builtin_amdgcn_sched_barrier(0)
#define WAIT_BAR(N) asm volatile("s_waitcnt vmcnt(" #N ") lgkmcnt(0)\n\ts_barrier" ::: "memory")
#define LGKM0() __builtin_amdgcn_s_waitcnt(0xC07F)
#define LDS_BAR() do { asm volatile("" ::: "memory"); LGKM0(); asm volatile("s_barrier" ::: "memory"); } while (0)

#define STAMP(th, i) do { const unsigned c_ = (unsigned)__builtin_amdgcn_s_memtime(), r_ = (unsigned)__builtin_amdgcn_s_memrealtime(); \
    sv = (lane == 2 * (i)) ? c_ : (lane == 2 * (i) + 1) ? r_ : sv; asm volatile("" : "+v"(sv)); } while (0)
#define STOUT(lo, hi) do { if (lane >= 2 * (lo) && lane < 2 * (hi)) ((u64*)(ws + (64u << 20)))[(size_t)blockIdx.x * 32 + lane] = sv; } while (0)
#define SVP , unsigned& sv
#define SVA , sv
#define TS(i) do {} while (0)
#define TSOUT() do {} while (0)
#define HF 1
#define VT ((HF) ? (threadIdx.x + 512) % 768 : threadIdx.x)
#define KA 1
#define HX(c) __builtin_expect(c, 1)
#define EL 1
#define CPF 10
#define TPF 0
#define HE 1
#define EPRIO 0
#define KVP 2
#define KVR 2
#define THR 0
#define THO 0
#define TH3 0
#define NF 3
#define ZREF 0
#define EPF 1
#define EMV 1
#define PFB 0
#define PF0 3
#define BROT (8 * (((x >> 1) ^ (x >> 2)) & 1))
#define NTX 0
#define NTW 0
#define KROT (2 * x)
#define MAPV 1
constexpr int D = 1024;
constexpr float LOG2E = 1.4426950408889634f;
constexpr float QSCALE = 0.17677669529663687f * LOG2E;

constexpr unsigned WS_QA  = 0;
constexpr unsigned WS_QB  = WS_QA + (8u << 20);
constexpr unsigned WS_OA  = WS_QB + (4u << 20);
constexpr unsigned WS_OB  = WS_OA + (4u << 20);
constexpr unsigned WS_KV  = WS_OB + (4u << 20);
constexpr unsigned WS_KVP = WS_KV + (256u << 10);
constexpr unsigned WS_ZP  = WS_KVP + (2u << 20);
constexpr unsigned WS_FLG = WS_ZP + (64u << 10);
constexpr unsigned WS_END = WS_FLG + (16u << 10);
constexpr int F_K = 0, F_Q = 128, F_OA = 256, F_OB = 512, F_KV = 768, F_EPOCH = 1024;

DI unsigned pk2(float lo, float hi) {
  f32x2 v = {lo, hi};
  return __builtin_bit_cast(unsigned, __builtin_convertvector(v, bf16x2));
}
template <int SS> DI bf16x8 pack8(const f32x16& x) {
  u32x4 p = {pk2(x[8 * SS + 0], x[8 * SS + 1]), pk2(x[8 * SS + 2], x[8 * SS + 3]), pk2(x[8 * SS + 4], x[8 * SS + 5]), pk2(x[8 * SS + 6], x[8 * SS + 7])};
  return __builtin_bit_cast(bf16x8, p);
}
DI bf16x8 lds_r128(unsigned a) { return *(const AS3 bf16x8*)a; }
DI bf16x8 lds_tr2(unsigned a0, unsigned a1) {
  s16x4 lo = __builtin_amdgcn_ds_read_tr16_b64_v4i16((AS3 s16x4*)a0);
  s16x4 hi = __builtin_amdgcn_ds_read_tr16_b64_v4i16((AS3 s16x4*)a1);
  s16x8 r = __builtin_shufflevector(lo, hi, 0, 1, 2, 3, 4, 5, 6, 7);
  return __builtin_bit_cast(bf16x8, r);
}
DI bf16x8 lds_trp(const AS3 char* p, int d) {
  s16x4 lo = __builtin_amdgcn_ds_read_tr16_b64_v4i16((AS3 s16x4*)p);
  s16x4 hi = __builtin_amdgcn_ds_read_tr16_b64_v4i16((AS3 s16x4*)(p + d));
  return __builtin_bit_cast(bf16x8, __builtin_shufflevector(lo, hi, 0, 1, 2, 3, 4, 5, 6, 7));
}
DI rsrc_t mk_rsrc(const void* p, unsigned bytes) { return __builtin_amdgcn_make_buffer_rsrc((void*)p, (short)0, (int)bytes, 0x00020000); }
DI void set_m0(unsigned v) { asm volatile("s_mov_b32 m0, %0\n\ts_nop 0" ::"s"(v) : "memory"); }
template <int IMM> DI void dma16(unsigned voff, rsrc_t srd, unsigned soff) {
  asm volatile("buffer_load_dwordx4 %0, %1, %2 offen offset:%3 lds" ::"v"(voff), "s"(srd), "s"(soff), "i"(IMM) : "memory");
}
template <int IMM> DI void dma16n(unsigned voff, rsrc_t srd, unsigned soff) {
  asm volatile("buffer_load_dwordx4 %0, %1, %2 offen offset:%3 nt lds" ::"v"(voff), "s"(srd), "s"(soff), "i"(IMM) : "memory");
}
#define DMAX dma16
#define DMAW dma16
template <int IMM> DI void dma16d(unsigned voff, rsrc_t srd, unsigned soff) {
  asm volatile("buffer_load_dwordx4 %0, %1, %2 offen offset:%3 sc1 lds" ::"v"(voff), "s"(srd), "s"(soff), "i"(IMM) : "memory");
}
DI void dma4k(unsigned lds, unsigned voff, rsrc_t srd, unsigned soff) {
  set_m0(lds); dma16d<0>(voff, srd, soff); dma16d<1024>(voff, srd, soff); dma16d<2048>(voff, srd, soff); dma16d<3072>(voff, srd, soff);
}
template <class T> DI void st16(rsrc_t r, unsigned voff, T v) { __builtin_amdgcn_raw_buffer_store_b128(__builtin_bit_cast(u32x4, v), r, (int)voff, 0, 16); }
DI void st8(rsrc_t r, unsigned voff, u32x2 v) { __builtin_amdgcn_raw_buffer_store_b64(v, r, (int)voff, 0, 16); }
DI void st4(rsrc_t r, unsigned voff, float v) { __builtin_amdgcn_raw_buffer_store_b32(__float_as_uint(v), r, (int)voff, 0, 16); }
DI u32x4 ld16(rsrc_t r, unsigned voff) { return __builtin_amdgcn_raw_buffer_load_b128(r, (int)voff, 0, 16); }
DI float ld4(rsrc_t r, unsigned voff) { return __uint_as_float(__builtin_amdgcn_raw_buffer_load_b32(r, (int)voff, 0, 16)); }
DI void post(char* ws, int f, u64 tag) { __hip_atomic_store((AS1 u64*)(ws + WS_FLG) + f, tag, __ATOMIC_RELAXED, __HIP_MEMORY_SCOPE_AGENT); }
DI void poll(char* ws, int f, u64 tag) {
#pragma unroll 1
  for (int it = 0; it < (1 << 17); ++it) {
    const u64 v = __hip_atomic_load((AS1 u64*)(ws + WS_FLG) + f, __ATOMIC_RELAXED, __HIP_MEMORY_SCOPE_AGENT);
    if (__all(v == tag)) break;
    __builtin_amdgcn_s_sleep(2);
  }
  asm volatile("" ::: "memory");
}
DI void pf_line(unsigned& sink, const void* p) { asm volatile("global_load_dword %0, %1, off" : "+v"(sink) : "v"(p) : "memory"); }
#define PIN(x) asm volatile("" : "+v"(x))
DI void pf_bin(unsigned bin, unsigned voff, const void* base) {
  asm volatile("s_mov_b32 m0, %0\n\ts_nop 0\n\tglobal_load_lds_dword %1, %2" ::"s"(bin), "v"(voff), "s"(base) : "memory");
}
DI float one(float v) { asm("" : "+v"(v)); return v; }
DI f32x4 add4(f32x4 a, f32x4 b) { return f32x4{one(a[0] + b[0]), one(a[1] + b[1]), one(a[2] + b[2]), one(a[3] + b[3])}; }
DI float xhalf_max(float v) {
  auto r = __builtin_amdgcn_permlane32_swap(__float_as_uint(v), __float_as_uint(v), false, false);
  return fmaxf(__uint_as_float(r[0]), __uint_as_float(r[1]));
}
DI float xhalf_min(float v) {
  auto r = __builtin_amdgcn_permlane32_swap(__float_as_uint(v), __float_as_uint(v), false, false);
  return fminf(__uint_as_float(r[0]), __uint_as_float(r[1]));
}
DI float xhalf_sum(float v) {
  auto r = __builtin_amdgcn_permlane32_swap(__float_as_uint(v), __float_as_uint(v), false, false);
  return __uint_as_float(r[0]) + __uint_as_float(r[1]);
}
DI float max16(const f32x16& x) {
  float a = fmaxf(fmaxf(x[0], x[1]), x[2]), b = fmaxf(fmaxf(x[3], x[4]), x[5]);
  a = fmaxf(fmaxf(a, x[6]), x[7]);   b = fmaxf(fmaxf(b, x[8]), x[9]);
  a = fmaxf(fmaxf(a, x[10]), x[11]); b = fmaxf(fmaxf(b, x[12]), x[13]);
  a = fmaxf(fmaxf(a, x[14]), x[15]);
  return fmaxf(a, b);
}
DI float min16(const f32x16& x) {
  float a = fminf(fminf(x[0], x[1]), x[2]), b = fminf(fminf(x[3], x[4]), x[5]);
  a = fminf(fminf(a, x[6]), x[7]);   b = fminf(fminf(b, x[8]), x[9]);
  a = fminf(fminf(a, x[10]), x[11]); b = fminf(fminf(b, x[12]), x[13]);
  a = fminf(fminf(a, x[14]), x[15]);
  return fminf(a, b);
}
template <bool NORM> DI void feat_map(const f32x16& p, f32x16& ep, f32x16& em) {
  const float mx = xhalf_max(max16(p)), mn = xhalf_min(min16(p));
  const f32x2 a = {-mx * LOG2E, -mx * LOG2E}, b = {mn * LOG2E, mn * LOG2E}, c = {LOG2E, LOG2E};
  f32x2 sp = {0.f, 0.f}, sm = {0.f, 0.f};
#pragma unroll
  for (int i = 0; i < 16; i += 2) {
    const f32x2 x = {p[i], p[i + 1]};
    const f32x2 u = __builtin_elementwise_fma(x, c, a), v = __builtin_elementwise_fma(x, -c, b);
    const f32x2 eu = {__builtin_amdgcn_exp2f(u[0]), __builtin_amdgcn_exp2f(u[1])}, ev = {__builtin_amdgcn_exp2f(v[0]), __builtin_amdgcn_exp2f(v[1])};
    ep[i] = eu[0]; ep[i + 1] = eu[1]; em[i] = ev[0]; em[i + 1] = ev[1];
    if (i == 0) { sp = eu; sm = ev; } else { sp += eu; sm += ev; }
  }
  if (NORM) {
    const float rp = __builtin_amdgcn_rcpf(xhalf_sum(sp[0] + sp[1])), rm = __builtin_amdgcn_rcpf(xhalf_sum(sm[0] + sm[1]));
    ep *= rp; em *= rm;
  }
}
DI bf16x8 row_sum_sel(int lane) {
  const unsigned u = ((lane & 11) == 0 && (((lane >> 2) ^ (lane >> 4)) & 1) == 0) ? 0x3f803f80u : 0u;
  return __builtin_bit_cast(bf16x8, u32x4{u, u, u, u});
}

constexpr int LDS_BYTES = 163840;
constexpr int P1_WOFF = 32768, P1_EOFF = 49152, P1_STAGE = 53248, P1_ZRED = 3 * P1_STAGE, P1_CNT = P1_ZRED + 2048, P1_GO = P1_CNT + 16, P1_BIN = P1_CNT + 256, P1_PHI = 65536;
constexpr int P2_Q = 131072, P2_KVF = 147456, P2_ZB = 151552;
constexpr int P2_XO = 32768, P2_XL = 0;
#define NO 4
constexpr int NW = 10 - NO;
constexpr int P3_O = 0, P3_W = NO * 16384;
DI constexpr int p3_wslot(int t) { return P3_W + ((t + NW - 4) % NW) * 16384; }
DI constexpr int p3_inflight(int t) {
  int n = 0, after = 0;
  for (int j = 0; j < NO; ++j) { n += 4; if (j == t + 1) after = n; }
  for (int u = 0; u < t; ++u) {
    if (u + NW < 16) { n += 4; if (u + NW == t + 1) after = n; }
    if (u + NO < 16) { n += 4; if (u + NO == t + 1) after = n; }
  }
  return n - after;
}
template <int N> DI void wait_bar() { asm volatile("s_waitcnt vmcnt(%0) lgkmcnt(0)\n\ts_barrier" ::"i"(N) : "memory"); }

template <int TYPE>
DI void p1_compute(unsigned lds0, char* __restrict__ ws, rsrc_t sws, int rb, int ct, int w, int lane, bool slow SVP) {
  const int wm = w & 3, wn = w >> 2, r32 = lane & 31, h = lane >> 5;
  if (w == 0 && lane == 0) *(AS3 unsigned*)(lds0 + P1_GO) = 0u;

  typedef const AS3 char* lp;
  const int xrow = wm * 64 + r32;
  const int q4 = (lane & 15) >> 2, p4 = lane & 3, chalf = (lane >> 4) & 1;
  lp xa[4], wa[2];
#pragma unroll
  for (int ks = 0; ks < 4; ++ks) xa[ks] = (lp)(lds0 + ((xrow * 128 + ((h ^ ((xrow >> 1) & 7)) << 4)) ^ (ks * 32)));
#pragma unroll
  for (int ns = 0; ns < 2; ++ns)
    wa[ns] = (lp)(lds0 + P1_WOFF + (8 * h + q4) * 256 + ((((wn * 2 + ns) ^ q4) << 2) | (chalf * 2 + (p4 >> 1))) * 16 + (p4 & 1) * 8);

  f32x16 acc[2][2] = {};
  bf16x8 xf[2][2], wfr[2][2];
  auto rdx = [&](int set, int b, int ks) {
#pragma unroll
    for (int ms = 0; ms < 2; ++ms) xf[set][ms] = *(const AS3 bf16x8*)(xa[ks] + b * P1_STAGE + ms * 4096);
  };
  auto rdw = [&](int set, int b, int ks, int ns) { wfr[set][ns] = lds_trp(wa[ns] + b * P1_STAGE + ks * 4096, 1024); };
  auto mm1 = [&](int set, int ms, int ns) {
    if (TYPE == 0 && ns == 1) acc[ms][1] = MFMA32(xf[set][ms], wfr[set][1], acc[ms][1]);
    else                      acc[ms][ns] = MFMA32(wfr[set][ns], xf[set][ms], acc[ms][ns]);
  };
  auto sub = [&](int set, int b, int ks) {
    SBAR(); mm1(set, 0, 0); SBAR(); if (b >= 0) rdx(set ^ 1, b, ks);
    SBAR(); mm1(set, 1, 0); SBAR(); if (b >= 0) rdw(set ^ 1, b, ks, 0);
    SBAR(); mm1(set, 0, 1); SBAR(); if (b >= 0) rdw(set ^ 1, b, ks, 1);
    SBAR(); mm1(set, 1, 1); SBAR();
  };
  auto stg = [&](int b, int nb) {
    sub(0, b, 1); sub(1, b, 2); sub(0, b, 3);
    LDS_BAR();
    if (THO && TYPE == 0) __builtin_amdgcn_s_sleep(THO);
    sub(1, nb, 0);
  };
  LDS_BAR();
  STAMP(0, 1);
  rdx(0, 0, 0); rdw(0, 0, 0, 0); rdw(0, 0, 0, 1);
  LGKM0();
#pragma unroll 1
  for (int t = 0; t < 5; ++t) {
    stg(0, 1); stg(1, 2); stg(2, 0);
    if (THR && TYPE == 0 && slow) __builtin_amdgcn_s_sleep(THR);
  }
  stg(0, -1);
  STAMP(0, 2);
  bf16x8 wf[2][2];
  if constexpr (TYPE <= 1) {
    const unsigned a = lds0 + P1_PHI + (TYPE == 0 ? wn : 2 * wn) * 2048 + (4 * h + q4) * 64 + chalf * 32 + p4 * 8;
#pragma unroll
    for (int n = 0; n <= TYPE; ++n)
#pragma unroll
      for (int s = 0; s < 2; ++s) wf[n][s] = lds_tr2(a + n * 2048 + s * 1024, a + n * 2048 + s * 1024 + 512);
  }

  if (TYPE >= 2) {
#pragma unroll
    for (int ms = 0; ms < 2; ++ms)
#pragma unroll
      for (int ns = 0; ns < 2; ++ns) {
        const f32x16 v = acc[ms][ns] * QSCALE;
        const unsigned p = WS_QB + ((rb * 8 + wm * 2 + ms) * 32 + ct * 4 + wn * 2 + ns) * 2048 + lane * 16;
        st16(sws, p, pack8<0>(v)); st16(sws, p + 1024, pack8<1>(v));
      }
    WAIT_BAR(0);
  } else if (TYPE == 1) {
    f32x16 pq[2][2];
#pragma unroll
    for (int ms = 0; ms < 2; ++ms)
#pragma unroll
      for (int ns = 0; ns < 2; ++ns) {
        pq[ms][ns] = MFMA32(wf[ns][0], pack8<0>(acc[ms][ns]), f32x16{});
        pq[ms][ns] = MFMA32(wf[ns][1], pack8<1>(acc[ms][ns]), pq[ms][ns]);
      }
#pragma unroll
    for (int ms = 0; ms < 2; ++ms)
#pragma unroll
      for (int ns = 0; ns < 2; ++ns) {
        f32x16 ep, em;
        feat_map<false>(pq[ms][ns], ep, em);
        const unsigned p = WS_QA + ((rb * 8 + wm * 2 + ms) * 32 + ct * 4 + wn * 2 + ns) * 4096 + lane * 16;
        st16(sws, p, pack8<0>(ep)); st16(sws, p + 1024, pack8<1>(ep)); st16(sws, p + 2048, pack8<0>(em)); st16(sws, p + 3072, pack8<1>(em));
      }
  } else {
    const unsigned img = lds0 + w * 8192;
    const bf16x8 sel = row_sum_sel(lane);
    f32x16 pk[2], kv[2] = {};
    f32x4 zz[2] = {};
    bf16x8 vf[2][2], af[2][2];
#pragma unroll
    for (int ms = 0; ms < 2; ++ms) {
      pk[ms] = MFMA32(wf[0][0], pack8<0>(acc[ms][0]), f32x16{});
      pk[ms] = MFMA32(wf[0][1], pack8<1>(acc[ms][0]), pk[ms]);
    }
#pragma unroll
    for (int ms = 0; ms < 2; ++ms) { vf[ms][0] = pack8<0>(acc[ms][1]); vf[ms][1] = pack8<1>(acc[ms][1]); PIN(vf[ms][0]); PIN(vf[ms][1]); }
    float off[4];
#pragma unroll
    for (int ms = 0; ms < 2; ++ms) { off[2 * ms] = -LOG2E * xhalf_max(max16(pk[ms])); off[2 * ms + 1] = LOG2E * xhalf_min(min16(pk[ms])); }
    const unsigned wr = img + 4096 + r32 * 64, sw = (r32 >> 1) & 7;
    const int t0 = 4 * h + q4, u8 = 4 * chalf + p4;
    const unsigned rd0 = img + 4096 + t0 * 64 + ((u8 ^ (t0 >> 1)) << 3), rd1 = img + 4096 + (t0 + 8) * 64 + ((u8 ^ ((t0 >> 1) + 4)) << 3);
    auto put = [&](int sg) {
#pragma unroll
      for (int g = 0; g < 4; ++g) *(AS3 f32x4*)(img + sg * 4096 + g * 1024 + lane * 16) = f32x4{kv[sg][4 * g], kv[sg][4 * g + 1], kv[sg][4 * g + 2], kv[sg][4 * g + 3]};
    };
    f32x16 e[2];
    float rs[2];
    auto mm = [&](int u, int j) {
      if (j & 1) zz[u & 1] = MFMA16(sel, af[u & 1][j >> 1], zz[u & 1]); else kv[u & 1] = MFMA32(af[u & 1][j >> 1], vf[u >> 1][j >> 1], kv[u & 1]);
    };
    auto exps = [&](int u, int mu) {
      const f32x16& p = pk[u >> 1];
      const float c = (u & 1) ? -LOG2E : LOG2E;
      f32x2 sm;
#pragma unroll
      for (int i = 0; i < 16; i += 2) {
        if (mu >= 0 && i >= 8) { SBAR(); mm(mu, (i - 8) >> 1); SBAR(); }
        const f32x2 v = __builtin_elementwise_fma(f32x2{p[i], p[i + 1]}, f32x2{c, c}, f32x2{off[u], off[u]});
        const f32x2 ev = {__builtin_amdgcn_exp2f(v[0]), __builtin_amdgcn_exp2f(v[1])};
        e[u & 1][i] = ev[0]; e[u & 1][i + 1] = ev[1];
        if (i == 0) sm = ev; else sm += ev;
      }
      rs[u & 1] = __builtin_amdgcn_rcpf(xhalf_sum(sm[0] + sm[1]));
    };
    auto rest = [&](int u) {
      const f32x16 n = e[u & 1] * rs[u & 1];
#pragma unroll
      for (int g = 0; g < 4; ++g) *(AS3 u32x2*)(wr + (u & 1) * 2048 + (((2 * g + h) ^ sw) << 3)) = u32x2{pk2(n[4 * g], n[4 * g + 1]), pk2(n[4 * g + 2], n[4 * g + 3])};
      asm volatile("" ::: "memory");
#pragma unroll
      for (int s = 0; s < 2; ++s) af[u & 1][s] = lds_tr2(rd0 + (u & 1) * 2048 + s * 1024, rd1 + (u & 1) * 2048 + s * 1024);
      asm volatile("" ::: "memory");
      SBAR();
    };
    exps(0, -1); exps(1, -1); rest(0);
    exps(2, 0);  rest(1);
    exps(3, 1);  rest(2);
    rest(3);
    mm(2, 0); mm(2, 2); mm(3, 0); mm(3, 2); SBAR(); mm(2, 1); mm(2, 3); mm(3, 1); mm(3, 3);
    put(0); put(1);
    if (lane < 32) {
      const unsigned a = lds0 + P1_ZRED + w * 256 + ((lane >> 2) & 1) * 64 + (lane >> 3) * 16 + (lane & 3) * 4;
      *(AS3 float*)a = zz[0][0]; *(AS3 float*)(a + 128) = zz[1][0];
    }
  }
  STAMP(0, 3);
}

DI void attn_pass_fast(unsigned lds0, unsigned qa, bf16x8 (&q)[2][2], bf16x8 sel, int kh, int lane, f32x16 (&O)[2], f32x4 (&L)[2], unsigned& tv SVP) {
  typedef const AS3 char* lp;
  lp l0 = (lp)(lds0 + lane * 16), la = l0 + kh * 16384;
#define RD(p) (*(const AS3 bf16x8*)(p))
  f32x16 negm[2], S0, S1 = {};
  bf16x8 vb = bf16x8{};
  int odd = 0xDDDD;
  O[0] = f32x16{}; O[1] = f32x16{}; L[0] = f32x4{}; L[1] = f32x4{};
  unsigned a0 = lds0 + lane * 16 + 98304, a1 = a0 + kh * 16384, qb = qa ^ 2048;
  PIN(O[0]); PIN(O[1]); PIN(L[0]); PIN(L[1]); PIN(S1); PIN(vb); PIN(odd); PIN(sel); PIN(a0); PIN(a1); PIN(qa); PIN(qb);
  LDS_BAR();
  STAMP(0, 4);
  TS(0);
  if (kh) __builtin_amdgcn_s_setprio(1);
  const bf16x8 ra = lds_r128(a0);
  q[0][0] = lds_r128(qa); q[1][0] = lds_r128(qb);
  const bf16x8 rb = lds_r128(a0 + 1024);
  q[0][1] = lds_r128(qa + 1024); q[1][1] = lds_r128(qb + 1024);
  bf16x8 ka = lds_r128(a1), kb = lds_r128(a1 + 1024), va = lds_r128(a1 + 2048);
  SBAR();
  negm[0] = MFMA32(ra, q[0][0], f32x16{}); negm[1] = MFMA32(ra, q[1][0], f32x16{});
  negm[0] = MFMA32(rb, q[0][1], negm[0]);  negm[1] = MFMA32(rb, q[1][1], negm[1]);
#pragma unroll
  for (int r = 0; r < 2; ++r) {
    const float m = xhalf_max(max16(negm[r]));
#pragma unroll
    for (int i = 0; i < 16; ++i) negm[r][i] = -m;
    PIN(negm[r]);
    SBAR();
    if (r == 0) { S0 = MFMA32(ka, q[0][0], negm[0]); S0 = MFMA32(kb, q[0][1], S0); PIN(S0); SBAR(); }
  }
#define EXN(SC, i, n) do { _Pragma("unroll") for (int k_ = 0; k_ < n; ++k_) SC[i + k_] = __builtin_amdgcn_exp2f(SC[i + k_]); PIN(SC); } while (0)
#define MM(D, E) do { SBAR(); D = E; PIN(D); SBAR(); } while (0)
#define TILE(TA, TN) do { \
    SBAR();                                                             EXN(S0, 0, 2);  \
    MM(O[1], SM32(vb, HI(S1), O[1]));       vb = RD((TA) + 3072);       EXN(S0, 2, 3);  \
    MM(L[1], SM16(sel, HI(S1), L[1]));                                  EXN(S0, 5, 2);  \
    MM(S1, MFMA32(ka, q[1][0], negm[1]));   ka = RD(TN);                EXN(S0, 7, 3);  \
    MM(S1, MFMA32(kb, q[1][1], S1));        kb = RD((TN) + 1024);       EXN(S0, 10, 3); \
    MM(O[0], SM32(va, LO(S0), O[0]));                                   EXN(S0, 13, 3); \
    MM(L[0], SM16(sel, LO(S0), L[0]));                                  EXN(S1, 0, 2);  \
    MM(O[0], SM32(vb, HI(S0), O[0]));                                   EXN(S1, 2, 3);  \
    MM(L[0], SM16(sel, HI(S0), L[0]));                                  EXN(S1, 5, 2);  \
    MM(S0, MFMA32(ka, q[0][0], negm[0]));                               EXN(S1, 7, 3);  \
    MM(S0, MFMA32(kb, q[0][1], S0));                                    EXN(S1, 10, 3); \
    MM(O[1], SM32(va, LO(S1), O[1]));       va = RD((TN) + 2048);       EXN(S1, 13, 3); \
    MM(L[1], SM16(sel, LO(S1), L[1])); } while (0)
  TS(1);
#pragma unroll 1
  for (int c = 0; c < 8; ++c) {
    const lp cb = la + ((3 - c) & 3) * 32768, nb = la + ((2 - c) & 3) * 32768;
    TILE(cb, cb + 4096);
    TILE(cb + 4096, cb + 8192);
    SBAR(); if (kh) __builtin_amdgcn_s_setprio(0);
    TILE(cb + 8192, cb + 12288);
    SBAR(); TS(2 + 2 * c);
    asm volatile("s_waitcnt vmcnt(0)\n\ts_barrier" ::: "memory"); TS(3 + 2 * c);
    if (kh) __builtin_amdgcn_s_setprio(1);
    TILE(cb + 12288, nb);
  }
  SBAR();
  O[1] = SM32(vb, HI(S1), O[1]); L[1] = SM16(sel, HI(S1), L[1]);
  __builtin_amdgcn_s_setprio(0);
  TS(18);
#undef TILE
#undef EXN
#undef MM
#undef RD
}

DI void attn_pass_safe(rsrc_t sws, const bf16x8 q0, const bf16x8 q1, const bf16x8 sel, int lane, f32x16& O, f32x4& L) {
  O = f32x16{}; L = f32x4{};
  float m = -INFINITY;
  const int odd = 0xDDDD;
#pragma unroll 1
  for (int t = 0; t < 64; ++t) {
    bf16x8 a[4];
#pragma unroll
    for (int j = 0; j < 4; ++j) a[j] = __builtin_bit_cast(bf16x8, ld16(sws, WS_KV + t * 4096 + j * 1024 + lane * 16));
    f32x16 Sc = {};
    Sc = MFMA32(a[0], q0, Sc); Sc = MFMA32(a[1], q1, Sc);
    const float mn = fmaxf(m, xhalf_max(max16(Sc)));
    const float al = __builtin_amdgcn_exp2f(m - mn);
    m = mn;
#pragma unroll
    for (int r = 0; r < 16; ++r) { O[r] *= al; Sc[r] = __builtin_amdgcn_exp2f(Sc[r] - mn); }
    L *= al;
    O = SM32(a[2], LO(Sc), O); L = SM16(sel, LO(Sc), L);
    O = SM32(a[3], HI(Sc), O); L = SM16(sel, HI(Sc), L);
  }
}

DI void helper_program(unsigned lds0, const unsigned short* __restrict__ X, const unsigned short* __restrict__ W, const unsigned short* __restrict__ E,
                       const unsigned short* __restrict__ woa, const unsigned short* __restrict__ wob, char* __restrict__ ws, rsrc_t sws,
                       const char* pc0, unsigned co, int type, int rb, int ct, int x, int idx, int lw, int lane) {
  unsigned sv = 0; (void)sv;
  __builtin_amdgcn_s_setprio(3);
  u64 e0;
  const unsigned vl = lane * 16;
  {
    const bool ext = type >= 2;
    const unsigned ldw = ext ? 2048u : 6144u;
    const rsrc_t sx = mk_rsrc(X + (size_t)rb * 256 * D, 256u * 2048u);
    const rsrc_t sw = mk_rsrc(W, 1024u * ldw);
    const rsrc_t se = mk_rsrc(E, 1024u * 64u);
    unsigned xv[8], wv[4];
#pragma unroll
    for (int j = 0; j < 8; ++j) {
      const int row = 8 * (8 * lw + j) + (lane >> 3), c = (lane & 7) ^ ((row >> 1) & 7);
      xv[j] = row * 2048 + c * 16 - (j & 3) * 1024;
    }
#pragma unroll
    for (int j = 0; j < 4; ++j) {
      const int k = 4 * (4 * lw + j) + (lane >> 4), c = (lane & 15) ^ (((lane >> 4) & 3) << 2);
      const int col = (type == 0) ? 1024 + ((c >> 2) & 1) * 1024 + (2 * ct + (c >> 3)) * 32 + (c & 3) * 8 : ct * 128 + c * 8;
      wv[j] = k * ldw + col * 2 - j * 1024;
    }
    const int erow = 64 * (ct >> 1) + (lane & 31);
    const unsigned exa = (erow * 128 + (((lane >> 5) ^ ((erow >> 1) & 7)) << 4)) ^ (lw * 32);
    const unsigned ewa = P1_EOFF + (16 * lw + 8 * (lane >> 5) + ((lane & 15) >> 2)) * 64 + ((lane >> 4) & 1) * 32 + (lane & 3) * 8;
    f32x16 eacc[2] = {};
    bf16x8 ex[2], ew;
    auto kvr = [&](unsigned buf) {
      if (ext) { ex[0] = lds_r128(buf + exa); ew = lds_tr2(buf + ewa, buf + ewa + 256); ex[1] = lds_r128(buf + exa + 4096); }
    };
    auto kvm = [&]() {
      if (ext)
        asm volatile("s_cmp_eq_u32 %5, 2\n\ts_cbranch_scc1 1f\n\t"
                     "v_mfma_f32_32x32x16_bf16 %0, %2, %4, %0\n\tv_mfma_f32_32x32x16_bf16 %1, %3, %4, %1\n\ts_branch 2f\n"
                     "1:\tv_mfma_f32_32x32x16_bf16 %0, %4, %2, %0\n\tv_mfma_f32_32x32x16_bf16 %1, %4, %3, %1\n2:"
                     : "+v"(eacc[0]), "+v"(eacc[1]) : "v"(ex[0]), "v"(ex[1]), "v"(ew), "s"(type) : "scc");
    };
    auto kvs = [&](unsigned buf) { kvr(buf); kvm(); };
    auto stage = [&](int t0, unsigned buf, bool kv, unsigned kb) {
      if (kv && KVR == 0) kvr(kb);
      const int t = (t0 + (ext ? BROT : KROT)) & 15;
      const unsigned sox = t * 128, sow = t * 64 * ldw;
      set_m0(buf + P1_WOFF + lw * 4096);
      dma16<0>(wv[0], sw, sow); dma16<1024>(wv[1], sw, sow); dma16<2048>(wv[2], sw, sow); dma16<3072>(wv[3], sw, sow);
      if (kv && KVP == 0) kvm();
      if (kv && KVR == 1) kvr(kb);
      set_m0(buf + lw * 8192);
      dma16<0>(xv[0], sx, sox); dma16<1024>(xv[1], sx, sox); dma16<2048>(xv[2], sx, sox); dma16<3072>(xv[3], sx, sox);
      if (kv && KVP == 1) kvm();
      if (kv && KVR == 2) kvr(kb);
      set_m0(buf + lw * 8192 + 4096);
      dma16<0>(xv[4], sx, sox); dma16<1024>(xv[5], sx, sox); dma16<2048>(xv[6], sx, sox); dma16<3072>(xv[7], sx, sox);
      if (ext) { set_m0(buf + P1_EOFF + lw * 1024); dma16<0>(vl, se, t * 4096 + lw * 1024); }
      if (kv && KVP == 2) kvm();
    };
    stage(0, lds0, false, 0); stage(1, lds0 + P1_STAGE, false, 0);
    if (ext) WAIT_BAR(13); else WAIT_BAR(12);
    stage(2, lds0 + 2 * P1_STAGE, true, lds0);
    unsigned buf = lds0;
#pragma unroll 1
    for (int t = 0; t < 13; ++t) {
      if (ext) WAIT_BAR(13); else WAIT_BAR(12);
      const unsigned nb = (buf == lds0 + 2 * P1_STAGE) ? lds0 : buf + P1_STAGE;
      stage(t + 3, buf, true, nb);
      buf = nb;
    }
    if (ext) WAIT_BAR(13); else WAIT_BAR(12);
    e0 = __hip_atomic_load((AS1 u64*)(ws + WS_FLG) + F_EPOCH, __ATOMIC_RELAXED, __HIP_MEMORY_SCOPE_AGENT);
    if (lw == 3) pf_bin(lds0 + P1_BIN, co, pc0);
    if (!ext && (type == 1 || lw < 2)) {
      set_m0(lds0 + P1_PHI + lw * 2048);
      const unsigned so = ((type == 0 ? 2 * ct : 4 * ct) + lw) * 2048;
      dma16<0>(vl, se, so); dma16<1024>(vl, se, so);
    }
    kvs(lds0 + 2 * P1_STAGE);
    WAIT_BAR(0);
    kvs(lds0);
    asm volatile("s_nop 15\n\ts_nop 7" : "+v"(eacc[0]), "+v"(eacc[1]));
    const unsigned red = lds0 + P1_STAGE + vl;
    if (ext) {
#pragma unroll
      for (int i = 0; i < 8; ++i) {
        const f32x16& a = eacc[i >> 2];
        *(AS3 f32x4*)(red + lw * 8192 + i * 1024) = f32x4{a[4 * (i & 3)], a[4 * (i & 3) + 1], a[4 * (i & 3) + 2], a[4 * (i & 3) + 3]};
      }
    }
    WAIT_BAR(0);
    if (ext) {
#pragma unroll
      for (int n = 0; n < 2; ++n) {
        const unsigned a = red + n * 4096 + lw * 1024;
        const f32x4 sm = add4(add4(*(AS3 f32x4*)a, *(AS3 f32x4*)(a + 8192)), add4(*(AS3 f32x4*)(a + 16384), *(AS3 f32x4*)(a + 24576)));
        unsigned p = WS_KV + (rb * 8 + 2 * (ct >> 1) + n) * 4096 + (lw >> 1) * 1024;
        if (type == 3) p += 2048 + ((lane & 31) + 32 * (lw & 1)) * 16 + (lane >> 5) * 8;
        else           p += vl + (lw & 1) * 8;
        st8(sws, p, u32x2{pk2(sm[0], sm[1]), pk2(sm[2], sm[3])});
      }
    }
  }
  const u64 tag = ((u64)(unsigned)__builtin_amdgcn_readfirstlane((unsigned)(e0 >> 32)) << 32 | (unsigned)__builtin_amdgcn_readfirstlane((unsigned)e0))
                  * 0x5851F42D4C957F2Dull + 0x14057B7EF767814Full;
  const bool lead = (lw == 0) && (lane == 0);
  if (type >= 2) { WAIT_BAR(0); if (lead) post(ws, F_KV + rb * 16 + ct, tag); }
  auto chunk = [&](int c) {
    const unsigned l = lds0 + ((3 - c) & 3) * 32768 + lw * 8192, so = WS_KV + ((c + x) & 7) * 32768 + lw * 8192;
    dma4k(l, vl, sws, so); dma4k(l + 4096, vl, sws, so + 4096);
  };
  STAMP(512, 10);
  if (lw == 0) {
    poll(ws, F_KV + x * 16 + (lane & 7), tag);
    __hip_atomic_store((AS3 unsigned*)(lds0 + P1_GO), 1u, __ATOMIC_RELAXED, __HIP_MEMORY_SCOPE_WORKGROUP);
  } else {
    while (__hip_atomic_load((AS3 unsigned*)(lds0 + P1_GO), __ATOMIC_RELAXED, __HIP_MEMORY_SCOPE_WORKGROUP) == 0u) __builtin_amdgcn_s_sleep(1);
    asm volatile("" ::: "memory");
  }
  STAMP(512, 11);
  dma4k(lds0 + P2_Q + lw * 4096, vl, sws, WS_QB + ((8 * x + (idx >> 2)) * 32 + 8 * (idx & 3)) * 2048 + lw * 4096);
  chunk(0);
  WAIT_BAR(0);
  poll(ws, F_KV + (lane >> 3) * 16 + (lane & 7), tag);
  chunk(1);
  if (type == 0) {
#pragma unroll
    for (int j = 0; j < 4; ++j) {
      const unsigned a = lds0 + (lw >> 1) * 32768 + ((lw & 1) * 4 + j) * 1024 + vl;
      const f32x4 sm = add4(add4(*(AS3 f32x4*)a, *(AS3 f32x4*)(a + 8192)), add4(*(AS3 f32x4*)(a + 16384), *(AS3 f32x4*)(a + 24576)));
      st16(sws, WS_KVP + ((2 * ct + (lw >> 1)) * 8 + rb) * 8192 + ((lw & 1) * 4 + j) * 1024 + vl, sm);
    }
    if (lw < 2) {
      const unsigned a = lds0 + P1_ZRED + lw * 1024 + lane * 4;
      st4(sws, WS_ZP + (((2 * ct + lw) * 8 + rb) * 64 + lane) * 4, one(*(AS3 float*)a + *(AS3 float*)(a + 256)) + one(*(AS3 float*)(a + 512) + *(AS3 float*)(a + 768)));
    }
  }
  WAIT_BAR(0);
  chunk(2); chunk(3);
  if (lead && type < 2) post(ws, type == 0 ? F_K + ct * 8 + rb : F_Q + rb * 8 + ct, tag);
  const int br = !(x & 1), rb3 = 4 * (x >> 1) + (idx & 3), cb = idx >> 2;
  const unsigned short* wo = br ? wob : woa;
  unsigned pfs = 0;
  auto warm_wo = [&]() {
    pf_line(pfs, (const char*)wo + idx * 65536 + lw * 16384 + lane * 128);
    pf_line(pfs, (const char*)wo + idx * 65536 + lw * 16384 + 8192 + lane * 128);
  };
  if (lw == 1) *(AS3 unsigned*)(lds0 + P2_ZB + 128 + lane * 4) = lane < 32 ? 0x3f803f80u : 0u;
  WAIT_BAR(8);
  if (br) {
    const int head = 4 * x + (idx & 7), tp = idx >> 3;
    poll(ws, lane < 8 ? F_K + (head >> 1) * 8 + lane : F_Q + (2 * tp + (lane & 1)) * 8 + (head >> 2), tag);
    STAMP(512, 12);
    chunk(4);
    u32x4 kvp[2][8], fq[4][4];
    float zs[8];
    auto ldq = [&](int e) {
#pragma unroll
      for (int j = 0; j < 4; ++j) fq[e][j] = ld16(sws, WS_QA + ((16 * tp + 4 * lw + e) * 32 + head) * 4096 + j * 1024 + vl);
    };
#pragma unroll
    for (int e = 0; e < 2; ++e)
#pragma unroll
      for (int p = 0; p < 8; ++p) kvp[e][p] = ld16(sws, WS_KVP + ((head * 8 + p) * 512 + (2 * lw + e) * 64 + lane) * 16);
#pragma unroll
    for (int p = 0; p < 8; ++p) zs[p] = ld4(sws, WS_ZP + ((head * 8 + p) * 64 + lane) * 4);
    ldq(0); ldq(1);
    asm volatile("s_barrier" ::: "memory");
#pragma unroll
    for (int e = 0; e < 2; ++e) {
#pragma unroll
      for (int p = 0; p < 8; ++p) PIN(kvp[e][p]);
#define F4(v) __builtin_bit_cast(f32x4, v)
      const f32x4 sm = add4(add4(add4(F4(kvp[e][0]), F4(kvp[e][1])), add4(F4(kvp[e][2]), F4(kvp[e][3]))), add4(add4(F4(kvp[e][4]), F4(kvp[e][5])), add4(F4(kvp[e][6]), F4(kvp[e][7]))));
#undef F4
      *(AS3 u32x2*)(lds0 + P2_KVF + lw * 1024 + vl + e * 8) = u32x2{pk2(sm[0], sm[1]), pk2(sm[2], sm[3])};
    }
    {
#pragma unroll
      for (int p = 0; p < 8; ++p) PIN(zs[p]);
      const float z = one(one(zs[0] + zs[1]) + one(zs[2] + zs[3])) + one(one(zs[4] + zs[5]) + one(zs[6] + zs[7]));
      if (lw == 0) *(AS3 unsigned short*)(lds0 + P2_ZB + lane * 2) = (unsigned short)(pk2(z, z) & 0xffffu);
    }
    chunk(5);
    ldq(2); ldq(3);
    const int row = lane & 15, kg = lane >> 4;
    const bool on = (((row >> 2) ^ kg) & 1) == 0;
    const unsigned za = lds0 + P2_ZB + ((on && (row & 3) == 0) ? (kg >> 1) * 32 : (on && (row & 3) == 1) ? 128 : 256);
    const unsigned ka = lds0 + P2_KVF + vl;
    auto otile = [&](int e) {
#define BF(v) __builtin_bit_cast(bf16x8, v)
      f32x16 np = MFMA32(lds_r128(ka), BF(fq[e][0]), f32x16{});        f32x4 dp = MFMA16(lds_r128(za), BF(fq[e][0]), f32x4{});
      np = MFMA32(lds_r128(ka + 1024), BF(fq[e][1]), np);              dp = MFMA16(lds_r128(za + 16), BF(fq[e][1]), dp);
      f32x16 nm = MFMA32(lds_r128(ka + 2048), BF(fq[e][2]), f32x16{}); f32x4 dm = MFMA16(lds_r128(za + 64), BF(fq[e][2]), f32x4{});
      nm = MFMA32(lds_r128(ka + 3072), BF(fq[e][3]), nm);              dm = MFMA16(lds_r128(za + 80), BF(fq[e][3]), dm);
#undef BF
      const float a = __builtin_amdgcn_rcpf(dp[1]), b = __builtin_amdgcn_rcpf(dm[1]);
      const float r = __builtin_amdgcn_rcpf(one(dp[0] * a) + one(dm[0] * b) + 1e-6f), ar = one(a * r), br = one(b * r);
      f32x16 o;
#pragma unroll
      for (int i = 0; i < 16; ++i) o[i] = one(__builtin_fmaf(nm[i], br, one(np[i] * ar)));
      const unsigned p = WS_OA + ((16 * tp + 4 * lw + e) * 32 + head) * 2048 + vl;
      st16(sws, p, pack8<0>(o)); st16(sws, p + 1024, pack8<1>(o));
    };
    WAIT_BAR(16);
#pragma unroll
    for (int j = 0; j < 4; ++j) { PIN(fq[0][j]); PIN(fq[1][j]); }
    chunk(6);
    otile(0); otile(1);
    WAIT_BAR(12);
#pragma unroll
    for (int j = 0; j < 4; ++j) { PIN(fq[2][j]); PIN(fq[3][j]); }
    chunk(7);
    otile(2); otile(3);
    WAIT_BAR(0);
    if (lead) post(ws, F_OA + tp * 32 + head, tag);
    warm_wo();
  } else {
    chunk(4); warm_wo(); WAIT_BAR(10);
    chunk(5); WAIT_BAR(10);
    chunk(6); WAIT_BAR(8);
    chunk(7);
    if (blockIdx.x == 1 && lw == 1) {
      poll(ws, lane, tag); poll(ws, 64 + lane, tag); poll(ws, 128 + lane, tag);
      if (lane == 0) post(ws, F_EPOCH, tag);
    }
    WAIT_BAR(8);
  }
  const rsrc_t sw = mk_rsrc(wo, 1024u * 2048u);
  unsigned wv[4];
#pragma unroll
  for (int j = 0; j < 4; ++j) {
    const int k = 4 * (4 * lw + j) + (lane >> 4), c = (lane & 15) ^ (((lane >> 4) & 3) << 2);
    wv[j] = k * 2048 + (cb * 128 + c * 8) * 2 - j * 1024;
  }
  const unsigned osrc = (br ? WS_OB : WS_OA) + (rb3 * 4 + lw) * 65536;
  auto wst = [&](int t) {
    const unsigned sow = t * 64 * 2048;
    set_m0(lds0 + p3_wslot(t) + lw * 4096);
    dma16<0>(wv[0], sw, sow); dma16<1024>(wv[1], sw, sow); dma16<2048>(wv[2], sw, sow); dma16<3072>(wv[3], sw, sow);
  };
  auto ost = [&](int t) { dma4k(lds0 + P3_O + (t % NO) * 16384 + lw * 4096, vl, sws, osrc + t * 4096); };
  wst(0); wst(1); wst(2); wst(3);
  if (br) asm volatile("s_barrier" ::: "memory"); else WAIT_BAR(16);
  if (NW > 4) wst(4);
  if (NW > 5) wst(5);
  if (!br && lw == 0) poll(ws, F_OA + (rb3 >> 2) * 32 + (lane & 31), tag);
  asm volatile("s_barrier" ::: "memory");
  asm volatile("s_barrier" ::: "memory");
  asm volatile("s_barrier" ::: "memory");
  if (lead) post(ws, F_OB + x * 32 + idx, tag);
  STAMP(512, 13);
  if (br && lw == 0) poll(ws, F_OB + (rb3 >> 1) * 32 + (rb3 & 1) * 16 + (lane & 15), tag);
  asm volatile("s_barrier" ::: "memory");
  STAMP(512, 14);
#pragma unroll
  for (int t = 0; t < NF; ++t) ost(t);
  wait_bar<4 * (NF - 1)>();
#pragma unroll
  for (int t = NF; t < NO; ++t) ost(t);
  PIN(pfs);
#define RND(t) do { wait_bar<p3_inflight(t)>(); if (t + NW < 16) wst(t + NW); if (t + NO < 16) ost(t + NO); } while (0)
  RND(0); RND(1); RND(2); RND(3); RND(4); RND(5); RND(6); RND(7); RND(8); RND(9); RND(10); RND(11); RND(12); RND(13); RND(14);
#undef RND
}

__global__ __launch_bounds__(768, 3) void fused_fwd(const unsigned short* __restrict__ xa, const unsigned short* __restrict__ wqkv,
                                                    const unsigned short* __restrict__ wphi, const unsigned short* __restrict__ woa,
                                                    const unsigned short* __restrict__ xb, const unsigned short* __restrict__ wq,
                                                    const unsigned short* __restrict__ wk, const unsigned short* __restrict__ wv,
                                                    const unsigned short* __restrict__ wob, char* __restrict__ ws, unsigned short* __restrict__ out) {
  __shared__ __attribute__((aligned(1024))) char smem[LDS_BYTES];
  const unsigned lds0 = (unsigned)(uintptr_t)(AS3 char*)smem;
  const int tid = threadIdx.x, lane = tid & 63, w0 = __builtin_amdgcn_readfirstlane(tid >> 6), w = HF ? (w0 < 4 ? w0 + 8 : w0 - 4) : w0;
  const char* pc0; unsigned clen;
  asm volatile("s_getpc_b64 %0\n.Lfw_head:\n\ts_mov_b32 %1, .Lfw_tail-.Lfw_head" : "=s"(pc0), "=s"(clen));
  unsigned co = (((blockIdx.x >> 3) & 7) * 40 + lane) * 128;
  co = lane < 40 && co < clen ? co : 0u;
  if (CPF & 1) pf_bin(lds0 + P1_BIN, (tid >> 6) == 4 ? co : 0u, pc0);
  if (CPF & 4) asm volatile("s_cmp_lg_u32 %3, 4\n\ts_cbranch_scc1 1f\n\ts_mov_b32 m0, %0\n\ts_nop 0\n\tglobal_load_lds_dword %1, %2\n1:" ::"s"(lds0 + P1_BIN), "v"(co), "s"(pc0), "s"(w0) : "memory", "scc");
  SBAR();
  asm volatile("" : "+s"(xa), "+s"(wqkv), "+s"(wphi), "+s"(xb), "+s"(wq), "+s"(wk), "+s"(wv), "+s"(ws));
  const int b = blockIdx.x, x = b & 7, idx = b >> 3;
  const int rb = 4 * ((x >> 1) & 1) + (idx & 3);
  const int ct = idx < 16 ? 8 * (x >> 2) + 4 * (x & 1) + (idx >> 2) : 4 * (x >> 2) + ((idx >> 2) & 3);
  const int type = idx < 16 ? 0 : (x & 1) ? 1 : 2 + (ct & 1);
  const rsrc_t sws = mk_rsrc(ws, WS_END);
  unsigned sv = 0; (void)sv;
  STAMP(0, 0);
  if (HX(w >= 8)) {
    helper_program(lds0, type >= 2 ? xb : xa, type >= 2 ? wq : wqkv, type == 3 ? wv : type == 2 ? wk : wphi, woa, wob, ws, sws, pc0, co, type, rb, ct, x, idx, w - 8, lane);
    asm volatile("s_endpgm" ::: "memory");
  }
  if ((CPF & 8) && w == 2) pf_bin(lds0 + P1_BIN, co, pc0);
  if (type == 0 && !(x & 1) && w < 2) pf_bin(lds0 + P1_BIN, (idx * 32 + (lane & 31)) * 128, w ? wv : wk);
  if (type == 0)      p1_compute<0>(lds0, ws, sws, rb, ct, w, lane, !(x & 1) SVA);
  else if (type == 1) p1_compute<1>(lds0, ws, sws, rb, ct, w, lane, !(x & 1) SVA);
  else                p1_compute<2>(lds0, ws, sws, rb, ct, w, lane, !(x & 1) SVA);

  const int kh = w >> 2, r32 = lane & 31, h = lane >> 5;
  {
    const unsigned s0 = (lane & 15) == 0 ? 0x3f803f80u : 0u, s4 = (lane & 15) == 4 ? 0x3f803f80u : 0u;
    const bf16x8 sel = __builtin_bit_cast(bf16x8, u32x4{s0, s0, s4, s4});
    bf16x8 q[2][2];
    f32x16 O[2]; f32x4 L[2];
    unsigned tv = 0;
    attn_pass_fast(lds0, lds0 + P2_Q + (2 * (w & 3) + (kh ^ 1)) * 2048 + lane * 16, q, sel, kh, lane, O, L, tv SVA);
    TSOUT();
    STAMP(0, 5);
    const unsigned xs = lds0 + P2_XO + w * 4096 + lane * 16, xr = lds0 + P2_XO + (w ^ 4) * 4096 + lane * 16;
#pragma unroll
    for (int g = 0; g < 4; ++g) *(AS3 f32x4*)(xs + g * 1024) = f32x4{O[0][4 * g], O[0][4 * g + 1], O[0][4 * g + 2], O[0][4 * g + 3]};
    *(AS3 float*)(lds0 + P2_XL + w * 256 + lane * 4) = L[0][0];
    LDS_BAR();
#pragma unroll
    for (int g = 0; g < 4; ++g) {
      const f32x4 o = *(AS3 f32x4*)(xr + g * 1024);
      O[1][4 * g] += o[0]; O[1][4 * g + 1] += o[1]; O[1][4 * g + 2] += o[2]; O[1][4 * g + 3] += o[3];
    }
    L[1][0] += *(AS3 float*)(lds0 + P2_XL + (w ^ 4) * 256 + lane * 4);
    bool bad = !(L[1][0] < 1.8446744e19f);
#pragma unroll
    for (int i = 0; i < 16; ++i) bad |= !(fabsf(O[1][i]) < 3.0e38f);
    if (__any(bad)) attn_pass_safe(sws, q[1][0], q[1][1], sel, lane, O[1], L[1]);
    const float r = __builtin_amdgcn_rcpf(xhalf_sum(L[1][0]));
    O[1] *= r;
    const unsigned p = WS_OB + ((8 * x + (idx >> 2)) * 32 + 8 * (idx & 3) + 2 * (w & 3) + kh) * 2048 + lane * 16;
    st16(sws, p, pack8<0>(O[1])); st16(sws, p + 1024, pack8<1>(O[1]));
    WAIT_BAR(0);
    STAMP(0, 6);
    asm volatile("s_barrier" ::: "memory");
    SBAR();
  }
  const int br = !(x & 1), rb3 = 4 * (x >> 1) + (idx & 3), cb = idx >> 2;
  const int wm = w & 1, wn = (w >> 1) & 1;
  const int q4 = (lane & 15) >> 2, p4 = lane & 3, chalf = (lane >> 4) & 1;
  typedef const AS3 char* lp;
  lp oa[2], wa[2];
#pragma unroll
  for (int ms = 0; ms < 2; ++ms) oa[ms] = (lp)(lds0 + P3_O + ((wm * 8192 + kh * (4096 + 2048) + lane * 16) ^ (ms * 4096)));
#pragma unroll
  for (int ns = 0; ns < 2; ++ns)
    wa[ns] = (lp)(lds0 + P3_W + (kh * 32 + 4 * h + q4) * 256 + ((((wn * 2 + ns) ^ q4) << 2) | (chalf * 2 + (p4 >> 1))) * 16 + (p4 & 1) * 8);
  f32x16 acc[2][2] = {};
  bf16x8 xf[2][2], wfr[2][2];
  auto rdx = [&](int set, int t, int ks) {
#pragma unroll
    for (int ms = 0; ms < 2; ++ms) xf[set][ms] = *(const AS3 bf16x8*)(oa[ms] + (t % NO) * 16384 + ks * 1024);
  };
  auto rdw = [&](int set, int t, int ks, int ns) { wfr[set][ns] = lds_trp(wa[ns] + (p3_wslot(t) - P3_W) + ks * 4096, 2048); };
  auto mm1 = [&](int set, int ms, int ns) { acc[ms][ns] = MFMA32(wfr[set][ns], xf[set][ms], acc[ms][ns]); };
  auto sub = [&](int set, int t, int ks) {
    SBAR(); mm1(set, 0, 0); SBAR(); if (t >= 0) rdx(set ^ 1, t, ks);
    SBAR(); mm1(set, 1, 0); SBAR(); if (t >= 0) rdw(set ^ 1, t, ks, 0);
    SBAR(); mm1(set, 0, 1); SBAR(); if (t >= 0) rdw(set ^ 1, t, ks, 1);
    SBAR(); mm1(set, 1, 1); SBAR();
  };
  PIN(acc[0][0]); PIN(acc[0][1]); PIN(acc[1][0]); PIN(acc[1][1]); PIN(oa[0]); PIN(oa[1]); PIN(wa[0]); PIN(wa[1]);
  LDS_BAR();
  STAMP(0, 7);
  rdx(0, 0, 0); rdw(0, 0, 0, 0); rdw(0, 0, 0, 1);
  LGKM0();
#pragma unroll
  for (int t = 0; t < 15; ++t) { sub(0, t, 1); LDS_BAR(); sub(1, t + 1, 0); }
  sub(0, 15, 1);
  sub(1, -1, 0);
  STAMP(0, 8);
  constexpr int XB = NO == 5 ? 16384 : 65536, IB = NO == 5 ? 131072 : 0;
  static_assert(NO == 4 || NO == 5 || NO == 6, "");
  const unsigned xs = lds0 + XB + w * 8192 + lane * 16, xr = lds0 + XB + (w ^ 4) * 8192 + lane * 16;
#pragma unroll
  for (int ns = 0; ns < 2; ++ns)
#pragma unroll
    for (int g = 0; g < 4; ++g) {
      const f32x16& a = acc[1][ns];
      *(AS3 f32x4*)(xs + ns * 4096 + g * 1024) = f32x4{a[4 * g], a[4 * g + 1], a[4 * g + 2], a[4 * g + 3]};
    }
  LDS_BAR();
  const unsigned img = lds0 + IB + w * 4096;
#pragma unroll
  for (int ns = 0; ns < 2; ++ns)
#pragma unroll
    for (int g = 0; g < 4; ++g) {
      const f32x16& a = acc[0][ns];
      const f32x4 o = *(AS3 f32x4*)(xr + ns * 4096 + g * 1024);
      *(AS3 u32x2*)(img + r32 * 128 + (((ns * 4 + g) ^ (r32 & 7)) << 4) + h * 8) =
          u32x2{pk2(a[4 * g] + o[0], a[4 * g + 1] + o[1]), pk2(a[4 * g + 2] + o[2], a[4 * g + 3] + o[3])};
    }
  asm volatile("" ::: "memory");
  const int tok0 = rb3 * 128 + wm * 64 + kh * 32;
  const rsrc_t so = mk_rsrc(out, 2048u * 4096u);
  const unsigned op = tok0 * 4096 + br * 2048 + cb * 256 + wn * 128;
#pragma unroll
  for (int i = 0; i < 4; ++i) {
    const int row = 8 * i + (lane >> 3), ch = lane & 7;
    const u32x4 v = *(AS3 u32x4*)(img + row * 128 + ((ch ^ (row & 7)) << 4));
    st16(so, op + row * 4096 + ch * 16, v);
  }
  STAMP(0, 9);
  if (w == 0 && blockIdx.x == 200) {
    unsigned d = 10000 + (__builtin_amdgcn_readlane(sv, 2 * 0 + 1) - __builtin_amdgcn_readlane(sv, 2 * 0 + 1)) * 10;
    d = d < 100000 ? d : 100000;
    const unsigned t0 = __builtin_amdgcn_readlane(sv, 1);
    for (int it = 0; it < 1000000 && (unsigned)__builtin_amdgcn_s_memrealtime() - t0 < d; ++it) __builtin_amdgcn_s_sleep(1);
  }
  asm volatile(".Lfw_tail:");
}

extern "C" void kernel_launch(void* const* d_in, const int* in_sizes, int n_in, void* d_out, int out_size, void* d_ws, size_t ws_size,
                              hipStream_t stream) {
  (void)in_sizes; (void)n_in; (void)out_size; (void)ws_size;
  typedef const unsigned short* cp;
  fused_fwd<<<256, 768, 0, stream>>>((cp)d_in[0], (cp)d_in[1], (cp)d_in[2], (cp)d_in[3], (cp)d_in[4], (cp)d_in[5], (cp)d_in[6], (cp)d_in[7],
                                     (cp)d_in[8], (char*)d_ws, (unsigned short*)d_out);
}
```

```cpp
#define STAMPS
#include <hip/hip_runtime.h>
#include <hip/hip_bf16.h>
#include <stdint.h>
#pragma clang diagnostic ignored "-Wint-to-pointer-cast"

typedef __bf16   bf16x8 __attribute__((ext_vector_type(8)));
typedef __bf16   bf16x16 __attribute__((ext_vector_type(16)));
typedef __bf16   bf16x2 __attribute__((ext_vector_type(2)));
typedef short    s16x4  __attribute__((ext_vector_type(4)));
typedef short    s16x8  __attribute__((ext_vector_type(8)));
typedef float    f32x16 __attribute__((ext_vector_type(16)));
typedef float    f32x4  __attribute__((ext_vector_type(4)));
typedef float    f32x2  __attribute__((ext_vector_type(2)));
typedef unsigned u32x4  __attribute__((ext_vector_type(4)));
typedef unsigned u32x2  __attribute__((ext_vector_type(2)));
typedef unsigned long long u64;
typedef __amdgpu_buffer_rsrc_t rsrc_t;

#define AS1 __attribute__((address_space(1)))
#define AS3 __attribute__((address_space(3)))
#define DI __device__ __forceinline__
#define MFMA32(a, b, c) __builtin_amdgcn_mfma_f32_32x32x16_bf16((a), (b), (c), 0, 0, 0)
#define MFMA16(a, b, c) __builtin_amdgcn_mfma_f32_16x16x32_bf16((a), (b), (c), 0, 0, 0)
#define SM32(a, b, c) __builtin_amdgcn_smfmac_f32_32x32x32_bf16((a), (b), (c), odd, 0, 0)
#define SM16(a, b, c) __builtin_amdgcn_smfmac_f32_16x16x64_bf16((a), (b), (c), odd, 0, 0)
#define LO(S) __builtin_bit_cast(bf16x16, __builtin_shufflevector(S, S, 0, 1, 2, 3, 4, 5, 6, 7))
#define HI(S) __builtin_bit_cast(bf16x16, __builtin_shufflevector(S, S, 8, 9, 10, 11, 12, 13, 14, 15))
#define SBAR() __builtin_amdgcn_sched_barrier(0)
#define WAIT_BAR(N) asm volatile("s_waitcnt vmcnt(" #N ") lgkmcnt(0)\n\ts_barrier" ::: "memory")
#define LGKM0() __builtin_amdgcn_s_waitcnt(0xC07F)
#define LDS_BAR() do { asm volatile("" ::: "memory"); LGKM0(); asm volatile("s_barrier" ::: "memory"); } while (0)

#define STAMP(th, i) do { const unsigned c_ = (unsigned)__builtin_amdgcn_s_memtime(), r_ = (unsigned)__builtin_amdgcn_s_memrealtime(); \
    sv = (lane == 2 * (i)) ? c_ : (lane == 2 * (i) + 1) ? r_ : sv; asm volatile("" : "+v"(sv)); } while (0)
#define STOUT(lo, hi) do { if (lane >= 2 * (lo) && lane < 2 * (hi)) ((u64*)(ws + (64u << 20)))[(size_t)blockIdx.x * 32 + lane] = sv; } while (0)
#define SVP , unsigned& sv
#define SVA , sv
#define TS(i) do {} while (0)
#define TSOUT() do {} while (0)
#define HF 1
#define VT ((HF) ? (threadIdx.x + 512) % 768 : threadIdx.x)
#define KA 1
#define HX(c) __builtin_expect(c, 1)
#define EL 1
#define CPF 10
#define TPF 0
#define HE 1
#define EPRIO 0
#define KVP 2
#define KVR 2
#define THR 0
#define THO 0
#define TH3 0
#define NF 3
#define WSEND 1
#define ZREF 0
#define EPF 1
#define EMV 1
#define PFB 0
#define PF0 3
#define BROT (8 * (((x >> 1) ^ (x >> 2)) & 1))
#define NTX 0
#define NTW 0
#define KROT (2 * x)
#define MAPV 1
constexpr int D = 1024;
constexpr float LOG2E = 1.4426950408889634f;
constexpr float QSCALE = 0.17677669529663687f * LOG2E;

constexpr unsigned WS_QA  = 0;
constexpr unsigned WS_QB  = WS_QA + (8u << 20);
constexpr unsigned WS_OA  = WS_QB + (4u << 20);
constexpr unsigned WS_OB  = WS_OA + (4u << 20);
constexpr unsigned WS_KV  = WS_OB + (4u << 20);
constexpr unsigned WS_KVP = WS_KV + (256u << 10);
constexpr unsigned WS_ZP  = WS_KVP + (2u << 20);
constexpr unsigned WS_FLG = WS_ZP + (64u << 10);
constexpr unsigned WS_END = WS_FLG + (16u << 10);
constexpr int F_K = 0, F_Q = 128, F_OA = 256, F_OB = 512, F_KV = 768, F_EPOCH = 1024;

DI unsigned pk2(float lo, float hi) {
  f32x2 v = {lo, hi};
  return __builtin_bit_cast(unsigned, __builtin_convertvector(v, bf16x2));
}
template <int SS> DI bf16x8 pack8(const f32x16& x) {
  u32x4 p = {pk2(x[8 * SS + 0], x[8 * SS + 1]), pk2(x[8 * SS + 2], x[8 * SS + 3]), pk2(x[8 * SS + 4], x[8 * SS + 5]), pk2(x[8 * SS + 6], x[8 * SS + 7])};
  return __builtin_bit_cast(bf16x8, p);
}
DI bf16x8 lds_r128(unsigned a) { return *(const AS3 bf16x8*)a; }
DI bf16x8 lds_tr2(unsigned a0, unsigned a1) {
  s16x4 lo = __builtin_amdgcn_ds_read_tr16_b64_v4i16((AS3 s16x4*)a0);
  s16x4 hi = __builtin_amdgcn_ds_read_tr16_b64_v4i16((AS3 s16x4*)a1);
  s16x8 r = __builtin_shufflevector(lo, hi, 0, 1, 2, 3, 4, 5, 6, 7);
  return __builtin_bit_cast(bf16x8, r);
}
DI bf16x8 lds_trp(const AS3 char* p, int d) {
  s16x4 lo = __builtin_amdgcn_ds_read_tr16_b64_v4i16((AS3 s16x4*)p);
  s16x4 hi = __builtin_amdgcn_ds_read_tr16_b64_v4i16((AS3 s16x4*)(p + d));
  return __builtin_bit_cast(bf16x8, __builtin_shufflevector(lo, hi, 0, 1, 2, 3, 4, 5, 6, 7));
}
DI rsrc_t mk_rsrc(const void* p, unsigned bytes) { return __builtin_amdgcn_make_buffer_rsrc((void*)p, (short)0, (int)bytes, 0x00020000); }
DI void set_m0(unsigned v) { asm volatile("s_mov_b32 m0, %0\n\ts_nop 0" ::"s"(v) : "memory"); }
template <int IMM> DI void dma16(unsigned voff, rsrc_t srd, unsigned soff) {
  asm volatile("buffer_load_dwordx4 %0, %1, %2 offen offset:%3 lds" ::"v"(voff), "s"(srd), "s"(soff), "i"(IMM) : "memory");
}
template <int IMM> DI void dma16n(unsigned voff, rsrc_t srd, unsigned soff) {
  asm volatile("buffer_load_dwordx4 %0, %1, %2 offen offset:%3 nt lds" ::"v"(voff), "s"(srd), "s"(soff), "i"(IMM) : "memory");
}
#define DMAX dma16
#define DMAW dma16
template <int IMM> DI void dma16d(unsigned voff, rsrc_t srd, unsigned soff) {
  asm volatile("buffer_load_dwordx4 %0, %1, %2 offen offset:%3 sc1 lds" ::"v"(voff), "s"(srd), "s"(soff), "i"(IMM) : "memory");
}
DI void dma4k(unsigned lds, unsigned voff, rsrc_t srd, unsigned soff) {
  set_m0(lds); dma16d<0>(voff, srd, soff); dma16d<1024>(voff, srd, soff); dma16d<2048>(voff, srd, soff); dma16d<3072>(voff, srd, soff);
}
template <class T> DI void st16(rsrc_t r, unsigned voff, T v) { __builtin_amdgcn_raw_buffer_store_b128(__builtin_bit_cast(u32x4, v), r, (int)voff, 0, 16); }
DI void st8(rsrc_t r, unsigned voff, u32x2 v) { __builtin_amdgcn_raw_buffer_store_b64(v, r, (int)voff, 0, 16); }
DI void st4(rsrc_t r, unsigned voff, float v) { __builtin_amdgcn_raw_buffer_store_b32(__float_as_uint(v), r, (int)voff, 0, 16); }
DI u32x4 ld16(rsrc_t r, unsigned voff) { return __builtin_amdgcn_raw_buffer_load_b128(r, (int)voff, 0, 16); }
DI float ld4(rsrc_t r, unsigned voff) { return __uint_as_float(__builtin_amdgcn_raw_buffer_load_b32(r, (int)voff, 0, 16)); }
DI void post(char* ws, int f, u64 tag) { __hip_atomic_store((AS1 u64*)(ws + WS_FLG) + f, tag, __ATOMIC_RELAXED, __HIP_MEMORY_SCOPE_AGENT); }
DI void poll(char* ws, int f, u64 tag) {
#pragma unroll 1
  for (int it = 0; it < (1 << 17); ++it) {
    const u64 v = __hip_atomic_load((AS1 u64*)(ws + WS_FLG) + f, __ATOMIC_RELAXED, __HIP_MEMORY_SCOPE_AGENT);
    if (__all(v == tag)) break;
    __builtin_amdgcn_s_sleep(2);
  }
  asm volatile("" ::: "memory");
}
DI void pf_line(unsigned& sink, const void* p) { asm volatile("global_load_dword %0, %1, off" : "+v"(sink) : "v"(p) : "memory"); }
#define PIN(x) asm volatile("" : "+v"(x))
DI void pf_bin(unsigned bin, unsigned voff, const void* base) {
  asm volatile("s_mov_b32 m0, %0\n\ts_nop 0\n\tglobal_load_lds_dword %1, %2" ::"s"(bin), "v"(voff), "s"(base) : "memory");
}
DI float one(float v) { asm("" : "+v"(v)); return v; }
DI f32x4 add4(f32x4 a, f32x4 b) { return f32x4{one(a[0] + b[0]), one(a[1] + b[1]), one(a[2] + b[2]), one(a[3] + b[3])}; }
DI float xhalf_max(float v) {
  auto r = __builtin_amdgcn_permlane32_swap(__float_as_uint(v), __float_as_uint(v), false, false);
  return fmaxf(__uint_as_float(r[0]), __uint_as_float(r[1]));
}
DI float xhalf_min(float v) {
  auto r = __builtin_amdgcn_permlane32_swap(__float_as_uint(v), __float_as_uint(v), false, false);
  return fminf(__uint_as_float(r[0]), __uint_as_float(r[1]));
}
DI float xhalf_sum(float v) {
  auto r = __builtin_amdgcn_permlane32_swap(__float_as_uint(v), __float_as_uint(v), false, false);
  return __uint_as_float(r[0]) + __uint_as_float(r[1]);
}
DI float max16(const f32x16& x) {
  float a = fmaxf(fmaxf(x[0], x[1]), x[2]), b = fmaxf(fmaxf(x[3], x[4]), x[5]);
  a = fmaxf(fmaxf(a, x[6]), x[7]);   b = fmaxf(fmaxf(b, x[8]), x[9]);
  a = fmaxf(fmaxf(a, x[10]), x[11]); b = fmaxf(fmaxf(b, x[12]), x[13]);
  a = fmaxf(fmaxf(a, x[14]), x[15]);
  return fmaxf(a, b);
}
DI float min16(const f32x16& x) {
  float a = fminf(fminf(x[0], x[1]), x[2]), b = fminf(fminf(x[3], x[4]), x[5]);
  a = fminf(fminf(a, x[6]), x[7]);   b = fminf(fminf(b, x[8]), x[9]);
  a = fminf(fminf(a, x[10]), x[11]); b = fminf(fminf(b, x[12]), x[13]);
  a = fminf(fminf(a, x[14]), x[15]);
  return fminf(a, b);
}
template <bool NORM> DI void feat_map(const f32x16& p, f32x16& ep, f32x16& em) {
  const float mx = xhalf_max(max16(p)), mn = xhalf_min(min16(p));
  const f32x2 a = {-mx * LOG2E, -mx * LOG2E}, b = {mn * LOG2E, mn * LOG2E}, c = {LOG2E, LOG2E};
  f32x2 sp = {0.f, 0.f}, sm = {0.f, 0.f};
#pragma unroll
  for (int i = 0; i < 16; i += 2) {
    const f32x2 x = {p[i], p[i + 1]};
    const f32x2 u = __builtin_elementwise_fma(x, c, a), v = __builtin_elementwise_fma(x, -c, b);
    const f32x2 eu = {__builtin_amdgcn_exp2f(u[0]), __builtin_amdgcn_exp2f(u[1])}, ev = {__builtin_amdgcn_exp2f(v[0]), __builtin_amdgcn_exp2f(v[1])};
    ep[i] = eu[0]; ep[i + 1] = eu[1]; em[i] = ev[0]; em[i + 1] = ev[1];
    if (i == 0) { sp = eu; sm = ev; } else { sp += eu; sm += ev; }
  }
  if (NORM) {
    const float rp = __builtin_amdgcn_rcpf(xhalf_sum(sp[0] + sp[1])), rm = __builtin_amdgcn_rcpf(xhalf_sum(sm[0] + sm[1]));
    ep *= rp; em *= rm;
  }
}
DI bf16x8 row_sum_sel(int lane) {
  const unsigned u = ((lane & 11) == 0 && (((lane >> 2) ^ (lane >> 4)) & 1) == 0) ? 0x3f803f80u : 0u;
  return __builtin_bit_cast(bf16x8, u32x4{u, u, u, u});
}

constexpr int LDS_BYTES = 163840;
constexpr int P1_WOFF = 32768, P1_EOFF = 49152, P1_STAGE = 53248, P1_ZRED = 3 * P1_STAGE, P1_CNT = P1_ZRED + 2048, P1_GO = P1_CNT + 16, P1_BIN = P1_CNT + 256, P1_PHI = 65536;
constexpr int P2_Q = 131072, P2_KVF = 147456, P2_ZB = 151552;
constexpr int P2_XO = 32768, P2_XL = 0;
#define NO 4
constexpr int NW = 10 - NO;
constexpr int P3_O = 0, P3_W = NO * 16384;
DI constexpr int p3_wslot(int t) { return P3_W + ((t + NW - 4) % NW) * 16384; }
DI constexpr int p3_inflight(int t) {
  int n = 0, after = 0;
  for (int j = 0; j < NO; ++j) { n += 4; if (j == t + 1) after = n; }
  for (int u = 0; u < t; ++u) {
    if (u + NW < 16) { n += 4; if (u + NW == t + 1) after = n; }
    if (u + NO < 16) { n += 4; if (u + NO == t + 1) after = n; }
  }
  return n - after;
}
template <int N> DI void wait_bar() { asm volatile("s_waitcnt vmcnt(%0) lgkmcnt(0)\n\ts_barrier" ::"i"(N) : "memory"); }

template <int TYPE>
DI void p1_compute(unsigned lds0, char* __restrict__ ws, rsrc_t sws, int rb, int ct, int w, int lane, bool slow SVP) {
  const int wm = w & 3, wn = w >> 2, r32 = lane & 31, h = lane >> 5;
  if (w == 0 && lane == 0) *(AS3 unsigned*)(lds0 + P1_GO) = 0u;

  typedef const AS3 char* lp;
  const int xrow = wm * 64 + r32;
  const int q4 = (lane & 15) >> 2, p4 = lane & 3, chalf = (lane >> 4) & 1;
  lp xa[4], wa[2];
#pragma unroll
  for (int ks = 0; ks < 4; ++ks) xa[ks] = (lp)(lds0 + ((xrow * 128 + ((h ^ ((xrow >> 1) & 7)) << 4)) ^ (ks * 32)));
#pragma unroll
  for (int ns = 0; ns < 2; ++ns)
    wa[ns] = (lp)(lds0 + P1_WOFF + (8 * h + q4) * 256 + ((((wn * 2 + ns) ^ q4) << 2) | (chalf * 2 + (p4 >> 1))) * 16 + (p4 & 1) * 8);

  f32x16 acc[2][2] = {};
  bf16x8 xf[2][2], wfr[2][2];
  auto rdx = [&](int set, int b, int ks) {
#pragma unroll
    for (int ms = 0; ms < 2; ++ms) xf[set][ms] = *(const AS3 bf16x8*)(xa[ks] + b * P1_STAGE + ms * 4096);
  };
  auto rdw = [&](int set, int b, int ks, int ns) { wfr[set][ns] = lds_trp(wa[ns] + b * P1_STAGE + ks * 4096, 1024); };
  auto mm1 = [&](int set, int ms, int ns) {
    if (TYPE == 0 && ns == 1) acc[ms][1] = MFMA32(xf[set][ms], wfr[set][1], acc[ms][1]);
    else                      acc[ms][ns] = MFMA32(wfr[set][ns], xf[set][ms], acc[ms][ns]);
  };
  auto sub = [&](int set, int b, int ks) {
    SBAR(); mm1(set, 0, 0); SBAR(); if (b >= 0) rdx(set ^ 1, b, ks);
    SBAR(); mm1(set, 1, 0); SBAR(); if (b >= 0) rdw(set ^ 1, b, ks, 0);
    SBAR(); mm1(set, 0, 1); SBAR(); if (b >= 0) rdw(set ^ 1, b, ks, 1);
    SBAR(); mm1(set, 1, 1); SBAR();
  };
  auto stg = [&](int b, int nb) {
    sub(0, b, 1); sub(1, b, 2); sub(0, b, 3);
    LDS_BAR();
    if (THO && TYPE == 0) __builtin_amdgcn_s_sleep(THO);
    sub(1, nb, 0);
  };
  LDS_BAR();
  STAMP(0, 1);
  rdx(0, 0, 0); rdw(0, 0, 0, 0); rdw(0, 0, 0, 1);
  LGKM0();
#pragma unroll 1
  for (int t = 0; t < 5; ++t) {
    stg(0, 1); stg(1, 2); stg(2, 0);
    if (THR && TYPE == 0 && slow) __builtin_amdgcn_s_sleep(THR);
  }
  stg(0, -1);
  STAMP(0, 2);
  bf16x8 wf[2][2];
  if constexpr (TYPE <= 1) {
    const unsigned a = lds0 + P1_PHI + (TYPE == 0 ? wn : 2 * wn) * 2048 + (4 * h + q4) * 64 + chalf * 32 + p4 * 8;
#pragma unroll
    for (int n = 0; n <= TYPE; ++n)
#pragma unroll
      for (int s = 0; s < 2; ++s) wf[n][s] = lds_tr2(a + n * 2048 + s * 1024, a + n * 2048 + s * 1024 + 512);
  }

  if (TYPE >= 2) {
#pragma unroll
    for (int ms = 0; ms < 2; ++ms)
#pragma unroll
      for (int ns = 0; ns < 2; ++ns) {
        const f32x16 v = acc[ms][ns] * QSCALE;
        const unsigned p = WS_QB + ((rb * 8 + wm * 2 + ms) * 32 + ct * 4 + wn * 2 + ns) * 2048 + lane * 16;
        st16(sws, p, pack8<0>(v)); st16(sws, p + 1024, pack8<1>(v));
      }
    WAIT_BAR(0);
  } else if (TYPE == 1) {
    f32x16 pq[2][2];
#pragma unroll
    for (int ms = 0; ms < 2; ++ms)
#pragma unroll
      for (int ns = 0; ns < 2; ++ns) {
        pq[ms][ns] = MFMA32(wf[ns][0], pack8<0>(acc[ms][ns]), f32x16{});
        pq[ms][ns] = MFMA32(wf[ns][1], pack8<1>(acc[ms][ns]), pq[ms][ns]);
      }
#pragma unroll
    for (int ms = 0; ms < 2; ++ms)
#pragma unroll
      for (int ns = 0; ns < 2; ++ns) {
        f32x16 ep, em;
        feat_map<false>(pq[ms][ns], ep, em);
        const unsigned p = WS_QA + ((rb * 8 + wm * 2 + ms) * 32 + ct * 4 + wn * 2 + ns) * 4096 + lane * 16;
        st16(sws, p, pack8<0>(ep)); st16(sws, p + 1024, pack8<1>(ep)); st16(sws, p + 2048, pack8<0>(em)); st16(sws, p + 3072, pack8<1>(em));
      }
  } else {
    const unsigned img = lds0 + w * 8192;
    const bf16x8 sel = row_sum_sel(lane);
    f32x16 pk[2], kv[2] = {};
    f32x4 zz[2] = {};
    bf16x8 vf[2][2], af[2][2];
#pragma unroll
    for (int ms = 0; ms < 2; ++ms) {
      pk[ms] = MFMA32(wf[0][0], pack8<0>(acc[ms][0]), f32x16{});
      pk[ms] = MFMA32(wf[0][1], pack8<1>(acc[ms][0]), pk[ms]);
    }
#pragma unroll
    for (int ms = 0; ms < 2; ++ms) { vf[ms][0] = pack8<0>(acc[ms][1]); vf[ms][1] = pack8<1>(acc[ms][1]); PIN(vf[ms][0]); PIN(vf[ms][1]); }
    float off[4];
#pragma unroll
    for (int ms = 0; ms < 2; ++ms) { off[2 * ms] = -LOG2E * xhalf_max(max16(pk[ms])); off[2 * ms + 1] = LOG2E * xhalf_min(min16(pk[ms])); }
    const unsigned wr = img + 4096 + r32 * 64, sw = (r32 >> 1) & 7;
    const int t0 = 4 * h + q4, u8 = 4 * chalf + p4;
    const unsigned rd0 = img + 4096 + t0 * 64 + ((u8 ^ (t0 >> 1)) << 3), rd1 = img + 4096 + (t0 + 8) * 64 + ((u8 ^ ((t0 >> 1) + 4)) << 3);
    auto put = [&](int sg) {
#pragma unroll
      for (int g = 0; g < 4; ++g) *(AS3 f32x4*)(img + sg * 4096 + g * 1024 + lane * 16) = f32x4{kv[sg][4 * g], kv[sg][4 * g + 1], kv[sg][4 * g + 2], kv[sg][4 * g + 3]};
    };
    f32x16 e[2];
    float rs[2];
    auto mm = [&](int u, int j) {
      if (j & 1) zz[u & 1] = MFMA16(sel, af[u & 1][j >> 1], zz[u & 1]); else kv[u & 1] = MFMA32(af[u & 1][j >> 1], vf[u >> 1][j >> 1], kv[u & 1]);
    };
    auto exps = [&](int u, int mu) {
      const f32x16& p = pk[u >> 1];
      const float c = (u & 1) ? -LOG2E : LOG2E;
      f32x2 sm;
#pragma unroll
      for (int i = 0; i < 16; i += 2) {
        if (mu >= 0 && i >= 8) { SBAR(); mm(mu, (i - 8) >> 1); SBAR(); }
        const f32x2 v = __builtin_elementwise_fma(f32x2{p[i], p[i + 1]}, f32x2{c, c}, f32x2{off[u], off[u]});
        const f32x2 ev = {__builtin_amdgcn_exp2f(v[0]), __builtin_amdgcn_exp2f(v[1])};
        e[u & 1][i] = ev[0]; e[u & 1][i + 1] = ev[1];
        if (i == 0) sm = ev; else sm += ev;
      }
      rs[u & 1] = __builtin_amdgcn_rcpf(xhalf_sum(sm[0] + sm[1]));
    };
    auto rest = [&](int u) {
      const f32x16 n = e[u & 1] * rs[u & 1];
#pragma unroll
      for (int g = 0; g < 4; ++g) *(AS3 u32x2*)(wr + (u & 1) * 2048 + (((2 * g + h) ^ sw) << 3)) = u32x2{pk2(n[4 * g], n[4 * g + 1]), pk2(n[4 * g + 2], n[4 * g + 3])};
      asm volatile("" ::: "memory");
#pragma unroll
      for (int s = 0; s < 2; ++s) af[u & 1][s] = lds_tr2(rd0 + (u & 1) * 2048 + s * 1024, rd1 + (u & 1) * 2048 + s * 1024);
      asm volatile("" ::: "memory");
      SBAR();
    };
    exps(0, -1); exps(1, -1); rest(0);
    exps(2, 0);  rest(1);
    exps(3, 1);  rest(2);
    rest(3);
    mm(2, 0); mm(2, 2); mm(3, 0); mm(3, 2); SBAR(); mm(2, 1); mm(2, 3); mm(3, 1); mm(3, 3);
    put(0); put(1);
    if (lane < 32) {
      const unsigned a = lds0 + P1_ZRED + w * 256 + ((lane >> 2) & 1) * 64 + (lane >> 3) * 16 + (lane & 3) * 4;
      *(AS3 float*)a = zz[0][0]; *(AS3 float*)(a + 128) = zz[1][0];
    }
  }
  STAMP(0, 3);
}

DI void attn_pass_fast(unsigned lds0, unsigned qa, bf16x8 (&q)[2][2], bf16x8 sel, int kh, int lane, f32x16 (&O)[2], f32x4 (&L)[2], unsigned& tv SVP) {
  typedef const AS3 char* lp;
  lp l0 = (lp)(lds0 + lane * 16), la = l0 + kh * 16384;
#define RD(p) (*(const AS3 bf16x8*)(p))
  f32x16 negm[2], S0, S1 = {};
  bf16x8 vb = bf16x8{};
  int odd = 0xDDDD;
  O[0] = f32x16{}; O[1] = f32x16{}; L[0] = f32x4{}; L[1] = f32x4{};
  unsigned a0 = lds0 + lane * 16 + 98304, a1 = a0 + kh * 16384, qb = qa ^ 2048;
  PIN(O[0]); PIN(O[1]); PIN(L[0]); PIN(L[1]); PIN(S1); PIN(vb); PIN(odd); PIN(sel); PIN(a0); PIN(a1); PIN(qa); PIN(qb);
  LDS_BAR();
  STAMP(0, 4);
  TS(0);
  if (kh) __builtin_amdgcn_s_setprio(1);
  const bf16x8 ra = lds_r128(a0);
  q[0][0] = lds_r128(qa); q[1][0] = lds_r128(qb);
  const bf16x8 rb = lds_r128(a0 + 1024);
  q[0][1] = lds_r128(qa + 1024); q[1][1] = lds_r128(qb + 1024);
  bf16x8 ka = lds_r128(a1), kb = lds_r128(a1 + 1024), va = lds_r128(a1 + 2048);
  SBAR();
  negm[0] = MFMA32(ra, q[0][0], f32x16{}); negm[1] = MFMA32(ra, q[1][0], f32x16{});
  negm[0] = MFMA32(rb, q[0][1], negm[0]);  negm[1] = MFMA32(rb, q[1][1], negm[1]);
#pragma unroll
  for (int r = 0; r < 2; ++r) {
    const float m = xhalf_max(max16(negm[r]));
#pragma unroll
    for (int i = 0; i < 16; ++i) negm[r][i] = -m;
    PIN(negm[r]);
    SBAR();
    if (r == 0) { S0 = MFMA32(ka, q[0][0], negm[0]); S0 = MFMA32(kb, q[0][1], S0); PIN(S0); SBAR(); }
  }
#define EXN(SC, i, n) do { _Pragma("unroll") for (int k_ = 0; k_ < n; ++k_) SC[i + k_] = __builtin_amdgcn_exp2f(SC[i + k_]); PIN(SC); } while (0)
#define MM(D, E) do { SBAR(); D = E; PIN(D); SBAR(); } while (0)
#define TILE(TA, TN) do { \
    SBAR();                                                             EXN(S0, 0, 2);  \
    MM(O[1], SM32(vb, HI(S1), O[1]));       vb = RD((TA) + 3072);       EXN(S0, 2, 3);  \
    MM(L[1], SM16(sel, HI(S1), L[1]));                                  EXN(S0, 5, 2);  \
    MM(S1, MFMA32(ka, q[1][0], negm[1]));   ka = RD(TN);                EXN(S0, 7, 3);  \
    MM(S1, MFMA32(kb, q[1][1], S1));        kb = RD((TN) + 1024);       EXN(S0, 10, 3); \
    MM(O[0], SM32(va, LO(S0), O[0]));                                   EXN(S0, 13, 3); \
    MM(L[0], SM16(sel, LO(S0), L[0]));                                  EXN(S1, 0, 2);  \
    MM(O[0], SM32(vb, HI(S0), O[0]));                                   EXN(S1, 2, 3);  \
    MM(L[0], SM16(sel, HI(S0), L[0]));                                  EXN(S1, 5, 2);  \
    MM(S0, MFMA32(ka, q[0][0], negm[0]));                               EXN(S1, 7, 3);  \
    MM(S0, MFMA32(kb, q[0][1], S0));                                    EXN(S1, 10, 3); \
    MM(O[1], SM32(va, LO(S1), O[1]));       va = RD((TN) + 2048);       EXN(S1, 13, 3); \
    MM(L[1], SM16(sel, LO(S1), L[1])); } while (0)
  TS(1);
#pragma unroll 1
  for (int c = 0; c < 8; ++c) {
    const lp cb = la + ((3 - c) & 3) * 32768, nb = la + ((2 - c) & 3) * 32768;
    TILE(cb, cb + 4096);
    TILE(cb + 4096, cb + 8192);
    SBAR(); if (kh) __builtin_amdgcn_s_setprio(0);
    TILE(cb + 8192, cb + 12288);
    SBAR(); TS(2 + 2 * c);
    asm volatile("s_waitcnt vmcnt(0)\n\ts_barrier" ::: "memory"); TS(3 + 2 * c);
    if (kh) __builtin_amdgcn_s_setprio(1);
    TILE(cb + 12288, nb);
  }
  SBAR();
  O[1] = SM32(vb, HI(S1), O[1]); L[1] = SM16(sel, HI(S1), L[1]);
  __builtin_amdgcn_s_setprio(0);
  TS(18);
#undef TILE
#undef EXN
#undef MM
#undef RD
}

DI void attn_pass_safe(rsrc_t sws, const bf16x8 q0, const bf16x8 q1, const bf16x8 sel, int lane, f32x16& O, f32x4& L) {
  O = f32x16{}; L = f32x4{};
  float m = -INFINITY;
  const int odd = 0xDDDD;
#pragma unroll 1
  for (int t = 0; t < 64; ++t) {
    bf16x8 a[4];
#pragma unroll
    for (int j = 0; j < 4; ++j) a[j] = __builtin_bit_cast(bf16x8, ld16(sws, WS_KV + t * 4096 + j * 1024 + lane * 16));
    f32x16 Sc = {};
    Sc = MFMA32(a[0], q0, Sc); Sc = MFMA32(a[1], q1, Sc);
    const float mn = fmaxf(m, xhalf_max(max16(Sc)));
    const float al = __builtin_amdgcn_exp2f(m - mn);
    m = mn;
#pragma unroll
    for (int r = 0; r < 16; ++r) { O[r] *= al; Sc[r] = __builtin_amdgcn_exp2f(Sc[r] - mn); }
    L *= al;
    O = SM32(a[2], LO(Sc), O); L = SM16(sel, LO(Sc), L);
    O = SM32(a[3], HI(Sc), O); L = SM16(sel, HI(Sc), L);
  }
}

DI void helper_program(unsigned lds0, const unsigned short* __restrict__ X, const unsigned short* __restrict__ W, const unsigned short* __restrict__ E,
                       const unsigned short* __restrict__ woa, const unsigned short* __restrict__ wob, char* __restrict__ ws, rsrc_t sws,
                       const char* pc0, unsigned co, int type, int rb, int ct, int x, int idx, int lw, int lane) {
  unsigned sv = 0; (void)sv;
  __builtin_amdgcn_s_setprio(3);
  u64 e0;
  const unsigned vl = lane * 16;
  {
    const bool ext = type >= 2;
    const unsigned ldw = ext ? 2048u : 6144u;
    const rsrc_t sx = mk_rsrc(X + (size_t)rb * 256 * D, 256u * 2048u);
    const rsrc_t sw = mk_rsrc(W, 1024u * ldw);
    const rsrc_t se = mk_rsrc(E, 1024u * 64u);
    unsigned xv[8], wv[4];
#pragma unroll
    for (int j = 0; j < 8; ++j) {
      const int row = 8 * (8 * lw + j) + (lane >> 3), c = (lane & 7) ^ ((row >> 1) & 7);
      xv[j] = row * 2048 + c * 16 - (j & 3) * 1024;
    }
#pragma unroll
    for (int j = 0; j < 4; ++j) {
      const int k = 4 * (4 * lw + j) + (lane >> 4), c = (lane & 15) ^ (((lane >> 4) & 3) << 2);
      const int col = (type == 0) ? 1024 + ((c >> 2) & 1) * 1024 + (2 * ct + (c >> 3)) * 32 + (c & 3) * 8 : ct * 128 + c * 8;
      wv[j] = k * ldw + col * 2 - j * 1024;
    }
    const int erow = 64 * (ct >> 1) + (lane & 31);
    const unsigned exa = (erow * 128 + (((lane >> 5) ^ ((erow >> 1) & 7)) << 4)) ^ (lw * 32);
    const unsigned ewa = P1_EOFF + (16 * lw + 8 * (lane >> 5) + ((lane & 15) >> 2)) * 64 + ((lane >> 4) & 1) * 32 + (lane & 3) * 8;
    f32x16 eacc[2] = {};
    bf16x8 ex[2], ew;
    auto kvr = [&](unsigned buf) {
      if (ext) { ex[0] = lds_r128(buf + exa); ew = lds_tr2(buf + ewa, buf + ewa + 256); ex[1] = lds_r128(buf + exa + 4096); }
    };
    auto kvm = [&]() {
      if (ext)
        asm volatile("s_cmp_eq_u32 %5, 2\n\ts_cbranch_scc1 1f\n\t"
                     "v_mfma_f32_32x32x16_bf16 %0, %2, %4, %0\n\tv_mfma_f32_32x32x16_bf16 %1, %3, %4, %1\n\ts_branch 2f\n"
                     "1:\tv_mfma_f32_32x32x16_bf16 %0, %4, %2, %0\n\tv_mfma_f32_32x32x16_bf16 %1, %4, %3, %1\n2:"
                     : "+v"(eacc[0]), "+v"(eacc[1]) : "v"(ex[0]), "v"(ex[1]), "v"(ew), "s"(type) : "scc");
    };
    auto kvs = [&](unsigned buf) { kvr(buf); kvm(); };
    auto stage = [&](int t0, unsigned buf, bool kv, unsigned kb) {
      if (kv && KVR == 0) kvr(kb);
      const int t = (t0 + (ext ? BROT : KROT)) & 15;
      const unsigned sox = t * 128, sow = t * 64 * ldw;
      set_m0(buf + P1_WOFF + lw * 4096);
      dma16<0>(wv[0], sw, sow); dma16<1024>(wv[1], sw, sow); dma16<2048>(wv[2], sw, sow); dma16<3072>(wv[3], sw, sow);
      if (kv && KVP == 0) kvm();
      if (kv && KVR == 1) kvr(kb);
      set_m0(buf + lw * 8192);
      dma16<0>(xv[0], sx, sox); dma16<1024>(xv[1], sx, sox); dma16<2048>(xv[2], sx, sox); dma16<3072>(xv[3], sx, sox);
      if (kv && KVP == 1) kvm();
      if (kv && KVR == 2) kvr(kb);
      set_m0(buf + lw * 8192 + 4096);
      dma16<0>(xv[4], sx, sox); dma16<1024>(xv[5], sx, sox); dma16<2048>(xv[6], sx, sox); dma16<3072>(xv[7], sx, sox);
      if (ext) { set_m0(buf + P1_EOFF + lw * 1024); dma16<0>(vl, se, t * 4096 + lw * 1024); }
      if (kv && KVP == 2) kvm();
    };
    stage(0, lds0, false, 0); stage(1, lds0 + P1_STAGE, false, 0);
    if (ext) WAIT_BAR(13); else WAIT_BAR(12);
    stage(2, lds0 + 2 * P1_STAGE, true, lds0);
    unsigned buf = lds0;
#pragma unroll 1
    for (int t = 0; t < 13; ++t) {
      if (ext) WAIT_BAR(13); else WAIT_BAR(12);
      const unsigned nb = (buf == lds0 + 2 * P1_STAGE) ? lds0 : buf + P1_STAGE;
      stage(t + 3, buf, true, nb);
      buf = nb;
    }
    if (ext) WAIT_BAR(13); else WAIT_BAR(12);
    e0 = __hip_atomic_load((AS1 u64*)(ws + WS_FLG) + F_EPOCH, __ATOMIC_RELAXED, __HIP_MEMORY_SCOPE_AGENT);
    if (lw == 3) pf_bin(lds0 + P1_BIN, co, pc0);
    if (!ext && (type == 1 || lw < 2)) {
      set_m0(lds0 + P1_PHI + lw * 2048);
      const unsigned so = ((type == 0 ? 2 * ct : 4 * ct) + lw) * 2048;
      dma16<0>(vl, se, so); dma16<1024>(vl, se, so);
    }
    kvs(lds0 + 2 * P1_STAGE);
    WAIT_BAR(0);
    kvs(lds0);
    asm volatile("s_nop 15\n\ts_nop 7" : "+v"(eacc[0]), "+v"(eacc[1]));
    const unsigned red = lds0 + P1_STAGE + vl;
    if (ext) {
#pragma unroll
      for (int i = 0; i < 8; ++i) {
        const f32x16& a = eacc[i >> 2];
        *(AS3 f32x4*)(red + lw * 8192 + i * 1024) = f32x4{a[4 * (i & 3)], a[4 * (i & 3) + 1], a[4 * (i & 3) + 2], a[4 * (i & 3) + 3]};
      }
    }
    WAIT_BAR(0);
    if (ext) {
#pragma unroll
      for (int n = 0; n < 2; ++n) {
        const unsigned a = red + n * 4096 + lw * 1024;
        const f32x4 sm = add4(add4(*(AS3 f32x4*)a, *(AS3 f32x4*)(a + 8192)), add4(*(AS3 f32x4*)(a + 16384), *(AS3 f32x4*)(a + 24576)));
        unsigned p = WS_KV + (rb * 8 + 2 * (ct >> 1) + n) * 4096 + (lw >> 1) * 1024;
        if (type == 3) p += 2048 + ((lane & 31) + 32 * (lw & 1)) * 16 + (lane >> 5) * 8;
        else           p += vl + (lw & 1) * 8;
        st8(sws, p, u32x2{pk2(sm[0], sm[1]), pk2(sm[2], sm[3])});
      }
    }
  }
  const u64 tag = ((u64)(unsigned)__builtin_amdgcn_readfirstlane((unsigned)(e0 >> 32)) << 32 | (unsigned)__builtin_amdgcn_readfirstlane((unsigned)e0))
                  * 0x5851F42D4C957F2Dull + 0x14057B7EF767814Full;
  const bool lead = (lw == 0) && (lane == 0);
  if (type >= 2) { WAIT_BAR(0); if (lead) post(ws, F_KV + rb * 16 + ct, tag); }
  auto chunk = [&](int c) {
    const unsigned l = lds0 + ((3 - c) & 3) * 32768 + lw * 8192, so = WS_KV + ((c + x) & 7) * 32768 + lw * 8192;
    dma4k(l, vl, sws, so); dma4k(l + 4096, vl, sws, so + 4096);
  };
  STAMP(512, 10);
  if (lw == 0) {
    poll(ws, F_KV + x * 16 + (lane & 7), tag);
    __hip_atomic_store((AS3 unsigned*)(lds0 + P1_GO), 1u, __ATOMIC_RELAXED, __HIP_MEMORY_SCOPE_WORKGROUP);
  } else {
    while (__hip_atomic_load((AS3 unsigned*)(lds0 + P1_GO), __ATOMIC_RELAXED, __HIP_MEMORY_SCOPE_WORKGROUP) == 0u) __builtin_amdgcn_s_sleep(1);
    asm volatile("" ::: "memory");
  }
  STAMP(512, 11);
  dma4k(lds0 + P2_Q + lw * 4096, vl, sws, WS_QB + ((8 * x + (idx >> 2)) * 32 + 8 * (idx & 3)) * 2048 + lw * 4096);
  chunk(0);
  WAIT_BAR(0);
  poll(ws, F_KV + (lane >> 3) * 16 + (lane & 7), tag);
  chunk(1);
  if (type == 0) {
#pragma unroll
    for (int j = 0; j < 4; ++j) {
      const unsigned a = lds0 + (lw >> 1) * 32768 + ((lw & 1) * 4 + j) * 1024 + vl;
      const f32x4 sm = add4(add4(*(AS3 f32x4*)a, *(AS3 f32x4*)(a + 8192)), add4(*(AS3 f32x4*)(a + 16384), *(AS3 f32x4*)(a + 24576)));
      st16(sws, WS_KVP + ((2 * ct + (lw >> 1)) * 8 + rb) * 8192 + ((lw & 1) * 4 + j) * 1024 + vl, sm);
    }
    if (lw < 2) {
      const unsigned a = lds0 + P1_ZRED + lw * 1024 + lane * 4;
      st4(sws, WS_ZP + (((2 * ct + lw) * 8 + rb) * 64 + lane) * 4, one(*(AS3 float*)a + *(AS3 float*)(a + 256)) + one(*(AS3 float*)(a + 512) + *(AS3 float*)(a + 768)));
    }
  }
  WAIT_BAR(0);
  chunk(2); chunk(3);
  if (lead && type < 2) post(ws, type == 0 ? F_K + ct * 8 + rb : F_Q + rb * 8 + ct, tag);
  const int br = !(x & 1), rb3 = 4 * (x >> 1) + (idx & 3), cb = idx >> 2;
  const unsigned short* wo = br ? wob : woa;
  unsigned pfs = 0;
  auto warm_wo = [&]() {
    pf_line(pfs, (const char*)wo + idx * 65536 + lw * 16384 + lane * 128);
    pf_line(pfs, (const char*)wo + idx * 65536 + lw * 16384 + 8192 + lane * 128);
  };
  if (lw == 1) *(AS3 unsigned*)(lds0 + P2_ZB + 128 + lane * 4) = lane < 32 ? 0x3f803f80u : 0u;
  WAIT_BAR(8);
  if (br) {
    const int head = 4 * x + (idx & 7), tp = idx >> 3;
    poll(ws, lane < 8 ? F_K + (head >> 1) * 8 + lane : F_Q + (2 * tp + (lane & 1)) * 8 + (head >> 2), tag);
    STAMP(512, 12);
    chunk(4);
    u32x4 kvp[2][8], fq[4][4];
    float zs[8];
    auto ldq = [&](int e) {
#pragma unroll
      for (int j = 0; j < 4; ++j) fq[e][j] = ld16(sws, WS_QA + ((16 * tp + 4 * lw + e) * 32 + head) * 4096 + j * 1024 + vl);
    };
#pragma unroll
    for (int e = 0; e < 2; ++e)
#pragma unroll
      for (int p = 0; p < 8; ++p) kvp[e][p] = ld16(sws, WS_KVP + ((head * 8 + p) * 512 + (2 * lw + e) * 64 + lane) * 16);
#pragma unroll
    for (int p = 0; p < 8; ++p) zs[p] = ld4(sws, WS_ZP + ((head * 8 + p) * 64 + lane) * 4);
    ldq(0); ldq(1);
    asm volatile("s_barrier" ::: "memory");
#pragma unroll
    for (int e = 0; e < 2; ++e) {
#pragma unroll
      for (int p = 0; p < 8; ++p) PIN(kvp[e][p]);
#define F4(v) __builtin_bit_cast(f32x4, v)
      const f32x4 sm = add4(add4(add4(F4(kvp[e][0]), F4(kvp[e][1])), add4(F4(kvp[e][2]), F4(kvp[e][3]))), add4(add4(F4(kvp[e][4]), F4(kvp[e][5])), add4(F4(kvp[e][6]), F4(kvp[e][7]))));
#undef F4
      *(AS3 u32x2*)(lds0 + P2_KVF + lw * 1024 + vl + e * 8) = u32x2{pk2(sm[0], sm[1]), pk2(sm[2], sm[3])};
    }
    {
#pragma unroll
      for (int p = 0; p < 8; ++p) PIN(zs[p]);
      const float z = one(one(zs[0] + zs[1]) + one(zs[2] + zs[3])) + one(one(zs[4] + zs[5]) + one(zs[6] + zs[7]));
      if (lw == 0) *(AS3 unsigned short*)(lds0 + P2_ZB + lane * 2) = (unsigned short)(pk2(z, z) & 0xffffu);
    }
    chunk(5);
    ldq(2); ldq(3);
    const int row = lane & 15, kg = lane >> 4;
    const bool on = (((row >> 2) ^ kg) & 1) == 0;
    const unsigned za = lds0 + P2_ZB + ((on && (row & 3) == 0) ? (kg >> 1) * 32 : (on && (row & 3) == 1) ? 128 : 256);
    const unsigned ka = lds0 + P2_KVF + vl;
    auto otile = [&](int e) {
#define BF(v) __builtin_bit_cast(bf16x8, v)
      f32x16 np = MFMA32(lds_r128(ka), BF(fq[e][0]), f32x16{});        f32x4 dp = MFMA16(lds_r128(za), BF(fq[e][0]), f32x4{});
      np = MFMA32(lds_r128(ka + 1024), BF(fq[e][1]), np);              dp = MFMA16(lds_r128(za + 16), BF(fq[e][1]), dp);
      f32x16 nm = MFMA32(lds_r128(ka + 2048), BF(fq[e][2]), f32x16{}); f32x4 dm = MFMA16(lds_r128(za + 64), BF(fq[e][2]), f32x4{});
      nm = MFMA32(lds_r128(ka + 3072), BF(fq[e][3]), nm);              dm = MFMA16(lds_r128(za + 80), BF(fq[e][3]), dm);
#undef BF
      const float a = __builtin_amdgcn_rcpf(dp[1]), b = __builtin_amdgcn_rcpf(dm[1]);
      const float r = __builtin_amdgcn_rcpf(one(dp[0] * a) + one(dm[0] * b) + 1e-6f), ar = one(a * r), br = one(b * r);
      f32x16 o;
#pragma unroll
      for (int i = 0; i < 16; ++i) o[i] = one(__builtin_fmaf(nm[i], br, one(np[i] * ar)));
      const unsigned p = WS_OA + ((16 * tp + 4 * lw + e) * 32 + head) * 2048 + vl;
      st16(sws, p, pack8<0>(o)); st16(sws, p + 1024, pack8<1>(o));
    };
    WAIT_BAR(16);
#pragma unroll
    for (int j = 0; j < 4; ++j) { PIN(fq[0][j]); PIN(fq[1][j]); }
    chunk(6);
    otile(0); otile(1);
    WAIT_BAR(12);
#pragma unroll
    for (int j = 0; j < 4; ++j) { PIN(fq[2][j]); PIN(fq[3][j]); }
    chunk(7);
    otile(2); otile(3);
    WAIT_BAR(0);
    if (lead) post(ws, F_OA + tp * 32 + head, tag);
    warm_wo();
  } else {
    chunk(4); warm_wo(); WAIT_BAR(10);
    chunk(5); WAIT_BAR(10);
    chunk(6); WAIT_BAR(8);
    chunk(7);
    if (blockIdx.x == 1 && lw == 1) {
      poll(ws, lane, tag); poll(ws, 64 + lane, tag); poll(ws, 128 + lane, tag);
      if (lane == 0) post(ws, F_EPOCH, tag);
    }
    WAIT_BAR(8);
  }
  const rsrc_t sw = mk_rsrc(wo, 1024u * 2048u);
  unsigned wv[4];
#pragma unroll
  for (int j = 0; j < 4; ++j) {
    const int k = 4 * (4 * lw + j) + (lane >> 4), c = (lane & 15) ^ (((lane >> 4) & 3) << 2);
    wv[j] = k * 2048 + (cb * 128 + c * 8) * 2 - j * 1024;
  }
  const unsigned osrc = (br ? WS_OB : WS_OA) + (rb3 * 4 + lw) * 65536;
  auto wst = [&](int t) {
    const unsigned sow = t * 64 * 2048;
    set_m0(lds0 + p3_wslot(t) + lw * 4096);
    dma16<0>(wv[0], sw, sow); dma16<1024>(wv[1], sw, sow); dma16<2048>(wv[2], sw, sow); dma16<3072>(wv[3], sw, sow);
  };
  auto ost = [&](int t) { dma4k(lds0 + P3_O + (t % NO) * 16384 + lw * 4096, vl, sws, osrc + t * 4096); };
  wst(0); wst(1); wst(2); wst(3);
  if (br) asm volatile("s_barrier" ::: "memory"); else WAIT_BAR(16);
  if (NW > 4) wst(4);
  if (NW > 5) wst(5);
  if (!br && lw == 0) poll(ws, F_OA + (rb3 >> 2) * 32 + (lane & 31), tag);
  asm volatile("s_barrier" ::: "memory");
  asm volatile("s_barrier" ::: "memory");
  asm volatile("s_barrier" ::: "memory");
  if (lead) post(ws, F_OB + x * 32 + idx, tag);
  STAMP(512, 13);
  if (br && lw == 0) poll(ws, F_OB + (rb3 >> 1) * 32 + (rb3 & 1) * 16 + (lane & 15), tag);
  asm volatile("s_barrier" ::: "memory");
  STAMP(512, 14);
#pragma unroll
  for (int t = 0; t < NF; ++t) ost(t);
  wait_bar<4 * (NF - 1)>();
#pragma unroll
  for (int t = NF; t < NO; ++t) ost(t);
  PIN(pfs);
#define RND(t) do { wait_bar<p3_inflight(t)>(); if (t + NW < 16) wst(t + NW); if (t + NO < 16) ost(t + NO); } while (0)
  RND(0); RND(1); RND(2); RND(3); RND(4); RND(5); RND(6); RND(7); RND(8); RND(9); RND(10); RND(11); RND(12); RND(13); RND(14);
#undef RND
}

__global__ __launch_bounds__(768, 3) void fused_fwd(const unsigned short* __restrict__ xa, const unsigned short* __restrict__ wqkv,
                                                    const unsigned short* __restrict__ wphi, const unsigned short* __restrict__ woa,
                                                    const unsigned short* __restrict__ xb, const unsigned short* __restrict__ wq,
                                                    const unsigned short* __restrict__ wk, const unsigned short* __restrict__ wv,
                                                    const unsigned short* __restrict__ wob, char* __restrict__ ws, unsigned short* __restrict__ out) {
  __shared__ __attribute__((aligned(1024))) char smem[LDS_BYTES];
  const unsigned lds0 = (unsigned)(uintptr_t)(AS3 char*)smem;
  const int tid = threadIdx.x, lane = tid & 63, w0 = __builtin_amdgcn_readfirstlane(tid >> 6), w = HF ? (w0 < 4 ? w0 + 8 : w0 - 4) : w0;
  const char* pc0; unsigned clen;
  asm volatile("s_getpc_b64 %0\n.Lfw_head:\n\ts_mov_b32 %1, .Lfw_tail-.Lfw_head" : "=s"(pc0), "=s"(clen));
  unsigned co = (((blockIdx.x >> 3) & 7) * 40 + lane) * 128;
  co = lane < 40 && co < clen ? co : 0u;
  if (CPF & 1) pf_bin(lds0 + P1_BIN, (tid >> 6) == 4 ? co : 0u, pc0);
  if (CPF & 4) asm volatile("s_cmp_lg_u32 %3, 4\n\ts_cbranch_scc1 1f\n\ts_mov_b32 m0, %0\n\ts_nop 0\n\tglobal_load_lds_dword %1, %2\n1:" ::"s"(lds0 + P1_BIN), "v"(co), "s"(pc0), "s"(w0) : "memory", "scc");
  SBAR();
  asm volatile("" : "+s"(xa), "+s"(wqkv), "+s"(wphi), "+s"(xb), "+s"(wq), "+s"(wk), "+s"(wv), "+s"(ws));
  const int b = blockIdx.x, x = b & 7, idx = b >> 3;
  const int rb = 4 * ((x >> 1) & 1) + (idx & 3);
  const int ct = idx < 16 ? 8 * (x >> 2) + 4 * (x & 1) + (idx >> 2) : 4 * (x >> 2) + ((idx >> 2) & 3);
  const int type = idx < 16 ? 0 : (x & 1) ? 1 : 2 + (ct & 1);
  const rsrc_t sws = mk_rsrc(ws, WS_END);
  unsigned sv = 0; (void)sv;
  STAMP(0, 0);
  if (HX(w >= 8)) {
    helper_program(lds0, type >= 2 ? xb : xa, type >= 2 ? wq : wqkv, type == 3 ? wv : type == 2 ? wk : wphi, woa, wob, ws, sws, pc0, co, type, rb, ct, x, idx, w - 8, lane);
    asm volatile("s_endpgm" ::: "memory");
  }
  if ((CPF & 8) && w == 2) pf_bin(lds0 + P1_BIN, co, pc0);
  if (type == 0 && !(x & 1) && w < 2) pf_bin(lds0 + P1_BIN, (idx * 32 + (lane & 31)) * 128, w ? wv : wk);
  if (PFB && type < 2 && w >= 3 && w < 6) {
    constexpr int NS = 8 - PF0, PER = (NS * 512 + 47) / 48;
    const int j = (x & 1) * 16 + idx, id = (w == 5 ? j : 2 * j + w - 3) * PER + min(lane, PER - 1);
    unsigned o;
    if (w == 5) { const int st = min(id >> 9, NS - 1), r = id & 511;  o = (64 * ((PF0 + st + BROT) & 15) + (r >> 3)) * 2048 + (x >> 2) * 1024 + (r & 7) * 128; }
    else        { const int st = min(id >> 10, NS - 1), r = id & 1023; o = (((x >> 1) & 1) * 1024 + r) * 2048 + ((PF0 + st + BROT) & 15) * 128; }
    pf_bin(lds0 + P1_BIN, o, w == 5 ? wq : xb);
  }
  if (type == 0)      p1_compute<0>(lds0, ws, sws, rb, ct, w, lane, !(x & 1) SVA);
  else if (type == 1) p1_compute<1>(lds0, ws, sws, rb, ct, w, lane, !(x & 1) SVA);
  else                p1_compute<2>(lds0, ws, sws, rb, ct, w, lane, !(x & 1) SVA);

  const int kh = w >> 2, r32 = lane & 31, h = lane >> 5;
  {
    const unsigned s0 = (lane & 15) == 0 ? 0x3f803f80u : 0u, s4 = (lane & 15) == 4 ? 0x3f803f80u : 0u;
    const bf16x8 sel = __builtin_bit_cast(bf16x8, u32x4{s0, s0, s4, s4});
    bf16x8 q[2][2];
    f32x16 O[2]; f32x4 L[2];
    unsigned tv = 0;
    attn_pass_fast(lds0, lds0 + P2_Q + (2 * (w & 3) + (kh ^ 1)) * 2048 + lane * 16, q, sel, kh, lane, O, L, tv SVA);
    TSOUT();
    STAMP(0, 5);
    const unsigned xs = lds0 + P2_XO + w * 4096 + lane * 16, xr = lds0 + P2_XO + (w ^ 4) * 4096 + lane * 16;
#pragma unroll
    for (int g = 0; g < 4; ++g) *(AS3 f32x4*)(xs + g * 1024) = f32x4{O[0][4 * g], O[0][4 * g + 1], O[0][4 * g + 2], O[0][4 * g + 3]};
    *(AS3 float*)(lds0 + P2_XL + w * 256 + lane * 4) = L[0][0];
    LDS_BAR();
#pragma unroll
    for (int g = 0; g < 4; ++g) {
      const f32x4 o = *(AS3 f32x4*)(xr + g * 1024);
      O[1][4 * g] += o[0]; O[1][4 * g + 1] += o[1]; O[1][4 * g + 2] += o[2]; O[1][4 * g + 3] += o[3];
    }
    L[1][0] += *(AS3 float*)(lds0 + P2_XL + (w ^ 4) * 256 + lane * 4);
    bool bad = !(L[1][0] < 1.8446744e19f);
#pragma unroll
    for (int i = 0; i < 16; ++i) bad |= !(fabsf(O[1][i]) < 3.0e38f);
    if (__any(bad)) attn_pass_safe(sws, q[1][0], q[1][1], sel, lane, O[1], L[1]);
    const float r = __builtin_amdgcn_rcpf(xhalf_sum(L[1][0]));
    O[1] *= r;
    const unsigned p = WS_OB + ((8 * x + (idx >> 2)) * 32 + 8 * (idx & 3) + 2 * (w & 3) + kh) * 2048 + lane * 16;
    st16(sws, p, pack8<0>(O[1])); st16(sws, p + 1024, pack8<1>(O[1]));
    WAIT_BAR(0);
    STAMP(0, 6);
    asm volatile("s_barrier" ::: "memory");
    SBAR();
  }
  const int br = !(x & 1), rb3 = 4 * (x >> 1) + (idx & 3), cb = idx >> 2;
  const int wm = w & 1, wn = (w >> 1) & 1;
  const int q4 = (lane & 15) >> 2, p4 = lane & 3, chalf = (lane >> 4) & 1;
  typedef const AS3 char* lp;
  lp oa[2], wa[2];
#pragma unroll
  for (int ms = 0; ms < 2; ++ms) oa[ms] = (lp)(lds0 + P3_O + ((wm * 8192 + kh * (4096 + 2048) + lane * 16) ^ (ms * 4096)));
#pragma unroll
  for (int ns = 0; ns < 2; ++ns)
    wa[ns] = (lp)(lds0 + P3_W + (kh * 32 + 4 * h + q4) * 256 + ((((wn * 2 + ns) ^ q4) << 2) | (chalf * 2 + (p4 >> 1))) * 16 + (p4 & 1) * 8);
  f32x16 acc[2][2] = {};
  bf16x8 xf[2][2], wfr[2][2];
  auto rdx = [&](int set, int t, int ks) {
#pragma unroll
    for (int ms = 0; ms < 2; ++ms) xf[set][ms] = *(const AS3 bf16x8*)(oa[ms] + (t % NO) * 16384 + ks * 1024);
  };
  auto rdw = [&](int set, int t, int ks, int ns) { wfr[set][ns] = lds_trp(wa[ns] + (p3_wslot(t) - P3_W) + ks * 4096, 2048); };
  auto mm1 = [&](int set, int ms, int ns) { acc[ms][ns] = MFMA32(wfr[set][ns], xf[set][ms], acc[ms][ns]); };
  auto sub = [&](int set, int t, int ks) {
    SBAR(); mm1(set, 0, 0); SBAR(); if (t >= 0) rdx(set ^ 1, t, ks);
    SBAR(); mm1(set, 1, 0); SBAR(); if (t >= 0) rdw(set ^ 1, t, ks, 0);
    SBAR(); mm1(set, 0, 1); SBAR(); if (t >= 0) rdw(set ^ 1, t, ks, 1);
    SBAR(); mm1(set, 1, 1); SBAR();
  };
  PIN(acc[0][0]); PIN(acc[0][1]); PIN(acc[1][0]); PIN(acc[1][1]); PIN(oa[0]); PIN(oa[1]); PIN(wa[0]); PIN(wa[1]);
  LDS_BAR();
  STAMP(0, 7);
  rdx(0, 0, 0); rdw(0, 0, 0, 0); rdw(0, 0, 0, 1);
  LGKM0();
#pragma unroll
  for (int t = 0; t < 15; ++t) { sub(0, t, 1); LDS_BAR(); sub(1, t + 1, 0); }
  sub(0, 15, 1);
  sub(1, -1, 0);
  STAMP(0, 8);
  constexpr int XB = NO == 5 ? 16384 : 65536, IB = NO == 5 ? 131072 : 0;
  static_assert(NO == 4 || NO == 5 || NO == 6, "");
  const unsigned xs = lds0 + XB + w * 8192 + lane * 16, xr = lds0 + XB + (w ^ 4) * 8192 + lane * 16;
#pragma unroll
  for (int ns = 0; ns < 2; ++ns)
#pragma unroll
    for (int g = 0; g < 4; ++g) {
      const f32x16& a = acc[1][ns];
      *(AS3 f32x4*)(xs + ns * 4096 + g * 1024) = f32x4{a[4 * g], a[4 * g + 1], a[4 * g + 2], a[4 * g + 3]};
    }
  LDS_BAR();
  const unsigned img = lds0 + IB + w * 4096;
#pragma unroll
  for (int ns = 0; ns < 2; ++ns)
#pragma unroll
    for (int g = 0; g < 4; ++g) {
      const f32x16& a = acc[0][ns];
      const f32x4 o = *(AS3 f32x4*)(xr + ns * 4096 + g * 1024);
      *(AS3 u32x2*)(img + r32 * 128 + (((ns * 4 + g) ^ (r32 & 7)) << 4) + h * 8) =
          u32x2{pk2(a[4 * g] + o[0], a[4 * g + 1] + o[1]), pk2(a[4 * g + 2] + o[2], a[4 * g + 3] + o[3])};
    }
  asm volatile("" ::: "memory");
  const int tok0 = rb3 * 128 + wm * 64 + kh * 32;
  const rsrc_t so = mk_rsrc(out, 2048u * 4096u);
  const unsigned op = tok0 * 4096 + br * 2048 + cb * 256 + wn * 128;
#pragma unroll
  for (int i = 0; i < 4; ++i) {
    const int row = 8 * i + (lane >> 3), ch = lane & 7;
    const u32x4 v = *(AS3 u32x4*)(img + row * 128 + ((ch ^ (row & 7)) << 4));
    st16(so, op + row * 4096 + ch * 16, v);
  }
  STAMP(0, 9);
  if (w == 0 && blockIdx.x == 200) {
    unsigned d = 10000 + (__builtin_amdgcn_readlane(sv, 2 * 3 + 1) - __builtin_amdgcn_readlane(sv, 2 * 2 + 1)) * 10;
    d = d < 100000 ? d : 100000;
    const unsigned t0 = __builtin_amdgcn_readlane(sv, 1);
    for (int it = 0; it < 1000000 && (unsigned)__builtin_amdgcn_s_memrealtime() - t0 < d; ++it) __builtin_amdgcn_s_sleep(1);
  }
  asm volatile(".Lfw_tail:");
}

extern "C" void kernel_launch(void* const* d_in, const int* in_sizes, int n_in, void* d_out, int out_size, void* d_ws, size_t ws_size,
                              hipStream_t stream) {
  (void)in_sizes; (void)n_in; (void)out_size; (void)ws_size;
  typedef const unsigned short* cp;
  fused_fwd<<<256, 768, 0, stream>>>((cp)d_in[0], (cp)d_in[1], (cp)d_in[2], (cp)d_in[3], (cp)d_in[4], (cp)d_in[5], (cp)d_in[6], (cp)d_in[7],
                                     (cp)d_in[8], (char*)d_ws + (WSEND ? ((ws_size - WS_END) & ~(size_t)0x1fffff) : 0), (unsigned short*)d_out);
}
```

```cpp
#include <hip/hip_runtime.h>
#include <hip/hip_bf16.h>
#include <stdint.h>
#pragma clang diagnostic ignored "-Wint-to-pointer-cast"

typedef __bf16   bf16x8 __attribute__((ext_vector_type(8)));
typedef __bf16   bf16x16 __attribute__((ext_vector_type(16)));
typedef __bf16   bf16x2 __attribute__((ext_vector_type(2)));
typedef short    s16x4  __attribute__((ext_vector_type(4)));
typedef short    s16x8  __attribute__((ext_vector_type(8)));
typedef float    f32x16 __attribute__((ext_vector_type(16)));
typedef float    f32x4  __attribute__((ext_vector_type(4)));
typedef float    f32x2  __attribute__((ext_vector_type(2)));
typedef unsigned u32x4  __attribute__((ext_vector_type(4)));
typedef unsigned u32x2  __attribute__((ext_vector_type(2)));
typedef unsigned long long u64;
typedef __amdgpu_buffer_rsrc_t rsrc_t;

#define AS1 __attribute__((address_space(1)))
#define AS3 __attribute__((address_space(3)))
#define DI __device__ __forceinline__
#define MFMA32(a, b, c) __builtin_amdgcn_mfma_f32_32x32x16_bf16((a), (b), (c), 0, 0, 0)
#define MFMA16(a, b, c) __builtin_amdgcn_mfma_f32_16x16x32_bf16((a), (b), (c), 0, 0, 0)
#define SM32(a, b, c) __builtin_amdgcn_smfmac_f32_32x32x32_bf16((a), (b), (c), odd, 0, 0)
#define SM16(a, b, c) __builtin_amdgcn_smfmac_f32_16x16x64_bf16((a), (b), (c), odd, 0, 0)
#define LO(S) __builtin_bit_cast(bf16x16, __builtin_shufflevector(S, S, 0, 1, 2, 3, 4, 5, 6, 7))
#define HI(S) __builtin_bit_cast(bf16x16, __builtin_shufflevector(S, S, 8, 9, 10, 11, 12, 13, 14, 15))
#define SBAR() __builtin_amdgcn_sched_barrier(0)
#define WAIT_BAR(N) asm volatile("s_waitcnt vmcnt(" #N ") lgkmcnt(0)\n\ts_barrier" ::: "memory")
#define LGKM0() __builtin_amdgcn_s_waitcnt(0xC07F)
#define LDS_BAR() do { asm volatile("" ::: "memory"); LGKM0(); asm volatile("s_barrier" ::: "memory"); } while (0)

#define STAMP(th, i) do {} while (0)
#define STOUT(lo, hi) do {} while (0)
#define SVP
#define SVA
#define TS(i) do {} while (0)
#define TSOUT() do {} while (0)
#define HF 1
#define VT ((HF) ? (threadIdx.x + 512) % 768 : threadIdx.x)
#define KA 1
#define HX(c) __builtin_expect(c, 1)
#define EL 1
#define CPF 10
#define TPF 0
#define HE 1
#define EPRIO 0
#define KVP 2
#define KVR 2
#define THR 0
#define THO 0
#define TH3 0
#define NF 3
#define WSEND 0
#define ZREF 0
#define EPF 1
#define EMV 1
#define PFB 1
#define PF0 3
#define BROT (8 * (((x >> 1) ^ (x >> 2)) & 1))
#define NTX 0
#define NTW 0
#define KROT (2 * x)
#define MAPV 1
constexpr int D = 1024;
constexpr float LOG2E = 1.4426950408889634f;
constexpr float QSCALE = 0.17677669529663687f * LOG2E;

constexpr unsigned WS_QA  = 0;
constexpr unsigned WS_QB  = WS_QA + (8u << 20);
constexpr unsigned WS_OA  = WS_QB + (4u << 20);
constexpr unsigned WS_OB  = WS_OA + (4u << 20);
constexpr unsigned WS_KV  = WS_OB + (4u << 20);
constexpr unsigned WS_KVP = WS_KV + (256u << 10);
constexpr unsigned WS_ZP  = WS_KVP + (2u << 20);
constexpr unsigned WS_FLG = WS_ZP + (64u << 10);
constexpr unsigned WS_END = WS_FLG + (16u << 10);
constexpr int F_K = 0, F_Q = 128, F_OA = 256, F_OB = 512, F_KV = 768, F_EPOCH = 1024;

DI unsigned pk2(float lo, float hi) {
  f32x2 v = {lo, hi};
  return __builtin_bit_cast(unsigned, __builtin_convertvector(v, bf16x2));
}
template <int SS> DI bf16x8 pack8(const f32x16& x) {
  u32x4 p = {pk2(x[8 * SS + 0], x[8 * SS + 1]), pk2(x[8 * SS + 2], x[8 * SS + 3]), pk2(x[8 * SS + 4], x[8 * SS + 5]), pk2(x[8 * SS + 6], x[8 * SS + 7])};
  return __builtin_bit_cast(bf16x8, p);
}
DI bf16x8 lds_r128(unsigned a) { return *(const AS3 bf16x8*)a; }
DI bf16x8 lds_tr2(unsigned a0, unsigned a1) {
  s16x4 lo = __builtin_amdgcn_ds_read_tr16_b64_v4i16((AS3 s16x4*)a0);
  s16x4 hi = __builtin_amdgcn_ds_read_tr16_b64_v4i16((AS3 s16x4*)a1);
  s16x8 r = __builtin_shufflevector(lo, hi, 0, 1, 2, 3, 4, 5, 6, 7);
  return __builtin_bit_cast(bf16x8, r);
}
DI bf16x8 lds_trp(const AS3 char* p, int d) {
  s16x4 lo = __builtin_amdgcn_ds_read_tr16_b64_v4i16((AS3 s16x4*)p);
  s16x4 hi = __builtin_amdgcn_ds_read_tr16_b64_v4i16((AS3 s16x4*)(p + d));
  return __builtin_bit_cast(bf16x8, __builtin_shufflevector(lo, hi, 0, 1, 2, 3, 4, 5, 6, 7));
}
DI rsrc_t mk_rsrc(const void* p, unsigned bytes) { return __builtin_amdgcn_make_buffer_rsrc((void*)p, (short)0, (int)bytes, 0x00020000); }
DI void set_m0(unsigned v) { asm volatile("s_mov_b32 m0, %0\n\ts_nop 0" ::"s"(v) : "memory"); }
template <int IMM> DI void dma16(unsigned voff, rsrc_t srd, unsigned soff) {
  asm volatile("buffer_load_dwordx4 %0, %1, %2 offen offset:%3 lds" ::"v"(voff), "s"(srd), "s"(soff), "i"(IMM) : "memory");
}
template <int IMM> DI void dma16n(unsigned voff, rsrc_t srd, unsigned soff) {
  asm volatile("buffer_load_dwordx4 %0, %1, %2 offen offset:%3 nt lds" ::"v"(voff), "s"(srd), "s"(soff), "i"(IMM) : "memory");
}
#define DMAX dma16
#define DMAW dma16
template <int IMM> DI void dma16d(unsigned voff, rsrc_t srd, unsigned soff) {
  asm volatile("buffer_load_dwordx4 %0, %1, %2 offen offset:%3 sc1 lds" ::"v"(voff), "s"(srd), "s"(soff), "i"(IMM) : "memory");
}
DI void dma4k(unsigned lds, unsigned voff, rsrc_t srd, unsigned soff) {
  set_m0(lds); dma16d<0>(voff, srd, soff); dma16d<1024>(voff, srd, soff); dma16d<2048>(voff, srd, soff); dma16d<3072>(voff, srd, soff);
}
template <class T> DI void st16(rsrc_t r, unsigned voff, T v) { __builtin_amdgcn_raw_buffer_store_b128(__builtin_bit_cast(u32x4, v), r, (int)voff, 0, 16); }
DI void st8(rsrc_t r, unsigned voff, u32x2 v) { __builtin_amdgcn_raw_buffer_store_b64(v, r, (int)voff, 0, 16); }
DI void st4(rsrc_t r, unsigned voff, float v) { __builtin_amdgcn_raw_buffer_store_b32(__float_as_uint(v), r, (int)voff, 0, 16); }
DI u32x4 ld16(rsrc_t r, unsigned voff) { return __builtin_amdgcn_raw_buffer_load_b128(r, (int)voff, 0, 16); }
DI float ld4(rsrc_t r, unsigned voff) { return __uint_as_float(__builtin_amdgcn_raw_buffer_load_b32(r, (int)voff, 0, 16)); }
DI void post(char* ws, int f, u64 tag) { __hip_atomic_store((AS1 u64*)(ws + WS_FLG) + f, tag, __ATOMIC_RELAXED, __HIP_MEMORY_SCOPE_AGENT); }
DI void poll(char* ws, int f, u64 tag) {
#pragma unroll 1
  for (int it = 0; it < (1 << 17); ++it) {
    const u64 v = __hip_atomic_load((AS1 u64*)(ws + WS_FLG) + f, __ATOMIC_RELAXED, __HIP_MEMORY_SCOPE_AGENT);
    if (__all(v == tag)) break;
    __builtin_amdgcn_s_sleep(2);
  }
  asm volatile("" ::: "memory");
}
DI void pf_line(unsigned& sink, const void* p) { asm volatile("global_load_dword %0, %1, off" : "+v"(sink) : "v"(p) : "memory"); }
#define PIN(x) asm volatile("" : "+v"(x))
DI void pf_bin(unsigned bin, unsigned voff, const void* base) {
  asm volatile("s_mov_b32 m0, %0\n\ts_nop 0\n\tglobal_load_lds_dword %1, %2" ::"s"(bin), "v"(voff), "s"(base) : "memory");
}
DI float one(float v) { asm("" : "+v"(v)); return v; }
DI f32x4 add4(f32x4 a, f32x4 b) { return f32x4{one(a[0] + b[0]), one(a[1] + b[1]), one(a[2] + b[2]), one(a[3] + b[3])}; }
DI float xhalf_max(float v) {
  auto r = __builtin_amdgcn_permlane32_swap(__float_as_uint(v), __float_as_uint(v), false, false);
  return fmaxf(__uint_as_float(r[0]), __uint_as_float(r[1]));
}
DI float xhalf_min(float v) {
  auto r = __builtin_amdgcn_permlane32_swap(__float_as_uint(v), __float_as_uint(v), false, false);
  return fminf(__uint_as_float(r[0]), __uint_as_float(r[1]));
}
DI float xhalf_sum(float v) {
  auto r = __builtin_amdgcn_permlane32_swap(__float_as_uint(v), __float_as_uint(v), false, false);
  return __uint_as_float(r[0]) + __uint_as_float(r[1]);
}
DI float max16(const f32x16& x) {
  float a = fmaxf(fmaxf(x[0], x[1]), x[2]), b = fmaxf(fmaxf(x[3], x[4]), x[5]);
  a = fmaxf(fmaxf(a, x[6]), x[7]);   b = fmaxf(fmaxf(b, x[8]), x[9]);
  a = fmaxf(fmaxf(a, x[10]), x[11]); b = fmaxf(fmaxf(b, x[12]), x[13]);
  a = fmaxf(fmaxf(a, x[14]), x[15]);
  return fmaxf(a, b);
}
DI float min16(const f32x16& x) {
  float a = fminf(fminf(x[0], x[1]), x[2]), b = fminf(fminf(x[3], x[4]), x[5]);
  a = fminf(fminf(a, x[6]), x[7]);   b = fminf(fminf(b, x[8]), x[9]);
  a = fminf(fminf(a, x[10]), x[11]); b = fminf(fminf(b, x[12]), x[13]);
  a = fminf(fminf(a, x[14]), x[15]);
  return fminf(a, b);
}
template <bool NORM> DI void feat_map(const f32x16& p, f32x16& ep, f32x16& em) {
  const float mx = xhalf_max(max16(p)), mn = xhalf_min(min16(p));
  const f32x2 a = {-mx * LOG2E, -mx * LOG2E}, b = {mn * LOG2E, mn * LOG2E}, c = {LOG2E, LOG2E};
  f32x2 sp = {0.f, 0.f}, sm = {0.f, 0.f};
#pragma unroll
  for (int i = 0; i < 16; i += 2) {
    const f32x2 x = {p[i], p[i + 1]};
    const f32x2 u = __builtin_elementwise_fma(x, c, a), v = __builtin_elementwise_fma(x, -c, b);
    const f32x2 eu = {__builtin_amdgcn_exp2f(u[0]), __builtin_amdgcn_exp2f(u[1])}, ev = {__builtin_amdgcn_exp2f(v[0]), __builtin_amdgcn_exp2f(v[1])};
    ep[i] = eu[0]; ep[i + 1] = eu[1]; em[i] = ev[0]; em[i + 1] = ev[1];
    if (i == 0) { sp = eu; sm = ev; } else { sp += eu; sm += ev; }
  }
  if (NORM) {
    const float rp = __builtin_amdgcn_rcpf(xhalf_sum(sp[0] + sp[1])), rm = __builtin_amdgcn_rcpf(xhalf_sum(sm[0] + sm[1]));
    ep *= rp; em *= rm;
  }
}
DI bf16x8 row_sum_sel(int lane) {
  const unsigned u = ((lane & 11) == 0 && (((lane >> 2) ^ (lane >> 4)) & 1) == 0) ? 0x3f803f80u : 0u;
  return __builtin_bit_cast(bf16x8, u32x4{u, u, u, u});
}

constexpr int LDS_BYTES = 163840;
constexpr int P1_WOFF = 32768, P1_EOFF = 49152, P1_STAGE = 53248, P1_ZRED = 3 * P1_STAGE, P1_CNT = P1_ZRED + 2048, P1_GO = P1_CNT + 16, P1_BIN = P1_CNT + 256, P1_PHI = 65536;
constexpr int P2_Q = 131072, P2_KVF = 147456, P2_ZB = 151552;
constexpr int P2_XO = 32768, P2_XL = 0;
#define NO 4
constexpr int NW = 10 - NO;
constexpr int P3_O = 0, P3_W = NO * 16384;
DI constexpr int p3_wslot(int t) { return P3_W + ((t + NW - 4) % NW) * 16384; }
DI constexpr int p3_inflight(int t) {
  int n = 0, after = 0;
  for (int j = 0; j < NO; ++j) { n += 4; if (j == t + 1) after = n; }
  for (int u = 0; u < t; ++u) {
    if (u + NW < 16) { n += 4; if (u + NW == t + 1) after = n; }
    if (u + NO < 16) { n += 4; if (u + NO == t + 1) after = n; }
  }
  return n - after;
}
template <int N> DI void wait_bar() { asm volatile("s_waitcnt vmcnt(%0) lgkmcnt(0)\n\ts_barrier" ::"i"(N) : "memory"); }

template <int TYPE>
DI void p1_compute(unsigned lds0, char* __restrict__ ws, rsrc_t sws, int rb, int ct, int w, int lane, bool slow SVP) {
  const int wm = w & 3, wn = w >> 2, r32 = lane & 31, h = lane >> 5;
  if (w == 0 && lane == 0) *(AS3 unsigned*)(lds0 + P1_GO) = 0u;

  typedef const AS3 char* lp;
  const int xrow = wm * 64 + r32;
  const int q4 = (lane & 15) >> 2, p4 = lane & 3, chalf = (lane >> 4) & 1;
  lp xa[4], wa[2];
#pragma unroll
  for (int ks = 0; ks < 4; ++ks) xa[ks] = (lp)(lds0 + ((xrow * 128 + ((h ^ ((xrow >> 1) & 7)) << 4)) ^ (ks * 32)));
#pragma unroll
  for (int ns = 0; ns < 2; ++ns)
    wa[ns] = (lp)(lds0 + P1_WOFF + (8 * h + q4) * 256 + ((((wn * 2 + ns) ^ q4) << 2) | (chalf * 2 + (p4 >> 1))) * 16 + (p4 & 1) * 8);

  f32x16 acc[2][2] = {};
  bf16x8 xf[2][2], wfr[2][2];
  auto rdx = [&](int set, int b, int ks) {
#pragma unroll
    for (int ms = 0; ms < 2; ++ms) xf[set][ms] = *(const AS3 bf16x8*)(xa[ks] + b * P1_STAGE + ms * 4096);
  };
  auto rdw = [&](int set, int b, int ks, int ns) { wfr[set][ns] = lds_trp(wa[ns] + b * P1_STAGE + ks * 4096, 1024); };
  auto mm1 = [&](int set, int ms, int ns) {
    if (TYPE == 0 && ns == 1) acc[ms][1] = MFMA32(xf[set][ms], wfr[set][1], acc[ms][1]);
    else                      acc[ms][ns] = MFMA32(wfr[set][ns], xf[set][ms], acc[ms][ns]);
  };
  auto sub = [&](int set, int b, int ks) {
    SBAR(); mm1(set, 0, 0); SBAR(); if (b >= 0) rdx(set ^ 1, b, ks);
    SBAR(); mm1(set, 1, 0); SBAR(); if (b >= 0) rdw(set ^ 1, b, ks, 0);
    SBAR(); mm1(set, 0, 1); SBAR(); if (b >= 0) rdw(set ^ 1, b, ks, 1);
    SBAR(); mm1(set, 1, 1); SBAR();
  };
  auto stg = [&](int b, int nb) {
    sub(0, b, 1); sub(1, b, 2); sub(0, b, 3);
    LDS_BAR();
    if (THO && TYPE == 0) __builtin_amdgcn_s_sleep(THO);
    sub(1, nb, 0);
  };
  LDS_BAR();
  STAMP(0, 1);
  rdx(0, 0, 0); rdw(0, 0, 0, 0); rdw(0, 0, 0, 1);
  LGKM0();
#pragma unroll 1
  for (int t = 0; t < 5; ++t) {
    stg(0, 1); stg(1, 2); stg(2, 0);
    if (THR && TYPE == 0 && slow) __builtin_amdgcn_s_sleep(THR);
  }
  stg(0, -1);
  STAMP(0, 2);
  bf16x8 wf[2][2];
  if constexpr (TYPE <= 1) {
    const unsigned a = lds0 + P1_PHI + (TYPE == 0 ? wn : 2 * wn) * 2048 + (4 * h + q4) * 64 + chalf * 32 + p4 * 8;
#pragma unroll
    for (int n = 0; n <= TYPE; ++n)
#pragma unroll
      for (int s = 0; s < 2; ++s) wf[n][s] = lds_tr2(a + n * 2048 + s * 1024, a + n * 2048 + s * 1024 + 512);
  }

  if (TYPE >= 2) {
#pragma unroll
    for (int ms = 0; ms < 2; ++ms)
#pragma unroll
      for (int ns = 0; ns < 2; ++ns) {
        const f32x16 v = acc[ms][ns] * QSCALE;
        const unsigned p = WS_QB + ((rb * 8 + wm * 2 + ms) * 32 + ct * 4 + wn * 2 + ns) * 2048 + lane * 16;
        st16(sws, p, pack8<0>(v)); st16(sws, p + 1024, pack8<1>(v));
      }
    WAIT_BAR(0);
  } else if (TYPE == 1) {
    f32x16 pq[2][2];
#pragma unroll
    for (int ms = 0; ms < 2; ++ms)
#pragma unroll
      for (int ns = 0; ns < 2; ++ns) {
        pq[ms][ns] = MFMA32(wf[ns][0], pack8<0>(acc[ms][ns]), f32x16{});
        pq[ms][ns] = MFMA32(wf[ns][1], pack8<1>(acc[ms][ns]), pq[ms][ns]);
      }
#pragma unroll
    for (int ms = 0; ms < 2; ++ms)
#pragma unroll
      for (int ns = 0; ns < 2; ++ns) {
        f32x16 ep, em;
        feat_map<false>(pq[ms][ns], ep, em);
        const unsigned p = WS_QA + ((rb * 8 + wm * 2 + ms) * 32 + ct * 4 + wn * 2 + ns) * 4096 + lane * 16;
        st16(sws, p, pack8<0>(ep)); st16(sws, p + 1024, pack8<1>(ep)); st16(sws, p + 2048, pack8<0>(em)); st16(sws, p + 3072, pack8<1>(em));
      }
  } else {
    const unsigned img = lds0 + w * 8192;
    const bf16x8 sel = row_sum_sel(lane);
    f32x16 pk[2], kv[2] = {};
    f32x4 zz[2] = {};
    bf16x8 vf[2][2], af[2][2];
#pragma unroll
    for (int ms = 0; ms < 2; ++ms) {
      pk[ms] = MFMA32(wf[0][0], pack8<0>(acc[ms][0]), f32x16{});
      pk[ms] = MFMA32(wf[0][1], pack8<1>(acc[ms][0]), pk[ms]);
    }
#pragma unroll
    for (int ms = 0; ms < 2; ++ms) { vf[ms][0] = pack8<0>(acc[ms][1]); vf[ms][1] = pack8<1>(acc[ms][1]); PIN(vf[ms][0]); PIN(vf[ms][1]); }
    float off[4];
#pragma unroll
    for (int ms = 0; ms < 2; ++ms) { off[2 * ms] = -LOG2E * xhalf_max(max16(pk[ms])); off[2 * ms + 1] = LOG2E * xhalf_min(min16(pk[ms])); }
    const unsigned wr = img + 4096 + r32 * 64, sw = (r32 >> 1) & 7;
    const int t0 = 4 * h + q4, u8 = 4 * chalf + p4;
    const unsigned rd0 = img + 4096 + t0 * 64 + ((u8 ^ (t0 >> 1)) << 3), rd1 = img + 4096 + (t0 + 8) * 64 + ((u8 ^ ((t0 >> 1) + 4)) << 3);
    auto put = [&](int sg) {
#pragma unroll
      for (int g = 0; g < 4; ++g) *(AS3 f32x4*)(img + sg * 4096 + g * 1024 + lane * 16) = f32x4{kv[sg][4 * g], kv[sg][4 * g + 1], kv[sg][4 * g + 2], kv[sg][4 * g + 3]};
    };
    f32x16 e[2];
    float rs[2];
    auto mm = [&](int u, int j) {
      if (j & 1) zz[u & 1] = MFMA16(sel, af[u & 1][j >> 1], zz[u & 1]); else kv[u & 1] = MFMA32(af[u & 1][j >> 1], vf[u >> 1][j >> 1], kv[u & 1]);
    };
    auto exps = [&](int u, int mu) {
      const f32x16& p = pk[u >> 1];
      const float c = (u & 1) ? -LOG2E : LOG2E;
      f32x2 sm;
#pragma unroll
      for (int i = 0; i < 16; i += 2) {
        if (mu >= 0 && i >= 8) { SBAR(); mm(mu, (i - 8) >> 1); SBAR(); }
        const f32x2 v = __builtin_elementwise_fma(f32x2{p[i], p[i + 1]}, f32x2{c, c}, f32x2{off[u], off[u]});
        const f32x2 ev = {__builtin_amdgcn_exp2f(v[0]), __builtin_amdgcn_exp2f(v[1])};
        e[u & 1][i] = ev[0]; e[u & 1][i + 1] = ev[1];
        if (i == 0) sm = ev; else sm += ev;
      }
      rs[u & 1] = __builtin_amdgcn_rcpf(xhalf_sum(sm[0] + sm[1]));
    };
    auto rest = [&](int u) {
      const f32x16 n = e[u & 1] * rs[u & 1];
#pragma unroll
      for (int g = 0; g < 4; ++g) *(AS3 u32x2*)(wr + (u & 1) * 2048 + (((2 * g + h) ^ sw) << 3)) = u32x2{pk2(n[4 * g], n[4 * g + 1]), pk2(n[4 * g + 2], n[4 * g + 3])};
      asm volatile("" ::: "memory");
#pragma unroll
      for (int s = 0; s < 2; ++s) af[u & 1][s] = lds_tr2(rd0 + (u & 1) * 2048 + s * 1024, rd1 + (u & 1) * 2048 + s * 1024);
      asm volatile("" ::: "memory");
      SBAR();
    };
    exps(0, -1); exps(1, -1); rest(0);
    exps(2, 0);  rest(1);
    exps(3, 1);  rest(2);
    rest(3);
    mm(2, 0); mm(2, 2); mm(3, 0); mm(3, 2); SBAR(); mm(2, 1); mm(2, 3); mm(3, 1); mm(3, 3);
    put(0); put(1);
    if (lane < 32) {
      const unsigned a = lds0 + P1_ZRED + w * 256 + ((lane >> 2) & 1) * 64 + (lane >> 3) * 16 + (lane & 3) * 4;
      *(AS3 float*)a = zz[0][0]; *(AS3 float*)(a + 128) = zz[1][0];
    }
  }
  STAMP(0, 3);
}

DI void attn_pass_fast(unsigned lds0, unsigned qa, bf16x8 (&q)[2][2], bf16x8 sel, int kh, int lane, f32x16 (&O)[2], f32x4 (&L)[2], unsigned& tv SVP) {
  typedef const AS3 char* lp;
  lp l0 = (lp)(lds0 + lane * 16), la = l0 + kh * 16384;
#define RD(p) (*(const AS3 bf16x8*)(p))
  f32x16 negm[2], S0, S1 = {};
  bf16x8 vb = bf16x8{};
  int odd = 0xDDDD;
  O[0] = f32x16{}; O[1] = f32x16{}; L[0] = f32x4{}; L[1] = f32x4{};
  unsigned a0 = lds0 + lane * 16 + 98304, a1 = a0 + kh * 16384, qb = qa ^ 2048;
  PIN(O[0]); PIN(O[1]); PIN(L[0]); PIN(L[1]); PIN(S1); PIN(vb); PIN(odd); PIN(sel); PIN(a0); PIN(a1); PIN(qa); PIN(qb);
  LDS_BAR();
  STAMP(0, 4);
  TS(0);
  if (kh) __builtin_amdgcn_s_setprio(1);
  const bf16x8 ra = lds_r128(a0);
  q[0][0] = lds_r128(qa); q[1][0] = lds_r128(qb);
  const bf16x8 rb = lds_r128(a0 + 1024);
  q[0][1] = lds_r128(qa + 1024); q[1][1] = lds_r128(qb + 1024);
  bf16x8 ka = lds_r128(a1), kb = lds_r128(a1 + 1024), va = lds_r128(a1 + 2048);
  SBAR();
  negm[0] = MFMA32(ra, q[0][0], f32x16{}); negm[1] = MFMA32(ra, q[1][0], f32x16{});
  negm[0] = MFMA32(rb, q[0][1], negm[0]);  negm[1] = MFMA32(rb, q[1][1], negm[1]);
#pragma unroll
  for (int r = 0; r < 2; ++r) {
    const float m = xhalf_max(max16(negm[r]));
#pragma unroll
    for (int i = 0; i < 16; ++i) negm[r][i] = -m;
    PIN(negm[r]);
    SBAR();
    if (r == 0) { S0 = MFMA32(ka, q[0][0], negm[0]); S0 = MFMA32(kb, q[0][1], S0); PIN(S0); SBAR(); }
  }
#define EXN(SC, i, n) do { _Pragma("unroll") for (int k_ = 0; k_ < n; ++k_) SC[i + k_] = __builtin_amdgcn_exp2f(SC[i + k_]); PIN(SC); } while (0)
#define MM(D, E) do { SBAR(); D = E; PIN(D); SBAR(); } while (0)
#define TILE(TA, TN) do { \
    SBAR();                                                             EXN(S0, 0, 2);  \
    MM(O[1], SM32(vb, HI(S1), O[1]));       vb = RD((TA) + 3072);       EXN(S0, 2, 3);  \
    MM(L[1], SM16(sel, HI(S1), L[1]));                                  EXN(S0, 5, 2);  \
    MM(S1, MFMA32(ka, q[1][0], negm[1]));   ka = RD(TN);                EXN(S0, 7, 3);  \
    MM(S1, MFMA32(kb, q[1][1], S1));        kb = RD((TN) + 1024);       EXN(S0, 10, 3); \
    MM(O[0], SM32(va, LO(S0), O[0]));                                   EXN(S0, 13, 3); \
    MM(L[0], SM16(sel, LO(S0), L[0]));                                  EXN(S1, 0, 2);  \
    MM(O[0], SM32(vb, HI(S0), O[0]));                                   EXN(S1, 2, 3);  \
    MM(L[0], SM16(sel, HI(S0), L[0]));                                  EXN(S1, 5, 2);  \
    MM(S0, MFMA32(ka, q[0][0], negm[0]));                               EXN(S1, 7, 3);  \
    MM(S0, MFMA32(kb, q[0][1], S0));                                    EXN(S1, 10, 3); \
    MM(O[1], SM32(va, LO(S1), O[1]));       va = RD((TN) + 2048);       EXN(S1, 13, 3); \
    MM(L[1], SM16(sel, LO(S1), L[1])); } while (0)
  TS(1);
#pragma unroll 1
  for (int c = 0; c < 8; ++c) {
    const lp cb = la + ((3 - c) & 3) * 32768, nb = la + ((2 - c) & 3) * 32768;
    TILE(cb, cb + 4096);
    TILE(cb + 4096, cb + 8192);
    SBAR(); if (kh) __builtin_amdgcn_s_setprio(0);
    TILE(cb + 8192, cb + 12288);
    SBAR(); TS(2 + 2 * c);
    asm volatile("s_waitcnt vmcnt(0)\n\ts_barrier" ::: "memory"); TS(3 + 2 * c);
    if (kh) __builtin_amdgcn_s_setprio(1);
    TILE(cb + 12288, nb);
  }
  SBAR();
  O[1] = SM32(vb, HI(S1), O[1]); L[1] = SM16(sel, HI(S1), L[1]);
  __builtin_amdgcn_s_setprio(0);
  TS(18);
#undef TILE
#undef EXN
#undef MM
#undef RD
}

DI void attn_pass_safe(rsrc_t sws, const bf16x8 q0, const bf16x8 q1, const bf16x8 sel, int lane, f32x16& O, f32x4& L) {
  O = f32x16{}; L = f32x4{};
  float m = -INFINITY;
  const int odd = 0xDDDD;
#pragma unroll 1
  for (int t = 0; t < 64; ++t) {
    bf16x8 a[4];
#pragma unroll
    for (int j = 0; j < 4; ++j) a[j] = __builtin_bit_cast(bf16x8, ld16(sws, WS_KV + t * 4096 + j * 1024 + lane * 16));
    f32x16 Sc = {};
    Sc = MFMA32(a[0], q0, Sc); Sc = MFMA32(a[1], q1, Sc);
    const float mn = fmaxf(m, xhalf_max(max16(Sc)));
    const float al = __builtin_amdgcn_exp2f(m - mn);
    m = mn;
#pragma unroll
    for (int r = 0; r < 16; ++r) { O[r] *= al; Sc[r] = __builtin_amdgcn_exp2f(Sc[r] - mn); }
    L *= al;
    O = SM32(a[2], LO(Sc), O); L = SM16(sel, LO(Sc), L);
    O = SM32(a[3], HI(Sc), O); L = SM16(sel, HI(Sc), L);
  }
}

DI void helper_program(unsigned lds0, const unsigned short* __restrict__ X, const unsigned short* __restrict__ W, const unsigned short* __restrict__ E,
                       const unsigned short* __restrict__ woa, const unsigned short* __restrict__ wob, char* __restrict__ ws, rsrc_t sws,
                       const char* pc0, unsigned co, int type, int rb, int ct, int x, int idx, int lw, int lane) {
  unsigned sv = 0; (void)sv;
  __builtin_amdgcn_s_setprio(3);
  u64 e0;
  const unsigned vl = lane * 16;
  {
    const bool ext = type >= 2;
    const unsigned ldw = ext ? 2048u : 6144u;
    const rsrc_t sx = mk_rsrc(X + (size_t)rb * 256 * D, 256u * 2048u);
    const rsrc_t sw = mk_rsrc(W, 1024u * ldw);
    const rsrc_t se = mk_rsrc(E, 1024u * 64u);
    unsigned xv[8], wv[4];
#pragma unroll
    for (int j = 0; j < 8; ++j) {
      const int row = 8 * (8 * lw + j) + (lane >> 3), c = (lane & 7) ^ ((row >> 1) & 7);
      xv[j] = row * 2048 + c * 16 - (j & 3) * 1024;
    }
#pragma unroll
    for (int j = 0; j < 4; ++j) {
      const int k = 4 * (4 * lw + j) + (lane >> 4), c = (lane & 15) ^ (((lane >> 4) & 3) << 2);
      const int col = (type == 0) ? 1024 + ((c >> 2) & 1) * 1024 + (2 * ct + (c >> 3)) * 32 + (c & 3) * 8 : ct * 128 + c * 8;
      wv[j] = k * ldw + col * 2 - j * 1024;
    }
    const int erow = 64 * (ct >> 1) + (lane & 31);
    const unsigned exa = (erow * 128 + (((lane >> 5) ^ ((erow >> 1) & 7)) << 4)) ^ (lw * 32);
    const unsigned ewa = P1_EOFF + (16 * lw + 8 * (lane >> 5) + ((lane & 15) >> 2)) * 64 + ((lane >> 4) & 1) * 32 + (lane & 3) * 8;
    f32x16 eacc[2] = {};
    bf16x8 ex[2], ew;
    auto kvr = [&](unsigned buf) {
      if (ext) { ex[0] = lds_r128(buf + exa); ew = lds_tr2(buf + ewa, buf + ewa + 256); ex[1] = lds_r128(buf + exa + 4096); }
    };
    auto kvm = [&]() {
      if (ext)
        asm volatile("s_cmp_eq_u32 %5, 2\n\ts_cbranch_scc1 1f\n\t"
                     "v_mfma_f32_32x32x16_bf16 %0, %2, %4, %0\n\tv_mfma_f32_32x32x16_bf16 %1, %3, %4, %1\n\ts_branch 2f\n"
                     "1:\tv_mfma_f32_32x32x16_bf16 %0, %4, %2, %0\n\tv_mfma_f32_32x32x16_bf16 %1, %4, %3, %1\n2:"
                     : "+v"(eacc[0]), "+v"(eacc[1]) : "v"(ex[0]), "v"(ex[1]), "v"(ew), "s"(type) : "scc");
    };
    auto kvs = [&](unsigned buf) { kvr(buf); kvm(); };
    auto stage = [&](int t0, unsigned buf, bool kv, unsigned kb) {
      if (kv && KVR == 0) kvr(kb);
      const int t = (t0 + (ext ? BROT : KROT)) & 15;
      const unsigned sox = t * 128, sow = t * 64 * ldw;
      set_m0(buf + P1_WOFF + lw * 4096);
      dma16<0>(wv[0], sw, sow); dma16<1024>(wv[1], sw, sow); dma16<2048>(wv[2], sw, sow); dma16<3072>(wv[3], sw, sow);
      if (kv && KVP == 0) kvm();
      if (kv && KVR == 1) kvr(kb);
      set_m0(buf + lw * 8192);
      dma16<0>(xv[0], sx, sox); dma16<1024>(xv[1], sx, sox); dma16<2048>(xv[2], sx, sox); dma16<3072>(xv[3], sx, sox);
      if (kv && KVP == 1) kvm();
      if (kv && KVR == 2) kvr(kb);
      set_m0(buf + lw * 8192 + 4096);
      dma16<0>(xv[4], sx, sox); dma16<1024>(xv[5], sx, sox); dma16<2048>(xv[6], sx, sox); dma16<3072>(xv[7], sx, sox);
      if (ext) { set_m0(buf + P1_EOFF + lw * 1024); dma16<0>(vl, se, t * 4096 + lw * 1024); }
      if (kv && KVP == 2) kvm();
    };
    stage(0, lds0, false, 0); stage(1, lds0 + P1_STAGE, false, 0);
    if (ext) WAIT_BAR(13); else WAIT_BAR(12);
    stage(2, lds0 + 2 * P1_STAGE, true, lds0);
    unsigned buf = lds0;
#pragma unroll 1
    for (int t = 0; t < 13; ++t) {
      if (ext) WAIT_BAR(13); else WAIT_BAR(12);
      const unsigned nb = (buf == lds0 + 2 * P1_STAGE) ? lds0 : buf + P1_STAGE;
      stage(t + 3, buf, true, nb);
      buf = nb;
    }
    if (ext) WAIT_BAR(13); else WAIT_BAR(12);
    e0 = __hip_atomic_load((AS1 u64*)(ws + WS_FLG) + F_EPOCH, __ATOMIC_RELAXED, __HIP_MEMORY_SCOPE_AGENT);
    if (lw == 3) pf_bin(lds0 + P1_BIN, co, pc0);
    if (!ext && (type == 1 || lw < 2)) {
      set_m0(lds0 + P1_PHI + lw * 2048);
      const unsigned so = ((type == 0 ? 2 * ct : 4 * ct) + lw) * 2048;
      dma16<0>(vl, se, so); dma16<1024>(vl, se, so);
    }
    kvs(lds0 + 2 * P1_STAGE);
    WAIT_BAR(0);
    kvs(lds0);
    asm volatile("s_nop 15\n\ts_nop 7" : "+v"(eacc[0]), "+v"(eacc[1]));
    const unsigned red = lds0 + P1_STAGE + vl;
    if (ext) {
#pragma unroll
      for (int i = 0; i < 8; ++i) {
        const f32x16& a = eacc[i >> 2];
        *(AS3 f32x4*)(red + lw * 8192 + i * 1024) = f32x4{a[4 * (i & 3)], a[4 * (i & 3) + 1], a[4 * (i & 3) + 2], a[4 * (i & 3) + 3]};
      }
    }
    WAIT_BAR(0);
    if (ext) {
#pragma unroll
      for (int n = 0; n < 2; ++n) {
        const unsigned a = red + n * 4096 + lw * 1024;
        const f32x4 sm = add4(add4(*(AS3 f32x4*)a, *(AS3 f32x4*)(a + 8192)), add4(*(AS3 f32x4*)(a + 16384), *(AS3 f32x4*)(a + 24576)));
        unsigned p = WS_KV + (rb * 8 + 2 * (ct >> 1) + n) * 4096 + (lw >> 1) * 1024;
        if (type == 3) p += 2048 + ((lane & 31) + 32 * (lw & 1)) * 16 + (lane >> 5) * 8;
        else           p += vl + (lw & 1) * 8;
        st8(sws, p, u32x2{pk2(sm[0], sm[1]), pk2(sm[2], sm[3])});
      }
    }
  }
  const u64 tag = ((u64)(unsigned)__builtin_amdgcn_readfirstlane((unsigned)(e0 >> 32)) << 32 | (unsigned)__builtin_amdgcn_readfirstlane((unsigned)e0))
                  * 0x5851F42D4C957F2Dull + 0x14057B7EF767814Full;
  const bool lead = (lw == 0) && (lane == 0);
  if (type >= 2) { WAIT_BAR(0); if (lead) post(ws, F_KV + rb * 16 + ct, tag); }
  auto chunk = [&](int c) {
    const unsigned l = lds0 + ((3 - c) & 3) * 32768 + lw * 8192, so = WS_KV + ((c + x) & 7) * 32768 + lw * 8192;
    dma4k(l, vl, sws, so); dma4k(l + 4096, vl, sws, so + 4096);
  };
  STAMP(512, 10);
  if (lw == 0) {
    poll(ws, F_KV + x * 16 + (lane & 7), tag);
    __hip_atomic_store((AS3 unsigned*)(lds0 + P1_GO), 1u, __ATOMIC_RELAXED, __HIP_MEMORY_SCOPE_WORKGROUP);
  } else {
    while (__hip_atomic_load((AS3 unsigned*)(lds0 + P1_GO), __ATOMIC_RELAXED, __HIP_MEMORY_SCOPE_WORKGROUP) == 0u) __builtin_amdgcn_s_sleep(1);
    asm volatile("" ::: "memory");
  }
  STAMP(512, 11);
  dma4k(lds0 + P2_Q + lw * 4096, vl, sws, WS_QB + ((8 * x + (idx >> 2)) * 32 + 8 * (idx & 3)) * 2048 + lw * 4096);
  chunk(0);
  WAIT_BAR(0);
  poll(ws, F_KV + (lane >> 3) * 16 + (lane & 7), tag);
  chunk(1);
  if (type == 0) {
#pragma unroll
    for (int j = 0; j < 4; ++j) {
      const unsigned a = lds0 + (lw >> 1) * 32768 + ((lw & 1) * 4 + j) * 1024 + vl;
      const f32x4 sm = add4(add4(*(AS3 f32x4*)a, *(AS3 f32x4*)(a + 8192)), add4(*(AS3 f32x4*)(a + 16384), *(AS3 f32x4*)(a + 24576)));
      st16(sws, WS_KVP + ((2 * ct + (lw >> 1)) * 8 + rb) * 8192 + ((lw & 1) * 4 + j) * 1024 + vl, sm);
    }
    if (lw < 2) {
      const unsigned a = lds0 + P1_ZRED + lw * 1024 + lane * 4;
      st4(sws, WS_ZP + (((2 * ct + lw) * 8 + rb) * 64 + lane) * 4, one(*(AS3 float*)a + *(AS3 float*)(a + 256)) + one(*(AS3 float*)(a + 512) + *(AS3 float*)(a + 768)));
    }
  }
  WAIT_BAR(0);
  chunk(2); chunk(3);
  if (lead && type < 2) post(ws, type == 0 ? F_K + ct * 8 + rb : F_Q + rb * 8 + ct, tag);
  const int br = !(x & 1), rb3 = 4 * (x >> 1) + (idx & 3), cb = idx >> 2;
  const unsigned short* wo = br ? wob : woa;
  unsigned pfs = 0;
  auto warm_wo = [&]() {
    pf_line(pfs, (const char*)wo + idx * 65536 + lw * 16384 + lane * 128);
    pf_line(pfs, (const char*)wo + idx * 65536 + lw * 16384 + 8192 + lane * 128);
  };
  if (lw == 1) *(AS3 unsigned*)(lds0 + P2_ZB + 128 + lane * 4) = lane < 32 ? 0x3f803f80u : 0u;
  WAIT_BAR(8);
  if (br) {
    const int head = 4 * x + (idx & 7), tp = idx >> 3;
    poll(ws, lane < 8 ? F_K + (head >> 1) * 8 + lane : F_Q + (2 * tp + (lane & 1)) * 8 + (head >> 2), tag);
    STAMP(512, 12);
    chunk(4);
    u32x4 kvp[2][8], fq[4][4];
    float zs[8];
    auto ldq = [&](int e) {
#pragma unroll
      for (int j = 0; j < 4; ++j) fq[e][j] = ld16(sws, WS_QA + ((16 * tp + 4 * lw + e) * 32 + head) * 4096 + j * 1024 + vl);
    };
#pragma unroll
    for (int e = 0; e < 2; ++e)
#pragma unroll
      for (int p = 0; p < 8; ++p) kvp[e][p] = ld16(sws, WS_KVP + ((head * 8 + p) * 512 + (2 * lw + e) * 64 + lane) * 16);
#pragma unroll
    for (int p = 0; p < 8; ++p) zs[p] = ld4(sws, WS_ZP + ((head * 8 + p) * 64 + lane) * 4);
    ldq(0); ldq(1);
    asm volatile("s_barrier" ::: "memory");
#pragma unroll
    for (int e = 0; e < 2; ++e) {
#pragma unroll
      for (int p = 0; p < 8; ++p) PIN(kvp[e][p]);
#define F4(v) __builtin_bit_cast(f32x4, v)
      const f32x4 sm = add4(add4(add4(F4(kvp[e][0]), F4(kvp[e][1])), add4(F4(kvp[e][2]), F4(kvp[e][3]))), add4(add4(F4(kvp[e][4]), F4(kvp[e][5])), add4(F4(kvp[e][6]), F4(kvp[e][7]))));
#undef F4
      *(AS3 u32x2*)(lds0 + P2_KVF + lw * 1024 + vl + e * 8) = u32x2{pk2(sm[0], sm[1]), pk2(sm[2], sm[3])};
    }
    {
#pragma unroll
      for (int p = 0; p < 8; ++p) PIN(zs[p]);
      const float z = one(one(zs[0] + zs[1]) + one(zs[2] + zs[3])) + one(one(zs[4] + zs[5]) + one(zs[6] + zs[7]));
      if (lw == 0) *(AS3 unsigned short*)(lds0 + P2_ZB + lane * 2) = (unsigned short)(pk2(z, z) & 0xffffu);
    }
    chunk(5);
    ldq(2); ldq(3);
    const int row = lane & 15, kg = lane >> 4;
    const bool on = (((row >> 2) ^ kg) & 1) == 0;
    const unsigned za = lds0 + P2_ZB + ((on && (row & 3) == 0) ? (kg >> 1) * 32 : (on && (row & 3) == 1) ? 128 : 256);
    const unsigned ka = lds0 + P2_KVF + vl;
    auto otile = [&](int e) {
#define BF(v) __builtin_bit_cast(bf16x8, v)
      f32x16 np = MFMA32(lds_r128(ka), BF(fq[e][0]), f32x16{});        f32x4 dp = MFMA16(lds_r128(za), BF(fq[e][0]), f32x4{});
      np = MFMA32(lds_r128(ka + 1024), BF(fq[e][1]), np);              dp = MFMA16(lds_r128(za + 16), BF(fq[e][1]), dp);
      f32x16 nm = MFMA32(lds_r128(ka + 2048), BF(fq[e][2]), f32x16{}); f32x4 dm = MFMA16(lds_r128(za + 64), BF(fq[e][2]), f32x4{});
      nm = MFMA32(lds_r128(ka + 3072), BF(fq[e][3]), nm);              dm = MFMA16(lds_r128(za + 80), BF(fq[e][3]), dm);
#undef BF
      const float a = __builtin_amdgcn_rcpf(dp[1]), b = __builtin_amdgcn_rcpf(dm[1]);
      const float r = __builtin_amdgcn_rcpf(one(dp[0] * a) + one(dm[0] * b) + 1e-6f), ar = one(a * r), br = one(b * r);
      f32x16 o;
#pragma unroll
      for (int i = 0; i < 16; ++i) o[i] = one(__builtin_fmaf(nm[i], br, one(np[i] * ar)));
      const unsigned p = WS_OA + ((16 * tp + 4 * lw + e) * 32 + head) * 2048 + vl;
      st16(sws, p, pack8<0>(o)); st16(sws, p + 1024, pack8<1>(o));
    };
    WAIT_BAR(16);
#pragma unroll
    for (int j = 0; j < 4; ++j) { PIN(fq[0][j]); PIN(fq[1][j]); }
    chunk(6);
    otile(0); otile(1);
    WAIT_BAR(12);
#pragma unroll
    for (int j = 0; j < 4; ++j) { PIN(fq[2][j]); PIN(fq[3][j]); }
    chunk(7);
    otile(2); otile(3);
    WAIT_BAR(0);
    if (lead) post(ws, F_OA + tp * 32 + head, tag);
    warm_wo();
  } else {
    chunk(4); warm_wo(); WAIT_BAR(10);
    chunk(5); WAIT_BAR(10);
    chunk(6); WAIT_BAR(8);
    chunk(7);
    if (blockIdx.x == 1 && lw == 1) {
      poll(ws, lane, tag); poll(ws, 64 + lane, tag); poll(ws, 128 + lane, tag);
      if (lane == 0) post(ws, F_EPOCH, tag);
    }
    WAIT_BAR(8);
  }
  const rsrc_t sw = mk_rsrc(wo, 1024u * 2048u);
  unsigned wv[4];
#pragma unroll
  for (int j = 0; j < 4; ++j) {
    const int k = 4 * (4 * lw + j) + (lane >> 4), c = (lane & 15) ^ (((lane >> 4) & 3) << 2);
    wv[j] = k * 2048 + (cb * 128 + c * 8) * 2 - j * 1024;
  }
  const unsigned osrc = (br ? WS_OB : WS_OA) + (rb3 * 4 + lw) * 65536;
  auto wst = [&](int t) {
    const unsigned sow = t * 64 * 2048;
    set_m0(lds0 + p3_wslot(t) + lw * 4096);
    dma16<0>(wv[0], sw, sow); dma16<1024>(wv[1], sw, sow); dma16<2048>(wv[2], sw, sow); dma16<3072>(wv[3], sw, sow);
  };
  auto ost = [&](int t) { dma4k(lds0 + P3_O + (t % NO) * 16384 + lw * 4096, vl, sws, osrc + t * 4096); };
  wst(0); wst(1); wst(2); wst(3);
  if (br) asm volatile("s_barrier" ::: "memory"); else WAIT_BAR(16);
  if (NW > 4) wst(4);
  if (NW > 5) wst(5);
  if (!br && lw == 0) poll(ws, F_OA + (rb3 >> 2) * 32 + (lane & 31), tag);
  asm volatile("s_barrier" ::: "memory");
  asm volatile("s_barrier" ::: "memory");
  asm volatile("s_barrier" ::: "memory");
  if (lead) post(ws, F_OB + x * 32 + idx, tag);
  STAMP(512, 13);
  if (br && lw == 0) poll(ws, F_OB + (rb3 >> 1) * 32 + (rb3 & 1) * 16 + (lane & 15), tag);
  asm volatile("s_barrier" ::: "memory");
  STAMP(512, 14);
#pragma unroll
  for (int t = 0; t < NF; ++t) ost(t);
  wait_bar<4 * (NF - 1)>();
#pragma unroll
  for (int t = NF; t < NO; ++t) ost(t);
  PIN(pfs);
#define RND(t) do { wait_bar<p3_inflight(t)>(); if (t + NW < 16) wst(t + NW); if (t + NO < 16) ost(t + NO); } while (0)
  RND(0); RND(1); RND(2); RND(3); RND(4); RND(5); RND(6); RND(7); RND(8); RND(9); RND(10); RND(11); RND(12); RND(13); RND(14);
#undef RND
  if (lw == 0) STOUT(10, 15);
}

__global__ __launch_bounds__(768, 3) void fused_fwd(const unsigned short* __restrict__ xa, const unsigned short* __restrict__ wqkv,
                                                    const unsigned short* __restrict__ wphi, const unsigned short* __restrict__ woa,
                                                    const unsigned short* __restrict__ xb, const unsigned short* __restrict__ wq,
                                                    const unsigned short* __restrict__ wk, const unsigned short* __restrict__ wv,
                                                    const unsigned short* __restrict__ wob, char* __restrict__ ws, unsigned short* __restrict__ out) {
  __shared__ __attribute__((aligned(1024))) char smem[LDS_BYTES];
  const unsigned lds0 = (unsigned)(uintptr_t)(AS3 char*)smem;
  const int tid = threadIdx.x, lane = tid & 63, w0 = __builtin_amdgcn_readfirstlane(tid >> 6), w = HF ? (w0 < 4 ? w0 + 8 : w0 - 4) : w0;
  const char* pc0; unsigned clen;
  asm volatile("s_getpc_b64 %0\n.Lfw_head:\n\ts_mov_b32 %1, .Lfw_tail-.Lfw_head" : "=s"(pc0), "=s"(clen));
  unsigned co = (((blockIdx.x >> 3) & 7) * 40 + lane) * 128;
  co = lane < 40 && co < clen ? co : 0u;
  if (CPF & 1) pf_bin(lds0 + P1_BIN, (tid >> 6) == 4 ? co : 0u, pc0);
  if (CPF & 4) asm volatile("s_cmp_lg_u32 %3, 4\n\ts_cbranch_scc1 1f\n\ts_mov_b32 m0, %0\n\ts_nop 0\n\tglobal_load_lds_dword %1, %2\n1:" ::"s"(lds0 + P1_BIN), "v"(co), "s"(pc0), "s"(w0) : "memory", "scc");
  SBAR();
  asm volatile("" : "+s"(xa), "+s"(wqkv), "+s"(wphi), "+s"(xb), "+s"(wq), "+s"(wk), "+s"(wv), "+s"(ws));
  const int b = blockIdx.x, x = b & 7, idx = b >> 3;
  const int rb = 4 * ((x >> 1) & 1) + (idx & 3);
  const int ct = idx < 16 ? 8 * (x >> 2) + 4 * (x & 1) + (idx >> 2) : 4 * (x >> 2) + ((idx >> 2) & 3);
  const int type = idx < 16 ? 0 : (x & 1) ? 1 : 2 + (ct & 1);
  const rsrc_t sws = mk_rsrc(ws, WS_END);
  unsigned sv = 0; (void)sv;
  STAMP(0, 0);
  if (HX(w >= 8)) {
    helper_program(lds0, type >= 2 ? xb : xa, type >= 2 ? wq : wqkv, type == 3 ? wv : type == 2 ? wk : wphi, woa, wob, ws, sws, pc0, co, type, rb, ct, x, idx, w - 8, lane);
    asm volatile("s_endpgm" ::: "memory");
  }
  if ((CPF & 8) && w == 2) pf_bin(lds0 + P1_BIN, co, pc0);
  if (type == 0 && !(x & 1) && w < 2) pf_bin(lds0 + P1_BIN, (idx * 32 + (lane & 31)) * 128, w ? wv : wk);
  if (PFB && type < 2 && w >= 3 && w < 6) {
    constexpr int NS = 8 - PF0, PER = (NS * 512 + 47) / 48;
    const int j = (x & 1) * 16 + idx, id = (w == 5 ? j : 2 * j + w - 3) * PER + min(lane, PER - 1);
    unsigned o;
    if (w == 5) { const int st = min(id >> 9, NS - 1), r = id & 511;  o = (64 * ((PF0 + st + BROT) & 15) + (r >> 3)) * 2048 + (x >> 2) * 1024 + (r & 7) * 128; }
    else        { const int st = min(id >> 10, NS - 1), r = id & 1023; o = (((x >> 1) & 1) * 1024 + r) * 2048 + ((PF0 + st + BROT) & 15) * 128; }
    pf_bin(lds0 + P1_BIN, o, w == 5 ? wq : xb);
  }
  if (type == 0)      p1_compute<0>(lds0, ws, sws, rb, ct, w, lane, !(x & 1) SVA);
  else if (type == 1) p1_compute<1>(lds0, ws, sws, rb, ct, w, lane, !(x & 1) SVA);
  else                p1_compute<2>(lds0, ws, sws, rb, ct, w, lane, !(x & 1) SVA);

  const int kh = w >> 2, r32 = lane & 31, h = lane >> 5;
  {
    const unsigned s0 = (lane & 15) == 0 ? 0x3f803f80u : 0u, s4 = (lane & 15) == 4 ? 0x3f803f80u : 0u;
    const bf16x8 sel = __builtin_bit_cast(bf16x8, u32x4{s0, s0, s4, s4});
    bf16x8 q[2][2];
    f32x16 O[2]; f32x4 L[2];
    unsigned tv = 0;
    attn_pass_fast(lds0, lds0 + P2_Q + (2 * (w & 3) + (kh ^ 1)) * 2048 + lane * 16, q, sel, kh, lane, O, L, tv SVA);
    TSOUT();
    STAMP(0, 5);
    const unsigned xs = lds0 + P2_XO + w * 4096 + lane * 16, xr = lds0 + P2_XO + (w ^ 4) * 4096 + lane * 16;
#pragma unroll
    for (int g = 0; g < 4; ++g) *(AS3 f32x4*)(xs + g * 1024) = f32x4{O[0][4 * g], O[0][4 * g + 1], O[0][4 * g + 2], O[0][4 * g + 3]};
    *(AS3 float*)(lds0 + P2_XL + w * 256 + lane * 4) = L[0][0];
    LDS_BAR();
#pragma unroll
    for (int g = 0; g < 4; ++g) {
      const f32x4 o = *(AS3 f32x4*)(xr + g * 1024);
      O[1][4 * g] += o[0]; O[1][4 * g + 1] += o[1]; O[1][4 * g + 2] += o[2]; O[1][4 * g + 3] += o[3];
    }
    L[1][0] += *(AS3 float*)(lds0 + P2_XL + (w ^ 4) * 256 + lane * 4);
    bool bad = !(L[1][0] < 1.8446744e19f);
#pragma unroll
    for (int i = 0; i < 16; ++i) bad |= !(fabsf(O[1][i]) < 3.0e38f);
    if (__any(bad)) attn_pass_safe(sws, q[1][0], q[1][1], sel, lane, O[1], L[1]);
    const float r = __builtin_amdgcn_rcpf(xhalf_sum(L[1][0]));
    O[1] *= r;
    const unsigned p = WS_OB + ((8 * x + (idx >> 2)) * 32 + 8 * (idx & 3) + 2 * (w & 3) + kh) * 2048 + lane * 16;
    st16(sws, p, pack8<0>(O[1])); st16(sws, p + 1024, pack8<1>(O[1]));
    WAIT_BAR(0);
    STAMP(0, 6);
    asm volatile("s_barrier" ::: "memory");
    SBAR();
  }
  const int br = !(x & 1), rb3 = 4 * (x >> 1) + (idx & 3), cb = idx >> 2;
  const int wm = w & 1, wn = (w >> 1) & 1;
  const int q4 = (lane & 15) >> 2, p4 = lane & 3, chalf = (lane >> 4) & 1;
  typedef const AS3 char* lp;
  lp oa[2], wa[2];
#pragma unroll
  for (int ms = 0; ms < 2; ++ms) oa[ms] = (lp)(lds0 + P3_O + ((wm * 8192 + kh * (4096 + 2048) + lane * 16) ^ (ms * 4096)));
#pragma unroll
  for (int ns = 0; ns < 2; ++ns)
    wa[ns] = (lp)(lds0 + P3_W + (kh * 32 + 4 * h + q4) * 256 + ((((wn * 2 + ns) ^ q4) << 2) | (chalf * 2 + (p4 >> 1))) * 16 + (p4 & 1) * 8);
  f32x16 acc[2][2] = {};
  bf16x8 xf[2][2], wfr[2][2];
  auto rdx = [&](int set, int t, int ks) {
#pragma unroll
    for (int ms = 0; ms < 2; ++ms) xf[set][ms] = *(const AS3 bf16x8*)(oa[ms] + (t % NO) * 16384 + ks * 1024);
  };
  auto rdw = [&](int set, int t, int ks, int ns) { wfr[set][ns] = lds_trp(wa[ns] + (p3_wslot(t) - P3_W) + ks * 4096, 2048); };
  auto mm1 = [&](int set, int ms, int ns) { acc[ms][ns] = MFMA32(wfr[set][ns], xf[set][ms], acc[ms][ns]); };
  auto sub = [&](int set, int t, int ks) {
    SBAR(); mm1(set, 0, 0); SBAR(); if (t >= 0) rdx(set ^ 1, t, ks);
    SBAR(); mm1(set, 1, 0); SBAR(); if (t >= 0) rdw(set ^ 1, t, ks, 0);
    SBAR(); mm1(set, 0, 1); SBAR(); if (t >= 0) rdw(set ^ 1, t, ks, 1);
    SBAR(); mm1(set, 1, 1); SBAR();
  };
  PIN(acc[0][0]); PIN(acc[0][1]); PIN(acc[1][0]); PIN(acc[1][1]); PIN(oa[0]); PIN(oa[1]); PIN(wa[0]); PIN(wa[1]);
  LDS_BAR();
  STAMP(0, 7);
  rdx(0, 0, 0); rdw(0, 0, 0, 0); rdw(0, 0, 0, 1);
  LGKM0();
#pragma unroll
  for (int t = 0; t < 15; ++t) { sub(0, t, 1); LDS_BAR(); sub(1, t + 1, 0); }
  sub(0, 15, 1);
  sub(1, -1, 0);
  STAMP(0, 8);
  constexpr int XB = NO == 5 ? 16384 : 65536, IB = NO == 5 ? 131072 : 0;
  static_assert(NO == 4 || NO == 5 || NO == 6, "");
  const unsigned xs = lds0 + XB + w * 8192 + lane * 16, xr = lds0 + XB + (w ^ 4) * 8192 + lane * 16;
#pragma unroll
  for (int ns = 0; ns < 2; ++ns)
#pragma unroll
    for (int g = 0; g < 4; ++g) {
      const f32x16& a = acc[1][ns];
      *(AS3 f32x4*)(xs + ns * 4096 + g * 1024) = f32x4{a[4 * g], a[4 * g + 1], a[4 * g + 2], a[4 * g + 3]};
    }
  LDS_BAR();
  const unsigned img = lds0 + IB + w * 4096;
#pragma unroll
  for (int ns = 0; ns < 2; ++ns)
#pragma unroll
    for (int g = 0; g < 4; ++g) {
      const f32x16& a = acc[0][ns];
      const f32x4 o = *(AS3 f32x4*)(xr + ns * 4096 + g * 1024);
      *(AS3 u32x2*)(img + r32 * 128 + (((ns * 4 + g) ^ (r32 & 7)) << 4) + h * 8) =
          u32x2{pk2(a[4 * g] + o[0], a[4 * g + 1] + o[1]), pk2(a[4 * g + 2] + o[2], a[4 * g + 3] + o[3])};
    }
  asm volatile("" ::: "memory");
  const int tok0 = rb3 * 128 + wm * 64 + kh * 32;
  const rsrc_t so = mk_rsrc(out, 2048u * 4096u);
  const unsigned op = tok0 * 4096 + br * 2048 + cb * 256 + wn * 128;
#pragma unroll
  for (int i = 0; i < 4; ++i) {
    const int row = 8 * i + (lane >> 3), ch = lane & 7;
    const u32x4 v = *(AS3 u32x4*)(img + row * 128 + ((ch ^ (row & 7)) << 4));
    st16(so, op + row * 4096 + ch * 16, v);
  }
  STAMP(0, 9);
  asm volatile(".Lfw_tail:");
}

extern "C" void kernel_launch(void* const* d_in, const int* in_sizes, int n_in, void* d_out, int out_size, void* d_ws, size_t ws_size,
                              hipStream_t stream) {
  (void)in_sizes; (void)n_in; (void)out_size; (void)ws_size;
  typedef const unsigned short* cp;
  fused_fwd<<<256, 768, 0, stream>>>((cp)d_in[0], (cp)d_in[1], (cp)d_in[2], (cp)d_in[3], (cp)d_in[4], (cp)d_in[5], (cp)d_in[6], (cp)d_in[7],
                                     (cp)d_in[8], (char*)d_ws + (WSEND ? ((ws_size - WS_END) & ~(size_t)0x1fffff) : 0), (unsigned short*)d_out);
}
```

```cpp
#include <hip/hip_runtime.h>
#include <hip/hip_bf16.h>
#include <stdint.h>
#pragma clang diagnostic ignored "-Wint-to-pointer-cast"

typedef __bf16   bf16x8 __attribute__((ext_vector_type(8)));
typedef __bf16   bf16x16 __attribute__((ext_vector_type(16)));
typedef __bf16   bf16x2 __attribute__((ext_vector_type(2)));
typedef short    s16x4  __attribute__((ext_vector_type(4)));
typedef short    s16x8  __attribute__((ext_vector_type(8)));
typedef float    f32x16 __attribute__((ext_vector_type(16)));
typedef float    f32x4  __attribute__((ext_vector_type(4)));
typedef float    f32x2  __attribute__((ext_vector_type(2)));
typedef unsigned u32x4  __attribute__((ext_vector_type(4)));
typedef unsigned u32x2  __attribute__((ext_vector_type(2)));
typedef unsigned long long u64;
typedef __amdgpu_buffer_rsrc_t rsrc_t;

#define AS1 __attribute__((address_space(1)))
#define AS3 __attribute__((address_space(3)))
#define DI __device__ __forceinline__
#define MFMA32(a, b, c) __builtin_amdgcn_mfma_f32_32x32x16_bf16((a), (b), (c), 0, 0, 0)
#define MFMA16(a, b, c) __builtin_amdgcn_mfma_f32_16x16x32_bf16((a), (b), (c), 0, 0, 0)
#define SM32(a, b, c) __builtin_amdgcn_smfmac_f32_32x32x32_bf16((a), (b), (c), odd, 0, 0)
#define SM16(a, b, c) __builtin_amdgcn_smfmac_f32_16x16x64_bf16((a), (b), (c), odd, 0, 0)
#define LO(S) __builtin_bit_cast(bf16x16, __builtin_shufflevector(S, S, 0, 1, 2, 3, 4, 5, 6, 7))
#define HI(S) __builtin_bit_cast(bf16x16, __builtin_shufflevector(S, S, 8, 9, 10, 11, 12, 13, 14, 15))
#define SBAR() __builtin_amdgcn_sched_barrier(0)
#define WAIT_BAR(N) asm volatile("s_waitcnt vmcnt(" #N ") lgkmcnt(0)\n\ts_barrier" ::: "memory")
#define LGKM0() __builtin_amdgcn_s_waitcnt(0xC07F)
#define LDS_BAR() do { asm volatile("" ::: "memory"); LGKM0(); asm volatile("s_barrier" ::: "memory"); } while (0)

#define STAMP(th, i) do {} while (0)
#define STOUT(lo, hi) do {} while (0)
#define SVP
#define SVA
#define TS(i) do {} while (0)
#define TSOUT() do {} while (0)
constexpr int D = 1024;
constexpr float LOG2E = 1.4426950408889634f;
constexpr float QSCALE = 0.17677669529663687f * LOG2E;

constexpr unsigned WS_QA  = 0;
constexpr unsigned WS_QB  = WS_QA + (8u << 20);
constexpr unsigned WS_OA  = WS_QB + (4u << 20);
constexpr unsigned WS_OB  = WS_OA + (4u << 20);
constexpr unsigned WS_KV  = WS_OB + (4u << 20);
constexpr unsigned WS_KVP = WS_KV + (256u << 10);
constexpr unsigned WS_ZP  = WS_KVP + (2u << 20);
constexpr unsigned WS_FLG = WS_ZP + (64u << 10);
constexpr unsigned WS_END = WS_FLG + (16u << 10);
constexpr int F_K = 0, F_Q = 128, F_OA = 256, F_OB = 512, F_KV = 768, F_EPOCH = 1024;

DI unsigned pk2(float lo, float hi) {
  f32x2 v = {lo, hi};
  return __builtin_bit_cast(unsigned, __builtin_convertvector(v, bf16x2));
}
template <int SS> DI bf16x8 pack8(const f32x16& x) {
  u32x4 p = {pk2(x[8 * SS + 0], x[8 * SS + 1]), pk2(x[8 * SS + 2], x[8 * SS + 3]), pk2(x[8 * SS + 4], x[8 * SS + 5]), pk2(x[8 * SS + 6], x[8 * SS + 7])};
  return __builtin_bit_cast(bf16x8, p);
}
DI u32x4 pack44(const f32x4& a, const f32x4& b) { return u32x4{pk2(a[0], a[1]), pk2(a[2], a[3]), pk2(b[0], b[1]), pk2(b[2], b[3])}; }
DI void to32(u32x4 x, u32x4 y, bf16x8 (&f)[2]) {
#pragma unroll
  for (int i = 0; i < 4; ++i) { auto r = __builtin_amdgcn_permlane16_swap(x[i], y[i], false, false); x[i] = r[0]; y[i] = r[1]; }
  f[0] = __builtin_bit_cast(bf16x8, x); f[1] = __builtin_bit_cast(bf16x8, y);
}
DI bf16x8 lds_r128(unsigned a) { return *(const AS3 bf16x8*)a; }
DI bf16x8 lds_tr2(unsigned a0, unsigned a1) {
  s16x4 lo = __builtin_amdgcn_ds_read_tr16_b64_v4i16((AS3 s16x4*)a0);
  s16x4 hi = __builtin_amdgcn_ds_read_tr16_b64_v4i16((AS3 s16x4*)a1);
  s16x8 r = __builtin_shufflevector(lo, hi, 0, 1, 2, 3, 4, 5, 6, 7);
  return __builtin_bit_cast(bf16x8, r);
}
DI bf16x8 lds_trp(const AS3 char* p, int d) {
  s16x4 lo = __builtin_amdgcn_ds_read_tr16_b64_v4i16((AS3 s16x4*)p);
  s16x4 hi = __builtin_amdgcn_ds_read_tr16_b64_v4i16((AS3 s16x4*)(p + d));
  return __builtin_bit_cast(bf16x8, __builtin_shufflevector(lo, hi, 0, 1, 2, 3, 4, 5, 6, 7));
}
DI rsrc_t mk_rsrc(const void* p, unsigned bytes) { return __builtin_amdgcn_make_buffer_rsrc((void*)p, (short)0, (int)bytes, 0x00020000); }
DI void set_m0(unsigned v) { asm volatile("s_mov_b32 m0, %0\n\ts_nop 0" ::"s"(v) : "memory"); }
template <int IMM> DI void dma16(unsigned voff, rsrc_t srd, unsigned soff) {
  asm volatile("buffer_load_dwordx4 %0, %1, %2 offen offset:%3 lds" ::"v"(voff), "s"(srd), "s"(soff), "i"(IMM) : "memory");
}
template <int IMM> DI void dma16d(unsigned voff, rsrc_t srd, unsigned soff) {
  asm volatile("buffer_load_dwordx4 %0, %1, %2 offen offset:%3 sc1 lds" ::"v"(voff), "s"(srd), "s"(soff), "i"(IMM) : "memory");
}
DI void dma4k(unsigned lds, unsigned voff, rsrc_t srd, unsigned soff) {
  set_m0(lds); dma16d<0>(voff, srd, soff); dma16d<1024>(voff, srd, soff); dma16d<2048>(voff, srd, soff); dma16d<3072>(voff, srd, soff);
}
template <class T> DI void st16(rsrc_t r, unsigned voff, T v) { __builtin_amdgcn_raw_buffer_store_b128(__builtin_bit_cast(u32x4, v), r, (int)voff, 0, 16); }
DI void st8(rsrc_t r, unsigned voff, u32x2 v) { __builtin_amdgcn_raw_buffer_store_b64(v, r, (int)voff, 0, 16); }
DI void st4(rsrc_t r, unsigned voff, float v) { __builtin_amdgcn_raw_buffer_store_b32(__float_as_uint(v), r, (int)voff, 0, 16); }
DI u32x4 ld16(rsrc_t r, unsigned voff) { return __builtin_amdgcn_raw_buffer_load_b128(r, (int)voff, 0, 16); }
DI float ld4(rsrc_t r, unsigned voff) { return __uint_as_float(__builtin_amdgcn_raw_buffer_load_b32(r, (int)voff, 0, 16)); }
DI void post(char* ws, int f, u64 tag) { __hip_atomic_store((AS1 u64*)(ws + WS_FLG) + f, tag, __ATOMIC_RELAXED, __HIP_MEMORY_SCOPE_AGENT); }
DI void poll(char* ws, int f, u64 tag) {
#pragma unroll 1
  for (int it = 0; it < (1 << 17); ++it) {
    const u64 v = __hip_atomic_load((AS1 u64*)(ws + WS_FLG) + f, __ATOMIC_RELAXED, __HIP_MEMORY_SCOPE_AGENT);
    if (__all(v == tag)) break;
    __builtin_amdgcn_s_sleep(2);
  }
  asm volatile("" ::: "memory");
}
DI void pf_line(unsigned& sink, const void* p) { asm volatile("global_load_dword %0, %1, off" : "+v"(sink) : "v"(p) : "memory"); }
#define PIN(x) asm volatile("" : "+v"(x))
DI void pf_bin(unsigned bin, unsigned voff, const void* base) {
  asm volatile("s_mov_b32 m0, %0\n\ts_nop 0\n\tglobal_load_lds_dword %1, %2" ::"s"(bin), "v"(voff), "s"(base) : "memory");
}
DI float one(float v) { asm("" : "+v"(v)); return v; }
DI f32x4 add4(f32x4 a, f32x4 b) { return f32x4{one(a[0] + b[0]), one(a[1] + b[1]), one(a[2] + b[2]), one(a[3] + b[3])}; }
DI float xhalf_max(float v) {
  auto r = __builtin_amdgcn_permlane32_swap(__float_as_uint(v), __float_as_uint(v), false, false);
  return fmaxf(__uint_as_float(r[0]), __uint_as_float(r[1]));
}
DI float xhalf_min(float v) {
  auto r = __builtin_amdgcn_permlane32_swap(__float_as_uint(v), __float_as_uint(v), false, false);
  return fminf(__uint_as_float(r[0]), __uint_as_float(r[1]));
}
DI float xhalf_sum(float v) {
  auto r = __builtin_amdgcn_permlane32_swap(__float_as_uint(v), __float_as_uint(v), false, false);
  return __uint_as_float(r[0]) + __uint_as_float(r[1]);
}
DI float max16(const f32x16& x) {
  float a = fmaxf(fmaxf(x[0], x[1]), x[2]), b = fmaxf(fmaxf(x[3], x[4]), x[5]);
  a = fmaxf(fmaxf(a, x[6]), x[7]);   b = fmaxf(fmaxf(b, x[8]), x[9]);
  a = fmaxf(fmaxf(a, x[10]), x[11]); b = fmaxf(fmaxf(b, x[12]), x[13]);
  a = fmaxf(fmaxf(a, x[14]), x[15]);
  return fmaxf(a, b);
}
DI float min16(const f32x16& x) {
  float a = fminf(fminf(x[0], x[1]), x[2]), b = fminf(fminf(x[3], x[4]), x[5]);
  a = fminf(fminf(a, x[6]), x[7]);   b = fminf(fminf(b, x[8]), x[9]);
  a = fminf(fminf(a, x[10]), x[11]); b = fminf(fminf(b, x[12]), x[13]);
  a = fminf(fminf(a, x[14]), x[15]);
  return fminf(a, b);
}
template <bool NORM> DI void feat_map(const f32x16& p, f32x16& ep, f32x16& em) {
  const float mx = xhalf_max(max16(p)), mn = xhalf_min(min16(p));
  const f32x2 a = {-mx * LOG2E, -mx * LOG2E}, b = {mn * LOG2E, mn * LOG2E}, c = {LOG2E, LOG2E};
  f32x2 sp = {0.f, 0.f}, sm = {0.f, 0.f};
#pragma unroll
  for (int i = 0; i < 16; i += 2) {
    const f32x2 x = {p[i], p[i + 1]};
    const f32x2 u = __builtin_elementwise_fma(x, c, a), v = __builtin_elementwise_fma(x, -c, b);
    const f32x2 eu = {__builtin_amdgcn_exp2f(u[0]), __builtin_amdgcn_exp2f(u[1])}, ev = {__builtin_amdgcn_exp2f(v[0]), __builtin_amdgcn_exp2f(v[1])};
    ep[i] = eu[0]; ep[i + 1] = eu[1]; em[i] = ev[0]; em[i + 1] = ev[1];
    if (i == 0) { sp = eu; sm = ev; } else { sp += eu; sm += ev; }
  }
  if (NORM) {
    const float rp = __builtin_amdgcn_rcpf(xhalf_sum(sp[0] + sp[1])), rm = __builtin_amdgcn_rcpf(xhalf_sum(sm[0] + sm[1]));
    ep *= rp; em *= rm;
  }
}
DI bf16x8 row_sum_sel(int lane) {
  const unsigned u = ((lane & 11) == 0 && (((lane >> 2) ^ (lane >> 4)) & 1) == 0) ? 0x3f803f80u : 0u;
  return __builtin_bit_cast(bf16x8, u32x4{u, u, u, u});
}

DI int b_rot(int x) { return 8 * (((x >> 1) ^ (x >> 2)) & 1); }

constexpr int LDS_BYTES = 163840;
constexpr int P1_WOFF = 32768, P1_EOFF = 49152, P1_STAGE = 53248, P1_ZRED = 3 * P1_STAGE, P1_CNT = P1_ZRED + 2048, P1_GO = P1_CNT + 16, P1_BIN = P1_CNT + 256, P1_PHI = 65536;
constexpr int P2_Q = 131072, P2_KVF = 147456, P2_ZB = 151552;
constexpr int P2_XO = 32768, P2_XL = 0;
constexpr int NO = 4, NW = 6, NF = 3;
constexpr int P3_O = 0, P3_W = NO * 16384;
DI constexpr int p3_wslot(int t) { return P3_W + ((t + NW - 4) % NW) * 16384; }
DI constexpr int p3_inflight(int t) {
  int n = 0, after = 0;
  for (int j = 0; j < NO; ++j) { n += 4; if (j == t + 1) after = n; }
  for (int u = 0; u < t; ++u) {
    if (u + NW < 16) { n += 4; if (u + NW == t + 1) after = n; }
    if (u + NO < 16) { n += 4; if (u + NO == t + 1) after = n; }
  }
  return n - after;
}
template <int N> DI void wait_bar() { asm volatile("s_waitcnt vmcnt(%0) lgkmcnt(0)\n\ts_barrier" ::"i"(N) : "memory"); }

DI void mac16(f32x4& c, bf16x8 a, bf16x8 b) { asm volatile("v_mfma_f32_16x16x32_bf16 %0, %1, %2, %0" : "+v"(c) : "v"(a), "v"(b)); }
DI void acc_fence(f32x4 (&c)[4][4]) {
  asm volatile("s_nop 7" : "+v"(c[0][0]), "+v"(c[0][1]), "+v"(c[0][2]), "+v"(c[0][3]), "+v"(c[1][0]), "+v"(c[1][1]), "+v"(c[1][2]), "+v"(c[1][3]),
                           "+v"(c[2][0]), "+v"(c[2][1]), "+v"(c[2][2]), "+v"(c[2][3]), "+v"(c[3][0]), "+v"(c[3][1]), "+v"(c[3][2]), "+v"(c[3][3]));
}
#define MT_OF(i) (((((i) >> 2) ^ ((i) >> 3)) & 1) * 2 + ((((i) >> 1) ^ (i)) & 1))
#define NT_OF(i) ((((i) >> 3) & 1) * 2 + (((i) >> 1) & 1))
#define RD_AT(i) ((i) < 12 && (i) % 3 != 2 ? (i) / 3 * 2 + (i) % 3 : -1)
template <int TYPE>
DI void p1_compute(unsigned lds0, char* __restrict__ ws, rsrc_t sws, int rb, int ct, int w, int lane SVP) {
  const int wm = w & 3, wn = w >> 2, r32 = lane & 31, h = lane >> 5;
  if (w == 0 && lane == 0) *(AS3 unsigned*)(lds0 + P1_GO) = 0u;

  typedef const AS3 char* lp;
  const int r16 = lane & 15, kg = lane >> 4, q4 = r16 >> 2, p4 = lane & 3, chalf = kg & 1;
  lp xa[2][2], wa[2][4];
#pragma unroll
  for (int kk = 0; kk < 2; ++kk) {
    xa[0][kk] = (lp)(lds0 + (wm * 64 + r16) * 128 + (((4 * kk + kg) ^ (r16 >> 1)) << 4));
    xa[1][kk] = xa[0][kk] + 2 * P1_STAGE; PIN(xa[0][kk]); PIN(xa[1][kk]);
  }
#pragma unroll
  for (int nt = 0; nt < 4; ++nt) {
    wa[0][nt] = (lp)(lds0 + P1_WOFF + (8 * kg + q4) * 256 + (((wn * 4 + nt) ^ (2 * q4 + chalf)) << 5) + p4 * 8);
    wa[1][nt] = wa[0][nt] + 2 * P1_STAGE; PIN(wa[0][nt]); PIN(wa[1][nt]);
  }

  f32x4 acc[4][4] = {};
  bf16x8 xf[2][4], wfr[2][4];
  auto rd = [&](int set, int b, int kk, int i) {
    constexpr int ord[8] = {0, 4, 1, 5, 2, 3, 6, 7};
    const int f = ord[i];
    if (f < 4) xf[set][f] = *(const AS3 bf16x8*)(xa[b >> 1][kk] + (b & 1) * P1_STAGE + f * 2048);
    else       wfr[set][f - 4] = lds_trp(wa[b >> 1][f - 4] + (b & 1) * P1_STAGE + kk * 8192, 1024);
  };
  auto sub = [&](int set, int b, int kk) {
#pragma unroll
    for (int i = 0; i < 16; ++i) {
      const int mt = MT_OF(i), nt = NT_OF(i);
      SBAR();
      if (TYPE == 0 && nt >= 2) mac16(acc[mt][nt], xf[set][mt], wfr[set][nt]);
      else                      mac16(acc[mt][nt], wfr[set][nt], xf[set][mt]);
      SBAR();
      if (b >= 0 && RD_AT(i) >= 0) rd(set ^ 1, b, kk, RD_AT(i));
    }
    SBAR();
  };
  auto stg = [&](int b, int nb) { sub(0, b, 1); LDS_BAR(); sub(1, nb, 0); };
  acc_fence(acc);
  LDS_BAR();
  STAMP(0, 1);
#pragma unroll
  for (int i = 0; i < 8; ++i) rd(0, 0, 0, i);
#pragma unroll 1
  for (int t = 0; t < 5; ++t) { stg(0, 1); stg(1, 2); stg(2, 0); }
  stg(0, -1);
  acc_fence(acc);
  STAMP(0, 2);
  bf16x8 wf[2][2];
  if constexpr (TYPE <= 1) {
    const unsigned a = lds0 + P1_PHI + (TYPE == 0 ? wn : 2 * wn) * 2048 + (8 * h + q4) * 64 + chalf * 32 + p4 * 8;
#pragma unroll
    for (int n = 0; n <= TYPE; ++n)
#pragma unroll
      for (int s = 0; s < 2; ++s) wf[n][s] = lds_tr2(a + n * 2048 + s * 256, a + n * 2048 + s * 256 + 1024);
  }
  auto ft32 = [&](int ms, int ns, bf16x8 (&f)[2]) { to32(pack44(acc[2 * ms][2 * ns], acc[2 * ms][2 * ns + 1]), pack44(acc[2 * ms + 1][2 * ns], acc[2 * ms + 1][2 * ns + 1]), f); };

  if (TYPE >= 2) {
#pragma unroll
    for (int mt = 0; mt < 4; ++mt)
#pragma unroll
      for (int ns = 0; ns < 2; ++ns) {
        const unsigned p = WS_QB + ((rb * 8 + wm * 2 + (mt >> 1)) * 32 + ct * 4 + wn * 2 + ns) * 2048 + chalf * 1024 + h * 512 + (mt & 1) * 256 + r16 * 16;
        st16(sws, p, pack44(acc[mt][2 * ns] * QSCALE, acc[mt][2 * ns + 1] * QSCALE));
      }
    WAIT_BAR(0);
  } else if (TYPE == 1) {
    f32x16 pq[2][2];
#pragma unroll
    for (int ms = 0; ms < 2; ++ms)
#pragma unroll
      for (int ns = 0; ns < 2; ++ns) {
        bf16x8 f[2];
        ft32(ms, ns, f);
        pq[ms][ns] = MFMA32(wf[ns][0], f[0], f32x16{});
        pq[ms][ns] = MFMA32(wf[ns][1], f[1], pq[ms][ns]);
      }
#pragma unroll
    for (int ms = 0; ms < 2; ++ms)
#pragma unroll
      for (int ns = 0; ns < 2; ++ns) {
        f32x16 ep, em;
        feat_map<false>(pq[ms][ns], ep, em);
        const unsigned p = WS_QA + ((rb * 8 + wm * 2 + ms) * 32 + ct * 4 + wn * 2 + ns) * 4096 + lane * 16;
        st16(sws, p, pack8<0>(ep)); st16(sws, p + 1024, pack8<1>(ep)); st16(sws, p + 2048, pack8<0>(em)); st16(sws, p + 3072, pack8<1>(em));
      }
  } else {
    const unsigned img = lds0 + w * 8192;
    const bf16x8 sel = row_sum_sel(lane);
    f32x16 pk[2], kv[2] = {};
    f32x4 zz[2] = {};
    bf16x8 vf[2][2], af[2][2];
#pragma unroll
    for (int ms = 0; ms < 2; ++ms) {
      bf16x8 f[2];
      ft32(ms, 0, f);
      pk[ms] = MFMA32(wf[0][0], f[0], f32x16{});
      pk[ms] = MFMA32(wf[0][1], f[1], pk[ms]);
    }
#pragma unroll
    for (int ms = 0; ms < 2; ++ms) { to32(pack44(acc[2 * ms][2], acc[2 * ms + 1][2]), pack44(acc[2 * ms][3], acc[2 * ms + 1][3]), vf[ms]); PIN(vf[ms][0]); PIN(vf[ms][1]); }
    float off[4];
#pragma unroll
    for (int ms = 0; ms < 2; ++ms) { off[2 * ms] = -LOG2E * xhalf_max(max16(pk[ms])); off[2 * ms + 1] = LOG2E * xhalf_min(min16(pk[ms])); }
    const unsigned wr = img + 4096 + r32 * 64, sw = (r32 >> 1) & 7;
    const int t0 = 8 * h + q4, u8 = 4 * chalf + p4;
    const unsigned rt[2] = {img + 4096 + t0 * 64 + ((u8 ^ (t0 >> 1)) << 3), img + 4096 + (t0 + 4) * 64 + ((u8 ^ ((t0 >> 1) + 2)) << 3)};
    auto put = [&](int sg) {
#pragma unroll
      for (int g = 0; g < 4; ++g) *(AS3 f32x4*)(img + sg * 4096 + g * 1024 + lane * 16) = f32x4{kv[sg][4 * g], kv[sg][4 * g + 1], kv[sg][4 * g + 2], kv[sg][4 * g + 3]};
    };
    f32x16 e[2];
    float rs[2];
    auto mm = [&](int u, int j) {
      if (j & 1) zz[u & 1] = MFMA16(sel, af[u & 1][j >> 1], zz[u & 1]); else kv[u & 1] = MFMA32(af[u & 1][j >> 1], vf[u >> 1][j >> 1], kv[u & 1]);
    };
    auto exps = [&](int u, int mu) {
      const f32x16& p = pk[u >> 1];
      const float c = (u & 1) ? -LOG2E : LOG2E;
      f32x2 sm;
#pragma unroll
      for (int i = 0; i < 16; i += 2) {
        if (mu >= 0 && i >= 8) { SBAR(); mm(mu, (i - 8) >> 1); SBAR(); }
        const f32x2 v = __builtin_elementwise_fma(f32x2{p[i], p[i + 1]}, f32x2{c, c}, f32x2{off[u], off[u]});
        const f32x2 ev = {__builtin_amdgcn_exp2f(v[0]), __builtin_amdgcn_exp2f(v[1])};
        e[u & 1][i] = ev[0]; e[u & 1][i + 1] = ev[1];
        if (i == 0) sm = ev; else sm += ev;
      }
      rs[u & 1] = __builtin_amdgcn_rcpf(xhalf_sum(sm[0] + sm[1]));
    };
    auto rest = [&](int u) {
      const f32x16 n = e[u & 1] * rs[u & 1];
#pragma unroll
      for (int g = 0; g < 4; ++g) *(AS3 u32x2*)(wr + (u & 1) * 2048 + (((2 * g + h) ^ sw) << 3)) = u32x2{pk2(n[4 * g], n[4 * g + 1]), pk2(n[4 * g + 2], n[4 * g + 3])};
      asm volatile("" ::: "memory");
#pragma unroll
      for (int s = 0; s < 2; ++s) af[u & 1][s] = lds_tr2(rt[s] + (u & 1) * 2048, rt[s] + (u & 1) * 2048 + 1024);
      asm volatile("" ::: "memory");
      SBAR();
    };
    exps(0, -1); exps(1, -1); rest(0);
    exps(2, 0);  rest(1);
    exps(3, 1);  rest(2);
    rest(3);
    mm(2, 0); mm(2, 2); mm(3, 0); mm(3, 2); SBAR(); mm(2, 1); mm(2, 3); mm(3, 1); mm(3, 3);
    put(0); put(1);
    if (lane < 32) {
      const unsigned a = lds0 + P1_ZRED + w * 256 + ((lane >> 2) & 1) * 64 + (lane >> 3) * 16 + (lane & 3) * 4;
      *(AS3 float*)a = zz[0][0]; *(AS3 float*)(a + 128) = zz[1][0];
    }
  }
  STAMP(0, 3);
}

DI void attn_pass_fast(unsigned lds0, unsigned qa, bf16x8 (&q)[2][2], bf16x8 sel, int kh, int lane, f32x16 (&O)[2], f32x4 (&L)[2], unsigned& tv SVP) {
  typedef const AS3 char* lp;
  lp l0 = (lp)(lds0 + lane * 16), la = l0 + kh * 16384;
#define RD(p) (*(const AS3 bf16x8*)(p))
  f32x16 negm[2], S0, S1 = {};
  bf16x8 vb = bf16x8{};
  int odd = 0xDDDD;
  O[0] = f32x16{}; O[1] = f32x16{}; L[0] = f32x4{}; L[1] = f32x4{};
  unsigned a0 = lds0 + lane * 16 + 98304, a1 = a0 + kh * 16384, qb = qa ^ 2048;
  PIN(O[0]); PIN(O[1]); PIN(L[0]); PIN(L[1]); PIN(S1); PIN(vb); PIN(odd); PIN(sel); PIN(a0); PIN(a1); PIN(qa); PIN(qb);
  LDS_BAR();
  STAMP(0, 4);
  TS(0);
  if (kh) __builtin_amdgcn_s_setprio(1);
  const bf16x8 ra = lds_r128(a0);
  q[0][0] = lds_r128(qa); q[1][0] = lds_r128(qb);
  const bf16x8 rb = lds_r128(a0 + 1024);
  q[0][1] = lds_r128(qa + 1024); q[1][1] = lds_r128(qb + 1024);
  bf16x8 ka = lds_r128(a1), kb = lds_r128(a1 + 1024), va = lds_r128(a1 + 2048);
  SBAR();
  negm[0] = MFMA32(ra, q[0][0], f32x16{}); negm[1] = MFMA32(ra, q[1][0], f32x16{});
  negm[0] = MFMA32(rb, q[0][1], negm[0]);  negm[1] = MFMA32(rb, q[1][1], negm[1]);
#pragma unroll
  for (int r = 0; r < 2; ++r) {
    const float m = xhalf_max(max16(negm[r]));
#pragma unroll
    for (int i = 0; i < 16; ++i) negm[r][i] = -m;
    PIN(negm[r]);
    SBAR();
    if (r == 0) { S0 = MFMA32(ka, q[0][0], negm[0]); S0 = MFMA32(kb, q[0][1], S0); PIN(S0); SBAR(); }
  }
#define EXN(SC, i, n) do { _Pragma("unroll") for (int k_ = 0; k_ < n; ++k_) SC[i + k_] = __builtin_amdgcn_exp2f(SC[i + k_]); PIN(SC); } while (0)
#define MM(D, E) do { SBAR(); D = E; PIN(D); SBAR(); } while (0)
#define TILE(TA, TN) do { \
    SBAR();                                                             EXN(S0, 0, 2);  \
    MM(O[1], SM32(vb, HI(S1), O[1]));       vb = RD((TA) + 3072);       EXN(S0, 2, 3);  \
    MM(L[1], SM16(sel, HI(S1), L[1]));                                  EXN(S0, 5, 2);  \
    MM(S1, MFMA32(ka, q[1][0], negm[1]));   ka = RD(TN);                EXN(S0, 7, 3);  \
    MM(S1, MFMA32(kb, q[1][1], S1));        kb = RD((TN) + 1024);       EXN(S0, 10, 3); \
    MM(O[0], SM32(va, LO(S0), O[0]));                                   EXN(S0, 13, 3); \
    MM(L[0], SM16(sel, LO(S0), L[0]));                                  EXN(S1, 0, 2);  \
    MM(O[0], SM32(vb, HI(S0), O[0]));                                   EXN(S1, 2, 3);  \
    MM(L[0], SM16(sel, HI(S0), L[0]));                                  EXN(S1, 5, 2);  \
    MM(S0, MFMA32(ka, q[0][0], negm[0]));                               EXN(S1, 7, 3);  \
    MM(S0, MFMA32(kb, q[0][1], S0));                                    EXN(S1, 10, 3); \
    MM(O[1], SM32(va, LO(S1), O[1]));       va = RD((TN) + 2048);       EXN(S1, 13, 3); \
    MM(L[1], SM16(sel, LO(S1), L[1])); } while (0)
  TS(1);
#pragma unroll 1
  for (int c = 0; c < 8; ++c) {
    const lp cb = la + ((3 - c) & 3) * 32768, nb = la + ((2 - c) & 3) * 32768;
    TILE(cb, cb + 4096);
    TILE(cb + 4096, cb + 8192);
    SBAR(); if (kh) __builtin_amdgcn_s_setprio(0);
    TILE(cb + 8192, cb + 12288);
    SBAR(); TS(2 + 2 * c);
    asm volatile("s_waitcnt vmcnt(0)\n\ts_barrier" ::: "memory"); TS(3 + 2 * c);
    if (kh) __builtin_amdgcn_s_setprio(1);
    TILE(cb + 12288, nb);
  }
  SBAR();
  O[1] = SM32(vb, HI(S1), O[1]); L[1] = SM16(sel, HI(S1), L[1]);
  __builtin_amdgcn_s_setprio(0);
  TS(18);
#undef TILE
#undef EXN
#undef MM
#undef RD
}

DI void attn_pass_safe(rsrc_t sws, const bf16x8 q0, const bf16x8 q1, const bf16x8 sel, int lane, f32x16& O, f32x4& L) {
  O = f32x16{}; L = f32x4{};
  float m = -INFINITY;
  const int odd = 0xDDDD;
#pragma unroll 1
  for (int t = 0; t < 64; ++t) {
    bf16x8 a[4];
#pragma unroll
    for (int j = 0; j < 4; ++j) a[j] = __builtin_bit_cast(bf16x8, ld16(sws, WS_KV + t * 4096 + j * 1024 + lane * 16));
    f32x16 Sc = {};
    Sc = MFMA32(a[0], q0, Sc); Sc = MFMA32(a[1], q1, Sc);
    const float mn = fmaxf(m, xhalf_max(max16(Sc)));
    const float al = __builtin_amdgcn_exp2f(m - mn);
    m = mn;
#pragma unroll
    for (int r = 0; r < 16; ++r) { O[r] *= al; Sc[r] = __builtin_amdgcn_exp2f(Sc[r] - mn); }
    L *= al;
    O = SM32(a[2], LO(Sc), O); L = SM16(sel, LO(Sc), L);
    O = SM32(a[3], HI(Sc), O); L = SM16(sel, HI(Sc), L);
  }
}

DI void helper_program(unsigned lds0, const unsigned short* __restrict__ X, const unsigned short* __restrict__ W, const unsigned short* __restrict__ E,
                       const unsigned short* __restrict__ woa, const unsigned short* __restrict__ wob, char* __restrict__ ws, rsrc_t sws,
                       const char* pc0, unsigned co, int type, int rb, int ct, int x, int idx, int lw, int lane) {
  unsigned sv = 0; (void)sv;
  __builtin_amdgcn_s_setprio(3);
  u64 e0;
  const unsigned vl = lane * 16;
  {
    const bool ext = type >= 2;
    const unsigned ldw = ext ? 2048u : 6144u;
    const rsrc_t sx = mk_rsrc(X + (size_t)rb * 256 * D, 256u * 2048u);
    const rsrc_t sw = mk_rsrc(W, 1024u * ldw);
    const rsrc_t se = mk_rsrc(E, 1024u * 64u);
    unsigned xv[8], wv[4];
#pragma unroll
    for (int j = 0; j < 8; ++j) {
      const int row = 8 * (8 * lw + j) + (lane >> 3), c = (lane & 7) ^ ((row >> 1) & 7);
      xv[j] = row * 2048 + c * 16 - (j & 3) * 1024;
    }
#pragma unroll
    for (int j = 0; j < 4; ++j) {
      const int k = 4 * (4 * lw + j) + (lane >> 4), c = (lane & 15) ^ ((lane >> 4) << 2) ^ (j & 2);
      const int col = (type == 0) ? 1024 + ((c >> 2) & 1) * 1024 + (2 * ct + (c >> 3)) * 32 + (c & 3) * 8 : ct * 128 + c * 8;
      wv[j] = k * ldw + col * 2 - j * 1024;
    }
    const int erow = 64 * (ct >> 1) + (lane & 31);
    const unsigned exa = (erow * 128 + (((lane >> 5) ^ ((erow >> 1) & 7)) << 4)) ^ (lw * 32);
    const unsigned ewa = P1_EOFF + (16 * lw + 8 * (lane >> 5) + ((lane & 15) >> 2)) * 64 + ((lane >> 4) & 1) * 32 + (lane & 3) * 8;
    f32x16 eacc[2] = {};
    bf16x8 ex[2], ew;
    auto kvr = [&](unsigned buf) {
      if (ext) { ex[0] = lds_r128(buf + exa); ew = lds_tr2(buf + ewa, buf + ewa + 256); ex[1] = lds_r128(buf + exa + 4096); }
    };
    auto kvm = [&]() {
      if (ext)
        asm volatile("s_cmp_eq_u32 %5, 2\n\ts_cbranch_scc1 1f\n\t"
                     "v_mfma_f32_32x32x16_bf16 %0, %2, %4, %0\n\tv_mfma_f32_32x32x16_bf16 %1, %3, %4, %1\n\ts_branch 2f\n"
                     "1:\tv_mfma_f32_32x32x16_bf16 %0, %4, %2, %0\n\tv_mfma_f32_32x32x16_bf16 %1, %4, %3, %1\n2:"
                     : "+v"(eacc[0]), "+v"(eacc[1]) : "v"(ex[0]), "v"(ex[1]), "v"(ew), "s"(type) : "scc");
    };
    auto kvs = [&](unsigned buf) { kvr(buf); kvm(); };
    auto stage = [&](int t0, unsigned buf, bool kv, unsigned kb) {
      const int t = (t0 + (ext ? b_rot(x) : 2 * x)) & 15;
      const unsigned sox = t * 128, sow = t * 64 * ldw;
      set_m0(buf + P1_WOFF + lw * 4096);
      dma16<0>(wv[0], sw, sow); dma16<1024>(wv[1], sw, sow); dma16<2048>(wv[2], sw, sow); dma16<3072>(wv[3], sw, sow);
      set_m0(buf + lw * 8192);
      dma16<0>(xv[0], sx, sox); dma16<1024>(xv[1], sx, sox); dma16<2048>(xv[2], sx, sox); dma16<3072>(xv[3], sx, sox);
      if (kv) kvr(kb);
      set_m0(buf + lw * 8192 + 4096);
      dma16<0>(xv[4], sx, sox); dma16<1024>(xv[5], sx, sox); dma16<2048>(xv[6], sx, sox); dma16<3072>(xv[7], sx, sox);
      if (ext) { set_m0(buf + P1_EOFF + lw * 1024); dma16<0>(vl, se, t * 4096 + lw * 1024); }
      if (kv) kvm();
    };
    stage(0, lds0, false, 0); stage(1, lds0 + P1_STAGE, false, 0);
    if (ext) WAIT_BAR(13); else WAIT_BAR(12);
    stage(2, lds0 + 2 * P1_STAGE, true, lds0);
    unsigned buf = lds0;
#pragma unroll 1
    for (int t = 0; t < 13; ++t) {
      if (ext) WAIT_BAR(13); else WAIT_BAR(12);
      const unsigned nb = (buf == lds0 + 2 * P1_STAGE) ? lds0 : buf + P1_STAGE;
      stage(t + 3, buf, true, nb);
      buf = nb;
    }
    if (ext) WAIT_BAR(13); else WAIT_BAR(12);
    e0 = __hip_atomic_load((AS1 u64*)(ws + WS_FLG) + F_EPOCH, __ATOMIC_RELAXED, __HIP_MEMORY_SCOPE_AGENT);
    if (lw == 3) pf_bin(lds0 + P1_BIN, co, pc0);
    if (!ext && (type == 1 || lw < 2)) {
      set_m0(lds0 + P1_PHI + lw * 2048);
      const unsigned so = ((type == 0 ? 2 * ct : 4 * ct) + lw) * 2048;
      dma16<0>(vl, se, so); dma16<1024>(vl, se, so);
    }
    kvs(lds0 + 2 * P1_STAGE);
    WAIT_BAR(0);
    kvs(lds0);
    asm volatile("s_nop 15\n\ts_nop 7" : "+v"(eacc[0]), "+v"(eacc[1]));
    const unsigned red = lds0 + P1_STAGE + vl;
    if (ext) {
#pragma unroll
      for (int i = 0; i < 8; ++i) {
        const f32x16& a = eacc[i >> 2];
        *(AS3 f32x4*)(red + lw * 8192 + i * 1024) = f32x4{a[4 * (i & 3)], a[4 * (i & 3) + 1], a[4 * (i & 3) + 2], a[4 * (i & 3) + 3]};
      }
    }
    WAIT_BAR(0);
    if (ext) {
#pragma unroll
      for (int n = 0; n < 2; ++n) {
        const unsigned a = red + n * 4096 + lw * 1024;
        const f32x4 sm = add4(add4(*(AS3 f32x4*)a, *(AS3 f32x4*)(a + 8192)), add4(*(AS3 f32x4*)(a + 16384), *(AS3 f32x4*)(a + 24576)));
        unsigned p = WS_KV + (rb * 8 + 2 * (ct >> 1) + n) * 4096 + ((lane & 31) + 32 * (lw & 1)) * 16;
        if (type == 3) p += 2048 + (lw >> 1) * 1024 + (lane >> 5) * 8;
        else           p += (lane >> 5) * 1024 + (lw >> 1) * 8;
        st8(sws, p, u32x2{pk2(sm[0], sm[1]), pk2(sm[2], sm[3])});
      }
    }
  }
  const u64 tag = ((u64)(unsigned)__builtin_amdgcn_readfirstlane((unsigned)(e0 >> 32)) << 32 | (unsigned)__builtin_amdgcn_readfirstlane((unsigned)e0))
                  * 0x5851F42D4C957F2Dull + 0x14057B7EF767814Full;
  const bool lead = (lw == 0) && (lane == 0);
  if (type >= 2) { WAIT_BAR(0); if (lead) post(ws, F_KV + rb * 16 + ct, tag); }
  auto chunk = [&](int c) {
    const unsigned l = lds0 + ((3 - c) & 3) * 32768 + lw * 8192, so = WS_KV + ((c + x) & 7) * 32768 + lw * 8192;
    dma4k(l, vl, sws, so); dma4k(l + 4096, vl, sws, so + 4096);
  };
  STAMP(512, 10);
  if (lw == 0) {
    poll(ws, F_KV + x * 16 + (lane & 7), tag);
    __hip_atomic_store((AS3 unsigned*)(lds0 + P1_GO), 1u, __ATOMIC_RELAXED, __HIP_MEMORY_SCOPE_WORKGROUP);
  } else {
    while (__hip_atomic_load((AS3 unsigned*)(lds0 + P1_GO), __ATOMIC_RELAXED, __HIP_MEMORY_SCOPE_WORKGROUP) == 0u) __builtin_amdgcn_s_sleep(1);
    asm volatile("" ::: "memory");
  }
  STAMP(512, 11);
  dma4k(lds0 + P2_Q + lw * 4096, vl, sws, WS_QB + ((8 * x + (idx >> 2)) * 32 + 8 * (idx & 3)) * 2048 + lw * 4096);
  chunk(0);
  WAIT_BAR(0);
  poll(ws, F_KV + (lane >> 3) * 16 + (lane & 7), tag);
  chunk(1);
  if (type == 0) {
#pragma unroll
    for (int j = 0; j < 4; ++j) {
      const unsigned a = lds0 + (lw >> 1) * 32768 + ((lw & 1) * 4 + j) * 1024 + vl;
      const f32x4 sm = add4(add4(*(AS3 f32x4*)a, *(AS3 f32x4*)(a + 8192)), add4(*(AS3 f32x4*)(a + 16384), *(AS3 f32x4*)(a + 24576)));
      st16(sws, WS_KVP + ((2 * ct + (lw >> 1)) * 8 + rb) * 8192 + ((lw & 1) * 4 + j) * 1024 + vl, sm);
    }
    if (lw < 2) {
      const unsigned a = lds0 + P1_ZRED + lw * 1024 + lane * 4;
      st4(sws, WS_ZP + (((2 * ct + lw) * 8 + rb) * 64 + lane) * 4, one(*(AS3 float*)a + *(AS3 float*)(a + 256)) + one(*(AS3 float*)(a + 512) + *(AS3 float*)(a + 768)));
    }
  }
  WAIT_BAR(0);
  chunk(2); chunk(3);
  if (lead && type < 2) post(ws, type == 0 ? F_K + ct * 8 + rb : F_Q + rb * 8 + ct, tag);
  const int br = !(x & 1), rb3 = 4 * (x >> 1) + (idx & 3), cb = idx >> 2;
  const unsigned short* wo = br ? wob : woa;
  unsigned pfs = 0;
  auto warm_wo = [&]() {
    pf_line(pfs, (const char*)wo + idx * 65536 + lw * 16384 + lane * 128);
    pf_line(pfs, (const char*)wo + idx * 65536 + lw * 16384 + 8192 + lane * 128);
  };
  if (lw == 1) *(AS3 unsigned*)(lds0 + P2_ZB + 128 + lane * 4) = lane < 32 ? 0x3f803f80u : 0u;
  WAIT_BAR(8);
  if (br) {
    const int head = 4 * x + (idx & 7), tp = idx >> 3;
    poll(ws, lane < 8 ? F_K + (head >> 1) * 8 + lane : F_Q + (2 * tp + (lane & 1)) * 8 + (head >> 2), tag);
    STAMP(512, 12);
    chunk(4);
    u32x4 kvp[2][8], fq[4][4];
    float zs[8];
    auto ldq = [&](int e) {
#pragma unroll
      for (int j = 0; j < 4; ++j) fq[e][j] = ld16(sws, WS_QA + ((16 * tp + 4 * lw + e) * 32 + head) * 4096 + j * 1024 + vl);
    };
#pragma unroll
    for (int e = 0; e < 2; ++e)
#pragma unroll
      for (int p = 0; p < 8; ++p) kvp[e][p] = ld16(sws, WS_KVP + ((head * 8 + p) * 512 + (2 * lw + e) * 64 + lane) * 16);
#pragma unroll
    for (int p = 0; p < 8; ++p) zs[p] = ld4(sws, WS_ZP + ((head * 8 + p) * 64 + lane) * 4);
    ldq(0); ldq(1);
    asm volatile("s_barrier" ::: "memory");
#pragma unroll
    for (int e = 0; e < 2; ++e) {
#pragma unroll
      for (int p = 0; p < 8; ++p) PIN(kvp[e][p]);
#define F4(v) __builtin_bit_cast(f32x4, v)
      const f32x4 sm = add4(add4(add4(F4(kvp[e][0]), F4(kvp[e][1])), add4(F4(kvp[e][2]), F4(kvp[e][3]))), add4(add4(F4(kvp[e][4]), F4(kvp[e][5])), add4(F4(kvp[e][6]), F4(kvp[e][7]))));
#undef F4
      *(AS3 u32x2*)(lds0 + P2_KVF + lw * 1024 + vl + e * 8) = u32x2{pk2(sm[0], sm[1]), pk2(sm[2], sm[3])};
    }
    {
#pragma unroll
      for (int p = 0; p < 8; ++p) PIN(zs[p]);
      const float z = one(one(zs[0] + zs[1]) + one(zs[2] + zs[3])) + one(one(zs[4] + zs[5]) + one(zs[6] + zs[7]));
      if (lw == 0) *(AS3 unsigned short*)(lds0 + P2_ZB + lane * 2) = (unsigned short)(pk2(z, z) & 0xffffu);
    }
    chunk(5);
    ldq(2); ldq(3);
    const int row = lane & 15, kg = lane >> 4;
    const bool on = (((row >> 2) ^ kg) & 1) == 0;
    const unsigned za = lds0 + P2_ZB + ((on && (row & 3) == 0) ? (kg >> 1) * 32 : (on && (row & 3) == 1) ? 128 : 256);
    const unsigned ka = lds0 + P2_KVF + vl;
    auto otile = [&](int e) {
#define BF(v) __builtin_bit_cast(bf16x8, v)
      f32x16 np = MFMA32(lds_r128(ka), BF(fq[e][0]), f32x16{});        f32x4 dp = MFMA16(lds_r128(za), BF(fq[e][0]), f32x4{});
      np = MFMA32(lds_r128(ka + 1024), BF(fq[e][1]), np);              dp = MFMA16(lds_r128(za + 16), BF(fq[e][1]), dp);
      f32x16 nm = MFMA32(lds_r128(ka + 2048), BF(fq[e][2]), f32x16{}); f32x4 dm = MFMA16(lds_r128(za + 64), BF(fq[e][2]), f32x4{});
      nm = MFMA32(lds_r128(ka + 3072), BF(fq[e][3]), nm);              dm = MFMA16(lds_r128(za + 80), BF(fq[e][3]), dm);
#undef BF
      const float a = __builtin_amdgcn_rcpf(dp[1]), b = __builtin_amdgcn_rcpf(dm[1]);
      const float r = __builtin_amdgcn_rcpf(one(dp[0] * a) + one(dm[0] * b) + 1e-6f), ar = one(a * r), br = one(b * r);
      f32x16 o;
#pragma unroll
      for (int i = 0; i < 16; ++i) o[i] = one(__builtin_fmaf(nm[i], br, one(np[i] * ar)));
      const unsigned p = WS_OA + ((16 * tp + 4 * lw + e) * 32 + head) * 2048 + vl;
      st16(sws, p, pack8<0>(o)); st16(sws, p + 1024, pack8<1>(o));
    };
    WAIT_BAR(16);
#pragma unroll
    for (int j = 0; j < 4; ++j) { PIN(fq[0][j]); PIN(fq[1][j]); }
    chunk(6);
    otile(0); otile(1);
    WAIT_BAR(12);
#pragma unroll
    for (int j = 0; j < 4; ++j) { PIN(fq[2][j]); PIN(fq[3][j]); }
    chunk(7);
    otile(2); otile(3);
    WAIT_BAR(0);
    if (lead) post(ws, F_OA + tp * 32 + head, tag);
    warm_wo();
  } else {
    chunk(4); warm_wo(); WAIT_BAR(10);
    chunk(5); WAIT_BAR(10);
    chunk(6); WAIT_BAR(8);
    chunk(7);
    if (blockIdx.x == 1 && lw == 1) {
      poll(ws, lane, tag); poll(ws, 64 + lane, tag); poll(ws, 128 + lane, tag);
      if (lane == 0) post(ws, F_EPOCH, tag);
    }
    WAIT_BAR(8);
  }
  const rsrc_t sw = mk_rsrc(wo, 1024u * 2048u);
  unsigned wv[4];
#pragma unroll
  for (int j = 0; j < 4; ++j) {
    const int k = 4 * (4 * lw + j) + (lane >> 4), c = (lane & 15) ^ ((lane >> 4) << 2) ^ ((j & 1) << 1);
    wv[j] = k * 2048 + (cb * 128 + c * 8) * 2 - j * 1024;
  }
  const unsigned osrc = (br ? WS_OB : WS_OA) + (rb3 * 4 + lw) * 65536;
  auto wst = [&](int t) {
    const unsigned sow = t * 64 * 2048;
    set_m0(lds0 + p3_wslot(t) + lw * 4096);
    dma16<0>(wv[0], sw, sow); dma16<1024>(wv[1], sw, sow); dma16<2048>(wv[2], sw, sow); dma16<3072>(wv[3], sw, sow);
  };
  auto ost = [&](int t) { dma4k(lds0 + P3_O + (t % NO) * 16384 + lw * 4096, vl, sws, osrc + t * 4096); };
  wst(0); wst(1); wst(2); wst(3);
  if (br) asm volatile("s_barrier" ::: "memory"); else WAIT_BAR(16);
  wst(4); wst(5);
  if (!br && lw == 0) poll(ws, F_OA + (rb3 >> 2) * 32 + (lane & 31), tag);
  asm volatile("s_barrier" ::: "memory");
  asm volatile("s_barrier" ::: "memory");
  asm volatile("s_barrier" ::: "memory");
  if (lead) post(ws, F_OB + x * 32 + idx, tag);
  STAMP(512, 13);
  if (br && lw == 0) poll(ws, F_OB + (rb3 >> 1) * 32 + (rb3 & 1) * 16 + (lane & 15), tag);
  asm volatile("s_barrier" ::: "memory");
  STAMP(512, 14);
#pragma unroll
  for (int t = 0; t < NF; ++t) ost(t);
  wait_bar<4 * (NF - 1)>();
#pragma unroll
  for (int t = NF; t < NO; ++t) ost(t);
  PIN(pfs);
#define RND(t) do { wait_bar<p3_inflight(t)>(); if (t + NW < 16) wst(t + NW); if (t + NO < 16) ost(t + NO); } while (0)
  RND(0); RND(1); RND(2); RND(3); RND(4); RND(5); RND(6); RND(7); RND(8); RND(9); RND(10); RND(11); RND(12); RND(13); RND(14);
#undef RND
  if (lw == 0) STOUT(10, 15);
}

__global__ __launch_bounds__(768, 3) void fused_fwd(const unsigned short* __restrict__ xa, const unsigned short* __restrict__ wqkv,
                                                    const unsigned short* __restrict__ wphi, const unsigned short* __restrict__ woa,
                                                    const unsigned short* __restrict__ xb, const unsigned short* __restrict__ wq,
                                                    const unsigned short* __restrict__ wk, const unsigned short* __restrict__ wv,
                                                    const unsigned short* __restrict__ wob, char* __restrict__ ws, unsigned short* __restrict__ out) {
  __shared__ __attribute__((aligned(1024))) char smem[LDS_BYTES];
  const unsigned lds0 = (unsigned)(uintptr_t)(AS3 char*)smem;
  const int tid = threadIdx.x, lane = tid & 63, w0 = __builtin_amdgcn_readfirstlane(tid >> 6), w = w0 < 4 ? w0 + 8 : w0 - 4;
  const char* pc0; unsigned clen;
  asm volatile("s_getpc_b64 %0\n.Lfw_head:\n\ts_mov_b32 %1, .Lfw_tail-.Lfw_head" : "=s"(pc0), "=s"(clen));
  unsigned co = (((blockIdx.x >> 3) & 7) * 40 + lane) * 128;
  co = lane < 40 && co < clen ? co : 0u;
  SBAR();
  asm volatile("" : "+s"(xa), "+s"(wqkv), "+s"(wphi), "+s"(xb), "+s"(wq), "+s"(wk), "+s"(wv), "+s"(ws));
  const int b = blockIdx.x, x = b & 7, idx = b >> 3;
  const int rb = 4 * ((x >> 1) & 1) + (idx & 3);
  const int ct = idx < 16 ? 8 * (x >> 2) + 4 * (x & 1) + (idx >> 2) : 4 * (x >> 2) + ((idx >> 2) & 3);
  const int type = idx < 16 ? 0 : (x & 1) ? 1 : 2 + (ct & 1);
  const rsrc_t sws = mk_rsrc(ws, WS_END);
  unsigned sv = 0; (void)sv;
  STAMP(0, 0);
  if (__builtin_expect(w >= 8, 1)) {
    helper_program(lds0, type >= 2 ? xb : xa, type >= 2 ? wq : wqkv, type == 3 ? wv : type == 2 ? wk : wphi, woa, wob, ws, sws, pc0, co, type, rb, ct, x, idx, w - 8, lane);
    asm volatile("s_endpgm" ::: "memory");
  }
  if (w == 2) pf_bin(lds0 + P1_BIN, co, pc0);
  if (type == 0 && !(x & 1) && w < 2) pf_bin(lds0 + P1_BIN, (idx * 32 + (lane & 31)) * 128, w ? wv : wk);
  if (type < 2 && w >= 3 && w < 6) {
    constexpr int PF0 = 3, NS = 8 - PF0, PER = (NS * 512 + 47) / 48;
    const int j = (x & 1) * 16 + idx, id = (w == 5 ? j : 2 * j + w - 3) * PER + min(lane, PER - 1);
    unsigned o;
    if (w == 5) { const int st = min(id >> 9, NS - 1), r = id & 511;  o = (64 * ((PF0 + st + b_rot(x)) & 15) + (r >> 3)) * 2048 + (x >> 2) * 1024 + (r & 7) * 128; }
    else        { const int st = min(id >> 10, NS - 1), r = id & 1023; o = (((x >> 1) & 1) * 1024 + r) * 2048 + ((PF0 + st + b_rot(x)) & 15) * 128; }
    pf_bin(lds0 + P1_BIN, o, w == 5 ? wq : xb);
  }
  if (type == 0)      p1_compute<0>(lds0, ws, sws, rb, ct, w, lane SVA);
  else if (type == 1) p1_compute<1>(lds0, ws, sws, rb, ct, w, lane SVA);
  else                p1_compute<2>(lds0, ws, sws, rb, ct, w, lane SVA);

  const int kh = w >> 2, h = lane >> 5;
  {
    const unsigned s0 = (lane & 15) == 0 ? 0x3f803f80u : 0u, s4 = (lane & 15) == 4 ? 0x3f803f80u : 0u;
    const bf16x8 sel = __builtin_bit_cast(bf16x8, u32x4{s0, s0, s4, s4});
    bf16x8 q[2][2];
    f32x16 O[2]; f32x4 L[2];
    unsigned tv = 0;
    attn_pass_fast(lds0, lds0 + P2_Q + (2 * (w & 3) + (kh ^ 1)) * 2048 + lane * 16, q, sel, kh, lane, O, L, tv SVA);
    TSOUT();
    STAMP(0, 5);
    const unsigned xs = lds0 + P2_XO + w * 4096 + lane * 16, xr = lds0 + P2_XO + (w ^ 4) * 4096 + lane * 16;
#pragma unroll
    for (int g = 0; g < 4; ++g) *(AS3 f32x4*)(xs + g * 1024) = f32x4{O[0][4 * g], O[0][4 * g + 1], O[0][4 * g + 2], O[0][4 * g + 3]};
    *(AS3 float*)(lds0 + P2_XL + w * 256 + lane * 4) = L[0][0];
    LDS_BAR();
#pragma unroll
    for (int g = 0; g < 4; ++g) {
      const f32x4 o = *(AS3 f32x4*)(xr + g * 1024);
      O[1][4 * g] += o[0]; O[1][4 * g + 1] += o[1]; O[1][4 * g + 2] += o[2]; O[1][4 * g + 3] += o[3];
    }
    L[1][0] += *(AS3 float*)(lds0 + P2_XL + (w ^ 4) * 256 + lane * 4);
    bool bad = !(L[1][0] < 1.8446744e19f);
#pragma unroll
    for (int i = 0; i < 16; ++i) bad |= !(fabsf(O[1][i]) < 3.0e38f);
    if (__any(bad)) attn_pass_safe(sws, q[1][0], q[1][1], sel, lane, O[1], L[1]);
    const float r = __builtin_amdgcn_rcpf(xhalf_sum(L[1][0]));
    O[1] *= r;
    const unsigned p = WS_OB + ((8 * x + (idx >> 2)) * 32 + 8 * (idx & 3) + 2 * (w & 3) + kh) * 2048 + lane * 16;
    st16(sws, p, pack8<0>(O[1])); st16(sws, p + 1024, pack8<1>(O[1]));
    WAIT_BAR(0);
    STAMP(0, 6);
    asm volatile("s_barrier" ::: "memory");
    SBAR();
  }
  const int br = !(x & 1), rb3 = 4 * (x >> 1) + (idx & 3), cb = idx >> 2;
  const int wm = w & 1, wn = (w >> 1) & 1;
  const int r16 = lane & 15, kg = lane >> 4, q4 = r16 >> 2, p4 = lane & 3;
  typedef const AS3 char* lp;
  lp oa[2], wa[2][4];
#pragma unroll
  for (int ms = 0; ms < 2; ++ms) oa[ms] = (lp)(lds0 + P3_O + ((wm * 8192 + kh * (4096 + 2048) + kg * 512 + r16 * 16) ^ (ms * 4096)));
#pragma unroll
  for (int nt = 0; nt < 4; ++nt) {
    wa[0][nt] = (lp)(lds0 + P3_W + (kh * 32 + 16 * h + 4 * (kg & 1) + q4) * 256 + (((wn * 4 + nt) ^ (2 * q4 + (kg & 1))) << 5) + p4 * 8);
    wa[1][nt] = wa[0][nt] + 49152; PIN(wa[0][nt]); PIN(wa[1][nt]);
  }
  f32x4 acc[4][4] = {};
  bf16x8 xf[2][4], wfr[2][4];
  auto rd = [&](int t, int i) {
    constexpr int ord[8] = {0, 4, 1, 5, 2, 3, 6, 7};
    const int f = ord[i];
    if (f < 4) xf[t & 1][f] = *(const AS3 bf16x8*)(oa[f >> 1] + (t % NO) * 16384 + (f & 1) * 256);
    else       wfr[t & 1][f - 4] = lds_trp(wa[(p3_wslot(t) - P3_W) / 49152][f - 4] + (p3_wslot(t) - P3_W) % 49152, 2048);
  };
  PIN(oa[0]); PIN(oa[1]);
  acc_fence(acc);
  LDS_BAR();
  STAMP(0, 7);
#pragma unroll
  for (int i = 0; i < 8; ++i) rd(0, i);
#pragma unroll
  for (int t = 0; t < 16; ++t) {
    if (t < 15) LDS_BAR();
#pragma unroll
    for (int i = 0; i < 16; ++i) {
      const int mt = MT_OF(i), nt = NT_OF(i);
      SBAR(); mac16(acc[mt][nt], wfr[t & 1][nt], xf[t & 1][mt]); SBAR();
      if (t < 15 && RD_AT(i) >= 0) rd(t + 1, RD_AT(i));
    }
    SBAR();
  }
  acc_fence(acc);
  STAMP(0, 8);
  constexpr int XB = 65536, IB = 0;
  const unsigned xs = lds0 + XB + w * 8192 + lane * 16, xr = lds0 + XB + (w ^ 4) * 8192 + lane * 16;
#pragma unroll
  for (int i = 0; i < 8; ++i) *(AS3 f32x4*)(xs + i * 1024) = acc[2 + (i >> 2)][i & 3];
  LDS_BAR();
  const unsigned img = lds0 + IB + w * 4096;
#pragma unroll
  for (int i = 0; i < 8; ++i) {
    const f32x4 o = acc[i >> 2][i & 3] + *(AS3 f32x4*)(xr + i * 1024);
    *(AS3 u32x2*)(img + ((i >> 2) * 16 + r16) * 128 + ((((i & 3) * 2 + h) ^ (r16 & 7)) << 4) + (kg & 1) * 8) = u32x2{pk2(o[0], o[1]), pk2(o[2], o[3])};
  }
  asm volatile("" ::: "memory");
  const int tok0 = rb3 * 128 + wm * 64 + kh * 32;
  const rsrc_t so = mk_rsrc(out, 2048u * 4096u);
  const unsigned op = tok0 * 4096 + br * 2048 + cb * 256 + wn * 128;
#pragma unroll
  for (int i = 0; i < 4; ++i) {
    const int row = 8 * i + (lane >> 3), ch = lane & 7;
    const u32x4 v = *(AS3 u32x4*)(img + row * 128 + ((ch ^ (row & 7)) << 4));
    st16(so, op + row * 4096 + ch * 16, v);
  }
  STAMP(0, 9);
  asm volatile(".Lfw_tail:");
}

extern "C" void kernel_launch(void* const* d_in, const int* in_sizes, int n_in, void* d_out, int out_size, void* d_ws, size_t ws_size,
                              hipStream_t stream) {
  (void)in_sizes; (void)n_in; (void)out_size; (void)ws_size;
  typedef const unsigned short* cp;
  fused_fwd<<<256, 768, 0, stream>>>((cp)d_in[0], (cp)d_in[1], (cp)d_in[2], (cp)d_in[3], (cp)d_in[4], (cp)d_in[5], (cp)d_in[6], (cp)d_in[7],
                                     (cp)d_in[8], (char*)d_ws, (unsigned short*)d_out);
}
```

```cpp
#include <hip/hip_runtime.h>
#include <hip/hip_bf16.h>
#include <stdint.h>
#pragma clang diagnostic ignored "-Wint-to-pointer-cast"

typedef __bf16   bf16x8 __attribute__((ext_vector_type(8)));
typedef __bf16   bf16x16 __attribute__((ext_vector_type(16)));
typedef __bf16   bf16x2 __attribute__((ext_vector_type(2)));
typedef short    s16x4  __attribute__((ext_vector_type(4)));
typedef short    s16x8  __attribute__((ext_vector_type(8)));
typedef float    f32x16 __attribute__((ext_vector_type(16)));
typedef float    f32x4  __attribute__((ext_vector_type(4)));
typedef float    f32x2  __attribute__((ext_vector_type(2)));
typedef unsigned u32x4  __attribute__((ext_vector_type(4)));
typedef unsigned u32x2  __attribute__((ext_vector_type(2)));
typedef unsigned long long u64;
typedef __amdgpu_buffer_rsrc_t rsrc_t;

#define AS1 __attribute__((address_space(1)))
#define AS3 __attribute__((address_space(3)))
#define DI __device__ __forceinline__
#define MFMA32(a, b, c) __builtin_amdgcn_mfma_f32_32x32x16_bf16((a), (b), (c), 0, 0, 0)
#define MFMA16(a, b, c) __builtin_amdgcn_mfma_f32_16x16x32_bf16((a), (b), (c), 0, 0, 0)
#define SM32(a, b, c) __builtin_amdgcn_smfmac_f32_32x32x32_bf16((a), (b), (c), odd, 0, 0)
#define SM16(a, b, c) __builtin_amdgcn_smfmac_f32_16x16x64_bf16((a), (b), (c), odd, 0, 0)
#define LO(S) __builtin_bit_cast(bf16x16, __builtin_shufflevector(S, S, 0, 1, 2, 3, 4, 5, 6, 7))
#define HI(S) __builtin_bit_cast(bf16x16, __builtin_shufflevector(S, S, 8, 9, 10, 11, 12, 13, 14, 15))
#define SBAR() __builtin_amdgcn_sched_barrier(0)
#define WAIT_BAR(N) asm volatile("s_waitcnt vmcnt(" #N ") lgkmcnt(0)\n\ts_barrier" ::: "memory")
#define LGKM0() __builtin_amdgcn_s_waitcnt(0xC07F)
#define LDS_BAR() do { asm volatile("" ::: "memory"); LGKM0(); asm volatile("s_barrier" ::: "memory"); } while (0)

#define ZREF 1
#define EPH 3
#define QEARLY 1
#define DEFQ 1
#define EPI 0
#define SWV 0
#define STAMP(th, i) do {} while (0)
#define ESTAMP(i) do {} while (0)
#define STOUT(lo, hi) do {} while (0)
#define SVP
#define SVA
#define TS(i) do {} while (0)
#define TSOUT() do {} while (0)
constexpr int D = 1024;
constexpr float LOG2E = 1.4426950408889634f;
#define FM_SAFE 40.f
constexpr float QSCALE = 0.17677669529663687f * LOG2E;

constexpr unsigned WS_QA  = 0;
constexpr unsigned WS_QB  = WS_QA + (8u << 20);
constexpr unsigned WS_OA  = WS_QB + (4u << 20);
constexpr unsigned WS_OB  = WS_OA + (4u << 20);
constexpr unsigned WS_KV  = WS_OB + (4u << 20);
constexpr unsigned WS_KVP = WS_KV + (256u << 10);
constexpr unsigned WS_ZP  = WS_KVP + (2u << 20);
constexpr unsigned WS_FLG = WS_ZP + (64u << 10);
constexpr unsigned WS_END = WS_FLG + (16u << 10);
constexpr int F_K = 0, F_Q = 128, F_OA = 256, F_OB = 512, F_KV = 768, F_EPOCH = 1024;

DI unsigned pk2(float lo, float hi) {
  f32x2 v = {lo, hi};
  return __builtin_bit_cast(unsigned, __builtin_convertvector(v, bf16x2));
}
template <int SS> DI bf16x8 pack8(const f32x16& x) {
  u32x4 p = {pk2(x[8 * SS + 0], x[8 * SS + 1]), pk2(x[8 * SS + 2], x[8 * SS + 3]), pk2(x[8 * SS + 4], x[8 * SS + 5]), pk2(x[8 * SS + 6], x[8 * SS + 7])};
  return __builtin_bit_cast(bf16x8, p);
}
DI u32x4 pack44(const f32x4& a, const f32x4& b) { return u32x4{pk2(a[0], a[1]), pk2(a[2], a[3]), pk2(b[0], b[1]), pk2(b[2], b[3])}; }
DI void to32(u32x4 x, u32x4 y, bf16x8 (&f)[2]) {
#pragma unroll
  for (int i = 0; i < 4; ++i) { auto r = __builtin_amdgcn_permlane16_swap(x[i], y[i], false, false); x[i] = r[0]; y[i] = r[1]; }
  f[0] = __builtin_bit_cast(bf16x8, x); f[1] = __builtin_bit_cast(bf16x8, y);
}
DI bf16x8 lds_r128(unsigned a) { return *(const AS3 bf16x8*)a; }
DI bf16x8 lds_tr2(unsigned a0, unsigned a1) {
  s16x4 lo = __builtin_amdgcn_ds_read_tr16_b64_v4i16((AS3 s16x4*)a0);
  s16x4 hi = __builtin_amdgcn_ds_read_tr16_b64_v4i16((AS3 s16x4*)a1);
  s16x8 r = __builtin_shufflevector(lo, hi, 0, 1, 2, 3, 4, 5, 6, 7);
  return __builtin_bit_cast(bf16x8, r);
}
DI bf16x8 lds_trp(const AS3 char* p, int d) {
  s16x4 lo = __builtin_amdgcn_ds_read_tr16_b64_v4i16((AS3 s16x4*)p);
  s16x4 hi = __builtin_amdgcn_ds_read_tr16_b64_v4i16((AS3 s16x4*)(p + d));
  return __builtin_bit_cast(bf16x8, __builtin_shufflevector(lo, hi, 0, 1, 2, 3, 4, 5, 6, 7));
}
DI rsrc_t mk_rsrc(const void* p, unsigned bytes) { return __builtin_amdgcn_make_buffer_rsrc((void*)p, (short)0, (int)bytes, 0x00020000); }
DI void set_m0(unsigned v) { asm volatile("s_mov_b32 m0, %0\n\ts_nop 0" ::"s"(v) : "memory"); }
template <int IMM> DI void dma16(unsigned voff, rsrc_t srd, unsigned soff) {
  asm volatile("buffer_load_dwordx4 %0, %1, %2 offen offset:%3 lds" ::"v"(voff), "s"(srd), "s"(soff), "i"(IMM) : "memory");
}
template <int IMM> DI void dma16d(unsigned voff, rsrc_t srd, unsigned soff) {
  asm volatile("buffer_load_dwordx4 %0, %1, %2 offen offset:%3 sc1 lds" ::"v"(voff), "s"(srd), "s"(soff), "i"(IMM) : "memory");
}
DI void dma4k(unsigned lds, unsigned voff, rsrc_t srd, unsigned soff) {
  set_m0(lds); dma16d<0>(voff, srd, soff); dma16d<1024>(voff, srd, soff); dma16d<2048>(voff, srd, soff); dma16d<3072>(voff, srd, soff);
}
template <class T> DI void st16(rsrc_t r, unsigned voff, T v) { __builtin_amdgcn_raw_buffer_store_b128(__builtin_bit_cast(u32x4, v), r, (int)voff, 0, 16); }
DI void st8(rsrc_t r, unsigned voff, u32x2 v) { __builtin_amdgcn_raw_buffer_store_b64(v, r, (int)voff, 0, 16); }
DI void st4(rsrc_t r, unsigned voff, float v) { __builtin_amdgcn_raw_buffer_store_b32(__float_as_uint(v), r, (int)voff, 0, 16); }
DI u32x4 ld16(rsrc_t r, unsigned voff) { return __builtin_amdgcn_raw_buffer_load_b128(r, (int)voff, 0, 16); }
DI float ld4(rsrc_t r, unsigned voff) { return __uint_as_float(__builtin_amdgcn_raw_buffer_load_b32(r, (int)voff, 0, 16)); }
DI void post(char* ws, int f, u64 tag) { __hip_atomic_store((AS1 u64*)(ws + WS_FLG) + f, tag, __ATOMIC_RELAXED, __HIP_MEMORY_SCOPE_AGENT); }
DI void poll(char* ws, int f, u64 tag) {
#pragma unroll 1
  for (int it = 0; it < (1 << 17); ++it) {
    const u64 v = __hip_atomic_load((AS1 u64*)(ws + WS_FLG) + f, __ATOMIC_RELAXED, __HIP_MEMORY_SCOPE_AGENT);
    if (__all(v == tag)) break;
    __builtin_amdgcn_s_sleep(2);
  }
  asm volatile("" ::: "memory");
}
DI void pf_line(unsigned& sink, const void* p) { asm volatile("global_load_dword %0, %1, off" : "+v"(sink) : "v"(p) : "memory"); }
#define PIN(x) asm volatile("" : "+v"(x))
DI void pf_bin(unsigned bin, unsigned voff, const void* base) {
  asm volatile("s_mov_b32 m0, %0\n\ts_nop 0\n\tglobal_load_lds_dword %1, %2" ::"s"(bin), "v"(voff), "s"(base) : "memory");
}
DI float one(float v) { asm("" : "+v"(v)); return v; }
DI f32x4 add4(f32x4 a, f32x4 b) { return f32x4{one(a[0] + b[0]), one(a[1] + b[1]), one(a[2] + b[2]), one(a[3] + b[3])}; }
DI float xhalf_max(float v) {
  auto r = __builtin_amdgcn_permlane32_swap(__float_as_uint(v), __float_as_uint(v), false, false);
  return fmaxf(__uint_as_float(r[0]), __uint_as_float(r[1]));
}
DI float xhalf_min(float v) {
  auto r = __builtin_amdgcn_permlane32_swap(__float_as_uint(v), __float_as_uint(v), false, false);
  return fminf(__uint_as_float(r[0]), __uint_as_float(r[1]));
}
DI float xhalf_sum(float v) {
  auto r = __builtin_amdgcn_permlane32_swap(__float_as_uint(v), __float_as_uint(v), false, false);
  return __uint_as_float(r[0]) + __uint_as_float(r[1]);
}
DI float max16(const f32x16& x) {
  float a = fmaxf(fmaxf(x[0], x[1]), x[2]), b = fmaxf(fmaxf(x[3], x[4]), x[5]);
  a = fmaxf(fmaxf(a, x[6]), x[7]);   b = fmaxf(fmaxf(b, x[8]), x[9]);
  a = fmaxf(fmaxf(a, x[10]), x[11]); b = fmaxf(fmaxf(b, x[12]), x[13]);
  a = fmaxf(fmaxf(a, x[14]), x[15]);
  return fmaxf(a, b);
}
DI float min16(const f32x16& x) {
  float a = fminf(fminf(x[0], x[1]), x[2]), b = fminf(fminf(x[3], x[4]), x[5]);
  a = fminf(fminf(a, x[6]), x[7]);   b = fminf(fminf(b, x[8]), x[9]);
  a = fminf(fminf(a, x[10]), x[11]); b = fminf(fminf(b, x[12]), x[13]);
  a = fminf(fminf(a, x[14]), x[15]);
  return fminf(a, b);
}
DI bool fm_wide(const f32x16& p) {
  float a = fmaxf(fmaxf(fabsf(p[0]), fabsf(p[1])), fabsf(p[2])), b = fmaxf(fmaxf(fabsf(p[3]), fabsf(p[4])), fabsf(p[5]));
#pragma unroll
  for (int i = 6; i < 14; i += 4) { a = fmaxf(fmaxf(a, fabsf(p[i])), fabsf(p[i + 1])); b = fmaxf(fmaxf(b, fabsf(p[i + 2])), fabsf(p[i + 3])); }
  a = fmaxf(fmaxf(a, fabsf(p[14])), fabsf(p[15]));
  return __any(fmaxf(a, b) > FM_SAFE);
}
DI void fm_shift(f32x16& p, int sign) {
  const float m = sign ? xhalf_min(min16(p)) : xhalf_max(max16(p));
#pragma unroll
  for (int i = 0; i < 16; ++i) p[i] -= m;
}
DI unsigned row_sum_sel(int lane) { return ((lane & 11) == 0 && (((lane >> 2) ^ (lane >> 4)) & 1) == 0) ? 0x3f803f80u : 0u; }

DI int b_rot(int x) { return 8 * (((x >> 1) ^ (x >> 2)) & 1); }

constexpr int LDS_BYTES = 163840;
constexpr int P1_WOFF = 32768, P1_EOFF = 49152, P1_STAGE = 53248, P1_ZRED = 3 * P1_STAGE, P1_CNT = P1_ZRED + 2048, P1_GO = P1_CNT + 16, P1_BIN = P1_CNT + 256, P1_PHI = 65536;
constexpr int P2_Q = 131072, P2_KVF = 147456, P2_ZB = 151552;
constexpr int P2_XO = 32768, P2_XL = 0;
constexpr int NO = 4, NW = 6, NF = 3;
constexpr int P3_O = 0, P3_W = NO * 16384;
DI constexpr int p3_wslot(int t) { return P3_W + ((t + NW - 4) % NW) * 16384; }
DI constexpr int p3_inflight(int t) {
  int n = 0, after = 0;
  for (int j = 0; j < NO; ++j) { n += 4; if (j == t + 1) after = n; }
  for (int u = 0; u < t; ++u) {
    if (u + NW < 16) { n += 4; if (u + NW == t + 1) after = n; }
    if (u + NO < 16) { n += 4; if (u + NO == t + 1) after = n; }
  }
  return n - after;
}
template <int N> DI void wait_bar() { asm volatile("s_waitcnt vmcnt(%0) lgkmcnt(0)\n\ts_barrier" ::"i"(N) : "memory"); }

DI void mac16(f32x4& c, bf16x8 a, bf16x8 b) { asm volatile("v_mfma_f32_16x16x32_bf16 %0, %1, %2, %0" : "+v"(c) : "v"(a), "v"(b)); }
DI void mul16(f32x4& c, bf16x8 a, bf16x8 b) { asm volatile("v_mfma_f32_16x16x32_bf16 %0, %1, %2, 0" : "=v"(c) : "v"(a), "v"(b)); }
DI void acc_fence(f32x4 (&c)[4][4]) {
  asm volatile("s_nop 7" : "+v"(c[0][0]), "+v"(c[0][1]), "+v"(c[0][2]), "+v"(c[0][3]), "+v"(c[1][0]), "+v"(c[1][1]), "+v"(c[1][2]), "+v"(c[1][3]),
                           "+v"(c[2][0]), "+v"(c[2][1]), "+v"(c[2][2]), "+v"(c[2][3]), "+v"(c[3][0]), "+v"(c[3][1]), "+v"(c[3][2]), "+v"(c[3][3]));
}
#define MT_OF(i) (((((i) >> 2) ^ ((i) >> 3)) & 1) * 2 + ((((i) >> 1) ^ (i)) & 1))
#define NT_OF(i) ((((i) >> 3) & 1) * 2 + (((i) >> 1) & 1))
#define RD_AT(i) ((i) < 12 && (i) % 3 != 2 ? (i) / 3 * 2 + (i) % 3 : -1)
template <int TYPE, class F>
DI void p1_compute(unsigned lds0, char* __restrict__ ws, rsrc_t sws, int rb, int ct, int w, int lane, F&& p2_init SVP) {
  const int wm = w & 3, wn = w >> 2, r32 = lane & 31, h = lane >> 5;
  if (w == 0 && lane == 0) *(AS3 unsigned*)(lds0 + P1_GO) = 0u;

  typedef const AS3 char* lp;
  const int r16 = lane & 15, kg = lane >> 4, q4 = r16 >> 2, p4 = lane & 3, chalf = kg & 1;
  lp xa[2][2], wa[2][4];
#pragma unroll
  for (int kk = 0; kk < 2; ++kk) {
    xa[0][kk] = (lp)(lds0 + (wm * 64 + r16) * 128 + (((4 * kk + kg) ^ (r16 >> 1)) << 4));
    xa[1][kk] = xa[0][kk] + 2 * P1_STAGE; PIN(xa[0][kk]); PIN(xa[1][kk]);
  }
#pragma unroll
  for (int nt = 0; nt < 4; ++nt) {
    wa[0][nt] = (lp)(lds0 + P1_WOFF + (8 * kg + q4) * 256 + (((wn * 4 + nt) ^ (2 * q4 + chalf)) << 5) + p4 * 8);
    wa[1][nt] = wa[0][nt] + 2 * P1_STAGE; PIN(wa[0][nt]); PIN(wa[1][nt]);
  }

  f32x4 acc[4][4];
  bf16x8 xf[2][4], wfr[2][4];
  auto rd = [&](int set, int b, int kk, int i) {
    constexpr int ord[8] = {0, 4, 1, 5, 2, 3, 6, 7};
    const int f = ord[i];
    if (f < 4) xf[set][f] = *(const AS3 bf16x8*)(xa[b >> 1][kk] + (b & 1) * P1_STAGE + f * 2048);
    else       wfr[set][f - 4] = lds_trp(wa[b >> 1][f - 4] + (b & 1) * P1_STAGE + kk * 8192, 1024);
  };
  auto sub = [&](int set, int b, int kk, bool first = false) {
#pragma unroll
    for (int i = 0; i < 16; ++i) {
      const int mt = MT_OF(i), nt = NT_OF(i);
      const bool tr = TYPE == 0 && nt >= 2;
      const bf16x8 a = tr ? xf[set][mt] : wfr[set][nt], bb = tr ? wfr[set][nt] : xf[set][mt];
      SBAR();
      if (first) mul16(acc[mt][nt], a, bb); else mac16(acc[mt][nt], a, bb);
      SBAR();
      if (b >= 0 && RD_AT(i) >= 0) rd(set ^ 1, b, kk, RD_AT(i));
    }
    SBAR();
  };
  LDS_BAR();
  STAMP(0, 1);
#pragma unroll
  for (int i = 0; i < 8; ++i) rd(0, 0, 0, i);
  SBAR();
  unsigned wfa = lds0 + P1_PHI + (TYPE == 0 ? wn : 2 * wn) * 2048 + (8 * h + q4) * 64 + chalf * 32 + p4 * 8;
  const unsigned img = lds0 + w * 8192;
  unsigned wr[4], rt[2], pa = img + lane * 16, za = lds0 + P1_ZRED + w * 256 + ((lane >> 2) & 1) * 64 + (lane >> 3) * 16 + (lane & 3) * 4, su = 0;
  unsigned go = TYPE == 1 ? WS_QA + ((rb * 8 + wm * 2) * 32 + ct * 4 + wn * 2) * 4096 + lane * 16
                          : WS_QB + ((rb * 8 + wm * 2) * 32 + ct * 4 + wn * 2) * 2048 + chalf * 1024 + h * 512 + r16 * 16;
  if (TYPE <= 1) { PIN(wfa); PIN(pa); }
  if (TYPE >= 1) PIN(go);
  if (TYPE == 0) {
    const int t0 = 8 * h + q4;
    wr[0] = img + 4096 + r32 * 64 + ((h ^ ((r32 >> 1) & 7)) << 3); PIN(wr[0]);
#pragma unroll
    for (int g = 1; g < 4; ++g) { wr[g] = wr[0] ^ (g << 4); PIN(wr[g]); }
    rt[0] = img + 4096 + t0 * 64 + (((4 * chalf + p4) ^ (t0 >> 1)) << 3); PIN(rt[0]);
    rt[1] = rt[0] ^ 16; PIN(rt[1]);
    su = row_sum_sel(lane);
    PIN(za); PIN(su);
  }
  sub(0, 0, 1, true);
#pragma unroll 1
  for (int t = 0; t < 5; ++t) {
    LDS_BAR(); sub(1, 1, 0); sub(0, 1, 1);
    LDS_BAR(); sub(1, 2, 0); sub(0, 2, 1);
    LDS_BAR(); sub(1, 0, 0); sub(0, 0, 1);
  }
  LDS_BAR();
  bf16x8 wf[2][2];
  auto qst = [&](int qd) {
    const int ms = MT_OF(4 * qd) >> 1, ns = NT_OF(4 * qd) >> 1;
#pragma unroll
    for (int tb = 0; tb < 2; ++tb) st16(sws, go + ms * 65536 + ns * 2048 + tb * 256, pack44(acc[2 * ms + tb][2 * ns], acc[2 * ms + tb][2 * ns + 1]));
  };
#pragma unroll
  for (int i = 0; i < 16; ++i) {
    const int mt = MT_OF(i), nt = NT_OF(i);
    SBAR();
    if (TYPE == 0 && nt >= 2) mac16(acc[mt][nt], xf[1][mt], wfr[1][nt]); else mac16(acc[mt][nt], wfr[1][nt], xf[1][mt]);
    SBAR();
    if (TYPE <= 1 && i < 2 * TYPE + 2) { const unsigned a = wfa + (i >> 1) * 2048 + (i & 1) * 256; wf[i >> 1][i & 1] = lds_tr2(a, a + 1024); }
    if (QEARLY && TYPE >= 2 && i >= 7 && i < 15 && (i & 3) == 3) {
      const int m0 = MT_OF(i - 7) & 2, n0 = NT_OF(i - 7) & 2;
      asm volatile("s_nop 3" : "+v"(acc[m0][n0]), "+v"(acc[m0][n0 + 1]), "+v"(acc[m0 + 1][n0]), "+v"(acc[m0 + 1][n0 + 1]));
      qst(i / 4 - 1);
    }
  }
  SBAR();
  acc_fence(acc);
  STAMP(0, 2);
  auto lead = [&](bool on) { if (wn) { if (on) __builtin_amdgcn_s_setprio(1); else __builtin_amdgcn_s_setprio(0); } };
  auto proj = [&](int ms, int ns) {
    bf16x8 f[2];
    to32(pack44(acc[2 * ms][2 * ns], acc[2 * ms][2 * ns + 1]), pack44(acc[2 * ms + 1][2 * ns], acc[2 * ms + 1][2 * ns + 1]), f);
    return MFMA32(wf[ns][1], f[1], MFMA32(wf[ns][0], f[0], f32x16{}));
  };

  if (TYPE >= 2) {
    if (!QEARLY) { qst(0); qst(1); }
    qst(2); qst(3);
    SBAR();
    p2_init();
    WAIT_BAR(0);
  } else if (TYPE == 1) {
    f32x16 pq[4];
    if (EPH) lead(true);
#pragma unroll
    for (int i = 0; i < 4; ++i) pq[i] = proj(i >> 1, i & 1);
#pragma unroll
    for (int i = 0; i < 4; ++i) {
      if (i == EPH) lead(false);
      if (i == 3) p2_init();
      pq[i] *= LOG2E;
      const bool wide = fm_wide(pq[i]);
      ESTAMP(5 + i);
#pragma unroll
      for (int sg = 0; sg < 2; ++sg) {
        if (__builtin_expect(wide, 0)) fm_shift(pq[i], sg);
        f32x16 e;
#pragma unroll
        for (int j = 0; j < 16; ++j) e[j] = __builtin_amdgcn_exp2f(sg ? -pq[i][j] : pq[i][j]);
        const unsigned o = (i & 1) * 4096 + sg * 2048;
        if (DEFQ && i >= 2) { *(AS3 bf16x8*)(pa + o) = pack8<0>(e); *(AS3 bf16x8*)(pa + o + 1024) = pack8<1>(e); }
        else { st16(sws, go + (i >> 1) * 131072 + o, pack8<0>(e)); st16(sws, go + (i >> 1) * 131072 + o + 1024, pack8<1>(e)); }
      }
    }
    if (EPH >= 4) lead(false);
  } else {
    const bf16x8 sel = __builtin_bit_cast(bf16x8, u32x4{su, su, su, su});
    f32x16 pk[2], kv[2] = {};
    f32x4 zz[2] = {};
    bf16x8 vf[2][2], af[2][2];
    bool wide[2];
    if (EPH) lead(true);
#pragma unroll
    for (int ms = 0; ms < 2; ++ms) pk[ms] = proj(ms, 0);
#pragma unroll
    for (int ms = 0; ms < 2; ++ms) { to32(pack44(acc[2 * ms][2], acc[2 * ms + 1][2]), pack44(acc[2 * ms][3], acc[2 * ms + 1][3]), vf[ms]); PIN(vf[ms][0]); PIN(vf[ms][1]); }
#pragma unroll
    for (int ms = 0; ms < 2; ++ms) { pk[ms] *= LOG2E; wide[ms] = fm_wide(pk[ms]); }
    auto put = [&](int sg) {
#pragma unroll
      for (int g = 0; g < 4; ++g) *(AS3 f32x4*)(pa + sg * 4096 + g * 1024) = f32x4{kv[sg][4 * g], kv[sg][4 * g + 1], kv[sg][4 * g + 2], kv[sg][4 * g + 3]};
    };
    f32x16 e[2];
    float rs[2];
    auto mm = [&](int u, int j) {
      if (j & 1) zz[u & 1] = MFMA16(sel, af[u & 1][j >> 1], zz[u & 1]); else kv[u & 1] = MFMA32(af[u & 1][j >> 1], vf[u >> 1][j >> 1], kv[u & 1]);
    };
    auto exps = [&](int u, int mu) {
      f32x16& p = pk[u >> 1];
      if (__builtin_expect(wide[u >> 1], 0)) fm_shift(p, u & 1);
      f32x2 sm;
#pragma unroll
      for (int i = 0; i < 16; i += 2) {
        if (mu >= 0 && i >= 8) { SBAR(); mm(mu, (i - 8) >> 1); SBAR(); }
        const f32x2 ev = {__builtin_amdgcn_exp2f((u & 1) ? -p[i] : p[i]), __builtin_amdgcn_exp2f((u & 1) ? -p[i + 1] : p[i + 1])};
        e[u & 1][i] = ev[0]; e[u & 1][i + 1] = ev[1];
        if (i == 0) sm = ev; else sm += ev;
      }
      rs[u & 1] = __builtin_amdgcn_rcpf(xhalf_sum(sm[0] + sm[1]));
    };
    auto rest = [&](int u, int mu = -1) {
      const f32x16 n = e[u & 1] * rs[u & 1];
#pragma unroll
      for (int g = 0; g < 4; ++g) {
        if (mu >= 0) { SBAR(); mm(mu, (g >> 1) + (g & 1) * 2); SBAR(); }
        *(AS3 u32x2*)(wr[g] + (u & 1) * 2048) = u32x2{pk2(n[4 * g], n[4 * g + 1]), pk2(n[4 * g + 2], n[4 * g + 3])};
      }
      asm volatile("" ::: "memory");
#pragma unroll
      for (int s = 0; s < 2; ++s) af[u & 1][s] = lds_tr2(rt[s] + s * 256 + (u & 1) * 2048, rt[s] + s * 256 + (u & 1) * 2048 + 1024);
      asm volatile("" ::: "memory");
      SBAR();
    };
    ESTAMP(5);
    if (EPH == 1) lead(false);
    exps(0, -1); exps(1, -1); rest(0);
    ESTAMP(6);
    if (EPH == 2) lead(false);
    exps(2, 0);  rest(1);
    ESTAMP(7);
    if (EPH == 3) lead(false);
    exps(3, 1);  rest(2);
    rest(3, 2);
    ESTAMP(8);
    put(0);
    SBAR(); mm(3, 0); mm(3, 2); mm(3, 1); mm(3, 3); SBAR();
    p2_init();
    SBAR();
    put(1);
    if (EPH >= 4) lead(false);
    if (lane < 32) { *(AS3 float*)za = zz[0][0]; *(AS3 float*)(za + 128) = zz[1][0]; }
  }
  STAMP(0, 3);
}

struct Attn { f32x16 O[2], S1; f32x4 L[2]; bf16x8 vb, sel; int odd; unsigned a1, qa, qb; };
DI bf16x8 attn_sel(int lane) {
  const unsigned s0 = (lane & 15) == 0 ? 0x3f803f80u : 0u, s4 = (lane & 15) == 4 ? 0x3f803f80u : 0u;
  return __builtin_bit_cast(bf16x8, u32x4{s0, s0, s4, s4});
}
DI void attn_init(Attn& a, unsigned lds0, int w, int lane) {
  const int kh = w >> 2;
  a.vb = bf16x8{};
  PIN(a.vb); a.S1 = MFMA32(a.vb, a.vb, f32x16{}); a.L[0] = MFMA16(a.vb, a.vb, f32x4{});
  PIN(a.vb); a.O[0] = MFMA32(a.vb, a.vb, f32x16{}); a.L[1] = MFMA16(a.vb, a.vb, f32x4{});
  PIN(a.vb); a.O[1] = MFMA32(a.vb, a.vb, f32x16{});
  a.sel = attn_sel(lane); a.odd = 0xDDDD;
  a.a1 = lds0 + lane * 16 + 98304 + kh * 16384; a.qa = lds0 + P2_Q + (2 * (w & 3) + (kh ^ 1)) * 2048 + lane * 16; a.qb = a.qa ^ 2048;
  PIN(a.sel); PIN(a.odd); PIN(a.a1); PIN(a.qa); PIN(a.qb); PIN(a.S1); PIN(a.L[0]); PIN(a.O[0]); PIN(a.L[1]); PIN(a.O[1]);
}
DI void attn_pass_fast(unsigned lds0, Attn& at, bf16x8 (&q)[2][2], int kh, int lane, unsigned& tv SVP) {
  typedef const AS3 char* lp;
  lp l0 = (lp)(lds0 + lane * 16), la = l0 + kh * 16384;
#define RD(p) (*(const AS3 bf16x8*)(p))
  f32x16 (&O)[2] = at.O; f32x4 (&L)[2] = at.L;
  f32x16 S0, S1 = at.S1;
  bf16x8 vb = at.vb;
  const bf16x8 sel = at.sel;
  const int odd = at.odd;
  const unsigned a1 = at.a1, qa = at.qa, qb = at.qb;
  LDS_BAR();
  STAMP(0, 4);
  TS(0);
  const f32x16 negm[2] = {};
  bf16x8 ka = lds_r128(a1);
  q[0][0] = lds_r128(qa);
  bf16x8 kb = lds_r128(a1 + 1024);
  q[0][1] = lds_r128(qa + 1024); q[1][0] = lds_r128(qb); q[1][1] = lds_r128(qb + 1024);
  bf16x8 va = lds_r128(a1 + 2048);
  SBAR();
  if (kh) __builtin_amdgcn_s_setprio(1);
  asm volatile("v_mfma_f32_32x32x16_bf16 %0, %1, %2, 0" : "=&v"(S0) : "v"(ka), "v"(q[0][0]));
  asm volatile("v_mfma_f32_32x32x16_bf16 %0, %1, %2, %0\n\ts_nop 11" : "+v"(S0) : "v"(kb), "v"(q[0][1]));
  SBAR();
#define EXN(SC, i, n) do { _Pragma("unroll") for (int k_ = 0; k_ < n; ++k_) SC[i + k_] = __builtin_amdgcn_exp2f(SC[i + k_]); PIN(SC); } while (0)
#define MM(D, E) do { SBAR(); D = E; PIN(D); SBAR(); } while (0)
#define TILE(TA, TN) do { \
    SBAR();                                                             EXN(S0, 0, 2);  \
    MM(O[1], SM32(vb, HI(S1), O[1]));       vb = RD((TA) + 3072);       EXN(S0, 2, 3);  \
    MM(L[1], SM16(sel, HI(S1), L[1]));                                  EXN(S0, 5, 2);  \
    MM(S1, MFMA32(ka, q[1][0], negm[1]));   ka = RD(TN);                EXN(S0, 7, 3);  \
    MM(S1, MFMA32(kb, q[1][1], S1));        kb = RD((TN) + 1024);       EXN(S0, 10, 3); \
    MM(O[0], SM32(va, LO(S0), O[0]));                                   EXN(S0, 13, 3); \
    MM(L[0], SM16(sel, LO(S0), L[0]));                                  EXN(S1, 0, 2);  \
    MM(O[0], SM32(vb, HI(S0), O[0]));                                   EXN(S1, 2, 3);  \
    MM(L[0], SM16(sel, HI(S0), L[0]));                                  EXN(S1, 5, 2);  \
    MM(S0, MFMA32(ka, q[0][0], negm[0]));                               EXN(S1, 7, 3);  \
    MM(S0, MFMA32(kb, q[0][1], S0));                                    EXN(S1, 10, 3); \
    MM(O[1], SM32(va, LO(S1), O[1]));       va = RD((TN) + 2048);       EXN(S1, 13, 3); \
    MM(L[1], SM16(sel, LO(S1), L[1])); } while (0)
  TS(1);
#pragma unroll 1
  for (int c = 0; c < 8; ++c) {
    const lp cb = la + ((3 - c) & 3) * 32768, nb = la + ((2 - c) & 3) * 32768;
    TILE(cb, cb + 4096);
    TILE(cb + 4096, cb + 8192);
    SBAR(); if (kh) __builtin_amdgcn_s_setprio(0);
    TILE(cb + 8192, cb + 12288);
    SBAR(); TS(2 + 2 * c);
    asm volatile("s_waitcnt vmcnt(0)\n\ts_barrier" ::: "memory"); TS(3 + 2 * c);
    if (kh) __builtin_amdgcn_s_setprio(1);
    TILE(cb + 12288, nb);
  }
  SBAR();
  L[1] = SM16(sel, HI(S1), L[1]); O[1] = SM32(vb, HI(S1), O[1]); PIN(L[1]); PIN(O[1]);
  __builtin_amdgcn_s_setprio(0);
  TS(18);
#undef TILE
#undef EXN
#undef MM
#undef RD
}

DI void attn_pass_safe(rsrc_t sws, const bf16x8 q0, const bf16x8 q1, int lane, f32x16& O, f32x4& L) {
  O = f32x16{}; L = f32x4{};
  const bf16x8 sel = attn_sel(lane);
  float m = -INFINITY;
  const int odd = 0xDDDD;
#pragma unroll 1
  for (int t = 0; t < 64; ++t) {
    bf16x8 a[4];
#pragma unroll
    for (int j = 0; j < 4; ++j) a[j] = __builtin_bit_cast(bf16x8, ld16(sws, WS_KV + t * 4096 + j * 1024 + lane * 16));
    f32x16 Sc = {};
    Sc = MFMA32(a[0], q0, Sc); Sc = MFMA32(a[1], q1, Sc);
    const float mn = fmaxf(m, xhalf_max(max16(Sc)));
    const float al = __builtin_amdgcn_exp2f(m - mn);
    m = mn;
#pragma unroll
    for (int r = 0; r < 16; ++r) { O[r] *= al; Sc[r] = __builtin_amdgcn_exp2f(Sc[r] - mn); }
    L *= al;
    O = SM32(a[2], LO(Sc), O); L = SM16(sel, LO(Sc), L);
    O = SM32(a[3], HI(Sc), O); L = SM16(sel, HI(Sc), L);
  }
}

DI void helper_program(unsigned lds0, const unsigned short* __restrict__ X, const unsigned short* __restrict__ W, const unsigned short* __restrict__ E,
                       const unsigned short* __restrict__ woa, const unsigned short* __restrict__ wob, char* __restrict__ ws, rsrc_t sws,
                       const char* pc0, unsigned co, int type, int rb, int ct, int x, int idx, int lw, int lane) {
  unsigned sv = 0; (void)sv;
  __builtin_amdgcn_s_setprio(3);
  u64 e0;
  const unsigned vl = lane * 16;
  {
    const bool ext = type >= 2;
    const unsigned ldw = ext ? 2048u : 6144u;
    const rsrc_t sx = mk_rsrc(X + (size_t)rb * 256 * D, 256u * 2048u);
    const rsrc_t sw = mk_rsrc(W, 1024u * ldw);
    const rsrc_t se = mk_rsrc(E, 1024u * 64u);
    unsigned xv[8], wv[4];
#pragma unroll
    for (int j = 0; j < 8; ++j) {
      const int row = 8 * (8 * lw + j) + (lane >> 3), c = (lane & 7) ^ ((row >> 1) & 7);
      xv[j] = row * 2048 + c * 16 - (j & 3) * 1024;
    }
#pragma unroll
    for (int j = 0; j < 4; ++j) {
      const int k = 4 * (4 * lw + j) + (lane >> 4), c = (lane & 15) ^ ((lane >> 4) << 2) ^ (j & 2);
      const int col = (type == 0) ? 1024 + ((c >> 2) & 1) * 1024 + (2 * ct + (c >> 3)) * 32 + (c & 3) * 8 : ct * 128 + c * 8;
      wv[j] = k * ldw + col * 2 - j * 1024;
    }
    const int erow = 64 * (ct >> 1) + (lane & 31);
    const unsigned exa = (erow * 128 + (((lane >> 5) ^ ((erow >> 1) & 7)) << 4)) ^ (lw * 32);
    const unsigned ewa = P1_EOFF + (16 * lw + 8 * (lane >> 5) + ((lane & 15) >> 2)) * 64 + ((lane >> 4) & 1) * 32 + (lane & 3) * 8;
    f32x16 eacc[2] = {};
    bf16x8 ex[2], ew;
    auto kvr = [&](unsigned buf) {
      if (ext) { ex[0] = lds_r128(buf + exa); ew = lds_tr2(buf + ewa, buf + ewa + 256); ex[1] = lds_r128(buf + exa + 4096); }
    };
    auto kvm = [&]() {
      if (ext)
        asm volatile("s_cmp_eq_u32 %5, 2\n\ts_cbranch_scc1 1f\n\t"
                     "v_mfma_f32_32x32x16_bf16 %0, %2, %4, %0\n\tv_mfma_f32_32x32x16_bf16 %1, %3, %4, %1\n\ts_branch 2f\n"
                     "1:\tv_mfma_f32_32x32x16_bf16 %0, %4, %2, %0\n\tv_mfma_f32_32x32x16_bf16 %1, %4, %3, %1\n2:"
                     : "+v"(eacc[0]), "+v"(eacc[1]) : "v"(ex[0]), "v"(ex[1]), "v"(ew), "s"(type) : "scc");
    };
    auto kvs = [&](unsigned buf) { kvr(buf); kvm(); };
    auto stage = [&](int t0, unsigned buf, bool kv, unsigned kb) {
      const int t = (t0 + (ext ? b_rot(x) : 2 * x)) & 15;
      const unsigned sox = t * 128, sow = t * 64 * ldw;
      set_m0(buf + P1_WOFF + lw * 4096);
      dma16<0>(wv[0], sw, sow); dma16<1024>(wv[1], sw, sow); dma16<2048>(wv[2], sw, sow); dma16<3072>(wv[3], sw, sow);
      set_m0(buf + lw * 8192);
      dma16<0>(xv[0], sx, sox); dma16<1024>(xv[1], sx, sox); dma16<2048>(xv[2], sx, sox); dma16<3072>(xv[3], sx, sox);
      if (kv) kvr(kb);
      set_m0(buf + lw * 8192 + 4096);
      dma16<0>(xv[4], sx, sox); dma16<1024>(xv[5], sx, sox); dma16<2048>(xv[6], sx, sox); dma16<3072>(xv[7], sx, sox);
      if (ext) { set_m0(buf + P1_EOFF + lw * 1024); dma16<0>(vl, se, t * 4096 + lw * 1024); }
      if (kv) kvm();
    };
    stage(0, lds0, false, 0); stage(1, lds0 + P1_STAGE, false, 0);
    if (ext) WAIT_BAR(13); else WAIT_BAR(12);
    stage(2, lds0 + 2 * P1_STAGE, true, lds0);
    unsigned buf = lds0;
#pragma unroll 1
    for (int t = 0; t < 13; ++t) {
      if (ext) WAIT_BAR(13); else WAIT_BAR(12);
      const unsigned nb = (buf == lds0 + 2 * P1_STAGE) ? lds0 : buf + P1_STAGE;
      stage(t + 3, buf, true, nb);
      buf = nb;
    }
    if (ext) WAIT_BAR(13); else WAIT_BAR(12);
    e0 = __hip_atomic_load((AS1 u64*)(ws + WS_FLG) + F_EPOCH, __ATOMIC_RELAXED, __HIP_MEMORY_SCOPE_AGENT);
    if (lw == 3) pf_bin(lds0 + P1_BIN, co, pc0);
    if (!ext && (type == 1 || lw < 2)) {
      set_m0(lds0 + P1_PHI + lw * 2048);
      const unsigned so = ((type == 0 ? 2 * ct : 4 * ct) + lw) * 2048;
      dma16<0>(vl, se, so); dma16<1024>(vl, se, so);
    }
    kvs(lds0 + 2 * P1_STAGE);
    WAIT_BAR(0);
    kvs(lds0);
    asm volatile("s_nop 15\n\ts_nop 7" : "+v"(eacc[0]), "+v"(eacc[1]));
    const unsigned red = lds0 + P1_STAGE + vl;
    if (ext) {
#pragma unroll
      for (int i = 0; i < 8; ++i) {
        const f32x16& a = eacc[i >> 2];
        *(AS3 f32x4*)(red + lw * 8192 + i * 1024) = f32x4{a[4 * (i & 3)], a[4 * (i & 3) + 1], a[4 * (i & 3) + 2], a[4 * (i & 3) + 3]};
      }
    }
    WAIT_BAR(0);
    if (ext) {
#pragma unroll
      for (int n = 0; n < 2; ++n) {
        const unsigned a = red + n * 4096 + lw * 1024;
        const f32x4 sm = add4(add4(*(AS3 f32x4*)a, *(AS3 f32x4*)(a + 8192)), add4(*(AS3 f32x4*)(a + 16384), *(AS3 f32x4*)(a + 24576)));
        unsigned p = WS_KV + (rb * 8 + 2 * (ct >> 1) + n) * 4096 + ((lane & 31) + 32 * (lw & 1)) * 16;
        if (type == 3) p += 2048 + (lw >> 1) * 1024 + (lane >> 5) * 8;
        else           p += (lane >> 5) * 1024 + (lw >> 1) * 8;
        const float c = type == 3 ? 1.f : QSCALE;
        st8(sws, p, u32x2{pk2(one(sm[0] * c), one(sm[1] * c)), pk2(one(sm[2] * c), one(sm[3] * c))});
      }
    }
  }
  const u64 tag = ((u64)(unsigned)__builtin_amdgcn_readfirstlane((unsigned)(e0 >> 32)) << 32 | (unsigned)__builtin_amdgcn_readfirstlane((unsigned)e0))
                  * 0x5851F42D4C957F2Dull + 0x14057B7EF767814Full;
  const bool lead = (lw == 0) && (lane == 0);
  if (type >= 2) { WAIT_BAR(0); if (lead) post(ws, F_KV + rb * 16 + ct, tag); }
  auto chunk = [&](int c) {
    const unsigned l = lds0 + ((3 - c) & 3) * 32768 + lw * 8192, so = WS_KV + ((c + x) & 7) * 32768 + lw * 8192;
    dma4k(l, vl, sws, so); dma4k(l + 4096, vl, sws, so + 4096);
  };
  STAMP(512, 10);
  if (lw == 0) {
    poll(ws, F_KV + x * 16 + (lane & 7), tag);
    __hip_atomic_store((AS3 unsigned*)(lds0 + P1_GO), 1u, __ATOMIC_RELAXED, __HIP_MEMORY_SCOPE_WORKGROUP);
  } else {
    while (__hip_atomic_load((AS3 unsigned*)(lds0 + P1_GO), __ATOMIC_RELAXED, __HIP_MEMORY_SCOPE_WORKGROUP) == 0u) __builtin_amdgcn_s_sleep(1);
    asm volatile("" ::: "memory");
  }
  STAMP(512, 11);
  dma4k(lds0 + P2_Q + lw * 4096, vl, sws, WS_QB + ((8 * x + (idx >> 2)) * 32 + 8 * (idx & 3)) * 2048 + lw * 4096);
  chunk(0);
  WAIT_BAR(0);
  poll(ws, F_KV + (lane >> 3) * 16 + (lane & 7), tag);
  chunk(1);
  if (type == 0) {
#pragma unroll
    for (int j = 0; j < 4; ++j) {
      const unsigned a = lds0 + (lw >> 1) * 32768 + ((lw & 1) * 4 + j) * 1024 + vl;
      const f32x4 sm = add4(add4(*(AS3 f32x4*)a, *(AS3 f32x4*)(a + 8192)), add4(*(AS3 f32x4*)(a + 16384), *(AS3 f32x4*)(a + 24576)));
      st16(sws, WS_KVP + ((2 * ct + (lw >> 1)) * 8 + rb) * 8192 + ((lw & 1) * 4 + j) * 1024 + vl, sm);
    }
    if (lw < 2) {
      const unsigned a = lds0 + P1_ZRED + lw * 1024 + lane * 4;
      st4(sws, WS_ZP + (((2 * ct + lw) * 8 + rb) * 64 + lane) * 4, one(*(AS3 float*)a + *(AS3 float*)(a + 256)) + one(*(AS3 float*)(a + 512) + *(AS3 float*)(a + 768)));
    }
  }
  if (DEFQ && type == 1) {
#pragma unroll
    for (int j = 0; j < 2; ++j) {
      u32x4 v[8];
#pragma unroll
      for (int i = 0; i < 8; ++i) v[i] = *(AS3 u32x4*)(lds0 + lw * 16384 + j * 8192 + i * 1024 + vl);
      const unsigned p = WS_QA + ((rb * 8 + (lw & 1) * 4 + j * 2 + 1) * 32 + ct * 4 + (lw >> 1) * 2) * 4096 + vl;
#pragma unroll
      for (int i = 0; i < 8; ++i) st16(sws, p + i * 1024, v[i]);
    }
    WAIT_BAR(16);
  } else WAIT_BAR(0);
  chunk(2); chunk(3);
  if (lead && type < (DEFQ ? 1 : 2)) post(ws, type == 0 ? F_K + ct * 8 + rb : F_Q + rb * 8 + ct, tag);
  const int br = !(x & 1), rb3 = 4 * (x >> 1) + (idx & 3), cb = idx >> 2;
  const unsigned short* wo = br ? wob : woa;
  unsigned pfs = 0;
  auto warm_wo = [&]() {
    pf_line(pfs, (const char*)wo + idx * 65536 + lw * 16384 + lane * 128);
    pf_line(pfs, (const char*)wo + idx * 65536 + lw * 16384 + 8192 + lane * 128);
  };
  if (lw == 1) *(AS3 unsigned*)(lds0 + P2_ZB + 128 + lane * 4) = lane < 32 ? 0x3f803f80u : 0u;
  WAIT_BAR(8);
  if (DEFQ && lead && type == 1) post(ws, F_Q + rb * 8 + ct, tag);
  if (br) {
    const int head = 4 * x + (idx & 7), tp = idx >> 3;
    poll(ws, F_K + (head >> 1) * 8 + (lane & 7), tag);
    STAMP(512, 12);
    chunk(4);
    u32x4 kvp[2][8], fq[4][4];
    float zs[8];
    auto ldq = [&](int e) {
#pragma unroll
      for (int j = 0; j < 4; ++j) fq[e][j] = ld16(sws, WS_QA + ((16 * tp + 4 * lw + e) * 32 + head) * 4096 + j * 1024 + vl);
    };
#pragma unroll
    for (int e = 0; e < 2; ++e)
#pragma unroll
      for (int p = 0; p < 8; ++p) kvp[e][p] = ld16(sws, WS_KVP + ((head * 8 + p) * 512 + (2 * lw + e) * 64 + lane) * 16);
#pragma unroll
    for (int p = 0; p < 8; ++p) zs[p] = ld4(sws, WS_ZP + ((head * 8 + p) * 64 + lane) * 4);
    asm volatile("s_barrier" ::: "memory");
#pragma unroll
    for (int e = 0; e < 2; ++e) {
#pragma unroll
      for (int p = 0; p < 8; ++p) PIN(kvp[e][p]);
#define F4(v) __builtin_bit_cast(f32x4, v)
      const f32x4 sm = add4(add4(add4(F4(kvp[e][0]), F4(kvp[e][1])), add4(F4(kvp[e][2]), F4(kvp[e][3]))), add4(add4(F4(kvp[e][4]), F4(kvp[e][5])), add4(F4(kvp[e][6]), F4(kvp[e][7]))));
#undef F4
      *(AS3 u32x2*)(lds0 + P2_KVF + lw * 1024 + vl + e * 8) = u32x2{pk2(sm[0], sm[1]), pk2(sm[2], sm[3])};
    }
    {
#pragma unroll
      for (int p = 0; p < 8; ++p) PIN(zs[p]);
      const float z = one(one(zs[0] + zs[1]) + one(zs[2] + zs[3])) + one(one(zs[4] + zs[5]) + one(zs[6] + zs[7]));
      if (lw == 0) *(AS3 unsigned short*)(lds0 + P2_ZB + lane * 2) = (unsigned short)(pk2(z, z) & 0xffffu);
    }
    poll(ws, F_Q + (2 * tp + (lane & 1)) * 8 + (head >> 2), tag);
    chunk(5);
    ldq(0); ldq(1); ldq(2); ldq(3);
    const int row = lane & 15, kg = lane >> 4;
    const bool on = (((row >> 2) ^ kg) & 1) == 0;
    const unsigned za = lds0 + P2_ZB + ((on && (row & 3) == 0) ? (kg >> 1) * 32 : (on && (row & 3) == 1) ? 128 : 256);
    const unsigned ka = lds0 + P2_KVF + vl;
    auto otile = [&](int e) {
#define BF(v) __builtin_bit_cast(bf16x8, v)
      f32x16 np = MFMA32(lds_r128(ka), BF(fq[e][0]), f32x16{});        f32x4 dp = MFMA16(lds_r128(za), BF(fq[e][0]), f32x4{});
      np = MFMA32(lds_r128(ka + 1024), BF(fq[e][1]), np);              dp = MFMA16(lds_r128(za + 16), BF(fq[e][1]), dp);
      f32x16 nm = MFMA32(lds_r128(ka + 2048), BF(fq[e][2]), f32x16{}); f32x4 dm = MFMA16(lds_r128(za + 64), BF(fq[e][2]), f32x4{});
      nm = MFMA32(lds_r128(ka + 3072), BF(fq[e][3]), nm);              dm = MFMA16(lds_r128(za + 80), BF(fq[e][3]), dm);
#undef BF
      const float a = __builtin_amdgcn_rcpf(dp[1]), b = __builtin_amdgcn_rcpf(dm[1]);
      const float r = __builtin_amdgcn_rcpf(one(dp[0] * a) + one(dm[0] * b) + 1e-6f), ar = one(a * r), br = one(b * r);
      f32x16 o;
#pragma unroll
      for (int i = 0; i < 16; ++i) o[i] = one(__builtin_fmaf(nm[i], br, one(np[i] * ar)));
      const unsigned p = WS_OA + ((16 * tp + 4 * lw + e) * 32 + head) * 2048 + vl;
      st16(sws, p, pack8<0>(o)); st16(sws, p + 1024, pack8<1>(o));
    };
    WAIT_BAR(24);
#pragma unroll
    for (int j = 0; j < 4; ++j) { PIN(fq[0][j]); PIN(fq[1][j]); }
    chunk(6);
    otile(0); otile(1);
    WAIT_BAR(12);
#pragma unroll
    for (int j = 0; j < 4; ++j) { PIN(fq[2][j]); PIN(fq[3][j]); }
    chunk(7);
    otile(2); otile(3);
    WAIT_BAR(0);
    if (lead) post(ws, F_OA + tp * 32 + head, tag);
    warm_wo();
  } else {
    chunk(4); warm_wo(); WAIT_BAR(10);
    chunk(5); WAIT_BAR(10);
    chunk(6); WAIT_BAR(8);
    chunk(7);
    if (blockIdx.x == 1 && lw == 1) {
      poll(ws, lane, tag); poll(ws, 64 + lane, tag); poll(ws, 128 + lane, tag);
      if (lane == 0) post(ws, F_EPOCH, tag);
    }
    WAIT_BAR(8);
  }
  const rsrc_t sw = mk_rsrc(wo, 1024u * 2048u);
  unsigned wv[4];
#pragma unroll
  for (int j = 0; j < 4; ++j) {
    const int k = 4 * (4 * lw + j) + (lane >> 4), c = (lane & 15) ^ ((lane >> 4) << 2) ^ ((j & 1) << 1);
    wv[j] = k * 2048 + (cb * 128 + c * 8) * 2 - j * 1024;
  }
  const unsigned osrc = (br ? WS_OB : WS_OA) + (rb3 * 4 + lw) * 65536;
  auto wst = [&](int t) {
    const unsigned sow = t * 64 * 2048;
    set_m0(lds0 + p3_wslot(t) + lw * 4096);
    dma16<0>(wv[0], sw, sow); dma16<1024>(wv[1], sw, sow); dma16<2048>(wv[2], sw, sow); dma16<3072>(wv[3], sw, sow);
  };
  auto ost = [&](int t) { dma4k(lds0 + P3_O + (t % NO) * 16384 + lw * 4096, vl, sws, osrc + t * 4096); };
  wst(0); wst(1); wst(2); wst(3);
  if (br) asm volatile("s_barrier" ::: "memory"); else WAIT_BAR(16);
  wst(4); wst(5);
  if (!br && lw == 0) poll(ws, F_OA + (rb3 >> 2) * 32 + (lane & 31), tag);
  asm volatile("s_barrier" ::: "memory");
  WAIT_BAR(0);
  asm volatile("s_barrier" ::: "memory");
  if (lead) post(ws, F_OB + x * 32 + idx, tag);
  STAMP(512, 13);
  if (br && lw == 0) poll(ws, F_OB + (rb3 >> 1) * 32 + (rb3 & 1) * 16 + (lane & 15), tag);
  asm volatile("s_barrier" ::: "memory");
  STAMP(512, 14);
#pragma unroll
  for (int t = 0; t < NF; ++t) ost(t);
  wait_bar<4 * (NF - 1)>();
#pragma unroll
  for (int t = NF; t < NO; ++t) ost(t);
  PIN(pfs);
#define RND(t) do { wait_bar<p3_inflight(t)>(); if (t + NW < 16) wst(t + NW); if (t + NO < 16) ost(t + NO); } while (0)
  RND(0); RND(1); RND(2); RND(3); RND(4); RND(5); RND(6); RND(7); RND(8); RND(9); RND(10); RND(11); RND(12); RND(13); RND(14);
#undef RND
  if (lw == 0) STOUT(10, 15);
}

__global__ __launch_bounds__(768, 3) void fused_fwd(const unsigned short* __restrict__ xa, const unsigned short* __restrict__ wqkv,
                                                    const unsigned short* __restrict__ wphi, const unsigned short* __restrict__ woa,
                                                    const unsigned short* __restrict__ xb, const unsigned short* __restrict__ wq,
                                                    const unsigned short* __restrict__ wk, const unsigned short* __restrict__ wv,
                                                    const unsigned short* __restrict__ wob, char* __restrict__ ws, unsigned short* __restrict__ out) {
  __shared__ __attribute__((aligned(1024))) char smem[LDS_BYTES];
  const unsigned lds0 = (unsigned)(uintptr_t)(AS3 char*)smem;
  const int tid = threadIdx.x, lane = tid & 63, w0 = __builtin_amdgcn_readfirstlane(tid >> 6), w = w0 < 4 ? w0 + 8 : w0 - 4;
  const char* pc0; unsigned clen;
  asm volatile("s_getpc_b64 %0\n.Lfw_head:\n\ts_mov_b32 %1, .Lfw_tail-.Lfw_head" : "=s"(pc0), "=s"(clen));
  unsigned co = (((blockIdx.x >> 3) & 7) * 40 + lane) * 128;
  co = lane < 40 && co < clen ? co : 0u;
  SBAR();
  asm volatile("" : "+s"(xa), "+s"(wqkv), "+s"(wphi), "+s"(xb), "+s"(wq), "+s"(wk), "+s"(wv), "+s"(ws), "+s"(out));
  const int b = blockIdx.x, x = b & 7, idx = b >> 3;
  const int rb = 4 * ((x >> 1) & 1) + (idx & 3);
  const int ct = idx < 16 ? 8 * (x >> 2) + 4 * (x & 1) + (idx >> 2) : 4 * (x >> 2) + ((idx >> 2) & 3);
  const int type = idx < 16 ? 0 : (x & 1) ? 1 : 2 + (ct & 1);
  const rsrc_t sws = mk_rsrc(ws, WS_END);
  unsigned sv = 0; (void)sv;
  STAMP(0, 0);
  if (__builtin_expect(w >= 8, 1)) {
    helper_program(lds0, type >= 2 ? xb : xa, type >= 2 ? wq : wqkv, type == 3 ? wv : type == 2 ? wk : wphi, woa, wob, ws, sws, pc0, co, type, rb, ct, x, idx, w - 8, lane);
    asm volatile("s_endpgm" ::: "memory");
  }
  if (w == 2) pf_bin(lds0 + P1_BIN, co, pc0);
  if (type == 0 && !(x & 1) && w < 2) pf_bin(lds0 + P1_BIN, (idx * 32 + (lane & 31)) * 128, w ? wv : wk);
  if (type < 2 && w >= 3 && w < 6) {
    constexpr int PF0 = 3, NS = 8 - PF0, PER = (NS * 512 + 47) / 48;
    const int j = (x & 1) * 16 + idx, id = (w == 5 ? j : 2 * j + w - 3) * PER + min(lane, PER - 1);
    unsigned o;
    if (w == 5) { const int st = min(id >> 9, NS - 1), r = id & 511;  o = (64 * ((PF0 + st + b_rot(x)) & 15) + (r >> 3)) * 2048 + (x >> 2) * 1024 + (r & 7) * 128; }
    else        { const int st = min(id >> 10, NS - 1), r = id & 1023; o = (((x >> 1) & 1) * 1024 + r) * 2048 + ((PF0 + st + b_rot(x)) & 15) * 128; }
    pf_bin(lds0 + P1_BIN, o, w == 5 ? wq : xb);
  }
  Attn at;
  auto p2_init = [&]() { attn_init(at, lds0, w, lane); };
  if (type == 0)      p1_compute<0>(lds0, ws, sws, rb, ct, w, lane, p2_init SVA);
  else if (type == 1) p1_compute<1>(lds0, ws, sws, rb, ct, w, lane, p2_init SVA);
  else                p1_compute<2>(lds0, ws, sws, rb, ct, w, lane, p2_init SVA);

  const int kh = w >> 2, h = lane >> 5;
  {
    bf16x8 q[2][2];
    f32x16 (&O)[2] = at.O; f32x4 (&L)[2] = at.L;
    unsigned tv = 0;
    attn_pass_fast(lds0, at, q, kh, lane, tv SVA);
    TSOUT();
    STAMP(0, 5);
    const unsigned xs = lds0 + P2_XO + w * 4096 + lane * 16;
    *(AS3 float*)(lds0 + P2_XL + w * 256 + lane * 4) = L[0][0];
#pragma unroll
    for (int g = 0; g < 4; ++g) *(AS3 f32x4*)(xs + g * 1024) = f32x4{O[0][4 * g], O[0][4 * g + 1], O[0][4 * g + 2], O[0][4 * g + 3]};
    unsigned xr = lds0 + P2_XO + (w ^ 4) * 4096 + lane * 16, xl = lds0 + P2_XL + (w ^ 4) * 256 + lane * 4;
    unsigned p = WS_OB + ((8 * x + (idx >> 2)) * 32 + 8 * (idx & 3) + 2 * (w & 3) + kh) * 2048 + lane * 16;
    PIN(xr); PIN(xl); PIN(p);
    LDS_BAR();
    const float lo = *(AS3 float*)xl;
    f32x4 o[4];
#pragma unroll
    for (int g = 0; g < 4; ++g) o[g] = *(AS3 f32x4*)(xr + g * 1024);
    SBAR();
    const float l = xhalf_sum(L[1][0] + lo);
#pragma unroll
    for (int i = 0; i < 16; ++i) O[1][i] += o[i >> 2][i & 3];
    auto out_b = [&](float den) { const f32x16 n = O[1] * __builtin_amdgcn_rcpf(den); st16(sws, p, pack8<0>(n)); st16(sws, p + 1024, pack8<1>(n)); };
    out_b(l);
    SBAR();
    bool bad = !(l < 1.8446744e19f && l > 8.6736174e-19f);
#pragma unroll
    for (int i = 0; i < 16; ++i) bad |= !(fabsf(O[1][i]) < 3.0e38f);
    if (__builtin_expect(__any(bad), 0)) {
      attn_pass_safe(sws, q[1][0], q[1][1], lane, O[1], L[1]);
      out_b(xhalf_sum(L[1][0]));
    }
    WAIT_BAR(0);
    STAMP(0, 6);
    asm volatile("s_barrier" ::: "memory");
    SBAR();
  }
  const int br = !(x & 1), rb3 = 4 * (x >> 1) + (idx & 3), cb = idx >> 2;
  const int wm = w & 1, wn = (w >> 1) & 1;
  const int r16 = lane & 15, kg = lane >> 4, q4 = r16 >> 2, p4 = lane & 3;
  typedef const AS3 char* lp;
  lp oa[2], wa[2][4];
#pragma unroll
  for (int ms = 0; ms < 2; ++ms) oa[ms] = (lp)(lds0 + P3_O + ((wm * 8192 + kh * (4096 + 2048) + kg * 512 + r16 * 16) ^ (ms * 4096)));
#pragma unroll
  for (int nt = 0; nt < 4; ++nt) {
    wa[0][nt] = (lp)(lds0 + P3_W + (kh * 32 + 16 * h + 4 * (kg & 1) + q4) * 256 + (((wn * 4 + nt) ^ (2 * q4 + (kg & 1))) << 5) + p4 * 8);
    wa[1][nt] = wa[0][nt] + 49152; PIN(wa[0][nt]); PIN(wa[1][nt]);
  }
  f32x4 acc[4][4];
  bf16x8 xf[2][4], wfr[2][4];
  auto rd = [&](int t, int i) {
    constexpr int ord[8] = {0, 4, 1, 5, 2, 3, 6, 7};
    const int f = ord[i];
    if (f < 4) xf[t & 1][f] = *(const AS3 bf16x8*)(oa[f >> 1] + (t % NO) * 16384 + (f & 1) * 256);
    else       wfr[t & 1][f - 4] = lds_trp(wa[(p3_wslot(t) - P3_W) / 49152][f - 4] + (p3_wslot(t) - P3_W) % 49152, 2048);
  };
  unsigned xs = lds0 + 65536 + w * 8192 + lane * 16, xr = xs ^ 32768;
  unsigned iw = lds0 + w * 4096 + r16 * 128 + ((h ^ (r16 & 7)) << 4) + (kg & 1) * 8, ir = lds0 + w * 4096 + (lane >> 3) * 128 + (((lane ^ (lane >> 3)) & 7) << 4);
  unsigned op = (rb3 * 128 + wm * 64 + kh * 32 + (lane >> 3)) * 4096 + br * 2048 + cb * 256 + wn * 128 + (lane & 7) * 16;
  const rsrc_t so = mk_rsrc(out, 2048u * 4096u);
  PIN(oa[0]); PIN(oa[1]); PIN(xs); PIN(xr); PIN(op); PIN(iw); PIN(ir);
  rd(0, 1); rd(0, 3); rd(0, 6); rd(0, 7);
  LDS_BAR();
  STAMP(0, 7);
  rd(0, 0); rd(0, 2); rd(0, 4); rd(0, 5);
#pragma unroll
  for (int t = 0; t < 14; ++t) {
    if (t) LDS_BAR();
#pragma unroll
    for (int i = 0; i < 16; ++i) {
      const int mt = MT_OF(i), nt = NT_OF(i);
      SBAR();
      if (t == 0) mul16(acc[mt][nt], wfr[0][nt], xf[0][mt]); else mac16(acc[mt][nt], wfr[t & 1][nt], xf[t & 1][mt]);
      SBAR();
      if (t == 0) { if (i == 5) LDS_BAR(); if (i >= 5 && i < 13) rd(1, i - 5); }
      else if (RD_AT(i) >= 0) rd(t + 1, RD_AT(i));
    }
    SBAR();
  }
  LDS_BAR();
#pragma unroll
  for (int i = 0; i < 32; ++i) {
    constexpr int ord[8] = {4, 1, 5, 3, 6, 7, 0, 2};
    const int j = ((i & 7) + 4 + (i >> 4) * 8) & 15, mt = MT_OF(j), nt = NT_OF(j), st = (i >> 3) & 1;
    SBAR(); mac16(acc[mt][nt], wfr[st][nt], xf[st][mt]); SBAR();
    if (i < 8) rd(15, ord[i]);
    if (i >= 16 && !(i & 1)) { const int ms = MT_OF(i / 2 - 4), ns = NT_OF(i / 2 - 4); *(AS3 f32x4*)(xs + ((ms - 2) * 4 + ns) * 1024) = acc[ms][ns]; }
  }
  SBAR();
  STAMP(0, 8);
  LDS_BAR();
  f32x4 o[8];
#pragma unroll
  for (int i = 0; i < 8; ++i) o[i] = *(AS3 f32x4*)(xr + i * 1024);
  SBAR();
  u32x4 v[4];
#pragma unroll
  for (int i = 0; i < 8; ++i) {
    const f32x4 a = acc[i >> 2][i & 3] + o[i];
    *(AS3 u32x2*)((iw ^ ((i & 3) << 5)) + (i >> 2) * 2048) = u32x2{pk2(a[0], a[1]), pk2(a[2], a[3])};
    if ((i & 3) == 3) {
      asm volatile("" ::: "memory");
      v[i >> 1 & 2] = *(AS3 u32x4*)(ir + (i >> 2) * 2048); v[(i >> 1 & 2) + 1] = *(AS3 u32x4*)(ir + (i >> 2) * 2048 + 1024);
      SBAR();
    }
  }
#pragma unroll
  for (int i = 0; i < 4; ++i) st16(so, op + i * 32768, v[i]);
  STAMP(0, 9);
  asm volatile(".Lfw_tail:");
}

extern "C" void kernel_launch(void* const* d_in, const int* in_sizes, int n_in, void* d_out, int out_size, void* d_ws, size_t ws_size,
                              hipStream_t stream) {
  (void)in_sizes; (void)n_in; (void)out_size; (void)ws_size;
  typedef const unsigned short* cp;
  fused_fwd<<<256, 768, 0, stream>>>((cp)d_in[0], (cp)d_in[1], (cp)d_in[2], (cp)d_in[3], (cp)d_in[4], (cp)d_in[5], (cp)d_in[6], (cp)d_in[7],
                                     (cp)d_in[8], (char*)d_ws, (unsigned short*)d_out);
}
```

```cpp
#include <hip/hip_runtime.h>
#include <hip/hip_bf16.h>
#include <stdint.h>
#pragma clang diagnostic ignored "-Wint-to-pointer-cast"
#pragma clang diagnostic ignored "-Winline-asm"

typedef __bf16   bf16x8 __attribute__((ext_vector_type(8)));
typedef __bf16   bf16x16 __attribute__((ext_vector_type(16)));
typedef __bf16   bf16x2 __attribute__((ext_vector_type(2)));
typedef short    s16x4  __attribute__((ext_vector_type(4)));
typedef short    s16x8  __attribute__((ext_vector_type(8)));
typedef float    f32x16 __attribute__((ext_vector_type(16)));
typedef float    f32x4  __attribute__((ext_vector_type(4)));
typedef float    f32x2  __attribute__((ext_vector_type(2)));
typedef unsigned u32x4  __attribute__((ext_vector_type(4)));
typedef unsigned u32x2  __attribute__((ext_vector_type(2)));
typedef unsigned long long u64;
typedef __amdgpu_buffer_rsrc_t rsrc_t;

#define AS1 __attribute__((address_space(1)))
#define AS3 __attribute__((address_space(3)))
#define DI __device__ __forceinline__
#define MFMA32(a, b, c) __builtin_amdgcn_mfma_f32_32x32x16_bf16((a), (b), (c), 0, 0, 0)
#define MFMA16(a, b, c) __builtin_amdgcn_mfma_f32_16x16x32_bf16((a), (b), (c), 0, 0, 0)
#define SM32(a, b, c) __builtin_amdgcn_smfmac_f32_32x32x32_bf16((a), (b), (c), odd, 0, 0)
#define SM16(a, b, c) __builtin_amdgcn_smfmac_f32_16x16x64_bf16((a), (b), (c), odd, 0, 0)
#define LO(S) __builtin_bit_cast(bf16x16, __builtin_shufflevector(S, S, 0, 1, 2, 3, 4, 5, 6, 7))
#define HI(S) __builtin_bit_cast(bf16x16, __builtin_shufflevector(S, S, 8, 9, 10, 11, 12, 13, 14, 15))
#define SBAR() __builtin_amdgcn_sched_barrier(0)
#define WAIT_BAR(N) asm volatile("s_waitcnt vmcnt(" #N ") lgkmcnt(0)\n\ts_barrier" ::: "memory")
#define LGKM0() __builtin_amdgcn_s_waitcnt(0xC07F)
#define LDS_BAR() do { asm volatile("" ::: "memory"); LGKM0(); asm volatile("s_barrier" ::: "memory"); } while (0)

#define RTA 0x00888800u
#define PF3 2
#define PFA 0
#define PD 2
#define SH 1
#define KW 0
#define PI 0
#define AL0 -1
#define AL1 -1
#define AL2 -1
#define AL3 -1
#define AL4 -1
template <int N> __device__ __forceinline__ void code_at() { if (N >= 0) asm volatile(".p2align 5\n\t.fill %0, 4, 0xBF800000" ::"i"(N < 0 ? 0 : N)); }
#define W1 1
#define W2 5
#define XK 0
#define QORD 0x1320
#define PDL 0
#define PDQ 0
#define DQS 8
#define VC1 0
#define SPL 1
#define AL5 -1
#define AL6 -1
#define HP0 30
#define HP1 44
#define HB 35
#define KA 1
#define SUP 0
#define FT 0
#define PKO 1
#define ROT 1
#define XE 0
#define SMS 0
#define NPK 0
#define UORD 0
#define SG(u) (UORD ? (u) >> 1 : (u) & 1)
#define TL(u) (UORD ? (u) & 1 : (u) >> 1)
#define PSX 0
#define PA0 3
#define PAW 3
#define PAX 1
#define PACE 3
#define GSLP 3
#define H1D 0
#define STG 1
#define NUMA 0
#define QS 0
#define PMX 0
#define FN 0
#define XB 1
#define XP 1
#define ROTB 0
#define ZREF 1
#define EPH 3
#define QEARLY 1
#define DEFQ 1
#define EPI 0
#define SWV 0
#define STAMP(th, i) do {} while (0)
#define ESTAMP(i) do {} while (0)
#define STOUT(lo, hi) do {} while (0)
#define SVP
#define SVA
#define TS(i) do {} while (0)
#define TSOUT() do {} while (0)
#define P1BAR() LDS_BAR()
constexpr int D = 1024;
constexpr float LOG2E = 1.4426950408889634f;
#define FM_SAFE 40.f
constexpr float QSCALE = 0.17677669529663687f * LOG2E;

constexpr unsigned WS_QA  = 0;
constexpr unsigned WS_QB  = WS_QA + (8u << 20);
constexpr unsigned WS_OA  = WS_QB + (4u << 20);
constexpr unsigned WS_OB  = WS_OA + (4u << 20);
constexpr unsigned WS_KV  = WS_OB + (4u << 20);
constexpr unsigned WS_KVP = WS_KV + (256u << 10);
constexpr unsigned WS_ZP  = WS_KVP + (2u << 20);
constexpr unsigned WS_FLG = WS_ZP + (64u << 10);
constexpr unsigned WS_END = WS_FLG + (16u << 10);
constexpr int F_K = 0, F_Q = 128, F_OA = 256, F_OB = 512, F_KV = 768, F_EPOCH = 1024, F_QB = 1280;

constexpr unsigned TTS = NUMA ? 131072 : 65536, HGS = NUMA ? 16384 : 8192;
DI unsigned tile_at(unsigned base, int tt, int head, unsigned pm, int flip) {
  if (!NUMA) return base + (tt * 32 + head) * 2048;
  const unsigned o = base + ((tt & 31) * 8 + (head >> 2)) * 16384;
  return o + ((((tt >> 5) ^ flip ^ (pm >> (o >> 21))) & 1) << 13) + (head & 3) * 2048;
}
DI unsigned pk2(float lo, float hi) {
  f32x2 v = {lo, hi};
  return __builtin_bit_cast(unsigned, __builtin_convertvector(v, bf16x2));
}
DI unsigned tr2(float lo, float hi) { return (XE & 2) ? __builtin_amdgcn_perm(__float_as_uint(hi), __float_as_uint(lo), 0x07060302u) : pk2(lo, hi); }
template <int SS> DI bf16x8 trunc8(const f32x16& x) {
  u32x4 p = {tr2(x[8 * SS + 0], x[8 * SS + 1]), tr2(x[8 * SS + 2], x[8 * SS + 3]), tr2(x[8 * SS + 4], x[8 * SS + 5]), tr2(x[8 * SS + 6], x[8 * SS + 7])};
  return __builtin_bit_cast(bf16x8, p);
}
template <int SS> DI bf16x8 pack8(const f32x16& x) {
  u32x4 p = {pk2(x[8 * SS + 0], x[8 * SS + 1]), pk2(x[8 * SS + 2], x[8 * SS + 3]), pk2(x[8 * SS + 4], x[8 * SS + 5]), pk2(x[8 * SS + 6], x[8 * SS + 7])};
  return __builtin_bit_cast(bf16x8, p);
}
DI u32x4 pack44(const f32x4& a, const f32x4& b) { return u32x4{pk2(a[0], a[1]), pk2(a[2], a[3]), pk2(b[0], b[1]), pk2(b[2], b[3])}; }
DI void to32(u32x4 x, u32x4 y, bf16x8 (&f)[2]) {
#pragma unroll
  for (int i = 0; i < 4; ++i) { auto r = __builtin_amdgcn_permlane16_swap(x[i], y[i], false, false); x[i] = r[0]; y[i] = r[1]; }
  f[0] = __builtin_bit_cast(bf16x8, x); f[1] = __builtin_bit_cast(bf16x8, y);
}
DI bf16x8 lds_r128(unsigned a) { return *(const AS3 bf16x8*)a; }
DI bf16x8 lds_tr2(unsigned a0, unsigned a1) {
  s16x4 lo = __builtin_amdgcn_ds_read_tr16_b64_v4i16((AS3 s16x4*)a0);
  s16x4 hi = __builtin_amdgcn_ds_read_tr16_b64_v4i16((AS3 s16x4*)a1);
  s16x8 r = __builtin_shufflevector(lo, hi, 0, 1, 2, 3, 4, 5, 6, 7);
  return __builtin_bit_cast(bf16x8, r);
}
DI bf16x8 lds_trp(const AS3 char* p, int d) {
  s16x4 lo = __builtin_amdgcn_ds_read_tr16_b64_v4i16((AS3 s16x4*)p);
  s16x4 hi = __builtin_amdgcn_ds_read_tr16_b64_v4i16((AS3 s16x4*)(p + d));
  return __builtin_bit_cast(bf16x8, __builtin_shufflevector(lo, hi, 0, 1, 2, 3, 4, 5, 6, 7));
}
DI rsrc_t mk_rsrc(const void* p, unsigned bytes) { return __builtin_amdgcn_make_buffer_rsrc((void*)p, (short)0, (int)bytes, 0x00020000); }
DI void set_m0(unsigned v) { asm volatile("s_mov_b32 m0, %0\n\ts_nop 0" ::"s"(v) : "memory"); }
template <int IMM> DI void dma16(unsigned voff, rsrc_t srd, unsigned soff) {
  asm volatile("buffer_load_dwordx4 %0, %1, %2 offen offset:%3 lds" ::"v"(voff), "s"(srd), "s"(soff), "i"(IMM) : "memory");
}
template <int IMM> DI void dma16d(unsigned voff, rsrc_t srd, unsigned soff) {
  asm volatile("buffer_load_dwordx4 %0, %1, %2 offen offset:%3 sc1 lds" ::"v"(voff), "s"(srd), "s"(soff), "i"(IMM) : "memory");
}
DI void dma4k(unsigned lds, unsigned voff, rsrc_t srd, unsigned soff) {
  set_m0(lds); dma16d<0>(voff, srd, soff); dma16d<1024>(voff, srd, soff); dma16d<2048>(voff, srd, soff); dma16d<3072>(voff, srd, soff);
}
template <class T> DI void st16(rsrc_t r, unsigned voff, T v, unsigned soff = 0) { __builtin_amdgcn_raw_buffer_store_b128(__builtin_bit_cast(u32x4, v), r, (int)voff, (int)soff, 16); }
DI void st8(rsrc_t r, unsigned voff, u32x2 v) { __builtin_amdgcn_raw_buffer_store_b64(v, r, (int)voff, 0, 16); }
DI void st4(rsrc_t r, unsigned voff, float v, unsigned soff) { __builtin_amdgcn_raw_buffer_store_b32(__float_as_uint(v), r, (int)voff, (int)soff, 16); }
DI u32x4 ld16(rsrc_t r, unsigned voff, unsigned soff = 0) { return __builtin_amdgcn_raw_buffer_load_b128(r, (int)voff, (int)soff, 16); }
DI float ld4(rsrc_t r, unsigned voff, unsigned soff) { return __uint_as_float(__builtin_amdgcn_raw_buffer_load_b32(r, (int)voff, (int)soff, 16)); }
DI void post(char* ws, int f, u64 tag) { __hip_atomic_store((AS1 u64*)(ws + WS_FLG) + f, tag, __ATOMIC_RELAXED, __HIP_MEMORY_SCOPE_AGENT); }
DI void poll(char* ws, int f, u64 tag) {
#pragma unroll 1
  for (int it = 0; it < (1 << 17); ++it) {
    const u64 v = __hip_atomic_load((AS1 u64*)(ws + WS_FLG) + f, __ATOMIC_RELAXED, __HIP_MEMORY_SCOPE_AGENT);
    if (__all(v == tag)) break;
    __builtin_amdgcn_s_sleep(2);
  }
  asm volatile("" ::: "memory");
}
DI void poll(rsrc_t r, unsigned voff, int f, u64 tag) {
#pragma unroll 1
  for (int it = 0; it < (1 << 17); ++it) {
    u64 v;
    asm volatile("buffer_load_dwordx2 %0, %1, %2, %3 offen sc1\n\ts_waitcnt vmcnt(0)" : "=v"(v) : "v"(voff), "s"(r), "s"(WS_FLG + f * 8) : "memory");
    if (__all(v == tag)) break;
    __builtin_amdgcn_s_sleep(2);
  }
}
DI unsigned arrive(unsigned cnt, int lane) {
  unsigned o = 0;
  if (lane == 0) o = __hip_atomic_fetch_add((AS3 unsigned*)cnt, 1u, __ATOMIC_RELAXED, __HIP_MEMORY_SCOPE_WORKGROUP);
  return __builtin_amdgcn_readfirstlane(o);
}
DI void meet(unsigned cnt, int lane) {
  asm volatile("" ::: "memory");
  if (lane == 0) __hip_atomic_fetch_add((AS3 unsigned*)cnt, 1u, __ATOMIC_RELAXED, __HIP_MEMORY_SCOPE_WORKGROUP);
  while (__hip_atomic_load((AS3 unsigned*)cnt, __ATOMIC_RELAXED, __HIP_MEMORY_SCOPE_WORKGROUP) < 4u) {}
  asm volatile("" ::: "memory");
}
DI void pf_line(unsigned& sink, const void* p) { asm volatile("global_load_dword %0, %1, off" : "+v"(sink) : "v"(p) : "memory"); }
#define PIN(x) asm volatile("" : "+v"(x))
DI void pf_bin(unsigned bin, unsigned voff, const void* base) {
  asm volatile("s_mov_b32 m0, %0\n\ts_nop 0\n\tglobal_load_lds_dword %1, %2" ::"s"(bin), "v"(voff), "s"(base) : "memory");
}
DI void pf_bin_d(unsigned bin, unsigned voff, const void* base) {
  asm volatile("s_mov_b32 m0, %0\n\ts_nop 0\n\tglobal_load_lds_dword %1, %2 sc1" ::"s"(bin), "v"(voff), "s"(base) : "memory");
}
#define SL(o) "s_load_dwordx2 s[100:101], %0, %1 offset:" #o "+60\n\t"
#define SL4 SL(0) SL(0x800) SL(0x1000) SL(0x1800)
#define SL7 SL4 SL(0x2000) SL(0x2800) SL(0x3000)
DI void touch7(const char* p, unsigned o) { asm volatile(SL7 ::"s"(p), "s"(o) : "s100", "s101", "memory"); }
DI float one(float v) { asm("" : "+v"(v)); return v; }
DI f32x4 add4(f32x4 a, f32x4 b) { return f32x4{one(a[0] + b[0]), one(a[1] + b[1]), one(a[2] + b[2]), one(a[3] + b[3])}; }
DI float xhalf_max(float v) {
  auto r = __builtin_amdgcn_permlane32_swap(__float_as_uint(v), __float_as_uint(v), false, false);
  return fmaxf(__uint_as_float(r[0]), __uint_as_float(r[1]));
}
DI float xhalf_min(float v) {
  auto r = __builtin_amdgcn_permlane32_swap(__float_as_uint(v), __float_as_uint(v), false, false);
  return fminf(__uint_as_float(r[0]), __uint_as_float(r[1]));
}
DI float xhalf_sum(float v) {
  auto r = __builtin_amdgcn_permlane32_swap(__float_as_uint(v), __float_as_uint(v), false, false);
  return __uint_as_float(r[0]) + __uint_as_float(r[1]);
}
DI float max16(const f32x16& x) {
  float a = fmaxf(fmaxf(x[0], x[1]), x[2]), b = fmaxf(fmaxf(x[3], x[4]), x[5]);
  a = fmaxf(fmaxf(a, x[6]), x[7]);   b = fmaxf(fmaxf(b, x[8]), x[9]);
  a = fmaxf(fmaxf(a, x[10]), x[11]); b = fmaxf(fmaxf(b, x[12]), x[13]);
  a = fmaxf(fmaxf(a, x[14]), x[15]);
  return fmaxf(a, b);
}
DI float min16(const f32x16& x) {
  float a = fminf(fminf(x[0], x[1]), x[2]), b = fminf(fminf(x[3], x[4]), x[5]);
  a = fminf(fminf(a, x[6]), x[7]);   b = fminf(fminf(b, x[8]), x[9]);
  a = fminf(fminf(a, x[10]), x[11]); b = fminf(fminf(b, x[12]), x[13]);
  a = fminf(fminf(a, x[14]), x[15]);
  return fminf(a, b);
}
DI bool fm_wide(const f32x16& p) {
  float a = fmaxf(fmaxf(fabsf(p[0]), fabsf(p[1])), fabsf(p[2])), b = fmaxf(fmaxf(fabsf(p[3]), fabsf(p[4])), fabsf(p[5]));
#pragma unroll
  for (int i = 6; i < 14; i += 4) { a = fmaxf(fmaxf(a, fabsf(p[i])), fabsf(p[i + 1])); b = fmaxf(fmaxf(b, fabsf(p[i + 2])), fabsf(p[i + 3])); }
  a = fmaxf(fmaxf(a, fabsf(p[14])), fabsf(p[15]));
  return __any(fmaxf(a, b) > FM_SAFE);
}
DI void fm_shift(f32x16& p, int sign) {
  const float m = sign ? xhalf_min(min16(p)) : xhalf_max(max16(p));
#pragma unroll
  for (int i = 0; i < 16; ++i) p[i] -= m;
}
DI unsigned row_sum_sel(int lane) { return ((lane & 11) == 0 && (((lane >> 2) ^ (lane >> 4)) & 1) == 0) ? 0x3f803f80u : 0u; }

DI int b_rot(int x) { return ROTB * (((x >> 1) ^ (x >> 2)) & 1); }

constexpr int NL = PSX ? 7 : 4, JB1 = PSX ? 19 : 32, JB2 = PSX ? 29 : 48;
DI unsigned ahead_off(int x, int jb, int lw) {
  const int bxr = (x >> 2) ^ 1, bxc = (x >> 1) & 1, i = (jb - JB1) * 4 + lw, ja = jb - JB2;
  const int rb0 = bxr * 1024 + bxc * 512, ra0 = bxc * 1024 + (x >> 2) * 512, r0 = jb < JB1 ? rb0 : ra0, j = jb < JB1 ? jb : ja;
  const unsigned ow = (bxr * 32 + min(NL * (i >> 3), 32 - NL)) * 2048 + bxc * 1024 + (i & 7) * 128, ox = (r0 + min(4 * NL * j + NL * lw, 512 - NL)) * 2048;
  return (unsigned)(jb - JB1) < JB2 - JB1 ? ow : ox;
}

struct Ahead {
  const char* p; unsigned step, rot;
  DI void touch(int t) const {
    if (NL == 7) touch7(p, ((t + rot) & 15) * step); else asm volatile(SL4 ::"s"(p), "s"(((t + rot) & 15) * step) : "s100", "s101", "memory");
  }
};
DI Ahead ahead(int x, int idx, int lw, int rot, const void* xa, const void* xb, const void* wq) {
  const int jb = (x & 1) * 16 + idx;
  const void* p1 = jb < JB2 ? wq : xa;
  return {(const char*)(jb < JB1 ? xb : p1) + ahead_off(x, jb, lw), (unsigned)(jb - JB1) < JB2 - JB1 ? 131072u : 128u, jb < JB2 ? 0u : (unsigned)rot};
}

constexpr int LDS_BYTES = 163840;
constexpr int P1_WOFF = 32768, P1_EOFF = 49152, P1_STAGE = 53248, P1_ZRED = 3 * P1_STAGE, P1_CNT = P1_ZRED + 2048, P1_GO = P1_CNT + 16, P1_BIN = P1_CNT + 256, P1_PHI = 65536;
constexpr int P2_Q = 131072, P2_KVF = 147456, P2_ZB = 151552;
constexpr int P2_XO = 32768, P2_XL = 0, P2_TAG = 2048;
constexpr int NO = 4, NW = 6, NF = 3;
constexpr int P3_O = 0, P3_W = NO * 16384;
DI constexpr int p3_wslot(int t) { return P3_W + ((t + NW - 4) % NW) * 16384; }
DI constexpr int p3_inflight(int t) {
  int n = 0, after = 0;
  for (int j = 0; j < NO; ++j) { n += 4; if (j == t + 1) after = n; }
  for (int u = 0; u < t; ++u) {
    if (u + NW < 16) { n += 4; if (u + NW == t + 1) after = n; }
    if (u + NO < 16) { n += 4; if (u + NO == t + 1) after = n; }
  }
  return n - after;
}
template <int N> DI void wait_bar() { asm volatile("s_waitcnt vmcnt(%0) lgkmcnt(0)\n\ts_barrier" ::"i"(N) : "memory"); }

DI void mac16(f32x4& c, bf16x8 a, bf16x8 b) { asm volatile("v_mfma_f32_16x16x32_bf16 %0, %1, %2, %0" : "+v"(c) : "v"(a), "v"(b)); }
DI void mul16(f32x4& c, bf16x8 a, bf16x8 b) { asm volatile("v_mfma_f32_16x16x32_bf16 %0, %1, %2, 0" : "=v"(c) : "v"(a), "v"(b)); }
DI void acc_fence(f32x4 (&c)[4][4]) {
  asm volatile("s_nop 7" : "+v"(c[0][0]), "+v"(c[0][1]), "+v"(c[0][2]), "+v"(c[0][3]), "+v"(c[1][0]), "+v"(c[1][1]), "+v"(c[1][2]), "+v"(c[1][3]),
                           "+v"(c[2][0]), "+v"(c[2][1]), "+v"(c[2][2]), "+v"(c[2][3]), "+v"(c[3][0]), "+v"(c[3][1]), "+v"(c[3][2]), "+v"(c[3][3]));
}
#define MT_OF(i) (((((i) >> 2) ^ ((i) >> 3)) & 1) * 2 + ((((i) >> 1) ^ (i)) & 1))
#define NT_OF(i) ((((i) >> 3) & 1) * 2 + (((i) >> 1) & 1))
#define RD_AT(i) ((i) < 12 && (i) % 3 != 2 ? (i) / 3 * 2 + (i) % 3 : -1)
template <int TYPE, class G, class F>
DI void p1_compute(unsigned lds0, char*& ws, rsrc_t& sws, unsigned pm, int rb, int ct, int w, int lane, G&& first, F&& p2_init SVP) {
  if (H1D == 4) STAMP(0, 5);
  unsigned hv = 0; int hc = 0; (void)hv; (void)hc;
  const int wm = w & 3, wn = w >> 2, r32 = lane & 31, h = lane >> 5;
  if (w == 0 && lane < 8) *(AS3 unsigned*)(lds0 + P1_CNT + lane * 4) = 0u;

  typedef const AS3 char* lp;
  const int r16 = lane & 15, kg = lane >> 4, q4 = r16 >> 2, p4 = lane & 3, chalf = kg & 1;
  lp xa[2][2], wa[2][4];
#pragma unroll
  for (int kk = 0; kk < 2; ++kk) {
    xa[0][kk] = (lp)(lds0 + (wm * 64 + r16) * 128 + (((4 * kk + kg) ^ (r16 >> 1)) << 4));
    xa[1][kk] = xa[0][kk] + 2 * P1_STAGE; PIN(xa[0][kk]); PIN(xa[1][kk]);
  }
#pragma unroll
  for (int nt = 0; nt < 4; ++nt) {
    wa[0][nt] = (lp)(lds0 + P1_WOFF + (8 * kg + q4) * 256 + (((wn * 4 + nt) ^ (2 * q4 + chalf)) << 5) + p4 * 8);
    wa[1][nt] = wa[0][nt] + 2 * P1_STAGE; PIN(wa[0][nt]); PIN(wa[1][nt]);
  }

  f32x4 acc[4][4];
  bf16x8 xf[2][4], wfr[2][4];
  auto rd = [&](int set, int b, int kk, int i) {
    constexpr int ord[8] = {0, 4, 1, 5, 2, 3, 6, 7};
    const int f = ord[i];
    if (f < 4) xf[set][f] = *(const AS3 bf16x8*)(xa[b >> 1][kk] + (b & 1) * P1_STAGE + f * 2048);
    else       wfr[set][f - 4] = lds_trp(wa[b >> 1][f - 4] + (b & 1) * P1_STAGE + kk * 8192, 1024);
  };
  auto sub = [&](int set, int b, int kk, bool first = false) {
    if (W1 && !first && set == 0) LGKM0();
#pragma unroll
    for (int i = 0; i < 16; ++i) {
      const int mt = MT_OF(i), nt = NT_OF(i);
      const bool tr = TYPE == 0 && nt >= 2;
      const bf16x8 a = tr ? xf[set][mt] : wfr[set][nt], bb = tr ? wfr[set][nt] : xf[set][mt];
      SBAR();
      if (first) mul16(acc[mt][nt], a, bb); else mac16(acc[mt][nt], a, bb);
      SBAR();
      if (b >= 0 && RD_AT(i) >= 0) rd(set ^ 1, b, kk, RD_AT(i));
    }
    SBAR();
  };
  if (ROT) {
#pragma unroll
    for (int i = 0; i < 16; ++i) { acc[i >> 2][i & 3] = f32x4{}; PIN(acc[i >> 2][i & 3]); }
  }
  if (H1D == 4) STAMP(0, 6);
  SBAR();
  first();
  if (H1D == 4) STAMP(0, 7);
  code_at<TYPE == 0 ? AL0 : TYPE == 1 ? AL1 : AL2>();
  P1BAR();
  STAMP(0, 1);
#pragma unroll
  for (int i = 0; i < 8; ++i) rd(0, 0, 0, i);
  SBAR();
  unsigned wfa = lds0 + P1_PHI + (TYPE == 0 ? wn : 2 * wn) * 2048 + (8 * h + q4) * 64 + chalf * 32 + p4 * 8;
  const unsigned img = lds0 + w * 8192;
  unsigned wr[4], rt[2], pa = img + lane * 16, za = lds0 + P1_ZRED + w * 256 + ((lane >> 2) & 1) * 64 + (lane >> 3) * 16 + (lane & 3) * 4, su = 0;
  unsigned go = TYPE == 1 ? WS_QA + ((rb * 8 + wm * 2) * 32 + ct * 4 + wn * 2) * 4096 + lane * 16
                          : tile_at(WS_QB, rb * 8 + wm * 2, ct * 4 + wn * 2, pm, QS) + chalf * 1024 + h * 512 + r16 * 16;
  unsigned c0 = (lane & 7) == 0 ? 0x3f803f80u : 0u, c4 = (lane & 7) == 4 ? 0x3f803f80u : 0u;
  int odd = 0xDDDD;
  if (TYPE <= 1) { PIN(wfa); PIN(pa); }
  if (TYPE >= 1) PIN(go);
  if (TYPE == 0) {
    const int t0 = 8 * h + q4;
    wr[0] = img + 4096 + r32 * 64 + ((h ^ ((r32 >> 1) & 7)) << 3); PIN(wr[0]);
#pragma unroll
    for (int g = 1; g < 4; ++g) { wr[g] = wr[0] ^ (g << 4); PIN(wr[g]); }
    rt[0] = img + 4096 + t0 * 64 + (((4 * chalf + p4) ^ (t0 >> 1)) << 3); PIN(rt[0]);
    rt[1] = rt[0] ^ 16; PIN(rt[1]);
    su = row_sum_sel(lane);
    PIN(za); PIN(su);
    if (SMS) { PIN(c0); PIN(c4); PIN(odd); }
  }
#pragma unroll 1
  for (int t = 0; t < (TYPE < 2 ? 5 : 4); ++t) {
    sub(0, 0, 1); P1BAR(); sub(1, 1, 0);
    sub(0, 1, 1); P1BAR(); sub(1, 2, 0);
    sub(0, 2, 1); P1BAR(); sub(1, 0, 0);
  }
  sub(0, 0, 1); P1BAR();
  if (TYPE >= 2) { sub(1, 1, 0); sub(0, 1, 1); P1BAR(); sub(1, 2, 0); sub(0, 2, 1); P1BAR(); }
  bf16x8 wf[2][2];
  auto qst = [&](int qd) {
    const int ms = MT_OF(4 * qd) >> 1, ns = NT_OF(4 * qd) >> 1;
#pragma unroll
    for (int tb = 0; tb < 2; ++tb) st16(sws, go + ms * TTS + ns * 2048 + tb * 256, pack44(acc[2 * ms + tb][2 * ns], acc[2 * ms + tb][2 * ns + 1]));
  };
  if (STG && TYPE >= 2) {
    bf16x8 xl[4], wl[4];
#pragma unroll
    for (int i = 0; i < 48; ++i) {
      constexpr int qd[12] = {0, 1, 2, 0, 0, 1, 1, 2, 2, 3, 3, 3}, ks[12] = {0, 0, 0, 1, 2, 1, 2, 1, 2, 0, 1, 2};
      constexpr int ord[16] = {0, 4, 1, 5, 8, 12, 9, 13, 2, 3, 10, 11, 6, 7, 14, 15};
      const int j = 4 * qd[i >> 2] + (i & 3), mt = MT_OF(j), nt = NT_OF(j), k = ks[i >> 2];
      SBAR();
      mac16(acc[mt][nt], k == 2 ? wl[nt] : wfr[k ^ 1][nt], k == 2 ? xl[mt] : xf[k ^ 1][mt]);
      SBAR();
      if (i < 24 && i % 3 != 2) {
        const int f = ord[i / 3 * 2 + i % 3], kk = f >> 3, e = f & 3;
        if (f & 4) (kk ? wl[e] : wfr[0][e]) = lds_trp(wa[0][e] + kk * 8192, 1024);
        else       (kk ? xl[e] : xf[0][e]) = *(const AS3 bf16x8*)(xa[0][kk] + e * 2048);
      }
      if (i == 23 || i == 31 || i == 39) {
        const int q = (i - 23) / 8, m0 = MT_OF(4 * q) & 2, n0 = NT_OF(4 * q) & 2;
        asm volatile("s_nop 3" : "+v"(acc[m0][n0]), "+v"(acc[m0][n0 + 1]), "+v"(acc[m0 + 1][n0]), "+v"(acc[m0 + 1][n0 + 1]));
        qst(q);
      }
    }
  } else
#pragma unroll
  for (int i = 0; i < 16; ++i) {
    const int mt = MT_OF(i), nt = NT_OF(i);
    SBAR();
    if (TYPE == 0 && nt >= 2) mac16(acc[mt][nt], xf[1][mt], wfr[1][nt]); else mac16(acc[mt][nt], wfr[1][nt], xf[1][mt]);
    SBAR();
    if (TYPE <= 1 && i < 2 * TYPE + 2) { const unsigned a = wfa + (i >> 1) * 2048 + (i & 1) * 256; wf[i >> 1][i & 1] = lds_tr2(a, a + 1024); }
    if (QEARLY && TYPE >= 2 && i >= 7 && i < 15 && (i & 3) == 3) {
      const int m0 = MT_OF(i - 7) & 2, n0 = NT_OF(i - 7) & 2;
      asm volatile("s_nop 3" : "+v"(acc[m0][n0]), "+v"(acc[m0][n0 + 1]), "+v"(acc[m0 + 1][n0]), "+v"(acc[m0 + 1][n0 + 1]));
      qst(i / 4 - 1);
    }
  }
  SBAR();
  if (STG && TYPE >= 2) asm volatile("s_nop 7" : "+v"(acc[0][2]), "+v"(acc[0][3]), "+v"(acc[1][2]), "+v"(acc[1][3])); else acc_fence(acc);
  STAMP(0, 2);
  auto lead = [&](bool on) { if (wn) { if (on) __builtin_amdgcn_s_setprio(1); else __builtin_amdgcn_s_setprio(0); } };
  auto proj = [&](int ms, int ns) {
    bf16x8 f[2];
    to32(pack44(acc[2 * ms][2 * ns], acc[2 * ms][2 * ns + 1]), pack44(acc[2 * ms + 1][2 * ns], acc[2 * ms + 1][2 * ns + 1]), f);
    return MFMA32(wf[ns][1], f[1], MFMA32(wf[ns][0], f[0], f32x16{}));
  };

  if (TYPE >= 2) {
    if (!QEARLY) { qst(0); qst(1); }
    if (!STG) qst(2);
    qst(3);
    SBAR();
    p2_init();
    WAIT_BAR(0);
  } else if (TYPE == 1) {
    f32x16 pq[4];
    if (EPH) lead(true);
#pragma unroll
    for (int n = 0; n < 4; ++n) { const int i = QORD >> 4 * n & 3; pq[i] = proj(i >> 1, i & 1); }
#pragma unroll
    for (int n = 0; n < 4; ++n) {
      const int i = QORD >> 4 * n & 3;
      if (n == (EPH > 4 ? 3 : EPH)) lead(false);
      if (n == 3) p2_init();
      if (!(XE & 1)) pq[i] *= LOG2E;
      const bool wide = fm_wide(pq[i]);
      ESTAMP(5 + n);
#pragma unroll
      for (int sg = 0; sg < 2; ++sg) {
        if (__builtin_expect(wide, 0)) fm_shift(pq[i], sg);
        f32x16 e;
#pragma unroll
        for (int j = 0; j < 16; ++j) e[j] = __builtin_amdgcn_exp2f(sg ? -pq[i][j] : pq[i][j]);
        const unsigned o = (i & 1) * 4096 + sg * 2048;
        if (DEFQ && i >= 2) { *(AS3 bf16x8*)(pa + o) = trunc8<0>(e); *(AS3 bf16x8*)(pa + o + 1024) = trunc8<1>(e); }
        else { st16(sws, go + (i >> 1) * 131072 + o, trunc8<0>(e)); st16(sws, go + (i >> 1) * 131072 + o + 1024, trunc8<1>(e)); }
      }
    }
    if (EPH == 4) lead(false);
  } else {
    const bf16x8 sel = __builtin_bit_cast(bf16x8, u32x4{su, su, su, su});
    f32x16 pk[2], kv[2] = {};
    f32x4 zz[2] = {};
    bf16x8 vf[2][2], af[2][2];
    bool wide[2];
    if (EPH) lead(true);
#pragma unroll
    for (int ms = 0; ms < 2; ++ms) pk[ms] = proj(ms, 0);
#pragma unroll
    for (int ms = 0; ms < 2; ++ms) { to32(pack44(acc[2 * ms][2], acc[2 * ms + 1][2]), pack44(acc[2 * ms][3], acc[2 * ms + 1][3]), vf[ms]); PIN(vf[ms][0]); PIN(vf[ms][1]); }
#pragma unroll
    for (int ms = 0; ms < 2; ++ms) { if (!(XE & 1)) pk[ms] *= LOG2E; wide[ms] = fm_wide(pk[ms]); }
    auto put = [&](int sg) {
#pragma unroll
      for (int g = 0; g < 4; ++g) *(AS3 f32x4*)(pa + sg * 4096 + g * 1024) = f32x4{kv[sg][4 * g], kv[sg][4 * g + 1], kv[sg][4 * g + 2], kv[sg][4 * g + 3]};
    };
    f32x16 e[2];
    float rs[2];
    f32x4 lsum[2];
    bf16x8 csel = __builtin_bit_cast(bf16x8, u32x4{c0, c0, c4, c4});
    asm volatile("" : "=v"(lsum[0]), "=v"(lsum[1]), "+v"(csel));
    auto scale = [&](int u) { rs[u & 1] = __builtin_amdgcn_rcpf(lsum[u & 1][0]); PIN(rs[u & 1]); };
    auto mm = [&](int u, int j) {
      if (j & 1) zz[SG(u)] = MFMA16(sel, af[u & 1][j >> 1], zz[SG(u)]); else kv[SG(u)] = MFMA32(af[u & 1][j >> 1], vf[TL(u)][j >> 1], kv[SG(u)]);
    };
    auto exps = [&](int u, int mu) {
      f32x16& p = pk[TL(u)];
      if (__builtin_expect(wide[TL(u)], 0)) fm_shift(p, SG(u));
      f32x2 sm;
      f32x4 ls = lsum[u & 1];
      ls[0] = 0.f; PIN(ls);
#pragma unroll
      for (int i = 0; i < 16; i += 2) {
        if (mu >= 0 && i >= 8) { SBAR(); mm(mu, (i - 8) >> 1); SBAR(); }
        const f32x2 ev = {__builtin_amdgcn_exp2f(SG(u) ? -p[i] : p[i]), __builtin_amdgcn_exp2f(SG(u) ? -p[i + 1] : p[i + 1])};
        e[u & 1][i] = ev[0]; e[u & 1][i + 1] = ev[1];
        if (!SMS) { if (i == 0) sm = ev; else sm += ev; }
        if (SMS && i == 8) { SBAR(); ls = SM16(csel, LO(e[u & 1]), ls); SBAR(); }
      }
      if (SMS) { if (SMS > 1 && u) scale(u - 1); SBAR(); lsum[u & 1] = SM16(csel, HI(e[u & 1]), ls); PIN(lsum[u & 1]); SBAR(); } else rs[u & 1] = __builtin_amdgcn_rcpf(xhalf_sum(sm[0] + sm[1]));
    };
    auto rest = [&](int u, int mu = -1) {
      if (SMS == 1 || (SMS > 1 && u == 3)) scale(u);
      f32x16 n;
      if (NPK) {
#pragma unroll
        for (int i = 0; i < 16; ++i) n[i] = one(e[u & 1][i] * rs[u & 1]);
      } else n = e[u & 1] * rs[u & 1];
#pragma unroll
      for (int g = 0; g < 4; ++g) {
        if (mu >= 0) { SBAR(); mm(mu, (g >> 1) + (g & 1) * 2); SBAR(); }
        *(AS3 u32x2*)(wr[g] + (u & 1) * 2048) = u32x2{tr2(n[4 * g], n[4 * g + 1]), tr2(n[4 * g + 2], n[4 * g + 3])};
      }
      asm volatile("" ::: "memory");
#pragma unroll
      for (int s = 0; s < 2; ++s) af[u & 1][s] = lds_tr2(rt[s] + s * 256 + (u & 1) * 2048, rt[s] + s * 256 + (u & 1) * 2048 + 1024);
      asm volatile("" ::: "memory");
      SBAR();
    };
    ESTAMP(5);
    if (EPH == 1) lead(false);
    exps(0, -1); exps(1, -1); rest(0);
    ESTAMP(6);
    if (EPH == 2) lead(false);
    exps(2, 0);  rest(1);
    ESTAMP(7);
    if (EPH == 3) lead(false);
    exps(3, 1);
    if (EPH == 5) lead(false);
    if (UORD == 2) { put(0); SBAR(); }
    rest(2);
    if (EPH == 6) lead(false);
    if (UORD == 1) { put(0); SBAR(); }
    rest(3, 2);
    ESTAMP(8);
    if (!UORD) put(0);
    SBAR(); mm(3, 0); mm(3, 2); mm(3, 1); mm(3, 3); SBAR();
    p2_init();
    SBAR();
    put(1);
    if (EPH == 4) lead(false);
    if (lane < 32) { *(AS3 float*)za = zz[0][0]; *(AS3 float*)(za + 128) = zz[1][0]; }
  }
  STAMP(0, 3);
}

struct Attn { f32x16 O[2], S1; f32x4 L[2]; bf16x8 vb, sel; int odd; unsigned a1, qa, qb; };
DI bf16x8 attn_sel(int lane) {
  const unsigned s0 = (lane & 15) == 0 ? 0x3f803f80u : 0u, s4 = (lane & 15) == 4 ? 0x3f803f80u : 0u;
  return __builtin_bit_cast(bf16x8, u32x4{s0, s0, s4, s4});
}
DI void attn_init(Attn& a, unsigned lds0, int w, int lane) {
  const int kh = w >> 2;
  a.vb = bf16x8{};
  PIN(a.vb); a.S1 = MFMA32(a.vb, a.vb, f32x16{}); a.L[0] = MFMA16(a.vb, a.vb, f32x4{});
  PIN(a.vb); a.O[0] = MFMA32(a.vb, a.vb, f32x16{}); a.L[1] = MFMA16(a.vb, a.vb, f32x4{});
  PIN(a.vb); a.O[1] = MFMA32(a.vb, a.vb, f32x16{});
  a.sel = attn_sel(lane); a.odd = 0xDDDD;
  a.a1 = lds0 + lane * 16 + 98304 + kh * 16384; a.qa = lds0 + P2_Q + (2 * (w & 3) + (kh ^ 1)) * 2048 + lane * 16; a.qb = a.qa ^ 2048;
  PIN(a.sel); PIN(a.odd); PIN(a.a1); PIN(a.qa); PIN(a.qb); PIN(a.S1); PIN(a.L[0]); PIN(a.O[0]); PIN(a.L[1]); PIN(a.O[1]);
}
DI void attn_pass_fast(unsigned lds0, Attn& at, bf16x8 (&q)[2][2], int kh, int lane, unsigned& tv SVP) {
  typedef const AS3 char* lp;
  lp l0 = (lp)(lds0 + lane * 16), la = l0 + kh * 16384;
#define RD(p) (*(const AS3 bf16x8*)(p))
  f32x16 (&O)[2] = at.O; f32x4 (&L)[2] = at.L;
  f32x16 S0, S1 = at.S1;
  bf16x8 vb = at.vb;
  const bf16x8 sel = at.sel;
  const int odd = at.odd;
  const unsigned a1 = at.a1, qa = at.qa, qb = at.qb;
  code_at<AL3>();
  LDS_BAR();
  STAMP(0, 4);
  TS(0);
  const f32x16 negm[2] = {};
  bf16x8 ka = lds_r128(a1);
  q[0][0] = lds_r128(qa);
  bf16x8 kb = lds_r128(a1 + 1024);
  q[0][1] = lds_r128(qa + 1024); q[1][0] = lds_r128(qb); q[1][1] = lds_r128(qb + 1024);
  bf16x8 va = lds_r128(a1 + 2048);
  SBAR();
  if (kh) __builtin_amdgcn_s_setprio(1);
  asm volatile("v_mfma_f32_32x32x16_bf16 %0, %1, %2, 0" : "=&v"(S0) : "v"(ka), "v"(q[0][0]));
  asm volatile("v_mfma_f32_32x32x16_bf16 %0, %1, %2, %0\n\ts_nop 11" : "+v"(S0) : "v"(kb), "v"(q[0][1]));
  SBAR();
  typedef float f32x8 __attribute__((ext_vector_type(8)));
#define HL(S) __builtin_shufflevector(S, S, 0, 1, 2, 3, 4, 5, 6, 7)
#define HH(S) __builtin_shufflevector(S, S, 8, 9, 10, 11, 12, 13, 14, 15)
#define BH(H) __builtin_bit_cast(bf16x16, H)
  f32x8 l0v = HL(S0), h0v = HH(S0), l1v, h1v = HH(S1);
  bf16x8 va2;
#define EXN(H, i, n) do { _Pragma("unroll") for (int k_ = 0; k_ < n; ++k_) H[i + k_] = __builtin_amdgcn_exp2f(H[i + k_]); PIN(H); } while (0)
#define MM(D, E) do { SBAR(); D = E; PIN(D); SBAR(); } while (0)
#define GAPS 0x142333
  constexpr int G0 = GAPS >> 20 & 15, G1 = G0 + (GAPS >> 16 & 15), G2 = G1 + (GAPS >> 12 & 15), G3 = G2 + (GAPS >> 8 & 15), G4 = G3 + (GAPS >> 4 & 15);
  static_assert(G4 + (GAPS & 15) == 16 && G4 >= 8, "");
#define EXR(Lo, Hi, i, j) do { _Pragma("unroll") for (int k_ = i; k_ < j; ++k_) { if (k_ < 8) Lo[k_ & 7] = __builtin_amdgcn_exp2f(Lo[k_ & 7]); else Hi[k_ & 7] = __builtin_amdgcn_exp2f(Hi[k_ & 7]); if (k_ == 7 && j > 8) PIN(Lo); } \
    if (j > i) { if (j <= 8) PIN(Lo); else PIN(Hi); } } while (0)
#define MH(n, D, E) do { MM(D, E); hook(VP * 12 + n); } while (0)
#undef TILE
#define TILE(TA, TN, VA, VN, VP_, VB) do { constexpr int VP = VP_; \
    SBAR();                                                                EXR(l0v, h0v, 0, G0);  \
    MH(1, O[1], SM32(vb, BH(h1v), O[1]));      vb = RD((TA) + 3072);       EXR(l0v, h0v, G0, G1);  \
    MH(2, L[1], SM16(sel, BH(h1v), L[1]));     VN = RD((TN) + 2048);       EXR(l0v, h0v, G1, G2);  \
    MH(3, S1, MFMA32(ka, q[1][0], negm[1]));   ka = RD(TN);                EXR(l0v, h0v, G2, G3); \
    MH(4, S1, MFMA32(kb, q[1][1], S1));        kb = RD((TN) + 1024);       EXR(l0v, h0v, G3, G4); \
    l1v = HL(S1); h1v = HH(S1); \
    MH(5, O[0], SM32(VA, BH(l0v), O[0]));                                  EXR(l0v, h0v, G4, 16); \
    MH(6, L[0], SM16(sel, BH(l0v), L[0]));     LGKM0(); SBAR();            EXR(l1v, h1v, 0, G0);  \
    MH(7, O[0], SM32(vb, BH(h0v), O[0]));                                  EXR(l1v, h1v, G0, G1);  \
    MH(8, L[0], SM16(sel, BH(h0v), L[0]));                                 EXR(l1v, h1v, G1, G2);  \
    MH(9, S0, MFMA32(ka, q[0][0], negm[0]));                               EXR(l1v, h1v, G2, G3); \
    MH(10, S0, MFMA32(kb, q[0][1], S0));                                   EXR(l1v, h1v, G3, G4); \
    l0v = HL(S0); h0v = HH(S0); \
    MH(11, O[1], SM32(VA, BH(l1v), O[1]));                                 EXR(l1v, h1v, G4, 16); \
    MH(12, L[1], SM16(sel, BH(l1v), L[1])); } while (0)
  LGKM0();
  TS(1);
  auto hook = [&](int n) {
    if (n == HP0) __builtin_amdgcn_s_setprio(0);
    if (n == HP1) asm volatile("s_cmp_eq_u32 %0, 0\n\ts_cbranch_scc1 1f\n\ts_setprio 1\n1:" ::"s"(kh) : "scc");
    if (n == HB) {
      asm volatile("" ::: "memory"); __builtin_amdgcn_s_waitcnt(0x0F70); __builtin_amdgcn_s_barrier(); asm volatile("" ::: "memory");
    }
    if (n == HP0 || n == HP1 || n == HB) SBAR();
  };
  lp cb = la + 98304;
#pragma unroll 1
  for (int c = 65536; c != 65536 - 8 * 32768; c -= 32768) {
    TILE(cb, cb + 4096, va, va2, 0, 0);
    TILE(cb + 4096, cb + 8192, va2, va, 1, 0);
    TILE(cb + 8192, cb + 12288, va, va2, 2, 0);
    const lp nb = la + (c & 98304);
    TILE(cb + 12288, nb, va2, va, 3, 0);
    cb = nb;
  }
  SBAR();
  L[1] = SM16(sel, BH(h1v), L[1]); O[1] = SM32(vb, BH(h1v), O[1]); PIN(L[1]); PIN(O[1]);
  __builtin_amdgcn_s_setprio(0);
  TS(18);
#undef TILE
#undef EXN
#undef MM
#undef RD
}

DI void attn_pass_safe(rsrc_t sws, const bf16x8 q0, const bf16x8 q1, int lane, f32x16& O, f32x4& L) {
  O = f32x16{}; L = f32x4{};
  const bf16x8 sel = attn_sel(lane);
  float m = -INFINITY;
  const int odd = 0xDDDD;
#pragma unroll 1
  for (int t = 0; t < 64; ++t) {
    bf16x8 a[4];
#pragma unroll
    for (int j = 0; j < 4; ++j) a[j] = __builtin_bit_cast(bf16x8, ld16(sws, WS_KV + t * 4096 + j * 1024 + lane * 16));
    f32x16 Sc = {};
    Sc = MFMA32(a[0], q0, Sc); Sc = MFMA32(a[1], q1, Sc);
    const float mn = fmaxf(m, xhalf_max(max16(Sc)));
    const float al = __builtin_amdgcn_exp2f(m - mn);
    m = mn;
#pragma unroll
    for (int r = 0; r < 16; ++r) { O[r] *= al; Sc[r] = __builtin_amdgcn_exp2f(Sc[r] - mn); }
    L *= al;
    O = SM32(a[2], LO(Sc), O); L = SM16(sel, LO(Sc), L);
    O = SM32(a[3], HI(Sc), O); L = SM16(sel, HI(Sc), L);
  }
}

DI void helper_program(unsigned lds0, const unsigned short* __restrict__ X, const unsigned short* __restrict__ W, const unsigned short* __restrict__ E,
                       const unsigned short* __restrict__ woa, const unsigned short* __restrict__ wob, const void* xb, const void* wq, char* __restrict__ ws, rsrc_t sws,
                       const char* pc0, unsigned co, unsigned pm, int type, int rb, int ct, int x, int idx, int lw, int lane, const unsigned (&xv)[8], const unsigned (&wv)[4], int rot, unsigned ldw) {
  unsigned sv = 0, hv = 0; (void)sv; (void)hv;
  __builtin_amdgcn_s_setprio(3);
  u64 e0 = 0;
  unsigned vl = lane * 16;
  typedef AS3 char* lp;
  unsigned l4, p7, pq, pk; lp lr, lz, za, ka, zw;
  auto lane_terms = [&]() __attribute__((always_inline)) {
    l4 = lane * 4; p7 = (lane & 7) * 8; pq = (lane & 1) * 64; pk = (lane >> 2) * 128 + (lane & 3) * 8;
    const unsigned r0 = (lw >> 1) * 32768 + (lw & 1) * 4096, r1 = lw * 16384;
    lr = (lp)(lds0 + vl + (type == 0 ? r0 : r1)); lz = (lp)(lds0 + P1_ZRED + lw * 1024 + l4);
    const int row = lane & 15, kg = lane >> 4;
    const bool on = (((row >> 2) ^ kg) & 1) == 0;
    za = (lp)(lds0 + P2_ZB + ((on && (row & 3) == 0) ? (kg >> 1) * 32 : (on && (row & 3) == 1) ? 128 : 256));
    ka = (lp)(lds0 + P2_KVF + vl); zw = (lp)(lds0 + P2_ZB + lane * 2);
    PIN(vl); PIN(l4); PIN(p7); PIN(pq); PIN(pk); PIN(lr); PIN(lz); PIN(za); PIN(ka); PIN(zw);
  };
  {
    const bool ext = type >= 2;
    const rsrc_t sx = mk_rsrc(X, 2048u * 2048u);
    const rsrc_t sw = mk_rsrc(W, 1024u * ldw);
    const rsrc_t se = mk_rsrc(E, 1024u * 64u);
    const int r16 = lane & 15, kg = lane >> 4;
    unsigned exa = (64 * (ct >> 1) + 16 * lw + r16) * 128 + ((kg ^ (r16 >> 1)) << 4);
    unsigned ewa = P1_EOFF + (8 * kg + (r16 >> 2)) * 64 + (kg & 1) * 32 + (lane & 3) * 8;
    const unsigned ev = vl ^ (lane & 32);
    const unsigned kpv = 2048 + (lw & 1) * 1024 + vl, kpk = (kg & 1) * 1024 + (kg >> 1) * 512 + (lw & 1) * 256 + r16 * 16;
    unsigned kp = WS_KV + (rb * 8 + 2 * (ct >> 1) + (lw >> 1)) * 4096 + (type == 3 ? kpv : kpk);
    f32x4 eacc[2] = {};
    bf16x8 ex[2], ew[2][2];
    auto kvr = [&](unsigned buf) {
      if (ext) {
#pragma unroll
        for (int kk = 0; kk < 2; ++kk) {
          ex[kk] = lds_r128(buf + (exa ^ (kk * 64)));
#pragma unroll
          for (int c = 0; c < 2; ++c) { const unsigned a = buf + (ewa ^ (c * 32)) + kk * 2048; ew[kk][c] = lds_tr2(a, a + 256); }
        }
      }
    };
    auto kvm = [&]() {
      if (ext)
        asm volatile("s_cmp_eq_u32 %8, 2\n\ts_cbranch_scc1 1f\n\t"
                     "v_mfma_f32_16x16x32_bf16 %0, %2, %4, %0\n\tv_mfma_f32_16x16x32_bf16 %1, %2, %5, %1\n\t"
                     "v_mfma_f32_16x16x32_bf16 %0, %3, %6, %0\n\tv_mfma_f32_16x16x32_bf16 %1, %3, %7, %1\n\ts_branch 2f\n"
                     "1:\tv_mfma_f32_16x16x32_bf16 %0, %4, %2, %0\n\tv_mfma_f32_16x16x32_bf16 %1, %5, %2, %1\n\t"
                     "v_mfma_f32_16x16x32_bf16 %0, %6, %3, %0\n\tv_mfma_f32_16x16x32_bf16 %1, %7, %3, %1\n2:"
                     : "+v"(eacc[0]), "+v"(eacc[1]) : "v"(ex[0]), "v"(ex[1]), "v"(ew[0][0]), "v"(ew[0][1]), "v"(ew[1][0]), "v"(ew[1][1]), "s"(type) : "scc");
    };
    auto kvs = [&](unsigned buf) { kvr(buf); kvm(); };
    Ahead ah;
    auto stage = [&](int t0, unsigned buf, bool kv, unsigned kb, int lo = 0, int hi = 12) {
      const int t = (t0 + rot) & 15;
      const unsigned sox = t * 128, sow = t * 64 * ldw;
      if (lo < 4 && hi > 0) {
        if (ext) { set_m0(buf + P1_EOFF + lw * 1024); dma16<0>(ev, se, t * 4096 + lw * 1024); }
        set_m0(buf + P1_WOFF + lw * 4096);
        dma16<0>(wv[0], sw, sow); dma16<1024>(wv[1], sw, sow); dma16<2048>(wv[2], sw, sow); dma16<3072>(wv[3], sw, sow);
      }
      if (lo < 8 && hi > 4) {
        set_m0(buf + lw * 8192);
        dma16<0>(xv[0], sx, sox); dma16<1024>(xv[1], sx, sox); dma16<2048>(xv[2], sx, sox); dma16<3072>(xv[3], sx, sox);
      }
      if (kv) kvr(kb);
      if (lo < 12 && hi > 8) {
        set_m0(buf + lw * 8192 + 4096);
        dma16<0>(xv[4], sx, sox); dma16<1024>(xv[5], sx, sox); dma16<2048>(xv[6], sx, sox); dma16<3072>(xv[7], sx, sox);
      }
      if (kv) kvm();
      if (PD && kv && !ext && t0 + PD < 16) ah.touch(t0 + PD);
    };
    if (H1D == 4) STAMP(512, 11);
    stage(0, lds0, false, 0);
    if (H1D == 4) STAMP(512, 12);
    stage(1, lds0 + P1_STAGE, false, 0, 0, SUP);
    SBAR();
    lane_terms();
    if (!ext) {
      const int i = ((x & 1) * 16 + idx) * 4 + lw, k = (x >> 1) * 20 + i - 165;
      const unsigned o0 = ((rot + 2) * 192 + min(7 * i, 1145)) * 2048 + (x >> 2) * 1024 + 384, o1 = min(7 * (k >> 4), 25) * 2048 + (k & 15) * 128;
      const unsigned o2 = WS_FLG + ((x >> 1) * 4 + min(i - 185, 3)) * 128, o12 = i < 185 ? o1 : o2;
      const char* p12 = i < 185 ? (const char*)E : ws;
      touch7(i < 165 ? (const char*)W : p12, i < 165 ? o0 : o12);
    }
    PIN(exa); PIN(ewa); PIN(kp); PIN(eacc[0]); PIN(eacc[1]); PIN(e0);
    ah = ahead(x, idx, lw, rot, X, xb, wq);
    asm volatile("" : "+s"(ah.p), "+s"(ah.step), "+s"(ah.rot));
    SBAR();
    stage(1, lds0 + P1_STAGE, false, 0, SUP, 12);
    if (H1D == 4) { STAMP(512, 13); if (ext) asm volatile("s_waitcnt vmcnt(13)" ::: "memory"); else asm volatile("s_waitcnt vmcnt(12)" ::: "memory"); STAMP(512, 14); }
    asm volatile("s_cmp_ge_u32 %0, 2\n\ts_waitcnt vmcnt(13)\n\ts_cbranch_scc1 1f\n\ts_waitcnt vmcnt(12)\n1:\ts_barrier" ::"s"(type) : "scc", "memory");
    stage(2, lds0 + 2 * P1_STAGE, true, lds0);
    unsigned buf = lds0;
#pragma unroll 1
    for (int t = 0; t < 13; ++t) {
      asm volatile("s_waitcnt vmcnt(12)\n\ts_barrier" ::: "memory");
      const unsigned nb = (buf == lds0 + 2 * P1_STAGE) ? lds0 : buf + P1_STAGE;
      if (t == 10) asm volatile("global_load_dwordx2 %0, %1, %2 sc1" : "+v"(e0) : "v"(WS_FLG + F_EPOCH * 8), "s"(ws) : "memory");
      stage(t + 3, buf, true, nb);
      buf = nb;
    }
    asm volatile("s_waitcnt vmcnt(12)\n\ts_barrier" ::: "memory");
    if (H1D == 3) STAMP(512, 12);
    PIN(e0);
    if (lw == 3 && !ext) pf_bin(lds0 + P1_BIN, co, pc0);
    if (!ext && (type == 1 || lw < 2)) {
      set_m0(lds0 + P1_PHI + lw * 2048);
      const unsigned so = ((type == 0 ? 2 * ct : 4 * ct) + lw) * 2048;
      dma16<0>(vl, se, so); dma16<1024>(vl, se, so);
    }
    kvs(lds0 + 2 * P1_STAGE);
    if (ext) {
      asm volatile("s_waitcnt vmcnt(0)" ::: "memory");
      meet(lds0 + P1_CNT, lane);
      if (H1D == 3) STAMP(512, 13);
      kvs(lds0);
      asm volatile("s_nop 7" : "+v"(eacc[0]), "+v"(eacc[1]));
      const float c = type == 3 ? 1.f : QSCALE;
      u32x4 v;
#pragma unroll
      for (int i = 0; i < 4; ++i) v[i] = pk2(one(eacc[i >> 1][2 * (i & 1)] * c), one(eacc[i >> 1][2 * (i & 1) + 1] * c));
      if (type == 3) {
#pragma unroll
        for (int i = 0; i < 2; ++i) { auto r = __builtin_amdgcn_permlane16_swap(v[i], v[i + 2], false, false); v[i] = r[0]; v[i + 2] = r[1]; }
      }
      st16(sws, kp, v);
      if (H1D == 3) STAMP(512, 14);
      asm volatile("s_barrier" ::: "memory");
      if (H1D == 2) STAMP(512, 12);
    } else { WAIT_BAR(0); WAIT_BAR(0); }
  }
  const u64 tag = ((u64)(unsigned)__builtin_amdgcn_readfirstlane((unsigned)(e0 >> 32)) << 32 | (unsigned)__builtin_amdgcn_readfirstlane((unsigned)e0))
                  * 0x5851F42D4C957F2Dull + 0x14057B7EF767814Full;
  const bool lead = (lw == 0) && (lane == 0);
  if (type >= 2) {
    const int f = rb * 32 + (ct >> 2) * 16 + (ct & 3);
    if (H1D == 2) STAMP(512, 13);
    asm volatile("s_waitcnt vmcnt(0)" ::: "memory");
    if (H1D == 2) STAMP(512, 14);
    if (arrive(lds0 + P1_CNT + 4, lane) == 3 && lane == 0) post(ws, F_KV + f, tag);
    asm volatile("s_barrier" ::: "memory");
    if (lead) post(ws, F_QB + f, tag);
  }
  auto chunk = [&](int c) {
    const unsigned l = lds0 + ((3 - c) & 3) * 32768 + lw * 8192, so = WS_KV + ((c + x) & 7) * 32768 + lw * 8192;
    dma4k(l, vl, sws, so); dma4k(l + 4096, vl, sws, so + 4096);
  };
  STAMP(512, 10);
  if (lw == 0) {
    const unsigned qo = tile_at(WS_QB, 8 * x + (idx >> 2), 8 * (idx & 3), pm, QS);
    if (PDL && type < 2) __builtin_amdgcn_s_sleep(PDL);
    poll(ws, F_KV + x * 32 + (lane & 4) * 4 + (lane & 3), tag);
    __hip_atomic_store((AS3 unsigned*)(lds0 + P1_GO), 1u, __ATOMIC_RELAXED, __HIP_MEMORY_SCOPE_WORKGROUP);
    if (H1D == 1) STAMP(512, 12);
    if (PDQ && type < 2) __builtin_amdgcn_s_sleep(PDQ);
    poll(ws, F_QB + x * 32 + (idx & 2) * 8 + (idx & 1) * 2 + (lane & 1), tag);
    if (H1D != 4) STAMP(512, 11);
#pragma unroll
    for (int j = 0; j < 4; ++j) dma4k(lds0 + P2_Q + j * 4096, vl, sws, qo + (j >> 1) * HGS + (j & 1) * 4096);
    if (H1D == 1) { asm volatile("s_waitcnt vmcnt(0)" ::: "memory"); STAMP(512, 14); }
  } else {
    while (__hip_atomic_load((AS3 unsigned*)(lds0 + P1_GO), __ATOMIC_RELAXED, __HIP_MEMORY_SCOPE_WORKGROUP) == 0u) __builtin_amdgcn_s_sleep(GSLP);
    asm volatile("" ::: "memory");
#pragma unroll 1
    for (int j = lw - 1; j < 32; j += 3) {
      set_m0(lds0 + 98304 + j * 1024); dma16d<0>(vl, sws, WS_KV + x * 32768 + j * 1024);
      if (type < 2) __builtin_amdgcn_s_sleep(PACE);
    }
  }
  WAIT_BAR(0);
  poll(sws, pk, F_KV, tag);
  chunk(1);
  if (type == 0) {
#pragma unroll
    for (int j = 0; j < 4; ++j) {
      const lp a = lr + j * 1024;
      const f32x4 sm = add4(add4(*(AS3 f32x4*)a, *(AS3 f32x4*)(a + 8192)), add4(*(AS3 f32x4*)(a + 16384), *(AS3 f32x4*)(a + 24576)));
      st16(sws, vl + j * 1024, sm, WS_KVP + ((2 * ct + (lw >> 1)) * 8 + rb) * 8192 + (lw & 1) * 4096);
    }
    if (lw < 2) st4(sws, l4, one(*(AS3 float*)lz + *(AS3 float*)(lz + 256)) + one(*(AS3 float*)(lz + 512) + *(AS3 float*)(lz + 768)), WS_ZP + ((2 * ct + lw) * 8 + rb) * 256);
  }
  if (DEFQ && type == 1) {
    u32x4 v[16];
#pragma unroll
    for (int i = 0; i < 16; ++i) v[i] = *(AS3 u32x4*)(lr + i * 1024);
    const unsigned p = WS_QA + ((rb * 8 + (lw & 1) * 4 + 1) * 32 + ct * 4 + (lw >> 1) * 2) * 4096;
#pragma unroll
    for (int i = 0; i < DQS; ++i) st16(sws, vl + (i & 3) * 1024, v[i], p + (i >> 3) * 262144 + (i & 4) * 1024);
    wait_bar<DQS>();
    chunk(2); chunk(3);
#pragma unroll
    for (int i = DQS; i < 16; ++i) st16(sws, vl + (i & 3) * 1024, v[i], p + (i >> 3) * 262144 + (i & 4) * 1024);
    asm volatile("s_waitcnt vmcnt(0)" ::: "memory");
  } else { WAIT_BAR(0); chunk(2); chunk(3); }
  if (lead && type < (DEFQ ? 1 : 2)) post(ws, type == 0 ? F_K + ct * 8 + rb : F_Q + rb * 8 + ct, tag);
  const int br = !(x & 1), rb3 = 4 * ((x >> 1) ^ XP) + (idx & 3), cb = idx >> 2;
  const unsigned short* wo = br ? wob : woa;
  unsigned pfs = 0;
  auto warm_wo = [&]() {
    pf_line(pfs, (const char*)wo + idx * 65536 + lw * 16384 + lane * 128);
    pf_line(pfs, (const char*)wo + idx * 65536 + lw * 16384 + 8192 + lane * 128);
  };
  if (lw == 1) *(AS3 unsigned*)(lds0 + P2_ZB + 128 + lane * 4) = lane < 32 ? 0x3f803f80u : 0u;
  WAIT_BAR(8);
  if (DEFQ && lead && type == 1) post(ws, F_Q + rb * 8 + ct, tag);
  if (br) {
    const int head = 4 * x + (idx & 7), tp = idx >> 3;
    poll(sws, p7, F_K + (head >> 1) * 8, tag);
    if (!H1D) STAMP(512, 12);
    chunk(4);
    u32x4 kvp[2][8], fq[4][4];
    float zs[8];
    auto ldq = [&](int e) {
#pragma unroll
      for (int j = 0; j < 4; ++j) fq[e][j] = ld16(sws, vl + j * 1024, WS_QA + ((16 * tp + 4 * lw + e) * 32 + head) * 4096);
    };
#pragma unroll
    for (int e = 0; e < 2; ++e)
#pragma unroll
      for (int p = 0; p < 8; ++p) kvp[e][p] = ld16(sws, vl + e * 1024, WS_KVP + (head * 8 + p) * 8192 + lw * 2048);
#pragma unroll
    for (int p = 0; p < 8; ++p) zs[p] = ld4(sws, l4 + p * 256, WS_ZP + head * 2048);
    asm volatile("s_barrier" ::: "memory");
    u32x4 kf;
#pragma unroll
    for (int e = 0; e < 2; ++e) {
#pragma unroll
      for (int p = 0; p < 8; ++p) PIN(kvp[e][p]);
#define F4(v) __builtin_bit_cast(f32x4, v)
      const f32x4 sm = add4(add4(add4(F4(kvp[e][0]), F4(kvp[e][1])), add4(F4(kvp[e][2]), F4(kvp[e][3]))), add4(add4(F4(kvp[e][4]), F4(kvp[e][5])), add4(F4(kvp[e][6]), F4(kvp[e][7]))));
#undef F4
      kf[2 * e] = pk2(sm[0], sm[1]); kf[2 * e + 1] = pk2(sm[2], sm[3]);
    }
    *(AS3 u32x4*)(ka + lw * 1024) = kf;
    {
#pragma unroll
      for (int p = 0; p < 8; ++p) PIN(zs[p]);
      const float z = one(one(zs[0] + zs[1]) + one(zs[2] + zs[3])) + one(one(zs[4] + zs[5]) + one(zs[6] + zs[7]));
      if (lw == 0) *(AS3 unsigned short*)zw = (unsigned short)pk2(z, z);
    }
    poll(sws, pq, F_Q + 16 * tp + (head >> 2), tag);
    chunk(5);
    ldq(0); ldq(1); ldq(2); ldq(3);
    auto otile = [&](int e) {
#define BF(v) __builtin_bit_cast(bf16x8, v)
#define RL(p) (*(AS3 bf16x8*)(p))
      f32x16 np = MFMA32(RL(ka), BF(fq[e][0]), f32x16{});        f32x4 dp = MFMA16(RL(za), BF(fq[e][0]), f32x4{});
      np = MFMA32(RL(ka + 1024), BF(fq[e][1]), np);              dp = MFMA16(RL(za + 16), BF(fq[e][1]), dp);
      f32x16 nm = MFMA32(RL(ka + 2048), BF(fq[e][2]), f32x16{}); f32x4 dm = MFMA16(RL(za + 64), BF(fq[e][2]), f32x4{});
      nm = MFMA32(RL(ka + 3072), BF(fq[e][3]), nm);              dm = MFMA16(RL(za + 80), BF(fq[e][3]), dm);
#undef BF
#undef RL
      const float a = __builtin_amdgcn_rcpf(dp[1]), b = __builtin_amdgcn_rcpf(dm[1]);
      const float r = __builtin_amdgcn_rcpf(one(dp[0] * a) + one(dm[0] * b) + 1e-6f), ar = one(a * r), br = one(b * r);
      f32x16 o;
#pragma unroll
      for (int i = 0; i < 16; ++i) o[i] = one(__builtin_fmaf(nm[i], br, one(np[i] * ar)));
      const unsigned p = tile_at(WS_OA, 16 * tp + 4 * lw + e, head, pm, 0);
      st16(sws, vl, pack8<0>(o), p); st16(sws, vl + 1024, pack8<1>(o), p);
    };
    WAIT_BAR(24);
#pragma unroll
    for (int j = 0; j < 4; ++j) { PIN(fq[0][j]); PIN(fq[1][j]); }
    chunk(6);
    otile(0); otile(1);
    WAIT_BAR(12);
#pragma unroll
    for (int j = 0; j < 4; ++j) { PIN(fq[2][j]); PIN(fq[3][j]); }
    chunk(7);
    otile(2); otile(3);
    WAIT_BAR(0);
    if (lead) post(ws, F_OA + tp * 32 + head, tag);
    warm_wo();
  } else {
    chunk(4); warm_wo(); WAIT_BAR(10);
    chunk(5); WAIT_BAR(10);
    chunk(6); WAIT_BAR(8);
    chunk(7);
    if (blockIdx.x == 1 && lw == 1) {
      poll(ws, lane, tag); poll(ws, 64 + lane, tag); poll(ws, 128 + lane, tag);
      if (lane == 0) post(ws, F_EPOCH, tag);
    }
    WAIT_BAR(8);
  }
  const rsrc_t sw = mk_rsrc(wo, 1024u * 2048u);
  unsigned w3[4];
#pragma unroll
  for (int j = 0; j < 4; ++j) {
    const int k = 4 * (4 * lw + j) + (lane >> 4), c = (lane & 15) ^ ((lane >> 4) << 2) ^ ((j & 1) << 1);
    w3[j] = k * 2048 + (cb * 128 + c * 8) * 2 - j * 1024;
  }
  const unsigned osrc = tile_at(br ? WS_OB : WS_OA, rb3 * 4 + lw, 0, pm, 0);
  unsigned fpi = WS_FLG + (F_OB + rb3 * 16 + (lane & 15)) * 8;
  PIN(fpi);
  auto wst = [&](int t) {
    const unsigned sow = t * 64 * 2048;
    set_m0(lds0 + p3_wslot(t) + lw * 4096);
    dma16<0>(w3[0], sw, sow); dma16<1024>(w3[1], sw, sow); dma16<2048>(w3[2], sw, sow); dma16<3072>(w3[3], sw, sow);
  };
  auto ost = [&](int t) { dma4k(lds0 + P3_O + (t % NO) * 16384 + lw * 4096, vl, sws, osrc + (t >> 1) * HGS + (t & 1) * 4096); };
  wst(0); wst(1); wst(2); wst(3);
  if (br) asm volatile("s_barrier" ::: "memory"); else WAIT_BAR(16);
  wst(4); wst(5);
  if (!br && lw == 0) poll(ws, F_OA + (rb3 >> 2) * 32 + (lane & 31), tag);
  asm volatile("s_barrier" ::: "memory");
  if (lw == 1 && lane == 0) *(AS3 u32x4*)(lds0 + P2_TAG) = u32x4{(unsigned)tag, (unsigned)(tag >> 32), 0u, 0u};
  WAIT_BAR(0);
  if (!H1D) STAMP(512, 13);
  if (br && lw == 0) {
#pragma unroll 1
    for (int it = 0; it < (1 << 17); ++it) {
      u64 v;
      asm volatile("buffer_load_dwordx2 %0, %1, %2, 0 offen sc1\n\ts_waitcnt vmcnt(0)" : "=v"(v) : "v"(fpi), "s"(sws) : "memory");
      const unsigned m = (unsigned)__ballot(v == tag);
      if (((m | m >> 16) & 0xffffu) == 0xffffu) break;
      __builtin_amdgcn_s_sleep(2);
    }
  }
  asm volatile("s_barrier" ::: "memory");
  if (!H1D) STAMP(512, 14);
#pragma unroll
  for (int t = 0; t < NF; ++t) ost(t);
  wait_bar<4 * (NF - 1)>();
#pragma unroll
  for (int t = NF; t < NO; ++t) ost(t);
  PIN(pfs);
#define RND(t) do { wait_bar<p3_inflight(t)>(); if (t + NW < 16) wst(t + NW); if (t + NO < 16) ost(t + NO); } while (0)
  RND(0); RND(1); RND(2); RND(3); RND(4); RND(5); RND(6); RND(7); RND(8); RND(9); RND(10); RND(11); RND(12); RND(13); RND(14);
#undef RND
  if (lw == 0) STOUT(10, 15);

}

#define KAL 4096
__global__ __launch_bounds__(768, 3) __attribute__((aligned(KAL))) void fused_fwd(const void*, const void*, const void*, const void*, const void*, const void*,
                                                                                  const void*, const void*, const void*, void*, void*) {
  const unsigned pm = 0;
  const int tid = threadIdx.x, lane = tid & 63;
  unsigned sv = 0; (void)sv;
  STAMP(0, 0);
  typedef u64 u64x8 __attribute__((ext_vector_type(8)));
  typedef u64 u64x2 __attribute__((ext_vector_type(2)));
  u64x8 a0; u64x2 a1;
  int b = blockIdx.x;
  const int w0 = __builtin_amdgcn_readfirstlane(tid >> 6), w = w0 < 4 ? w0 + 8 : w0 - 4;
  asm volatile("s_cmp_ge_u32 %4, 4\n\ts_cbranch_scc1 1f\n\ts_load_dwordx16 %0, %3, 0x0\n\ts_load_dwordx4 %1, %3, 0x40\n1:"
               : "=&s"(a0), "=&s"(a1), "+s"(b) : "s"(__builtin_amdgcn_kernarg_segment_ptr()), "s"(w0) : "scc");
  SBAR();
  __shared__ __attribute__((aligned(1024))) char smem[LDS_BYTES];
  const unsigned lds0 = (unsigned)(uintptr_t)(AS3 char*)smem;
  const char* pc0; unsigned clen;
  asm volatile("s_getpc_b64 %0\n.Lfw_head:\n\ts_mov_b32 %1, .Lfw_tail-.Lfw_head" : "=s"(pc0), "=s"(clen));
  unsigned co = (((b >> 3) & 7) * 40 + lane) * 128;
  co = ((lane < 40) & (co < clen)) ? co : 0u;
  asm volatile("s_cmp_lg_u32 %0, 4\n\ts_cbranch_scc1 1f\n\ts_mov_b32 m0, %1\n\ts_nop 0\n\tglobal_load_lds_dword %2, %3\n1:" ::"s"(w0), "s"(lds0 + P1_BIN), "v"(co), "s"(pc0) : "scc", "memory");
#define PFB 0
#define PFK 1
  const int x = b & 7, idx = b >> 3;
  const int bxr = XB ? (x >> 2) ^ 1 : (x >> 1) & 1, bxc = XB ? (x >> 1) & 1 : x >> 2;
  const bool bb = (idx >= 16) & !(x & 1);
  const int axr = (x >> 1) & 1, axc = x >> 2, rb = 4 * (bb ? bxr : axr) + (idx & 3);
  const int ct0 = 8 * axc + 4 * (x & 1) + (idx >> 2), ct1 = 4 * (bb ? bxc : axc) + ((idx >> 2) & 3), ct = idx < 16 ? ct0 : ct1;
  const int ty2 = 2 + (ct & 1), ty1 = (x & 1) ? 1 : ty2, type_ = idx < 16 ? 0 : ty1;
  const int rota = RTA >> (4 * x), rotb = b_rot(x);
  int rot = (bb ? rotb : rota) & 15, type = type_, ldw = bb ? 2048 : 6144;
  asm volatile("" : "+s"(rot), "+s"(type), "+s"(ldw));
  typedef const unsigned short* cp;
  const bool more = NL == 7 && PD > 2 && w0 == 7;
  const int job = more ? 0 : w0 - 5 + (type & (w0 >> 1) & 1);
  if (__builtin_expect(((unsigned)(w0 - 5) <= (unsigned)(x & 1) || more) && type < 2, 1)) {
    const int jb = (x & 1) * 16 + idx, a1 = jb < JB2 ? 40 : 0, a2 = jb < JB1 ? 32 : a1, a3 = 48 + (idx & 8), a4 = job == 1 ? a3 : 8, l = NL == 4 ? lane & 15 : min(lane & 31, 4 * NL - 1), q = l / NL;
    const char* p;
    asm volatile("s_load_dwordx2 %0, %1, %2" : "=&s"(p) : "s"(__builtin_amdgcn_kernarg_segment_ptr()), "s"(job == 0 ? a2 : a4));
    const Ahead h = ahead(x, idx, 0, rot, nullptr, nullptr, nullptr);
    const unsigned o0 = ahead_off(x, jb, q) + (l - NL * q) * 2048 + ((2 + (NL == 4 ? min(lane >> 4, PD - 1) : min(w0 - 5 + (lane >> 5), PD - 1)) + h.rot) & 15) * h.step;
    const unsigned o1 = 8192 + (((idx & 7) * 4 + (x >> 1)) * 14 + min(lane, 13)) * 128, o2 = ((rot + 2) * 64 + (idx & 15) * 24 + min(lane, 23)) * 6144 + (x >> 2) * 1024 + 256, o12 = job == 1 ? o1 : o2;
    unsigned o = job == 0 ? o0 : o12;
    asm volatile("s_waitcnt lgkmcnt(0)" : "+s"(p), "+v"(o));
    pf_bin(lds0 + P1_BIN, o, p);
  }
  if (__builtin_expect(w >= 8, 1)) {
  unsigned xv[8], wd[4];
  {
    const unsigned ldw = bb ? 2048u : 6144u, wb0 = 2048 + ct * 128, wb1 = ct * 256, wb = idx < 16 ? wb0 : wb1, m2 = idx < 16 ? 2048 : 64, m3 = idx < 16 ? 64 : 128;
    const unsigned x0 = (rb * 256 + 64 * w0 + (lane >> 3)) * 2048 + (((lane & 7) ^ (lane >> 4)) << 4), x1 = x0 ^ 64;
#pragma unroll
    for (int j = 0; j < 8; ++j) { xv[j] = (j & 1 ? x1 : x0) + j * 16384 - (j & 3) * 1024; PIN(xv[j]); }
#pragma unroll
    for (int j = 0; j < 4; ++j) {
      const int k = 4 * (4 * w0 + j) + (lane >> 4), c = (lane & 15) ^ ((lane >> 4) << 2) ^ (j & 2);
      wd[j] = k * ldw + (c & 3) * 16 + ((c >> 2) & 1) * m2 + (c >> 3) * m3 + wb - j * 1024; PIN(wd[j]);
    }
  }
    SBAR();
    asm volatile("s_waitcnt lgkmcnt(0)" : "+s"(a0), "+s"(a1));
    char* const ws = (char*)a1[1];
    const rsrc_t sws = mk_rsrc(ws, WS_END);
    const cp xa = (cp)a0[0], wqkv = (cp)a0[1], wphi = (cp)a0[2], woa = (cp)a0[3], xb = (cp)a0[4], wq = (cp)a0[5], wk = (cp)a0[6], wv = (cp)a0[7], wob = (cp)a1[0];
    const unsigned short* e2 = type == 2 ? wk : wphi;
    helper_program(lds0, type >= 2 ? xb : xa, type >= 2 ? wq : wqkv, type == 3 ? wv : e2, woa, wob, xb, wq, ws, sws, pc0, co, pm, type, rb, ct, x, idx, w0, lane, xv, wd, rot, ldw);
    asm volatile("s_endpgm" ::: "memory");
  }
  char* ws; rsrc_t sws; u64x2 a2;
  auto first = [&]() {
    asm volatile("s_load_dwordx4 %0, %1, 0x48\n\ts_waitcnt lgkmcnt(0)" : "=&s"(a2) : "s"(__builtin_amdgcn_kernarg_segment_ptr()));
    ws = (char*)a2[0]; sws = mk_rsrc(ws, WS_END);
  };
  Attn at;
  auto p2_init = [&]() { attn_init(at, lds0, w, lane); };
  if (type < 2) {
    if (type == 1) p1_compute<1>(lds0, ws, sws, pm, rb, ct, w, lane, first, p2_init SVA);
    else           p1_compute<0>(lds0, ws, sws, pm, rb, ct, w, lane, first, p2_init SVA);
  } else           p1_compute<2>(lds0, ws, sws, pm, rb, ct, w, lane, first, p2_init SVA);
  unsigned short* const out = (unsigned short*)a2[1];

  const int kh = w >> 2, h = lane >> 5;
  {
    bf16x8 q[2][2];
    f32x16 (&O)[2] = at.O; f32x4 (&L)[2] = at.L;
    unsigned tv = 0;
    attn_pass_fast(lds0, at, q, kh, lane, tv SVA);
    TSOUT();
    if (H1D != 4) STAMP(0, 5);
    const unsigned xs = lds0 + P2_XO + w * 4096 + lane * 16;
    *(AS3 float*)(lds0 + P2_XL + w * 256 + lane * 4) = L[0][0];
#pragma unroll
    for (int g = 0; g < 4; ++g) *(AS3 f32x4*)(xs + g * 1024) = f32x4{O[0][4 * g], O[0][4 * g + 1], O[0][4 * g + 2], O[0][4 * g + 3]};
    unsigned xr = lds0 + P2_XO + (w ^ 4) * 4096 + lane * 16, xl = lds0 + P2_XL + (w ^ 4) * 256 + lane * 4;
    unsigned p = tile_at(WS_OB, 8 * x + (idx >> 2), 8 * (idx & 3) + 2 * (w & 3) + kh, pm, 0) + lane * 16;
    PIN(xr); PIN(xl); PIN(p);
    LDS_BAR();
    const float lo = *(AS3 float*)xl;
    const u32x2 tg = *(AS3 u32x2*)(lds0 + P2_TAG);
    f32x4 o[4];
#pragma unroll
    for (int g = 0; g < 4; ++g) o[g] = *(AS3 f32x4*)(xr + g * 1024);
    SBAR();
    const float l = xhalf_sum(L[1][0] + lo);
#pragma unroll
    for (int i = 0; i < 16; ++i) O[1][i] += o[i >> 2][i & 3];
    auto out_b = [&](float den) { const f32x16 n = O[1] * __builtin_amdgcn_rcpf(den); st16(sws, p, pack8<0>(n)); st16(sws, p + 1024, pack8<1>(n)); };
    out_b(l);
    SBAR();
    bool bad = !(l < 1.8446744e19f && l > 8.6736174e-19f);
#pragma unroll
    for (int i = 0; i < 16; ++i) bad |= !(fabsf(O[1][i]) < 3.0e38f);
    if (__builtin_expect(__any(bad), 0)) {
      attn_pass_safe(sws, q[1][0], q[1][1], lane, O[1], L[1]);
      out_b(xhalf_sum(L[1][0]));
    }
    asm volatile("s_waitcnt vmcnt(0)" ::: "memory");
    if (arrive(lds0 + P2_TAG + 8, lane) == 7 && lane < 1) st8(sws, WS_FLG + (F_OB + x * 32 + idx) * 8 + lane * 8192, tg);
    if (H1D != 4) STAMP(0, 6);
    asm volatile("s_barrier" ::: "memory");
    SBAR();
  }
  const int br = !(x & 1), rb3 = 4 * ((x >> 1) ^ XP) + (idx & 3), cb = idx >> 2;
  const int wm = w & 1, wn = (w >> 1) & 1;
  const int r16 = lane & 15, kg = lane >> 4, q4 = r16 >> 2, p4 = lane & 3;
  typedef const AS3 char* lp;
  lp oa[2], wa[2][4];
#pragma unroll
  for (int ms = 0; ms < 2; ++ms) oa[ms] = (lp)(lds0 + P3_O + ((wm * 8192 + kh * (4096 + 2048) + kg * 512 + r16 * 16) ^ (ms * 4096)));
#pragma unroll
  for (int nt = 0; nt < 4; ++nt) {
    wa[0][nt] = (lp)(lds0 + P3_W + (kh * 32 + 16 * h + 4 * (kg & 1) + q4) * 256 + (((wn * 4 + nt) ^ (2 * q4 + (kg & 1))) << 5) + p4 * 8);
    wa[1][nt] = wa[0][nt] + 49152; PIN(wa[0][nt]); PIN(wa[1][nt]);
  }
  f32x4 acc[4][4];
  bf16x8 xf[2][4], wfr[2][4];
  auto rd = [&](int t, int i) {
    constexpr int ord[8] = {0, 4, 1, 5, 2, 3, 6, 7};
    const int f = ord[i];
    if (f < 4) xf[t & 1][f] = *(const AS3 bf16x8*)(oa[f >> 1] + (t % NO) * 16384 + (f & 1) * 256);
    else       wfr[t & 1][f - 4] = lds_trp(wa[(p3_wslot(t) - P3_W) / 49152][f - 4] + (p3_wslot(t) - P3_W) % 49152, 2048);
  };
  unsigned xs = lds0 + 65536 + w * 8192 + lane * 16, xr = xs ^ 32768;
  unsigned iw = lds0 + w * 4096 + r16 * 128 + ((h ^ (r16 & 7)) << 4) + (kg & 1) * 8, ir = lds0 + w * 4096 + (lane >> 3) * 128 + (((lane ^ (lane >> 3)) & 7) << 4);
  unsigned op = (rb3 * 128 + wm * 64 + kh * 32 + (lane >> 3)) * 4096 + br * 2048 + cb * 256 + wn * 128 + (lane & 7) * 16;
  const rsrc_t so = mk_rsrc(out, 2048u * 4096u);
  PIN(oa[0]); PIN(oa[1]); PIN(xs); PIN(xr); PIN(op); PIN(iw); PIN(ir);
  rd(0, 1); rd(0, 3); rd(0, 6); rd(0, 7);
  code_at<AL4>();
  LDS_BAR();
  if (H1D != 4) STAMP(0, 7);
  rd(0, 0); rd(0, 2); rd(0, 4); rd(0, 5);
#pragma unroll
  for (int t = 0; t < 14; ++t) {
    if (t) LDS_BAR();
#pragma unroll
    for (int i = 0; i < 16; ++i) {
      const int mt = MT_OF(i), nt = NT_OF(i);
      SBAR();
      if (t == 0) mul16(acc[mt][nt], wfr[0][nt], xf[0][mt]); else mac16(acc[mt][nt], wfr[t & 1][nt], xf[t & 1][mt]);
      SBAR();
      if (t == 0) { if (i == 5) LDS_BAR(); if (i >= 5 && i < 13) rd(1, i - 5); }
      else if (RD_AT(i) >= 0) rd(t + 1, RD_AT(i));
    }
    SBAR();
  }
  LDS_BAR();
#pragma unroll
  for (int i = 0; i < 32; ++i) {
    constexpr int ord[8] = {4, 1, 5, 3, 6, 7, 0, 2};
    const int j = ((i & 7) + 4 + (i >> 4) * 8) & 15, mt = MT_OF(j), nt = NT_OF(j), st = (i >> 3) & 1;
    SBAR(); mac16(acc[mt][nt], wfr[st][nt], xf[st][mt]); SBAR();
    if (i < 8) rd(15, ord[i]);
    if (i >= 16 && !(i & 1)) { const int ms = MT_OF(i / 2 - 4), ns = NT_OF(i / 2 - 4); *(AS3 f32x4*)(xs + ((ms - 2) * 4 + ns) * 1024) = acc[ms][ns]; }
  }
  SBAR();
  STAMP(0, 8);
  LDS_BAR();
  f32x4 o[8];
#pragma unroll
  for (int i = 0; i < 8; ++i) o[i] = *(AS3 f32x4*)(xr + i * 1024);
  SBAR();
  u32x4 v[4];
#pragma unroll
  for (int i = 0; i < 8; ++i) {
    const f32x4 a = acc[i >> 2][i & 3] + o[i];
    *(AS3 u32x2*)((iw ^ ((i & 3) << 5)) + (i >> 2) * 2048) = u32x2{pk2(a[0], a[1]), pk2(a[2], a[3])};
    if ((i & 3) == 3) {
      asm volatile("" ::: "memory");
      v[i >> 1 & 2] = *(AS3 u32x4*)(ir + (i >> 2) * 2048); v[(i >> 1 & 2) + 1] = *(AS3 u32x4*)(ir + (i >> 2) * 2048 + 1024);
      SBAR();
    }
  }
#pragma unroll
  for (int i = 0; i < 4; ++i) st16(so, op + i * 32768, v[i]);
  STAMP(0, 9);
  asm volatile(".Lfw_tail:");
}

extern "C" void kernel_launch(void* const* d_in, const int* in_sizes, int n_in, void* d_out, int out_size, void* d_ws, size_t ws_size,
                              hipStream_t stream) {
  (void)in_sizes; (void)n_in; (void)out_size; (void)ws_size;
  typedef const unsigned short* cp;
  fused_fwd<<<256, 768, 0, stream>>>((cp)d_in[0], (cp)d_in[1], (cp)d_in[2], (cp)d_in[3], (cp)d_in[4], (cp)d_in[5], (cp)d_in[6], (cp)d_in[7],
                                     (cp)d_in[8], (char*)d_ws, (unsigned short*)d_out);
}
```
